```python
import torch

def _softmax(x, axis=-1):
    return torch.softmax(x, dim=axis)

def _gelu(x):
    return torch.nn.functional.gelu(x, approximate='tanh')

def _silu(x):
    return torch.nn.functional.silu(x)

def _sigmoid(x):
    return torch.sigmoid(x)

def _layernorm(x, axis=-1, eps=1e-05):
    m = x.mean(dim=axis, keepdim=True)
    v = x.var(dim=axis, keepdim=True, unbiased=False)
    return (x - m) / torch.sqrt(v + eps)

def _scatter_add(tgt, idx, src, axis=0):
    return tgt.clone().index_add_(axis, idx, src)

def _segment_sum(src, seg, n):
    out = torch.zeros((n,) + src.shape[1:], dtype=src.dtype)
    return out.index_add_(0, seg, src)

def _pad(x, pw):
    flat = tuple((v for pair in reversed(pw) for v in pair))
    return torch.nn.functional.pad(x, flat)
kernel_name = 'parallel_sum__memory_aug_recurrent_mem_tr__dense_activation_squared_relu__prompt'

def setup_inputs():

    _w = lambda a: (lambda t: t.to(torch.bfloat16) if t.is_floating_point() else t)(torch.as_tensor(a))

    def _A_setup():
        B, S, Dm = (1, 2048, 448)
        H, Dh = (16, 256)
        M, seg = (8, 32)
        seg = min(seg, S)
        S = S // seg * seg
        return {'x': _w((lambda *s: torch.randn(*s, dtype=torch.bfloat16) * 0.05)(B, S, Dm)), 'mem0': _w((lambda *s: torch.randn(*s, dtype=torch.bfloat16) * 1.6)(B, M, Dm)), 'Wq': _w((lambda *s: torch.randn(*s, dtype=torch.bfloat16) * 0.8)(Dm, H, Dh) * Dm ** (-0.5)), 'Wk': _w((lambda *s: torch.randn(*s, dtype=torch.bfloat16) * 0.8)(Dm, H, Dh) * Dm ** (-0.5)), 'Wv': _w((lambda *s: torch.randn(*s, dtype=torch.bfloat16) * 1.6)(Dm, H, Dh) * Dm ** (-0.5)), 'Wo': _w((lambda *s: torch.randn(*s, dtype=torch.bfloat16) * 1.6)(H, Dh, Dm) * (H * Dh) ** (-0.5)), 'seg_len': seg}
    _A_in = _A_setup()

    def _B_setup():
        B, S, D, F = (1, 2048, 448, 1792)
        return {'x': _w((lambda *s: torch.randn(*s, dtype=torch.bfloat16) * 0.05)(B, S, D)), 'w1': _w((lambda *s: torch.randn(*s, dtype=torch.bfloat16) * 0.05)(D, F)), 'b1': _w((lambda *s: torch.randn(*s, dtype=torch.bfloat16) * 0.4)(F)), 'w2': _w((lambda *s: torch.randn(*s, dtype=torch.bfloat16) * 0.05)(F, D)), 'b2': _w((lambda *s: torch.randn(*s, dtype=torch.bfloat16) * 0.8)(D))}
    _B_in = _B_setup()
    return {'A_x': _A_in['x'], 'A_mem0': _A_in['mem0'], 'A_Wq': _A_in['Wq'], 'A_Wk': _A_in['Wk'], 'A_Wv': _A_in['Wv'], 'A_Wo': _A_in['Wo'], 'A_seg_len': _A_in['seg_len'], 'B_x': _B_in['x'], 'B_w1': _B_in['w1'], 'B_b1': _B_in['b1'], 'B_w2': _B_in['w2'], 'B_b2': _B_in['b2']}

def reference(A_x, A_mem0, A_Wq, A_Wk, A_Wv, A_Wo, A_seg_len, B_x, B_w1, B_b1, B_w2, B_b2):

    def _A_ref(x, mem0, Wq, Wk, Wv, Wo, seg_len):
        B, S, Dm = x.shape
        H, Dh = (Wq.shape[1], Wq.shape[2])
        M = mem0.shape[1]
        scale = Dh ** (-0.5)
        n_seg = S // seg_len
        L = M + seg_len
        idx = torch.arange(L)
        mask = (idx[:, None] < M) | (idx[:, None] >= idx[None, :])
        mem = mem0
        outs = []
        for s in range(n_seg):
            xs = x[:, s * seg_len:(s + 1) * seg_len, :].clone()
            h = torch.concatenate([mem, xs], axis=1)
            q = torch.einsum('btd,dhe->bhte', h, Wq)
            k = torch.einsum('btd,dhe->bhte', h, Wk)
            v = torch.einsum('btd,dhe->bhte', h, Wv)
            scores = torch.einsum('bhqe,bhke->bhqk', q, k) * scale
            scores = torch.where(mask[None, None], scores, -1000000000.0)
            p = _softmax(scores, axis=-1)
            o = torch.einsum('bhqk,bhke->bhqe', p, v)
            o = torch.einsum('bhte,hed->btd', o, Wo)
            mem = o[:, :M, :].clone()
            outs.append(o[:, M:, :])
        return torch.concatenate(outs, axis=1)

    def _B_ref(x, w1, b1, w2, b2):
        h = torch.einsum('bsd,df->bsf', x, w1) + b1
        zero = torch.zeros_like(h)
        r = torch.where(h > zero, h, zero)
        h = r * r
        out = torch.einsum('bsf,fd->bsd', h, w2) + b2
        return out
    x = A_x
    xn = _layernorm(x, axis=-1)
    return x + _A_ref(xn, A_mem0, A_Wq, A_Wk, A_Wv, A_Wo, A_seg_len) + _B_ref(xn, B_w1, B_b1, B_w2, B_b2)
```

```cpp
#include <hip/hip_runtime.h>
#include <hip/hip_bf16.h>
#include <stdint.h>

typedef __bf16 bf2 __attribute__((ext_vector_type(2)));
typedef __bf16 bf8 __attribute__((ext_vector_type(8)));
typedef __bf16 bf16x __attribute__((ext_vector_type(16)));
typedef short s4v __attribute__((ext_vector_type(4)));
typedef float f2v __attribute__((ext_vector_type(2)));
typedef float f4v __attribute__((ext_vector_type(4)));
typedef float f16v __attribute__((ext_vector_type(16)));
typedef unsigned u2v __attribute__((ext_vector_type(2)));
typedef unsigned u4v __attribute__((ext_vector_type(4)));
typedef unsigned u8v __attribute__((ext_vector_type(8)));
#define LDS __attribute__((address_space(3)))
#define GLB __attribute__((address_space(1)))
#define DEV __device__ __forceinline__

constexpr int DM = 448, NH = 16, DH = 256, NSEG = 64, FF = 1792;
constexpr unsigned SENT = 0xFFFFFFFFu;

constexpr size_t MB = 1u << 20;
constexpr size_t O_HB = 0;
constexpr size_t O_FL = 4 * MB;
constexpr size_t O_SEL = O_FL + 256 * 1024;
constexpr size_t O_XN = 5 * MB;
constexpr size_t O_WA = 7 * MB;
constexpr size_t O_WB = 14 * MB;
constexpr size_t O_W1 = 21 * MB;
constexpr size_t O_W2 = 23 * MB;
constexpr size_t O_G = 25 * MB;
constexpr size_t O_U = 55 * MB;
constexpr size_t O_ST = 85 * MB;
constexpr size_t O_SM = 87 * MB;
constexpr size_t O_YG = 100 * MB;
constexpr size_t O_F = 111 * MB;
constexpr size_t O_END = 115 * MB;

DEV unsigned pk2(float a, float b) { f2v v = {a, b}; return __builtin_bit_cast(unsigned, __builtin_convertvector(v, bf2)); }
DEV float bflo(unsigned u) { return __builtin_bit_cast(float, u << 16); }
DEV float bfhi(unsigned u) { return __builtin_bit_cast(float, u & 0xffff0000u); }
DEV float rbf(float a) { return bflo(pk2(a, 0.f) & 0xffffu); }
DEV bf8 asbf8(u4v v) { return __builtin_bit_cast(bf8, v); }
DEV u4v lds_r16(unsigned a) { return *(LDS u4v*)(size_t)a; }
DEV void lds_w16(unsigned a, u4v v) { *(LDS u4v*)(size_t)a = v; }
DEV u2v lds_tr(unsigned a) { return __builtin_bit_cast(u2v, __builtin_amdgcn_ds_read_tr16_b64_v4i16((LDS s4v*)(size_t)a)); }
DEV unsigned rdtime() { unsigned long long t; asm volatile("s_memtime %0\n\ts_waitcnt lgkmcnt(0)" : "=s"(t) :: "memory"); return (unsigned)t; }
DEV int swap13(int x) { return (x & 5) | ((x >> 2) & 2) | ((x << 2) & 8); }
DEV void dma16(const void* g, unsigned ldsaddr) {
  unsigned keep;
  asm volatile("s_mov_b32 %0, m0\n\ts_mov_b32 m0, %2\n\ts_nop 0\n\tglobal_load_lds_dwordx4 %1, off\n\ts_mov_b32 m0, %0" : "=&s"(keep) : "v"(g), "s"(ldsaddr) : "memory");
}
DEV void dma16c(const void* g, unsigned ldsaddr) {
  unsigned keep;
  asm volatile("s_mov_b32 %0, m0\n\ts_mov_b32 m0, %2\n\ts_nop 0\n\tglobal_load_lds_dwordx4 %1, off sc1\n\ts_mov_b32 m0, %0" : "=&s"(keep) : "v"(g), "s"(ldsaddr) : "memory");
}
DEV void vm0() { asm volatile("s_waitcnt vmcnt(0)" ::: "memory"); }
DEV void bar() { asm volatile("s_waitcnt lgkmcnt(0)\n\ts_barrier" ::: "memory"); }

DEV void stwt16(void* p, u4v v) { asm volatile("global_store_dwordx4 %0, %1, off sc1\n\ts_nop 1" :: "v"(p), "v"(v) : "memory"); }
DEV float wavesum(float v) {
  v += __builtin_bit_cast(float, __builtin_amdgcn_update_dpp(0, __builtin_bit_cast(int, v), 0xb1, 0xf, 0xf, true));
  v += __builtin_bit_cast(float, __builtin_amdgcn_update_dpp(0, __builtin_bit_cast(int, v), 0x4e, 0xf, 0xf, true));
  v += __builtin_bit_cast(float, __builtin_amdgcn_update_dpp(0, __builtin_bit_cast(int, v), 0x124, 0xf, 0xf, true));
  v += __builtin_bit_cast(float, __builtin_amdgcn_update_dpp(0, __builtin_bit_cast(int, v), 0x128, 0xf, 0xf, true));
  const unsigned u = __builtin_bit_cast(unsigned, v);
  const u2v a = __builtin_amdgcn_permlane16_swap(u, u, false, false);
  const unsigned ax = a.x, ay = a.y;
  const unsigned w = __builtin_bit_cast(unsigned, __builtin_bit_cast(float, ax) + __builtin_bit_cast(float, ay));
  const u2v c = __builtin_amdgcn_permlane32_swap(w, w, false, false);
  const unsigned cx = c.x, cy = c.y;
  return __builtin_bit_cast(float, cx) + __builtin_bit_cast(float, cy);
}
#define K0S(k)
template <bool ISA>
DEV void prep_gemm(const unsigned short* __restrict__ Rw, const unsigned short* __restrict__ Cw, const unsigned short* __restrict__ xrow,
                   unsigned char* __restrict__ ws, int b, int h, int cg, int wave, int lane, u4v& xv) {
  const int g = lane >> 4, c16 = lane & 15, hl = lane >> 5, sl = lane & 31;
#pragma unroll
  for (int j = 0; j < 4; ++j) {
    const int id = wave * 4 + j;
    if (ISA) {
      const int lr = 2 * id + hl;
      const int kp = 64 * cg + (lr & 48) + swap13(lr & 15);
      dma16(Cw + (size_t)kp * 4096 + h * 256 + (sl ^ (lr & 15)) * 8, id * 1024);
    } else {
      const int rw = id * 8 + (lane >> 3), s = lane & 7;
      const int xr = ((rw >> 1) & 1) | (((rw >> 3) & 1) << 1);
      dma16(Cw + ((size_t)h * 256 + rw) * DM + 64 * cg + (s ^ (xr << 1)) * 8, id * 1024);
    }
  }
  if (wave == 7) {
    if (lane < 56) xv = *(const u4v*)xrow;
    __builtin_amdgcn_sched_barrier(0);
    asm volatile("s_waitcnt vmcnt(1)\n\ts_barrier" ::: "memory");
    asm volatile("" : "+v"(xv));
    const u4v ones = {SENT, SENT, SENT, SENT}, zero = {0u, 0u, 0u, 0u};
#pragma unroll
    for (int j = 0; j < 16; ++j) if (b >= 2) stwt16(ws + O_HB + (size_t)b * 16384 + j * 1024 + lane * 16, ones);
    stwt16(ws + O_FL + b * 1024 + lane * 16, zero);
    return;
  }
  const float sc = ISA ? 0.0625f * 1.4426950408889634f : 1.f;
  unsigned char* dst = ws + (ISA ? O_WA : O_WB) + (size_t)(h * 28 + 4 * cg) * 14336 + 2 * wave * 1024 + lane * 16;
  const unsigned short* R = Rw + (size_t)(64 * wave) * 4096 + h * 256;
  const unsigned reg = 32768 + wave * 16384;
  auto rsrc_row = [&](int jj) -> const unsigned short* {
    const int lr = 2 * jj + hl, rho = lr & 15, gr = rho >> 2, r = rho & 3;
    return R + (size_t)(16 * (gr >> 1) + 8 * (r >> 1) + 4 * (gr & 1) + (r & 1) + 2 * (lr >> 4)) * 4096 + (sl ^ rho) * 8;
  };
  const int rot = (cg * 16) / 7;
  u4v nx[16];
#pragma unroll
  for (int j = 0; j < 16; ++j) { const int jj = (j + rot) & 15; dma16(rsrc_row(jj), reg + jj * 1024); }
#pragma unroll
  for (int j = 0; j < 16; ++j) nx[j] = *(const u4v*)(rsrc_row((j + rot) & 15) + 32 * 4096);
  if (lane < 56) xv = *(const u4v*)xrow;
  __builtin_amdgcn_sched_barrier(0);
  K0S(0);
  asm volatile("s_waitcnt vmcnt(17)\n\ts_barrier" ::: "memory");
  K0S(2);
  const int q = c16 >> 2, p = c16 & 3;
  const int xr = (q >> 1) | ((g & 1) << 1);
  auto bfrag = [&](int es, int ti) -> u4v {
    if (ISA) return lds_r16((ti * 16 + c16) * 512 + (((es * 4 + g) ^ c16) << 4));
    const unsigned ad = (32 * es + 8 * g + q) * 128 + ((ti ^ xr) << 5) + p * 8;
    const u2v lo = lds_tr(ad), hi = lds_tr(ad + 512);
    return (u4v){lo.x, lo.y, hi.x, hi.y};
  };
  auto afrag = [&](int es, int t) -> u4v { return lds_r16(reg + (t * 16 + c16) * 512 + (((es * 4 + g) ^ c16) << 4)); };
#pragma unroll
  for (int rb = 0; rb < 2; ++rb) {
    u4v bb[2][4], aa[2][2];
#pragma unroll
    for (int ti = 0; ti < 4; ++ti) bb[0][ti] = bfrag(0, ti);
    aa[0][0] = afrag(0, 0); aa[0][1] = afrag(0, 1);
    f4v acc[2][4];
#pragma unroll
    for (int t = 0; t < 2; ++t)
#pragma unroll
      for (int ti = 0; ti < 4; ++ti) acc[t][ti] = (f4v){0.f, 0.f, 0.f, 0.f};
#pragma unroll
    for (int es = 0; es < 8; ++es) {
      if (es < 7) {
        aa[(es + 1) & 1][0] = afrag(es + 1, 0); aa[(es + 1) & 1][1] = afrag(es + 1, 1);
#pragma unroll
        for (int ti = 0; ti < 4; ++ti) bb[(es + 1) & 1][ti] = bfrag(es + 1, ti);
      }
#pragma unroll
      for (int ti = 0; ti < 4; ++ti)
#pragma unroll
        for (int t = 0; t < 2; ++t)
          acc[t][ti] = __builtin_amdgcn_mfma_f32_16x16x32_bf16(asbf8(aa[es & 1][t]), asbf8(bb[es & 1][ti]), acc[t][ti], 0, 0, 0);
      __builtin_amdgcn_sched_barrier(0);
    }
    if (rb == 0) {
#pragma unroll
      for (int j = 0; j < 16; ++j) lds_w16(reg + ((j + rot) & 15) * 1024 + lane * 16, nx[j]);
      asm volatile("" : "+v"(xv));
    }
#pragma unroll
    for (int ti = 0; ti < 4; ++ti) {
      const u4v o = {pk2(acc[0][ti][0] * sc, acc[0][ti][1] * sc), pk2(acc[0][ti][2] * sc, acc[0][ti][3] * sc),
                     pk2(acc[1][ti][0] * sc, acc[1][ti][1] * sc), pk2(acc[1][ti][2] * sc, acc[1][ti][3] * sc)};
      stwt16(dst + (size_t)ti * 14336 + rb * 1024, o);
    }
  }
  K0S(3);
}

__global__ void __launch_bounds__(512, 2)
prep_kernel(const unsigned short* __restrict__ x, const unsigned short* __restrict__ mem0,
            const unsigned short* __restrict__ Wq, const unsigned short* __restrict__ Wk,
            const unsigned short* __restrict__ Wv, const unsigned short* __restrict__ Wo,
            const unsigned short* __restrict__ w1, const unsigned short* __restrict__ w2,
            unsigned char* __restrict__ ws) {
  const int b = blockIdx.x, tid = threadIdx.x, lane = tid & 63;
  const int wave = __builtin_amdgcn_readfirstlane(tid >> 6);
  const int xc = b & 7, slot = b >> 3;
  const int row = b * 8 + wave;
  const unsigned short* xrow = x + (size_t)row * DM + lane * 8;
  u4v xv = {0u, 0u, 0u, 0u};
  if (slot < 28) {
    const int m = xc * 4 + slot / 7, cg = slot % 7;
    if (m < 16) prep_gemm<true>(Wk, Wq, xrow, ws, b, m, cg, wave, lane, xv);
    else prep_gemm<false>(Wv, Wo, xrow, ws, b, m - 16, cg, wave, lane, xv);
  } else {
    const int misc = (slot - 28) * 8 + xc;
    const int job = (misc - 1) * 8 + wave;
    const unsigned reg = wave * 14336;
    if (misc == 0) {
      if (wave == 7) {
        u4v m0[7];
#pragma unroll
        for (int j = 0; j < 7; ++j) { const int n = j * 64 + lane; m0[j] = *(const u4v*)(mem0 + ((n >> 1) & 7) * DM + (n >> 4) * 16 + (n & 1) * 8); }
        const u4v ones = {SENT, SENT, SENT, SENT};
#pragma unroll
        for (int j = 0; j < 32; ++j) stwt16(ws + O_HB + j * 1024 + lane * 16, ones);
        stwt16(ws + O_HB + 2 * MB + (lane >> 4) * 8192 + (lane & 15) * 64, ones);
#pragma unroll
        for (int j = 0; j < 7; ++j) asm volatile("" : "+v"(m0[j]));
        vm0();
        unsigned best[8], v;
#pragma unroll
        for (int c = 0; c < 8; ++c) best[c] = 0x7fffffff;
#pragma unroll 1
        for (int it = 0; it < 3; ++it) {
#pragma unroll
          for (int c = 0; c < 8; ++c) {
            const unsigned char* pc = ws + O_HB + (size_t)(c >> 2) * 2 * MB + (c & 3) * 8192 + it * 256;
            const unsigned c0 = rdtime();
            asm volatile("global_load_dword %0, %1, %2 sc1\n\ts_waitcnt vmcnt(0)" : "=&v"(v) : "v"(0u), "s"(pc) : "memory");
            best[c] = min(best[c], rdtime() - c0);
          }
        }
        unsigned sel = 0;
#pragma unroll
        for (int rg = 0; rg < 2; ++rg) {
          unsigned bi = 0, bv = best[4 * rg];
#pragma unroll
          for (unsigned k = 1; k < 4; ++k) if (best[4 * rg + k] < bv) { bv = best[4 * rg + k]; bi = k; }
          sel |= bi << (2 * rg);
        }
        if (lane == 0) *(unsigned*)(ws + O_SEL) = sel;
#pragma unroll
        for (int j = 0; j < 7; ++j) stwt16(ws + O_HB + (sel & 3u) * 8192 + j * 1024 + lane * 16, m0[j]);
      }
    } else if (job < 224) {
      const bool first = job < 112;
      const int step = first ? job >> 2 : job - 112;
      const unsigned short* src = first ? w1 + (size_t)step * 16 * FF + (job & 3) * 448 : w2 + (size_t)step * 16 * DM;
      const int ld = first ? FF : DM;
#pragma unroll
      for (int j = 0; j < 14; ++j) { const int n = j * 64 + lane, r = n / 56; dma16(src + (size_t)r * ld + (n - r * 56) * 8, reg + j * 1024); }
    }
    if (lane < 56) xv = *(const u4v*)xrow;
    asm volatile("" : "+v"(xv));
    vm0();
    if (misc > 0 && job < 224) {
      const bool first = job < 112;
      const int hi = lane >> 5, q = (lane & 15) >> 2;
      const int r0 = first ? 4 * hi + 8 * (q >> 1) + (q & 1) : 4 * hi + q, dr = first ? 2 : 8;
      const unsigned ad = reg + r0 * 896 + ((lane >> 4) & 1) * 32 + (lane & 3) * 8;
      unsigned char* dst = ws + (first ? O_W1 + (size_t)((job & 3) * 14 * 28 + (job >> 2)) * 1024 : O_W2 + (size_t)(job - 112) * 1024) + lane * 16;
      const size_t dstep = first ? 28672 : 114688;
#pragma unroll
      for (int t = 0; t < 14; ++t) {
        const u2v lo = lds_tr(ad + t * 64), hv = lds_tr(ad + t * 64 + dr * 896);
        stwt16(dst + t * dstep, (u4v){lo.x, lo.y, hv.x, hv.y});
      }
    }
    const u4v ones = {SENT, SENT, SENT, SENT}, zero = {0u, 0u, 0u, 0u};
    stwt16(ws + O_HB + (size_t)b * 16384 + tid * 16, ones);
    stwt16(ws + O_HB + (size_t)b * 16384 + 8192 + tid * 16, ones);
    if (wave == 0) stwt16(ws + O_FL + b * 1024 + lane * 16, zero);
  }
  {
    float e[8];
#pragma unroll
    for (int j = 0; j < 4; ++j) { e[2 * j] = bflo(xv[j]); e[2 * j + 1] = bfhi(xv[j]); }
    float s = 0.f;
#pragma unroll
    for (int j = 0; j < 8; ++j) s += e[j];
    const float mean = wavesum(s) * (1.f / DM);
    float q = 0.f;
    if (lane < 56) {
#pragma unroll
      for (int j = 0; j < 8; ++j) { const float d = e[j] - mean; q += d * d; }
    }
    const float mb = rbf(mean), vb = rbf(wavesum(q) * (1.f / DM));
    const float den = rbf(__builtin_sqrtf(rbf(vb + 1e-5f)));
    unsigned o4[4];
#pragma unroll
    for (int j = 0; j < 4; ++j) o4[j] = pk2(rbf(e[2 * j] - mb) / den, rbf(e[2 * j + 1] - mb) / den);
    const bool odd = lane & 1;
    const unsigned r0 = __builtin_amdgcn_update_dpp(0u, odd ? o4[0] : o4[2], 0xb1, 0xf, 0xf, true);
    const unsigned r1 = __builtin_amdgcn_update_dpp(0u, odd ? o4[1] : o4[3], 0xb1, 0xf, 0xf, true);
    u4v p;
    if (odd) p = (u4v){r0, o4[2], r1, o4[3]}; else p = (u4v){o4[0], r0, o4[1], r1};
    if (lane < 56) *(u4v*)(ws + O_XN + (size_t)(row >> 5) * 28672 + (lane >> 1) * 1024 + (lane & 1) * 512 + (row & 31) * 16) = p;
  }
  K0S(1);
}
constexpr int C_G = 0, C_U = 512, C_F = 1856, C_ANCHOR = 1900, C_ABORT = 1901;
typedef __amdgpu_buffer_rsrc_t rsrc_t;
DEV rsrc_t mkrsrc(const void* p, unsigned bytes) { return __builtin_amdgcn_make_buffer_rsrc((void*)p, 0, bytes, 0x00020000); }
DEV u4v ldc16(rsrc_t r, unsigned off) { return __builtin_amdgcn_raw_buffer_load_b128(r, off, 0, 16); }
DEV u2v ldc8(rsrc_t r, unsigned off) { return __builtin_amdgcn_raw_buffer_load_b64(r, off, 0, 16); }
DEV void stc16(rsrc_t r, unsigned off, u4v v) { __builtin_amdgcn_raw_buffer_store_b128(v, r, off, 0, 16); }
DEV void stc4(rsrc_t r, unsigned off, unsigned v) { __builtin_amdgcn_raw_buffer_store_b32(v, r, off, 0, 16); }
DEV unsigned* cntp(unsigned char* ws, int idx) { return (unsigned*)(ws + O_FL) + idx * 16; }
DEV unsigned ld_cnt(const unsigned* p) { unsigned v; asm volatile("global_load_dword %0, %1, off sc1\n\ts_waitcnt vmcnt(0)" : "=&v"(v) : "v"(p) : "memory"); return v; }
DEV void add_cnt(unsigned* p, int lane) {
  if (lane == 0) { const unsigned one = 1; asm volatile("global_atomic_add %0, %1, off sc1" :: "v"(p), "v"(one) : "memory"); }
}
constexpr int SPIN_CAP = 1 << 17;
DEV void wait_cnt(unsigned char* ws, const unsigned* p, unsigned target) {
  for (int it = 0;; ++it) {
    const unsigned v = ld_cnt(p);
    if (__all(v >= target)) return;
    if ((it & 63) == 63) {
      if (it >= SPIN_CAP) { const unsigned one = 1; asm volatile("global_store_dword %0, %1, off sc1" :: "v"(cntp(ws, C_ABORT)), "v"(one) : "memory"); return; }
      if (__any(ld_cnt(cntp(ws, C_ABORT)) != 0)) return;
    }
    __builtin_amdgcn_s_sleep(6);
  }
}
DEV unsigned hoff(unsigned sel, int gi, int kind) {
  const unsigned k = ((sel >> (2 * (gi >> 6))) & 3u) ^ ((((unsigned)gi >> 4) & 1u) << 1) ^ (unsigned)kind;
  return (unsigned)O_HB + (unsigned)gi * 32768u + k * 8192u;
}
DEV unsigned char* hblk(unsigned char* ws, unsigned sel, int gi, int kind) { return ws + hoff(sel, gi, kind); }
DEV unsigned char* flagS(unsigned char* ws, unsigned sel, int s, int id) { return hblk(ws, sel, s, 3) + id * 128; }
DEV unsigned char* flagY(unsigned char* ws, unsigned sel, int s, int v) { return hblk(ws, sel, 64 + s, 3) + v * 128; }
DEV void wait_flags(unsigned char* ws, const unsigned char* p) {
  for (int it = 0;; ++it) {
    if (!__any(ld_cnt((const unsigned*)p) == SENT)) return;
    if ((it & 63) == 63) {
      if (it >= SPIN_CAP) { const unsigned one = 1; asm volatile("global_store_dword %0, %1, off sc1" :: "v"(cntp(ws, C_ABORT)), "v"(one) : "memory"); return; }
      if (__any(ld_cnt(cntp(ws, C_ABORT)) != 0)) return;
    }
    __builtin_amdgcn_s_sleep(6);
  }
}
DEV f16v mfma32(u4v a, u4v b, f16v c) { return __builtin_amdgcn_mfma_f32_32x32x16_bf16(asbf8(a), asbf8(b), c, 0, 0, 0); }
DEV f4v mfma16(u4v a, u4v b, f4v c) { return __builtin_amdgcn_mfma_f32_16x16x32_bf16(asbf8(a), asbf8(b), c, 0, 0, 0); }
DEV void st16c_asm(void* p, u4v v) { asm volatile("global_store_dwordx4 %0, %1, off sc1\n\ts_nop 1" :: "v"(p), "v"(v) : "memory"); }

#define RTS(k)
template <bool ISG>
DEV void role_w1(unsigned char* __restrict__ ws, int j, int wave, int lane) {
  const int ct = wave & 3, rh = wave >> 2, hi = lane >> 5, c32 = lane & 31;
  int h, tile0, cidx;
  if (ISG) { const int hp = j / 7, ctile = (j % 7) * 4 + ct; h = 2 * hp + ctile / 14; tile0 = 2 * (ctile % 14); cidx = C_G + hp; }
  else { const int V = j >> 2; h = 4 * (j & 3) + ct; tile0 = 2 * V; cidx = C_U + V; }
  const unsigned char* xn = ws + O_XN + wave * 7168 + lane * 16;
#pragma unroll
  for (int i = 0; i < 7; ++i) dma16(xn + i * 1024, wave * 7168 + i * 1024);
  u4v w[28];
  {
    const unsigned char* p = ws + (ISG ? O_WA : O_WB) + (size_t)(h * 28 + tile0 + (c32 >> 4)) * 14336 + hi * 256 + (c32 & 15) * 16;
#pragma unroll
    for (int s = 0; s < 28; ++s) w[s] = *(const u4v*)(p + (s >> 1) * 1024 + (s & 1) * 512);
  }
  unsigned char* out = ISG ? ws + O_G + (size_t)(h * 2 + (c32 >> 4)) * 14336 + (tile0 >> 1) * 1024 + hi * 256 + (c32 & 15) * 16
                           : ws + O_U + (size_t)((tile0 + (c32 >> 4)) * 16 + h) * 1024 + hi * 256 + (c32 & 15) * 16;
  out += (size_t)rh * 458752;
  const int tok = ISG ? c32 : ((c32 & 19) | ((c32 >> 1) & 4) | ((c32 << 1) & 8));
  const unsigned rd = rh * 28672 + hi * 512 + tok * 16;
  unsigned* cnt = cntp(ws, cidx + rh * (ISG ? 8 : 14));
  constexpr int CSTEP = (ISG ? 16 : 28) * 16;
#pragma unroll
  for (int s = 0; s < 28; ++s) asm volatile("" : "+v"(w[s]));
  vm0();
  RTS(1);
  for (int p = 0; p < 32; ++p) {
    asm volatile("s_waitcnt vmcnt(2)\n\ts_barrier" ::: "memory");
    const unsigned buf = (p & 1) * 57344;
    const unsigned char* src = xn + (size_t)(p < 31 ? p + 1 : p) * 57344;
    const unsigned dstl = (57344 - buf) + wave * 7168;
    f16v acc;
#pragma unroll
    for (int i = 0; i < 16; ++i) acc[i] = 0.f;
    const LDS u4v* xp = (const LDS u4v*)(size_t)(buf + rd);
    u4v xr[5];
#pragma unroll
    for (int s = 0; s < 4; ++s) xr[s] = xp[s * 64];
#pragma unroll
    for (int s = 0; s < 28; ++s) {
      if (s + 4 < 28) xr[(s + 4) % 5] = xp[(s + 4) * 64];
      acc = ISG ? mfma32(w[s], xr[s % 5], acc) : mfma32(xr[s % 5], w[s], acc);
      __builtin_amdgcn_sched_barrier(0);
      if (s % 3 == 0 && s / 3 < 7) { dma16(src + (s / 3) * 1024, dstl + (s / 3) * 1024); __builtin_amdgcn_sched_barrier(0); }
      if (s == 16) {
        asm volatile("s_waitcnt vmcnt(6)" ::: "memory");
        if (p >= 2) add_cnt(cnt + (p - 1) * CSTEP, lane);
        __builtin_amdgcn_sched_barrier(0);
      }
    }
    const u4v o0 = {pk2(acc[0], acc[1]), pk2(acc[2], acc[3]), pk2(acc[4], acc[5]), pk2(acc[6], acc[7])};
    const u4v o1 = {pk2(acc[8], acc[9]), pk2(acc[10], acc[11]), pk2(acc[12], acc[13]), pk2(acc[14], acc[15])};
    unsigned char* o = out + (size_t)p * 917504;
    st16c_asm(o, o0); st16c_asm(o + 512, o1);
    if (p == 0) { RTS(4); vm0(); add_cnt(cnt, lane); RTS(2); vm0(); }
  }
  vm0();
  add_cnt(cnt + 31 * CSTEP, lane);
  RTS(3);
}

template <bool P2>
DEV void ffn_body(unsigned char* __restrict__ ws, const unsigned short* __restrict__ x, const unsigned short* __restrict__ b1,
                  const unsigned short* __restrict__ b2, int job, int wave, int lane) {
  const int hi = lane >> 5, c32 = lane & 31;
  constexpr int NL = P2 ? 60 : 28, RING = P2 ? 12 : 14, DIST = RING - 1;
  const u4v* w1p = (const u4v*)(ws + O_W1 + (size_t)wave * 28672) + lane;
  const u4v* w2p = (const u4v*)(ws + O_W2 + (size_t)(2 * wave) * 114688) + lane;
  auto wload = [&](int c, int i) -> u4v {
    if (i < 28) return w1p[(size_t)c * 14336 + i * 64];
    return w2p[(size_t)c * 1024 + ((i - 28) >> 1) * 64 + ((i - 28) & 1) * 7168];
  };
  u4v wr[RING];
#pragma unroll
  for (int i = 0; i < DIST; ++i) wr[i] = wload(0, i);
  {
    const unsigned char* xn = ws + O_XN + (size_t)job * 57344 + wave * 7168 + lane * 16;
#pragma unroll
    for (int i = 0; i < 7; ++i) dma16(xn + i * 1024, wave * 7168 + i * 1024);
  }
  f16v o[2][2];
#pragma unroll
  for (int a = 0; a < 2; ++a)
#pragma unroll
    for (int bq = 0; bq < 2; ++bq)
#pragma unroll
      for (int i = 0; i < 16; ++i) o[a][bq][i] = 0.f;
  vm0();
  bar();
  const LDS u4v* xp = (const LDS u4v*)(size_t)(lane * 16);
  for (int c = 0; c < 7; ++c) {
    const int cn = c < 6 ? c + 1 : 6;
    const unsigned hbuf = 57344 + (c & 1) * 32768;
    u2v bv[4];
#pragma unroll
    for (int q = 0; q < 4; ++q) bv[q] = *(const u2v*)(b1 + 32 * (8 * c + wave) + 4 * hi + 8 * q);
    f16v a0, a1;
#pragma unroll
    for (int i = 0; i < 16; ++i) { a0[i] = 0.f; a1[i] = 0.f; }
    u4v xa[3], xb[3];
#pragma unroll
    for (int s = 0; s < 2; ++s) { xa[s] = xp[s * 64]; xb[s] = xp[1792 + s * 64]; }
#pragma unroll
    for (int s = 0; s < 28; ++s) {
      wr[(s + DIST) % RING] = (s + DIST < NL) ? wload(c, s + DIST) : wload(cn, s + DIST - NL);
      if (s + 2 < 28) { xa[(s + 2) % 3] = xp[(s + 2) * 64]; xb[(s + 2) % 3] = xp[1792 + (s + 2) * 64]; }
      a0 = mfma32(wr[s % RING], xa[s % 3], a0);
      a1 = mfma32(wr[s % RING], xb[s % 3], a1);
      __builtin_amdgcn_sched_barrier(0);
    }
#pragma unroll
    for (int i = 0; i < 16; ++i) {
      const unsigned bw = bv[i >> 2][(i >> 1) & 1];
      const float bs = (i & 1) ? bfhi(bw) : bflo(bw);
      const float t0 = fmaxf(a0[i] + bs, 0.f), t1 = fmaxf(a1[i] + bs, 0.f);
      a0[i] = t0 * t0; a1[i] = t1 * t1;
    }
#pragma unroll
    for (int bq = 0; bq < 2; ++bq) {
      lds_w16(hbuf + ((wave * 2 + bq) * 2 + 0) * 1024 + lane * 16,
              (u4v){pk2(a0[8 * bq], a0[8 * bq + 1]), pk2(a0[8 * bq + 2], a0[8 * bq + 3]), pk2(a0[8 * bq + 4], a0[8 * bq + 5]), pk2(a0[8 * bq + 6], a0[8 * bq + 7])});
      lds_w16(hbuf + ((wave * 2 + bq) * 2 + 1) * 1024 + lane * 16,
              (u4v){pk2(a1[8 * bq], a1[8 * bq + 1]), pk2(a1[8 * bq + 2], a1[8 * bq + 3]), pk2(a1[8 * bq + 4], a1[8 * bq + 5]), pk2(a1[8 * bq + 6], a1[8 * bq + 7])});
    }
    bar();
    if (P2) {
      const LDS u4v* hp = (const LDS u4v*)(size_t)(hbuf + lane * 16);
      u4v h0[2], h1[2];
      h0[0] = hp[0]; h1[0] = hp[64];
#pragma unroll
      for (int s = 0; s < 16; ++s) {
        const int i = 28 + 2 * s;
        wr[(i + DIST) % RING] = (i + DIST < NL) ? wload(c, i + DIST) : wload(cn, i + DIST - NL);
        if (s + 1 < 16) { h0[(s + 1) & 1] = hp[(s + 1) * 128]; h1[(s + 1) & 1] = hp[(s + 1) * 128 + 64]; }
        o[0][0] = mfma32(wr[i % RING], h0[s & 1], o[0][0]); o[0][1] = mfma32(wr[i % RING], h1[s & 1], o[0][1]);
        __builtin_amdgcn_sched_barrier(0);
        wr[(i + 1 + DIST) % RING] = (i + 1 + DIST < NL) ? wload(c, i + 1 + DIST) : wload(cn, i + 1 + DIST - NL);
        o[1][0] = mfma32(wr[(i + 1) % RING], h0[s & 1], o[1][0]); o[1][1] = mfma32(wr[(i + 1) % RING], h1[s & 1], o[1][1]);
        __builtin_amdgcn_sched_barrier(0);
      }
    }
  }
  if (P2) {
    const rsrc_t rf = mkrsrc(ws + O_F, 4u << 20);
#pragma unroll
    for (int a = 0; a < 2; ++a)
#pragma unroll
      for (int sg = 0; sg < 2; ++sg)
#pragma unroll
        for (int Q = 0; Q < 4; ++Q) {
          const int n = 32 * (2 * wave + a) + 8 * Q + 4 * hi, seg = 2 * job + sg;
          const u2v xv = *(const u2v*)(x + (size_t)(seg * 32 + c32) * DM + n), bv = *(const u2v*)(b2 + n);
          const f4v r = {o[a][sg][4 * Q] + bflo(bv.x) + bflo(xv.x), o[a][sg][4 * Q + 1] + bfhi(bv.x) + bfhi(xv.x),
                         o[a][sg][4 * Q + 2] + bflo(bv.y) + bflo(xv.y), o[a][sg][4 * Q + 3] + bfhi(bv.y) + bfhi(xv.y)};
          const int v = 2 * (2 * wave + a) + (Q >> 1), g = 2 * (Q & 1) + hi;
          stc16(rf, ((seg * 28 + v) * 2 + (c32 >> 4)) * 1024 + (g * 16 + (c32 & 15)) * 16, __builtin_bit_cast(u4v, r));
        }
    vm0();
    add_cnt(cntp(ws, C_F + job), lane);
    RTS(3);
  }
}
DEV void role_ffn(unsigned char* __restrict__ ws, const unsigned short* __restrict__ x, const unsigned short* __restrict__ b1,
                  const unsigned short* __restrict__ b2, int job, int wave, int lane) {
  if (wave < 7) ffn_body<true>(ws, x, b1, b2, job, wave, lane);
  else ffn_body<false>(ws, x, b1, b2, job, wave, lane);
}

DEV float asf(unsigned u) { return __builtin_bit_cast(float, u); }
DEV unsigned asu(float f) { return __builtin_bit_cast(unsigned, f); }
DEV float xmax16_32(float v) {
  const u2v a = __builtin_amdgcn_permlane16_swap(asu(v), asu(v), false, false);
  v = fmaxf(asf(a.x), asf(a.y));
  const u2v c = __builtin_amdgcn_permlane32_swap(asu(v), asu(v), false, false);
  return fmaxf(asf(c.x), asf(c.y));
}
DEV float xsum16_32(float v) {
  const u2v a = __builtin_amdgcn_permlane16_swap(asu(v), asu(v), false, false);
  v = asf(a.x) + asf(a.y);
  const u2v c = __builtin_amdgcn_permlane32_swap(asu(v), asu(v), false, false);
  return asf(c.x) + asf(c.y);
}
DEV float halfsum(float a, float b) {
  const u2v s = __builtin_amdgcn_permlane32_swap(asu(a), asu(b), false, false);
  return asf(s.x) + asf(s.y);
}

constexpr int W3_GROUPS = 12;
DEV void role_w3(unsigned char* __restrict__ ws, unsigned short* __restrict__ out, int grp, int C, int wave, int lane) {
  const unsigned sel = *(const unsigned*)(ws + O_SEL);
  const int g = lane >> 4, c16 = lane & 15, v = 7 * C + (wave < 7 ? wave : 6);
  const rsrc_t ru = mkrsrc(ws + O_U, 30u << 20), rf = mkrsrc(ws + O_F, 4u << 20);
  constexpr unsigned L_P = 0, L_PM = 32768, L_ST = 40960, L_SM = L_ST + 32768, L_Y = L_SM + 32768;
  for (int s = grp; s < NSEG; s += W3_GROUPS) {
    if (wave == 7) {
      wait_cnt(ws, cntp(ws, C_U + s * 14 + ((7 * C) >> 1) + (lane & 3)), 16);
      wait_cnt(ws, cntp(ws, C_F + (s >> 1)), 7);
    }
    bar();
    u4v uf[16], ff[2];
    if (wave < 7) {
#pragma unroll
      for (int hh = 0; hh < 16; ++hh) uf[hh] = ldc16(ru, ((s * 28 + v) * 16 + hh) * 1024 + lane * 16);
#pragma unroll
      for (int q = 0; q < 2; ++q) ff[q] = ldc16(rf, ((s * 28 + v) * 2 + q) * 1024 + lane * 16);
    } else {
      wait_flags(ws, flagY(ws, sel, s, 7 * C + min(lane, 6)));
      wait_flags(ws, flagS(ws, sel, s, lane));
    }
    __builtin_amdgcn_sched_barrier(0);
    bar();
#pragma unroll
    for (int it = 0; it < 4; ++it) {
      dma16c(ws + O_ST + (size_t)(s * 32 + wave * 4 + it) * 1024 + lane * 16, L_ST + (wave * 4 + it) * 1024);
      dma16c(ws + O_SM + (size_t)(s * 32 + wave * 4 + it) * 1024 + lane * 16, L_SM + (wave * 4 + it) * 1024);
    }
    if (wave < 7) {
#pragma unroll
      for (int H = 0; H < 4; ++H) dma16c(ws + O_YG + (size_t)((s * 28 + v) * 4 + H) * 1024 + lane * 16, L_Y + (wave * 4 + H) * 1024);
      asm volatile("s_waitcnt vmcnt(4)" ::: "memory");
    } else vm0();
#pragma unroll
    for (int it = 0; it < 4; ++it) {
      const int q = it & 1, hh = 2 * wave + (it >> 1), tq = 16 * q + c16;
      const u4v st = lds_r16(L_ST + (wave * 4 + it) * 1024 + (g >> 1) * 512 + c16 * 32 + (g & 1) * 16);
      const LDS unsigned* sm = (const LDS unsigned*)(size_t)(L_SM + (wave * 4 + it) * 1024 + lane * 4);
      const unsigned s0 = sm[0], s1 = sm[64], s2 = sm[128], s3 = sm[192];
      float e[8];
      float m0 = (bflo(s0) + bflo(s1)) + (bflo(s2) + bflo(s3));
      float m1 = (bfhi(s0) + bfhi(s1)) + (bfhi(s2) + bfhi(s3));
#pragma unroll
      for (int i = 0; i < 8; ++i) { const unsigned w = st[i >> 1]; e[i] = (8 * g + i <= tq) ? ((i & 1) ? bfhi(w) : bflo(w)) : -__builtin_inff(); }
      float mx = fmaxf(m0, m1);
#pragma unroll
      for (int i = 0; i < 8; ++i) mx = fmaxf(mx, e[i]);
      mx = xmax16_32(mx);
      m0 = __builtin_amdgcn_exp2f(m0 - mx); m1 = __builtin_amdgcn_exp2f(m1 - mx);
      float sum = m0 + m1;
#pragma unroll
      for (int i = 0; i < 8; ++i) { e[i] = __builtin_amdgcn_exp2f(e[i] - mx); sum += e[i]; }
      const float r = __builtin_amdgcn_rcpf(xsum16_32(sum));
      lds_w16(L_P + (q * 16 + hh) * 1024 + lane * 16,
              (u4v){pk2(e[0] * r, e[1] * r), pk2(e[2] * r, e[3] * r), pk2(e[4] * r, e[5] * r), pk2(e[6] * r, e[7] * r)});
      *(LDS unsigned*)(size_t)(L_PM + (q * 4 + (hh >> 2)) * 1024 + lane * 16 + (hh & 3) * 4) = pk2(m0 * r, m1 * r);
    }
    vm0();
    bar();
    if (wave < 7) {
      u4v yf[4];
#pragma unroll
      for (int H = 0; H < 4; ++H) {
        const u2v lo = *(const LDS u2v*)(size_t)(L_Y + (wave * 4 + H) * 1024 + lane * 8), hi = *(const LDS u2v*)(size_t)(L_Y + (wave * 4 + H) * 1024 + 512 + lane * 8);
        yf[H] = (u4v){lo.x, lo.y, hi.x, hi.y};
      }
      f4v a0 = __builtin_bit_cast(f4v, ff[0]), a1 = __builtin_bit_cast(f4v, ff[1]);
      const LDS u4v* pp = (const LDS u4v*)(size_t)(L_P + lane * 16);
      u4v p0[4], p1[4];
#pragma unroll
      for (int k = 0; k < 3; ++k) { p0[k] = pp[k * 64]; p1[k] = pp[(16 + k) * 64]; }
#pragma unroll
      for (int k = 0; k < 20; ++k) {
        const int n = k + 3;
        if (n < 16) { p0[n & 3] = pp[n * 64]; p1[n & 3] = pp[(16 + n) * 64]; }
        else if (n < 20) { p0[n & 3] = pp[(32 + n - 16) * 64]; p1[n & 3] = pp[(36 + n - 16) * 64]; }
        const u4v w = k < 16 ? uf[k] : yf[k - 16];
        a0 = mfma16(w, p0[k & 3], a0); a1 = mfma16(w, p1[k & 3], a1);
        __builtin_amdgcn_sched_barrier(0);
      }
      *(u2v*)(out + (size_t)(s * 32 + c16) * DM + v * 16 + g * 4) = (u2v){pk2(a0[0], a0[1]), pk2(a0[2], a0[3])};
      *(u2v*)(out + (size_t)(s * 32 + 16 + c16) * DM + v * 16 + g * 4) = (u2v){pk2(a1[0], a1[1]), pk2(a1[2], a1[3])};
    }
  }
  RTS(4);
}
#define TUNE_ARG
#define TUNE_PASS
#define K_PERIOD 800
#define K_DE 1330
#define K_DS 1580
#define K_SMK 2
#define K_YOFF 0
#define DBG(slot, v)
#define ST(n)
#define ARR(s_)
#define EV(code)
#define EV_OUT(base)
#define ST_CLOB
#define ST_OUT(base, ref)
constexpr int RET_E = 24;
constexpr int TRY_CAP = 1 << 16;

DEV int rt32() { unsigned long long t; asm volatile("s_memrealtime %0\n\ts_waitcnt lgkmcnt(0)" : "=s"(t) :: "memory"); return (int)(unsigned)t; }
struct Lat { int anchor, e, f; };
DEV void lat_fwd(Lat& L, int pe, int pf) { L.f += pf; const int c = L.f >= 24; L.f -= c ? 24 : 0; L.e += pe + c; }
DEV void lat_back(Lat& L, int pe, int pf) { L.f -= pf; const int c = L.f < 0; L.f += c ? 24 : 0; L.e -= pe + c; }
DEV void lat_prep(const Lat& L, int& tgt, int& jmp) { tgt = L.anchor + 4 * L.e; jmp = 108 - 4 * L.f; }
DEV void lat_commit(Lat& L, int tgt, int jmp) {
  if (tgt == (int)0x80000000) return;
  int r = tgt - rt32();
  while (__builtin_expect(r < 0, 0)) { L.e += RET_E; tgt += 4 * RET_E; r += 4 * RET_E; }
  if (r > 0) {
    int d;
#define RT_STEP "s_memrealtime vcc\n\ts_waitcnt lgkmcnt(0)\n\ts_sub_i32 %0, %1, vcc_lo\n\ts_cmp_lt_i32 %0, 1\n\t"
    asm volatile("1:\n\t" RT_STEP "s_cbranch_scc1 2f\n\t" RT_STEP "s_cbranch_scc1 2f\n\t" RT_STEP "s_cbranch_scc1 2f\n\t"
                 RT_STEP "s_cbranch_scc0 1b\n\t"
                 "2:\n\t"
                 "s_getpc_b64 vcc\n\ts_add_u32 vcc_lo, vcc_lo, %2\n\ts_addc_u32 vcc_hi, vcc_hi, 0\n\ts_setpc_b64 vcc\n\t"
                 ".rept 24\n\ts_nop 0\n\t.endr"
                 : "=&s"(d) : "s"(tgt), "s"(jmp) : "scc", "vcc", "memory");
  }
}
DEV void lat_wait(Lat& L) { int tgt, jmp; lat_prep(L, tgt, jmp); lat_commit(L, tgt, jmp); }
DEV int get_anchor(unsigned char* ws, bool publisher) {
  unsigned* p = cntp(ws, C_ANCHOR);
  if (publisher) { const unsigned v = ((unsigned)rt32() << 1) | 1u; asm volatile("global_store_dword %0, %1, off sc1" :: "v"(p), "v"(v) : "memory"); }
  wait_cnt(ws, p, 1);
  const unsigned v = __builtin_amdgcn_readfirstlane(ld_cnt(p));
  const int n = rt32();
  const int d = (int)((v & ~1u) - ((unsigned)n << 1)) >> 1;
  return (d > 0 || d < -(1 << 22)) ? n : n + d;
}
DEV f4v smf(u4v a, u8v b, f4v c, int idx) {
  return __builtin_amdgcn_smfmac_f32_16x16x64_bf16(asbf8(a), __builtin_bit_cast(bf16x, b), c, idx, 0, 0);
}
DEV unsigned umax4(u4v v) { return max(max(v.x, v.y), max(v.z, v.w)); }
DEV float dpp_ror8(float v) { return __builtin_bit_cast(float, __builtin_amdgcn_update_dpp(0, __builtin_bit_cast(int, v), 0x128, 0xf, 0xf, true)); }
DEV float dpp_xor1(float v) { return __builtin_bit_cast(float, __builtin_amdgcn_update_dpp(0, __builtin_bit_cast(int, v), 0xb1, 0xf, 0xf, true)); }
DEV void st4c_asm(void* p, unsigned v) { asm volatile("global_store_dword %0, %1, off sc1" :: "v"(p), "v"(v) : "memory"); }
DEV void st8c_asm(void* p, u2v v) { asm volatile("global_store_dwordx2 %0, %1, off sc1" :: "v"(p), "v"(v) : "memory"); }
DEV void ld16c_asm(u4v& v, const void* p) { asm volatile("global_load_dwordx4 %0, %1, off sc1" : "=&v"(v) : "v"(p) : "memory"); }
DEV void rawbar() { asm volatile("s_barrier" ::: "memory"); }
DEV unsigned lds_r4(unsigned a) { return *(volatile LDS unsigned*)(size_t)a; }
DEV void lds_w4(unsigned a, unsigned v) { *(volatile LDS unsigned*)(size_t)a = v; }
DEV u8v cat8(u4v a, u4v b) { return (u8v){a.x, a.y, a.z, a.w, b.x, b.y, b.z, b.w}; }
DEV void set_abort(unsigned char* ws) { st4c_asm(cntp(ws, C_ABORT), 1u); }

constexpr unsigned L_MEM = 0, L_KT = 8192, L_FLAG = 12288, L_RED = 16384;
constexpr unsigned L_CG = 12544, L_G = 32768, L_XN = 77824;
#define CLOB_V200_247 "v200","v201","v202","v203","v204","v205","v206","v207","v208","v209","v210","v211","v212","v213","v214","v215", \
  "v216","v217","v218","v219","v220","v221","v222","v223","v224","v225","v226","v227","v228","v229","v230","v231", \
  "v232","v233","v234","v235","v236","v237","v238","v239","v240","v241","v242","v243","v244","v245","v246","v247","v248"
#define SMF "v_smfmac_f32_16x16x64_bf16 "
#define R1_HEAD \
  "v_mov_b32 v228, 0\n\tv_mov_b32 v229, 0\n\tv_mov_b32 v230, 0\n\tv_mov_b32 v231, 0\n\t" \
  "v_mov_b32 v232, 0\n\tv_mov_b32 v233, 0\n\tv_mov_b32 v234, 0\n\tv_mov_b32 v235, 0\n\t" \
  "v_mov_b32 v244, 0\n\tv_mov_b32 v245, 0\n\tv_mov_b32 v246, 0\n\tv_mov_b32 v247, 0\n\t" \
  "s_mov_b32 m0, %[m0v]\n\t" \
  "s_barrier\n\t" ST(80) \
  "global_load_lds_dwordx4 %[moff], %[src] sc1\n\t" \
  "s_waitcnt vmcnt(0)\n\t" \
  "s_barrier\n\t" ST(82) \
  "ds_read_b128 v[200:203], %[rd]\n\tds_read_b128 v[204:207], %[rd] offset:1024\n\tds_read_b128 v[208:211], %[rd] offset:2048\n\t" \
  "ds_read_b128 v[212:215], %[rd] offset:3072\n\tds_read_b128 v[216:219], %[rd] offset:4096\n\tds_read_b128 v[220:223], %[rd] offset:5120\n\t" \
  "ds_read_b128 v[224:227], %[rd] offset:6144\n\t"
#define R1_TWO(n6, n5, n4, n3, n2, n1, n0) \
  "s_waitcnt lgkmcnt(" #n6 ")\n\t" ST(84) SMF "v[228:231], v[200:203], %[a0], %[idx]\n\t" SMF "v[232:235], v[200:203], %[b0], %[idx]\n\t" \
  "s_waitcnt lgkmcnt(" #n5 ")\n\t" SMF "v[228:231], v[204:207], %[a1], %[idx]\n\t" SMF "v[232:235], v[204:207], %[b1], %[idx]\n\t" \
  "s_waitcnt lgkmcnt(" #n4 ")\n\t" SMF "v[228:231], v[208:211], %[a2], %[idx]\n\t" SMF "v[232:235], v[208:211], %[b2], %[idx]\n\t" \
  "s_waitcnt lgkmcnt(" #n3 ")\n\t" SMF "v[228:231], v[212:215], %[a3], %[idx]\n\t" SMF "v[232:235], v[212:215], %[b3], %[idx]\n\t" \
  "s_waitcnt lgkmcnt(" #n2 ")\n\t" SMF "v[228:231], v[216:219], %[a4], %[idx]\n\t" SMF "v[232:235], v[216:219], %[b4], %[idx]\n\t" \
  "s_waitcnt lgkmcnt(" #n1 ")\n\t" SMF "v[228:231], v[220:223], %[a5], %[idx]\n\t" \
  "s_waitcnt lgkmcnt(" #n0 ")\n\t" SMF "v[228:231], v[224:227], %[a6], %[idx]\n\t" \
  SMF "v[232:235], v[220:223], %[b5], %[idx]\n\t" SMF "v[232:235], v[224:227], %[b6], %[idx]\n\t" \
  ST(86) "s_nop 1\n\t" \
  "v_cvt_pk_bf16_f32 v228, v228, v229\n\tv_cvt_pk_bf16_f32 v229, v230, v231\n\t" \
  "ds_write_b64 %[ktw], v[228:229]\n\t" \
  "s_nop 2\n\t" \
  "v_cvt_pk_bf16_f32 v232, v232, v233\n\tv_cvt_pk_bf16_f32 v233, v234, v235\n\t" \
  "ds_write_b64 %[ktw], v[232:233] offset:512\n\t" \
  "s_waitcnt lgkmcnt(0)\n\t" ST(88) \
  "s_barrier\n\t" ST(90)
#define R1_WOPS [a0] "v"(w0[0]), [a1] "v"(w0[1]), [a2] "v"(w0[2]), [a3] "v"(w0[3]), [a4] "v"(w0[4]), [a5] "v"(w0[5]), [a6] "v"(w0[6])
#define R1_WOPS2 [b0] "v"(w1[0]), [b1] "v"(w1[1]), [b2] "v"(w1[2]), [b3] "v"(w1[3]), [b4] "v"(w1[4]), [b5] "v"(w1[5]), [b6] "v"(w1[6])
#define R1_COMMON [idx] "v"(idx), [rd] "v"(rd), [ktw] "v"(ktw), [moff] "v"(moff), [src] "s"(src), [m0v] "s"(m0v)

template <int KIND>
DEV void r1_mfma(unsigned char* __restrict__ ws, int h, int q, int wave, int lane, unsigned sel, int smk) {
  const int g = lane >> 4, c16 = lane & 15;
  const int idx = (lane & 8) ? 0xEEEE : 0x4444;
  const unsigned moff = wave * 1024 + g * 256 + (lane & 7) * 32 + ((lane >> 3) & 1) * 16;
  u8v w0[7], w1[7];
  {
    const unsigned char* p = ws + O_WA + (size_t)(h * 28 + 7 * q + 2 * wave) * 14336 + lane * 16;
#pragma unroll
    for (int k = 0; k < 7; ++k) {
      w0[k] = cat8(*(const u4v*)(p + k * 2048), *(const u4v*)(p + k * 2048 + 1024));
      if (KIND != 2) w1[k] = cat8(*(const u4v*)(p + 14336 + k * 2048), *(const u4v*)(p + 14336 + k * 2048 + 1024));
    }
#pragma unroll
    for (int k = 0; k < 7; ++k) { asm volatile("" : "+v"(w0[k])); if (KIND != 2) asm volatile("" : "+v"(w1[k])); }
  }
  RTS(1);
  const unsigned rd = L_MEM + lane * 16, ktw = L_KT + 2 * wave * 512 + c16 * 32 + g * 8, m0v = L_MEM + wave * 1024;
  const unsigned a0a = L_MEM + (7 * q + g) * 256 + c16 * 16, a1a = L_MEM + min(7 * q + 4 + g, 27) * 256 + c16 * 16;
  const unsigned trb = L_KT + (g >> 1) * 512 + (4 * (g & 1) + (c16 >> 2)) * 32 + (c16 & 3) * 8;
  const unsigned sto = h * 512 + q * 128 + (4 * (g & 1) + 2 * (g >> 1) + (c16 >> 3)) * 16 + (c16 & 7) * 2;
  const unsigned long long lo8 = 0x00FF00FF00FF00FFull;
  unsigned ep = 0;
  int budget = TRY_CAP;
  rawbar();
  rawbar();
  for (int s = 0; s < NSEG; ++s) {
    const unsigned char* src = hblk(ws, sel, s, 0);
    unsigned char* dst = hblk(ws, sel, 64 + s, smk);
    for (;;) {
      if (KIND == 0) {
        unsigned long long okm;
        asm volatile(R1_HEAD
          "ds_read_b128 v[236:239], %[a0a]\n\tds_read_b128 v[240:243], %[a1a]\n\t"
          R1_TWO(8, 7, 6, 5, 4, 3, 2)
          "ds_read_b64_tr_b16 v[200:201], %[trb]\n\tds_read_b64_tr_b16 v[202:203], %[trb] offset:256\n\t"
          "ds_read_b64_tr_b16 v[204:205], %[trb] offset:1024\n\tds_read_b64_tr_b16 v[206:207], %[trb] offset:1280\n\t"
          "ds_read_b64_tr_b16 v[208:209], %[trb] offset:2048\n\tds_read_b64_tr_b16 v[210:211], %[trb] offset:2304\n\t"
          "ds_read_b64_tr_b16 v[212:213], %[trb] offset:3072\n\tds_read_b64_tr_b16 v[214:215], %[trb] offset:3328\n\t"
          "s_waitcnt lgkmcnt(4)\n\t" SMF "v[244:247], v[236:239], v[200:207], %[idx]\n\t"
          "s_waitcnt lgkmcnt(0)\n\t" SMF "v[244:247], v[240:243], v[208:215], %[idx]\n\t"
          "s_nop 7\n\t"
          "v_permlane32_swap_b32 v244, v246\n\tv_permlane32_swap_b32 v245, v247\n\t"
          "v_add_f32 v244, v244, v246\n\tv_add_f32 v245, v245, v247\n\t"
          "v_cndmask_b32 v246, v244, v245, %[lo8]\n\t"
          "v_cndmask_b32 v247, v245, v244, %[lo8]\n\t"
          "s_nop 1\n\t"
          "v_add_f32_dpp v247, v246, v247 row_ror:8 row_mask:0xf bank_mask:0xf\n\t"
          "v_cvt_pk_bf16_f32 v246, v247, v247\n\t"
          "v_cmpx_o_f32 vcc, v247, v247\n\t"
          "global_store_short %[sto], v246, %[dst] sc1\n\t" ST(92)
          "s_mov_b64 exec, -1\n\t"
          "s_mov_b64 %[okm], vcc\n\t"
          : [okm] "=s"(okm)
          : R1_WOPS, R1_WOPS2, R1_COMMON, [a0a] "v"(a0a), [a1a] "v"(a1a), [trb] "v"(trb), [sto] "v"(sto), [dst] "s"(dst), [lo8] "s"(lo8)
          : CLOB_V200_247, "vcc", "memory" ST_CLOB);
        if (okm != ~0ull) {
          if (budget > 0) { if (lane == 0) lds_w4(L_FLAG, ep); }
          else asm volatile("global_store_short %0, %1, %2 sc1" :: "v"(sto), "v"(0x7FC0u), "s"(dst) : "memory");
        }
      } else if (KIND == 1) {
        asm volatile(R1_HEAD R1_TWO(6, 5, 4, 3, 2, 1, 0) :: R1_WOPS, R1_WOPS2, R1_COMMON : CLOB_V200_247, "memory" ST_CLOB);
      } else {
        asm volatile(R1_HEAD
          "s_waitcnt lgkmcnt(6)\n\t" SMF "v[228:231], v[200:203], %[a0], %[idx]\n\t"
          "s_waitcnt lgkmcnt(5)\n\t" SMF "v[228:231], v[204:207], %[a1], %[idx]\n\t"
          "s_waitcnt lgkmcnt(4)\n\t" SMF "v[228:231], v[208:211], %[a2], %[idx]\n\t"
          "s_waitcnt lgkmcnt(3)\n\t" SMF "v[228:231], v[212:215], %[a3], %[idx]\n\t"
          "s_waitcnt lgkmcnt(2)\n\t" SMF "v[228:231], v[216:219], %[a4], %[idx]\n\t"
          "s_waitcnt lgkmcnt(1)\n\t" SMF "v[228:231], v[220:223], %[a5], %[idx]\n\t"
          "s_waitcnt lgkmcnt(0)\n\t" SMF "v[228:231], v[224:227], %[a6], %[idx]\n\t"
          "s_nop 7\n\t"
          "v_cvt_pk_bf16_f32 v228, v228, v229\n\tv_cvt_pk_bf16_f32 v229, v230, v231\n\t"
          "ds_write_b64 %[ktw], v[228:229]\n\t"
          "s_waitcnt lgkmcnt(0)\n\t"
          "s_barrier\n\t"
          :: R1_WOPS, R1_COMMON : CLOB_V200_247, "memory" ST_CLOB);
      }
      bar();
      const bool bad = __builtin_amdgcn_readfirstlane(lds_r4(L_FLAG)) == ep;
      ++ep;
      if (!bad) break;
      --budget;
      rawbar();
    }
    if (KIND == 1 && q < 2) {
      const LDS u4v* gp = (const LDS u4v*)(size_t)(L_G + (s % 3) * 14336 + lane * 16);
      const LDS u4v* xp = (const LDS u4v*)(size_t)(L_XN + (s & 1) * 28672 + (g >> 1) * 1024 + (g & 1) * 512 + (wave - 1) * 256 + c16 * 16);
      f4v c = {0.f, 0.f, 0.f, 0.f};
      u4v ga[6], xa[6];
#pragma unroll
      for (int T = 0; T < 5; ++T) { ga[T] = gp[T * 64]; xa[T] = xp[T * 128]; }
#pragma unroll
      for (int T = 0; T < 14; ++T) {
        if (T + 5 < 14) { ga[(T + 5) % 6] = gp[(T + 5) * 64]; xa[(T + 5) % 6] = xp[(T + 5) * 128]; }
        c = mfma16(ga[T % 6], xa[T % 6], c);
        __builtin_amdgcn_sched_barrier(0);
      }
      st8c_asm(ws + O_ST + (size_t)(s * 16 + h) * 2048 + (wave - 1) * 1024 + q * 512 + c16 * 32 + g * 8, (u2v){pk2(c[0], c[1]), pk2(c[2], c[3])});
      if (s == NSEG - 1) vm0();
    }
    ARR(s);
    rawbar();
  }
}

DEV void role_r1(unsigned char* __restrict__ ws, int id, int wave, int lane, unsigned sel TUNE_ARG) {
  const int h = id >> 2, q = id & 3;
  if (wave == 0) { r1_mfma<0>(ws, h, q, wave, lane, sel, K_SMK); return; }
  if (wave < 3) { r1_mfma<1>(ws, h, q, wave, lane, sel, K_SMK); return; }
  if (wave == 3) { r1_mfma<2>(ws, h, q, wave, lane, sel, K_SMK); return; }
  const int g = lane >> 4, c16 = lane & 15;
  const bool act = q < 2;
  const int fr = wave - 1;
  const unsigned moff = fr * 1024 + g * 256 + (lane & 7) * 32 + ((lane >> 3) & 1) * 16, m0v = L_MEM + fr * 1024;
  const unsigned xo = L_XN + (g >> 1) * 1024 + (g & 1) * 512 + c16 * 16;
  unsigned ep = 0;
  if (wave == 7) {
    const int idx = (lane & 8) ? 0xEEEE : 0x4444;
    u8v w0[7];
    const unsigned rd = lane * 16;
    const unsigned sto = h * 512 + (q & 1) * 256 + (4 * (g & 1) + 2 * (g >> 1) + (lane & 1)) * 32 + (c16 >> 1) * 4;
    const unsigned long long odd = 0xAAAAAAAAAAAAAAAAull;
    __builtin_amdgcn_s_setprio(3);
    rawbar();
    for (int s = 0; s < NSEG; ++s) {
      unsigned char* dst = hblk(ws, sel, s, 2);
      unsigned char* raw = hblk(ws, sel, s, 1);
      if (act) {
#pragma unroll
        for (int k = 0; k < 7; ++k) w0[k] = cat8(lds_r16(L_G + (s % 3) * 14336 + rd + k * 2048), lds_r16(L_G + (s % 3) * 14336 + rd + k * 2048 + 1024));
      }
      for (;;) {
        if (act) {
          asm volatile(
            "v_mov_b32 v228, 0\n\tv_mov_b32 v229, 0\n\tv_mov_b32 v230, 0\n\tv_mov_b32 v231, 0\n\t"
            "s_barrier\n\ts_barrier\n\ts_barrier\n\t" ST(80)
            "ds_read_b128 v[200:203], %[rd]\n\tds_read_b128 v[204:207], %[rd] offset:1024\n\tds_read_b128 v[208:211], %[rd] offset:2048\n\t"
            "ds_read_b128 v[212:215], %[rd] offset:3072\n\tds_read_b128 v[216:219], %[rd] offset:4096\n\tds_read_b128 v[220:223], %[rd] offset:5120\n\t"
            "ds_read_b128 v[224:227], %[rd] offset:6144\n\t"
            "s_waitcnt lgkmcnt(6)\n\t" SMF "v[228:231], v[200:203], %[a0], %[idx]\n\t"
            "s_waitcnt lgkmcnt(5)\n\t" SMF "v[228:231], v[204:207], %[a1], %[idx]\n\t"
            "s_waitcnt lgkmcnt(4)\n\t" SMF "v[228:231], v[208:211], %[a2], %[idx]\n\t"
            "s_waitcnt lgkmcnt(3)\n\t" SMF "v[228:231], v[212:215], %[a3], %[idx]\n\t"
            "s_waitcnt lgkmcnt(2)\n\t" SMF "v[228:231], v[216:219], %[a4], %[idx]\n\t"
            "s_waitcnt lgkmcnt(1)\n\t" SMF "v[228:231], v[220:223], %[a5], %[idx]\n\t"
            "s_waitcnt lgkmcnt(0)\n\t" SMF "v[228:231], v[224:227], %[a6], %[idx]\n\t"
            "s_nop 7\n\t"
            "v_permlane32_swap_b32 v228, v230\n\tv_permlane32_swap_b32 v229, v231\n\t"
            "v_add_f32 v228, v228, v230\n\tv_add_f32 v229, v229, v231\n\t"
            "v_cndmask_b32 v230, v229, v228, %[odd]\n\t"
            "s_nop 1\n\t"
            "v_mov_b32_dpp v230, v230 quad_perm:[1,0,3,2] row_mask:0xf bank_mask:0xf\n\t"
            "v_cndmask_b32 v228, v228, v230, %[odd]\n\tv_cndmask_b32 v229, v230, v229, %[odd]\n\t"
            "v_exp_f32 v230, v228\n\tv_exp_f32 v231, v229\n\t"
            "v_cvt_pk_bf16_f32 v232, v228, v229\n\t"
            "v_cvt_pk_bf16_f32 v233, v230, v231\n\t"
            "v_cmpx_o_f32 vcc, v228, v229\n\t"
            "global_store_dword %[sto], v233, %[dst] sc1\n\t" ST(82)
            "global_store_dword %[sto], v232, %[raw] sc1\n\t"
            "s_mov_b64 exec, -1\n\t"
            "s_barrier\n\t"
            :: R1_WOPS, [idx] "v"(idx), [rd] "v"(rd), [sto] "v"(sto), [dst] "s"(dst), [raw] "s"(raw), [odd] "s"(odd)
            : CLOB_V200_247, "vcc", "memory" ST_CLOB);
          rawbar();
        } else {
          rawbar(); rawbar(); rawbar(); rawbar(); rawbar();
        }
        const bool bad = __builtin_amdgcn_readfirstlane(lds_r4(L_FLAG)) == ep;
        ++ep;
        if (!bad) break;
      }
    }
    rawbar();
  } else if (wave == 4) {
    const unsigned trb = L_KT + (g >> 1) * 512 + (4 * (g & 1) + (c16 >> 2)) * 32 + (c16 & 3) * 8;
    unsigned char* so = ws + O_SM + (size_t)h * 2048 + q * 256 + lane * 4;
    const unsigned x3 = xo + 7 * q * 1024, xlast = g < 2 ? 6144 : 5120;
    lds_w4(L_KT + 7 * 512 + lane * 8, 0u); lds_w4(L_KT + 7 * 512 + lane * 8 + 4, 0u);
    if (lane == 0) lds_w4(L_FLAG, SENT);
    rawbar();
    for (int s = 0; s < NSEG; ++s) {
      bool sig = s > 0;
      for (;;) {
        bar(); rawbar(); rawbar();
        if (sig) { vm0(); if (lane == 0) st4c_asm(flagS(ws, sel, s - 1, id), 0u); sig = false; }
        rawbar(); rawbar();
        const bool bad = __builtin_amdgcn_readfirstlane(lds_r4(L_FLAG)) == ep;
        ++ep;
        if (!bad) break;
      }
      f4v c0 = {0.f, 0.f, 0.f, 0.f}, c1 = c0;
      u4v ka[4], xa[4], xb[4];
#pragma unroll
      for (int u = 0; u < 4; ++u) {
        const u2v lo = lds_tr(trb + u * 1024), hi = lds_tr(trb + u * 1024 + 256);
        ka[u] = (u4v){lo.x, lo.y, hi.x, hi.y};
        const unsigned ad = x3 + (s & 1) * 28672 + (u < 3 ? u * 2048 : xlast);
        xa[u] = lds_r16(ad); xb[u] = lds_r16(ad + 256);
      }
      __builtin_amdgcn_sched_barrier(0);
#pragma unroll
      for (int u = 0; u < 4; ++u) { c0 = mfma16(ka[u], xa[u], c0); c1 = mfma16(ka[u], xb[u], c1); }
      st4c_asm(so + (size_t)s * 32768, pk2(halfsum(c0[0], c0[1]), halfsum(c0[2], c0[3])));
      st4c_asm(so + (size_t)s * 32768 + 1024, pk2(halfsum(c1[0], c1[1]), halfsum(c1[2], c1[3])));
      ARR(s);
    }
    vm0();
    rawbar();
    if (lane == 0) st4c_asm(flagS(ws, sel, NSEG - 1, id), 0u);
  } else if (wave == 5) {
    const unsigned char* gp = ws + O_G + (size_t)(h * 2 + (q & 1)) * 14336;
    const unsigned v0 = lane * 16, v1 = v0 + 4096, v2 = v0 + 8192, v3 = v0 + 12288;
    auto fetch = [&](int t) {
#define GL4(v) "global_load_lds_dwordx4 " v ", %4 sc1\n\tglobal_load_lds_dwordx4 " v ", %4 offset:1024 sc1\n\t"
#define GL8(v) GL4(v) "global_load_lds_dwordx4 " v ", %4 offset:2048 sc1\n\tglobal_load_lds_dwordx4 " v ", %4 offset:3072 sc1\n\t"
      asm volatile("s_mov_b32 m0, %5\n\ts_nop 0\n\t" GL8("%0") "s_add_u32 m0, m0, 4096\n\ts_nop 0\n\t" GL8("%1")
                   "s_add_u32 m0, m0, 4096\n\ts_nop 0\n\t" GL8("%2") "s_add_u32 m0, m0, 4096\n\ts_nop 0\n\t" GL4("%3")
                   :: "v"(v0), "v"(v1), "v"(v2), "v"(v3), "s"(gp + (size_t)t * 458752), "s"(L_G + (t % 3) * 14336) : "scc", "memory");
    };
    auto fetchx = [&](int t) {
      const unsigned char* src = ws + O_XN + (size_t)t * 28672;
      const unsigned dstl = L_XN + (t & 1) * 28672;
      if (act) {
#pragma unroll
        for (int k = 0; k < 7; ++k)
          asm volatile("s_mov_b32 m0, %2\n\ts_nop 0\n\tglobal_load_lds_dwordx4 %0, %1\n\tglobal_load_lds_dwordx4 %0, %1 offset:1024\n\t"
                       "global_load_lds_dwordx4 %0, %1 offset:2048\n\tglobal_load_lds_dwordx4 %0, %1 offset:3072"
                       :: "v"(v0), "s"(src + k * 4096), "s"(dstl + k * 4096) : "memory");
      } else {
#pragma unroll
        for (int k = 0; k < 7; ++k) dma16(src + (7 * q + k) * 1024 + v0, dstl + (7 * q + k) * 1024);
      }
    };
    auto peek = [&](int t) {
      unsigned keep;
      asm volatile("s_mov_b32 %0, m0\n\ts_mov_b32 m0, %2\n\ts_nop 0\n\tglobal_load_lds_dword %1, off sc1\n\ts_mov_b32 m0, %0"
                   : "=&s"(keep) : "v"(cntp(ws, C_G + t * 8 + (h >> 1))), "s"(L_CG) : "memory");
    };
    if (act) {
      wait_cnt(ws, cntp(ws, C_G + (lane & 1) * 8 + (h >> 1)), 28);
      fetch(0); fetch(1); peek(2);
    }
    fetchx(0);
    vm0();
    RTS(1);
    rawbar();
    for (int s = 0; s < NSEG; ++s) {
      const unsigned char* src = hblk(ws, sel, s, 0) + moff;
      bool todo = act && s + 2 < NSEG;
      for (;;) {
        rawbar(); rawbar();
        dma16c(src, m0v);
        vm0();
        rawbar();
        if (todo && __builtin_amdgcn_readfirstlane(lds_r4(L_CG)) >= 28) { fetch(s + 2); peek(min(s + 3, NSEG - 1)); todo = false; }
        rawbar();
        rawbar();
        const bool bad = __builtin_amdgcn_readfirstlane(lds_r4(L_FLAG)) == ep;
        ++ep;
        if (!bad) break;
      }
      if (todo) {
        wait_cnt(ws, cntp(ws, C_G + (s + 2) * 8 + (h >> 1)), 28);
        fetch(s + 2); peek(min(s + 3, NSEG - 1));
      }
      if (s + 1 < NSEG) fetchx(s + 1);
      ARR(s);
    }
    vm0();
    bar();
  } else {
    const int pe = K_PERIOD / 24, pf = K_PERIOD % 24;
    Lat L = {get_anchor(ws, id == 0), 0, 0};
    rawbar();
    for (int s = 0; s < NSEG; ++s) {
      const unsigned char* src = hblk(ws, sel, s, 0) + moff;
      for (;;) {
        rawbar();
        lat_wait(L);
        rawbar();
        asm volatile("s_mov_b32 m0, %1\n\ts_nop 0\n\tglobal_load_lds_dwordx4 %0, off sc1\n\tglobal_load_lds_dwordx4 %0, off offset:1024 sc1\n\ts_waitcnt vmcnt(0)"
                     :: "v"(src), "s"(m0v) : "memory");
        rawbar(); rawbar(); rawbar();
        const bool bad = __builtin_amdgcn_readfirstlane(lds_r4(L_FLAG)) == ep;
        ++ep;
        EV(bad ? 1 : 0);
        if (!bad) break;
        L.e += RET_E;
      }
      lat_fwd(L, pe, pf);
      ARR(s);
    }
    bar();
    EV_OUT(id);
  }
}

#define XRED(op, v, t) \
  "v_mov_b32 " t ", " v "\n\ts_nop 1\n\tv_permlane16_swap_b32 " v ", " t "\n\t" op " " v ", " v ", " t "\n\t" \
  "v_mov_b32 " t ", " v "\n\ts_nop 1\n\tv_permlane32_swap_b32 " v ", " t "\n\t" op " " v ", " v ", " t "\n\t"
#define MF16 "v_mfma_f32_16x16x32_bf16 "
#define WAIT_CYC(T) \
  "s_memtime vcc\n\ts_waitcnt lgkmcnt(0)\n\t" \
  "s_sub_i32 s70, " T ", vcc_lo\n\ts_max_i32 s70, s70, 0\n\ts_mul_i32 s70, s70, 13\n\ts_lshr_b32 s70, s70, 6\n\t" \
  "s_min_i32 s70, s70, 128\n\ts_lshl_b32 s70, s70, 2\n\ts_sub_i32 s70, 524, s70\n\t" \
  "s_getpc_b64 vcc\n\ts_add_u32 vcc_lo, vcc_lo, s70\n\ts_addc_u32 vcc_hi, vcc_hi, 0\n\ts_setpc_b64 vcc\n\t" \
  ".rept 128\n\ts_nop 0\n\t.endr\n\t"
#define R3_Y_A \
  "ds_read_b128 v[200:203], %[rd]\n\tds_read_b128 v[204:207], %[rd] offset:1024\n\tds_read_b128 v[208:211], %[rd] offset:2048\n\t" \
  "ds_read_b128 v[212:215], %[rd] offset:3072\n\tds_read_b128 v[216:219], %[rd] offset:4096\n\tds_read_b128 v[220:223], %[rd] offset:5120\n\t" \
  "ds_read_b128 v[224:227], %[rd] offset:6144\n\t" \
  "s_waitcnt lgkmcnt(6)\n\t" SMF "v[228:231], v[200:203], %[a0], %[idx]\n\t" SMF "v[232:235], v[200:203], %[b0], %[idx]\n\t" \
  "s_waitcnt lgkmcnt(5)\n\t" SMF "v[228:231], v[204:207], %[a1], %[idx]\n\t" SMF "v[232:235], v[204:207], %[b1], %[idx]\n\t" \
  "s_waitcnt lgkmcnt(4)\n\t" SMF "v[228:231], v[208:211], %[a2], %[idx]\n\t" SMF "v[232:235], v[208:211], %[b2], %[idx]\n\t" \
  "s_waitcnt lgkmcnt(3)\n\t" SMF "v[228:231], v[212:215], %[a3], %[idx]\n\t" SMF "v[232:235], v[212:215], %[b3], %[idx]\n\t" \
    \
  "s_waitcnt lgkmcnt(0)\n\t" \
  "v_max3_u32 v244, v216, v217, v218\n\tv_max3_u32 v245, v219, v220, v221\n\tv_max3_u32 v244, v244, v222, v223\n\t" \
  "v_max3_u32 v245, v245, v224, v225\n\tv_max3_u32 v244, v244, v226, v227\n\tv_max_u32 v244, v244, v245\n\t" \
  "v_cmp_eq_u32 s[74:75], -1, v244\n\t" \
  "v_cmp_u_f32 vcc, v228, v229\n\t" \
  "s_or_b64 s[74:75], s[74:75], vcc\n\t" \
  "v_cmp_u_f32 vcc, v234, v235\n\t" \
  "s_or_b64 vcc, s[74:75], vcc\n\t"
#define R3_Y_B \
  SMF "v[228:231], v[216:219], %[a4], %[idx]\n\t" SMF "v[232:235], v[216:219], %[b4], %[idx]\n\t" \
  SMF "v[228:231], v[220:223], %[a5], %[idx]\n\t" SMF "v[228:231], v[224:227], %[a6], %[idx]\n\t" \
  SMF "v[232:235], v[220:223], %[b5], %[idx]\n\t" SMF "v[232:235], v[224:227], %[b6], %[idx]\n\t" \
  "s_nop 1\n\t" \
  "v_permlane32_swap_b32 v228, v229\n\tv_permlane32_swap_b32 v230, v231\n\t" \
  "v_add_f32 v228, v228, v229\n\tv_add_f32 v230, v230, v231\n\t" \
  "v_cvt_pk_bf16_f32 v164, v228, v230\n\t" \
  "s_nop 0\n\t" \
  "v_permlane32_swap_b32 v232, v233\n\tv_permlane32_swap_b32 v234, v235\n\t" \
  "v_add_f32 v232, v232, v233\n\tv_add_f32 v234, v234, v235\n\t" \
  "v_cvt_pk_bf16_f32 v165, v232, v234\n\t" \
  ST(82)
#define R3_POLL \
  WAIT_CYC("s68") \
  "global_load_dwordx4 v[236:239], %[to], s[60:61] sc1\n\t" \
  WAIT_CYC("s69") \
  "global_load_dwordx4 v[240:243], %[tm], s[60:61] sc1\n\t" ST(86)
#define R3_POLL2   \
  "55:\n\ts_memtime vcc\n\ts_waitcnt lgkmcnt(0)\n\ts_sub_i32 s70, s69, vcc_lo\n\ts_cmp_gt_i32 s70, 0\n\ts_cbranch_scc1 55b\n\t" \
  "global_load_dwordx4 v[236:239], %[to], s[60:61] sc1\n\tglobal_load_dwordx4 v[240:243], %[tm], s[60:61] sc1\n\t"
#define R3_S_PHASE(SIG, NS) \
  "s_waitcnt vmcnt(1)\n\t" ST(84) \
  "v_cndmask_b32 v208, 0, v236, s[76:77]\n\tv_cndmask_b32 v209, 0, v237, s[76:77]\n\tv_cndmask_b32 v210, 0, v238, s[76:77]\n\tv_cndmask_b32 v211, 0, v239, s[76:77]\n\t" \
  MF16 "v[220:223], v[160:163], v[236:239], 0\n\t" \
  "v_cndmask_b32 v212, v236, 0, s[76:77]\n\tv_cndmask_b32 v213, v237, 0, s[76:77]\n\tv_cndmask_b32 v214, v238, 0, s[76:77]\n\tv_cndmask_b32 v215, v239, 0, s[76:77]\n\t" \
  MF16 "v[216:219], v[192:195], v[208:211], 0\n\t" \
  "s_nop 0\n\t" \
  MF16 "v[216:219], v[196:199], v[212:215], v[216:219]\n\t" \
  "global_store_dwordx2 %[yo], v[164:165], s[60:61] sc1\n\t"        \
  SIG "s_waitcnt vmcnt(" #NS ")\n\t" ST(88) \
  MF16 "v[232:235], %[selA], v[240:243], 0\n\t" \
  "s_nop 7\n\t" \
  "v_exp_f32 v234, v232\n\tv_exp_f32 v235, v233\n\t" \
  "s_nop 0\n\t" \
  "v_cvt_pk_bf16_f32 v240, v234, v235\n\t" \
  "v_cndmask_b32 v168, 0, v240, s[76:77]\n\tv_cndmask_b32 v169, v240, 0, s[76:77]\n\t" \
  "s_nop 1\n\t" \
  MF16 "v[220:223], v[160:163], v[168:171], v[220:223]\n\t" \
  MF16 "v[244:247], v[164:167], v[168:171], v[216:219]\n\t" \
  "s_nop 6\n\t" \
  "v_rcp_f32 v242, v220\n\t" \
  "v_med3_f32 v241, v220, v172, v173\n\t" \
  "v_cmp_lg_f32 vcc, v241, v220\n\t" \
  "s_cbranch_vccnz 12f\n\t" \
  "11:\n\t" \
  "v_pk_mul_f32 v[244:245], v[244:245], v[242:243] op_sel_hi:[1,0]\n\t" \
  "v_pk_mul_f32 v[246:247], v[246:247], v[242:243] op_sel_hi:[1,0]\n\t" \
  "s_nop 0\n\t" \
  "v_add_f32_dpp v244, v244, v244 row_ror:8 row_mask:0xf bank_mask:0xf\n\tv_add_f32_dpp v245, v245, v245 row_ror:8 row_mask:0xf bank_mask:0xf\n\t" \
  "v_add_f32_dpp v246, v246, v246 row_ror:8 row_mask:0xf bank_mask:0xf\n\tv_add_f32_dpp v247, v247, v247 row_ror:8 row_mask:0xf bank_mask:0xf\n\t" \
  ST(90) "ds_write_b128 %[redw], v[244:247]\n\t" \
  "s_waitcnt lgkmcnt(0)\n\t" \
  "s_barrier\n\t" ST(92)
#define R3_S_RARE \
    "12:\n\t" \
  "v_xor_b32 v241, 0x6000, %[to]\n\t" \
  "global_load_dwordx4 v[200:203], v241, s[60:61] sc1\n\t" \
  "s_waitcnt vmcnt(0)\n\t" \
  "v_lshlrev_b32 v208, 16, v200\n\tv_and_b32 v209, 0xffff0000, v200\n\tv_lshlrev_b32 v210, 16, v201\n\tv_and_b32 v211, 0xffff0000, v201\n\t" \
  "v_lshlrev_b32 v212, 16, v202\n\tv_and_b32 v213, 0xffff0000, v202\n\tv_lshlrev_b32 v214, 16, v203\n\tv_and_b32 v215, 0xffff0000, v203\n\t" \
  "v_max3_f32 v240, v208, v209, v210\n\tv_max3_f32 v241, v211, v212, v213\n\tv_max3_f32 v240, v240, v214, v215\n\t" \
  "v_max3_f32 v241, v241, v232, v233\n\tv_max_f32 v240, v240, v241\n\t" \
  XRED("v_max_f32", "v240", "v241") \
  "v_sub_f32 v208, v208, v240\n\tv_sub_f32 v209, v209, v240\n\tv_sub_f32 v210, v210, v240\n\tv_sub_f32 v211, v211, v240\n\t" \
  "v_sub_f32 v212, v212, v240\n\tv_sub_f32 v213, v213, v240\n\tv_sub_f32 v214, v214, v240\n\tv_sub_f32 v215, v215, v240\n\t" \
  "v_sub_f32 v234, v232, v240\n\tv_sub_f32 v235, v233, v240\n\t" \
  "v_exp_f32 v208, v208\n\tv_exp_f32 v209, v209\n\tv_exp_f32 v210, v210\n\tv_exp_f32 v211, v211\n\t" \
  "v_exp_f32 v212, v212\n\tv_exp_f32 v213, v213\n\tv_exp_f32 v214, v214\n\tv_exp_f32 v215, v215\n\t" \
  "v_exp_f32 v234, v234\n\tv_exp_f32 v235, v235\n\ts_nop 1\n\t" \
  "v_cvt_pk_bf16_f32 v200, v208, v209\n\tv_cvt_pk_bf16_f32 v201, v210, v211\n\tv_cvt_pk_bf16_f32 v202, v212, v213\n\tv_cvt_pk_bf16_f32 v203, v214, v215\n\t" \
  "v_cvt_pk_bf16_f32 v240, v234, v235\n\t" \
  "v_cndmask_b32 v208, 0, v200, s[76:77]\n\tv_cndmask_b32 v209, 0, v201, s[76:77]\n\tv_cndmask_b32 v210, 0, v202, s[76:77]\n\tv_cndmask_b32 v211, 0, v203, s[76:77]\n\t" \
  "v_cndmask_b32 v212, v200, 0, s[76:77]\n\tv_cndmask_b32 v213, v201, 0, s[76:77]\n\tv_cndmask_b32 v214, v202, 0, s[76:77]\n\tv_cndmask_b32 v215, v203, 0, s[76:77]\n\t" \
  "v_cndmask_b32 v168, 0, v240, s[76:77]\n\tv_cndmask_b32 v169, v240, 0, s[76:77]\n\ts_nop 1\n\t" \
  MF16 "v[220:223], v[160:163], v[200:203], 0\n\t" \
  MF16 "v[220:223], v[160:163], v[168:171], v[220:223]\n\t" \
  MF16 "v[244:247], v[192:195], v[208:211], 0\n\t" \
  MF16 "v[244:247], v[196:199], v[212:215], v[244:247]\n\t" \
  MF16 "v[244:247], v[164:167], v[168:171], v[244:247]\n\t" \
  "s_nop 7\n\t" \
  "v_rcp_f32 v242, v220\n\ts_nop 0\n\t" \
  "s_branch 11b\n\t"
#define R3_PREP "s_lshl_b32 s44, s65, 2\n\ts_add_i32 s44, s44, s67\n\ts_lshl_b32 s45, s66, 2\n\ts_sub_i32 s45, 108, s45\n\t"
#define R3_FWD "s_add_i32 s66, s66, s47\n\ts_add_i32 s65, s65, s46\n\ts_cmp_ge_i32 s66, 24\n\ts_cselect_b32 s70, 24, 0\n\ts_addc_u32 s65, s65, 0\n\ts_sub_i32 s66, s66, s70\n\t"
#define R3_RT "s_memrealtime vcc\n\ts_waitcnt lgkmcnt(0)\n\ts_sub_i32 s70, s44, vcc_lo\n\ts_cmp_lt_i32 s70, 1\n\t"
#define R3_TW_WAIT \
  R3_RT "s_cbranch_scc1 40f\n\t" \
  "20:\n\t" R3_RT "s_cbranch_scc1 21f\n\t" R3_RT "s_cbranch_scc1 21f\n\t" R3_RT "s_cbranch_scc1 21f\n\t" R3_RT "s_cbranch_scc0 20b\n\t" \
  "21:\n\t" \
  "s_getpc_b64 vcc\n\ts_add_u32 vcc_lo, vcc_lo, s45\n\ts_addc_u32 vcc_hi, vcc_hi, 0\n\ts_setpc_b64 vcc\n\t" \
  ".rept 24\n\ts_nop 0\n\t.endr\n\t" \
  "22:\n\t"
#define R3_TW_LATE \
  "40:\n\t" \
  "s_cmp_eq_u32 s70, 0\n\ts_cbranch_scc1 22b\n\t" \
  "41:\n\ts_add_i32 s65, s65, 24\n\ts_add_i32 s44, s44, 4*24\n\ts_add_i32 s70, s70, 4*24\n\ts_cmp_lt_i32 s70, 0\n\ts_cbranch_scc1 41b\n\t" \
  "s_branch 20b\n\t"
#define R3_STEP(op, uop)   \
  op " %[moff], 0x8000, %[moff]\n\t" op " %[to], 0x8000, %[to]\n\t" op " %[tm], 0x8000, %[tm]\n\t" op " %[sto], 0x8000, %[sto]\n\t" \
  uop " %[uo], 0x70000, %[uo]\n\t" uop " %[yo], 0x1c000, %[yo]\n\t"
#define R3_FLIP \
  "s_and_b32 s70, s62, 15\n\ts_cselect_b32 s70, 0, 0x4000\n\ts_add_i32 s71, s62, 1\n\ts_and_b32 s71, s71, 15\n\ts_cselect_b32 s71, 0, 0x4000\n\t" \
  "v_xor_b32 %[moff], s70, %[moff]\n\tv_xor_b32 %[to], s70, %[to]\n\tv_xor_b32 %[tm], s70, %[tm]\n\tv_xor_b32 %[sto], s71, %[sto]\n\t"
#define R3_DMA "global_load_lds_dwordx4 %[moff], s[60:61] sc1\n\t"
#define R3_LOOP(DMA, TWWAIT, CNTLOAD, NW, CNTSIG, NS, TWFWD, FIN, CNTCHK, OUTOFLINE) \
  "s_mov_b64 s[60:61], %[wsp]\n\ts_mov_b32 s62, 0\n\ts_mov_b32 s63, 0\n\ts_mov_b32 s64, 0x10000\n\ts_mov_b32 s78, 0\n\t" \
  "s_mov_b32 s76, 0x00ff00ff\n\ts_mov_b32 s77, s76\n\t" \
  "s_and_b32 s46, %[p0], 0xff\n\ts_bfe_u32 s47, %[p0], 0x80008\n\ts_bfe_u32 s65, %[p0], 0x80010\n\ts_lshr_b32 s66, %[p0], 24\n\t" \
  "s_and_b32 s48, %[p1], 0xffff\n\ts_bfe_u32 s49, %[p1], 0xc0010\n\ts_lshr_b32 s50, %[p1], 28\n\ts_lshl_b32 s50, s50, 10\n\t" \
  "v_mov_b32 v188, 0\n\tv_mov_b32 v160, 0x3f803f80\n\tv_mov_b32 v161, v160\n\tv_mov_b32 v162, v160\n\tv_mov_b32 v163, v160\n\t" \
  "v_mov_b32 v166, 0\n\tv_mov_b32 v167, 0\n\tv_mov_b32 v170, 0\n\tv_mov_b32 v171, 0\n\tv_mov_b32 v172, 0x1f800000\n\tv_mov_b32 v173, 0x71800000\n\t" \
  R3_PREP \
  "10:\n\t" \
  TWWAIT \
  "s_mov_b32 m0, s50\n\t" \
  "s_waitcnt lgkmcnt(0)\n\ts_barrier\n\t" \
  "s_memtime s[72:73]\n\t" \
  DMA \
  "ds_read_b32 v200, v188 offset:12288\n\t" \
  "s_waitcnt lgkmcnt(0)\n\t" \
  "v_readfirstlane_b32 s74, v200\n\t" \
  "s_cmp_eq_u32 s74, s63\n\ts_cbranch_scc1 50f\n\t" \
  "s_add_i32 s63, s63, 1\n\t" \
  "v_mov_b32 v228, 0\n\tv_mov_b32 v229, 0\n\tv_mov_b32 v230, 0\n\tv_mov_b32 v231, 0\n\t" \
  "v_mov_b32 v232, 0\n\tv_mov_b32 v233, 0\n\tv_mov_b32 v234, 0\n\tv_mov_b32 v235, 0\n\t" \
  "global_load_dwordx4 v[192:195], %[uo], s[60:61] sc1\n\t" \
  "global_load_dwordx4 v[196:199], %[uo], s[60:61] offset:1024 sc1\n\t" \
  CNTLOAD \
  "s_add_i32 s68, s72, s48\n\ts_add_i32 s69, s68, s49\n\t" \
  "s_waitcnt vmcnt(" #NW ")\n\t" \
  "s_barrier\n\t" ST(80) \
  R3_Y_A \
  "s_cbranch_vccnz 60f\n\t" \
  "14:\n\t" \
  "s_cmp_eq_u32 s62, 63\n\ts_cbranch_scc1 98f\n\t" \
  R3_POLL R3_Y_B \
  TWFWD \
  "15:\n\t" \
  R3_S_PHASE(CNTSIG, NS) FIN CNTCHK \
  "s_add_i32 s62, s62, 1\n\t" \
  R3_STEP("v_add_u32", "v_add_u32") R3_FLIP DIAG_R3 \
  "s_branch 10b\n\t" \
  R3_S_RARE \
    \
  "50:\n\t" \
  "s_sub_i32 s64, s64, 1\n\ts_add_i32 s63, s63, 1\n\t" \
  R3_FLIP R3_STEP("v_subrev_u32", "v_subrev_u32") \
  "s_sub_i32 s62, s62, 1\n\t" \
  "s_memtime vcc\n\ts_waitcnt lgkmcnt(0)\n\t" \
  "s_sub_i32 s70, s69, s68\n\ts_cmp_eq_u32 s70, s49\n\ts_cselect_b32 s70, 256, 0\n\ts_sub_i32 s69, s69, s70\n\t"       \
  "53:\n\ts_add_i32 s68, s68, 96*24\n\ts_add_i32 s69, s69, 96*24\n\ts_add_i32 s65, s65, 24\n\t" \
  "s_sub_i32 s70, s69, vcc_lo\n\ts_cmp_lt_i32 s70, 0\n\ts_cbranch_scc1 53b\n\t" \
  R3_PREP R3_POLL2 \
  "s_branch 15b\n\t" \
    \
  "60:\n\ts_sub_i32 s64, s64, 1\n\ts_cmp_gt_i32 s64, 0\n\ts_cbranch_scc0 14b\n\t" \
  "s_add_i32 s65, s65, 24\n\t" R3_PREP "s_branch 10b\n\t" \
  OUTOFLINE \
  "98:\n\t" R3_Y_B \
  "global_store_dwordx2 %[yo], v[164:165], s[60:61] sc1\n\t" \
  "s_waitcnt vmcnt(0)\n\ts_barrier\n\t"
#define R3_FIN \
  "ds_read_b128 v[200:203], %[redr]\n\tds_read_b128 v[204:207], %[redr] offset:512\n\t" \
  "ds_read_b128 v[208:211], %[redr] offset:1024\n\tds_read_b128 v[212:215], %[redr] offset:1536\n\t" \
  "s_waitcnt lgkmcnt(2)\n\t" \
  "v_pk_add_f32 v[200:201], v[200:201], v[204:205]\n\tv_pk_add_f32 v[202:203], v[202:203], v[206:207]\n\t" \
  "s_waitcnt lgkmcnt(1)\n\t" \
  "v_pk_add_f32 v[200:201], v[200:201], v[208:209]\n\tv_pk_add_f32 v[202:203], v[202:203], v[210:211]\n\t" \
  "s_waitcnt lgkmcnt(0)\n\t" \
  "v_pk_add_f32 v[200:201], v[200:201], v[212:213]\n\tv_pk_add_f32 v[202:203], v[202:203], v[214:215]\n\t" \
  "s_nop 1\n\t" \
  "v_permlane32_swap_b32 v200, v202\n\tv_permlane32_swap_b32 v201, v203\n\t" \
  "v_add_f32 v200, v200, v202\n\tv_add_f32 v201, v201, v203\n\t" \
  "v_cvt_pk_bf16_f32 v202, v200, v201\n\t" \
  "v_cmpx_o_f32 vcc, v200, v201\n\t" \
  "global_store_dword %[sto], v202, s[60:61] sc1\n\t" ST(94) \
  "s_mov_b64 exec, -1\n\t" \
  "s_cmp_eq_u64 vcc, -1\n\ts_cbranch_scc0 80f\n\t" \
  "16:\n\t"
#define R3_FIN_OOL \
  "80:\n\ts_cmp_gt_i32 s64, 0\n\ts_cbranch_scc0 81f\n\t" \
  "v_mov_b32 v200, s63\n\tds_write_b32 v188, v200 offset:12288\n\ts_branch 16b\n\t" \
  "81:\n\tv_mov_b32 v200, 0x7fc07fc0\n\tglobal_store_dword %[sto], v200, s[60:61] sc1\n\ts_branch 16b\n\t"
#define R3_CNT_SIG        \
  "global_store_dword v187, v188, s[60:61] sc1\n\t"
#define R3_CNT_LOAD \
  "v_mov_b32 v187, s43\n\t" \
  "s_min_u32 s70, s62, 62\n\ts_mul_i32 s70, s70, 896\n\tv_mov_b32 v186, s70\n\tglobal_load_dword v189, v186, s[40:41] sc1\n\t"
#define R3_CNT_CHK "v_readfirstlane_b32 s43, %[tm]\n\ts_and_b32 s43, s43, 0xffffe000\n\ts_xor_b32 s43, s43, s42\n\tv_readfirstlane_b32 s70, v189\n\ts_cmp_lt_u32 s70, 16\n\ts_cbranch_scc1 85f\n\t17:\n\t"
#define R3_CNT_OOL        \
  "85:\n\ts_mov_b32 s71, 0x40000\n\t" \
  "86:\n\tglobal_load_dword v189, v186, s[40:41] sc1\n\ts_waitcnt vmcnt(0)\n\tv_readfirstlane_b32 s70, v189\n\t" \
  "s_cmp_lt_u32 s70, 16\n\ts_cbranch_scc0 17b\n\ts_sleep 2\n\ts_sub_i32 s71, s71, 1\n\ts_cmp_gt_i32 s71, 0\n\ts_cbranch_scc1 86b\n\ts_branch 17b\n\t"
#define DIAG_R3
#define DIAG_R3_END
#define DIAG_R3_OUT
#define R3_OUTS [moff] "+v"(moff), [to] "+v"(to), [tm] "+v"(tm), [sto] "+v"(sto), [uo] "+v"(uo), [yo] "+v"(yo) DIAG_R3_OUT
#define R3_INS R1_WOPS, R1_WOPS2, [idx] "v"(idx), [rd] "v"(rd), [redw] "v"(redw), [selA] "v"(selA), [wsp] "s"(ws), [p0] "s"(p0), [p1] "s"(p1)
#define R3_CLOB CLOB_V200_247, "v160","v161","v162","v163","v164","v165","v166","v167","v168","v169","v170","v171","v172","v173", "v179","v180","v181","v182","v183","s96","s97","v184","v185","v186","v187","v188","v189","v190","v191","v192","v193","v194","v195","v196","v197","v198","v199", \
  "s40","s41","s42","s43","s44","s45","s46","s47","s48","s49","s50","s60","s61","s62","s63","s64","s65","s66","s67","s68","s69","s70","s71", \
  "s72","s73","s74","s75","s76","s77","s78","vcc","scc","memory" ST_CLOB
#define STR2(x) #x
#define STR(x) STR2(x)

template <int KIND>
DEV void r3_body(unsigned char* __restrict__ ws, int v, int wave, int lane, unsigned sel TUNE_ARG) {
  const int g = lane >> 4, hs = (lane >> 3) & 1, i8 = lane & 7;
  const int idx = (lane & 8) ? 0xEEEE : 0x4444;
  const int dw = wave < 7 ? wave : 6;
  const unsigned rd = lane * 16;
  u8v w0[7], w1[7];
  {
    const unsigned char* p = ws + O_WB + (size_t)(2 * wave * 28 + v) * 14336 + lane * 16;
#pragma unroll
    for (int k = 0; k < 7; ++k) {
      w0[k] = cat8(*(const u4v*)(p + k * 2048), *(const u4v*)(p + k * 2048 + 1024));
      w1[k] = cat8(*(const u4v*)(p + 28 * 14336 + k * 2048), *(const u4v*)(p + 28 * 14336 + k * 2048 + 1024));
    }
#pragma unroll
    for (int k = 0; k < 7; ++k) asm volatile("" : "+v"(w0[k]), "+v"(w1[k]));
  }
  RTS(1);
  int anchor = 0;
  if (KIND == 3) { anchor = get_anchor(ws, false); if (lane == 0) lds_w4(L_FLAG, SENT); }
  if (KIND == 2) wait_cnt(ws, cntp(ws, C_U + (v >> 1)), 16);
  bar();
  RTS(2);
  unsigned moff = hoff(sel, 0, 0) + dw * 1024 + g * 256 + i8 * 32 + hs * 16;
  unsigned to = hoff(sel, 0, 2) + (2 * wave + hs) * 512 + (g >> 1) * 256 + i8 * 32 + (g & 1) * 16;
  unsigned tm = hoff(sel, 64, K_SMK) + (2 * wave + hs) * 512 + g * 128 + i8 * 16;
  u4v selA = {0u, 0u, 0u, 0u};
  if (((lane & 3) >> 1) == 0) {
    const int gq = (lane >> 2) & 3, j = 4 * (gq & 1) + (gq >> 1) + 2 * (lane & 1);
    const unsigned one = (j & 1) ? 0x3f800000u : 0x00003f80u;
    selA = (u4v){(j >> 1) == 0 ? one : 0u, (j >> 1) == 1 ? one : 0u, (j >> 1) == 2 ? one : 0u, (j >> 1) == 3 ? one : 0u};
  }
  unsigned sto = hoff(sel, 1, 0) + v * 256 + i8 * 32 + ((lane >> 3) & 3) * 8 + (lane >> 5) * 4;
  unsigned uo = (unsigned)O_U + (v * 16 + 2 * wave) * 1024 + lane * 16;
  unsigned yo = (unsigned)O_YG + (v * 8 + wave) * 512 + lane * 8;
  const unsigned redw = L_RED + hs * 4096 + wave * 512 + (g * 8 + i8) * 16;
  const unsigned redr = L_RED + (lane >> 5) * 2048 + (lane & 31) * 16;
  const unsigned p0 = (K_PERIOD / 24) | ((K_PERIOD % 24) << 8) | ((K_YOFF / 24) << 16) | ((K_YOFF % 24) << 24);
  const unsigned p1 = (K_DE - 80) | ((K_DS - K_DE) << 16) | (dw << 28);
  if (KIND == 0) {
    asm volatile(R3_LOOP(R3_DMA, "", "", 2, "", 1, "", R3_FIN, "", R3_FIN_OOL) DIAG_R3_END : R3_OUTS : R3_INS, [redr] "v"(redr) : R3_CLOB);
  } else if (KIND == 1) {
    asm volatile(R3_LOOP(R3_DMA, "", "", 2, "", 1, "", "", "", "") DIAG_R3_END : R3_OUTS : R3_INS : R3_CLOB);
  } else if (KIND == 2) {
    const unsigned cc = (C_U + 14 + (v >> 1)) | ((((unsigned)(K_SMK ^ 3) << 13) | (unsigned)(v * 128)) << 16);
    asm volatile("s_mov_b64 s[40:41], %[wsp]\n\ts_add_u32 s40, s40, 0x400000\n\ts_addc_u32 s41, s41, 0\n\t"
                 "s_and_b32 s70, %[cc], 0xffff\n\ts_lshl_b32 s70, s70, 6\n\ts_add_u32 s40, s40, s70\n\t"
                 "s_lshr_b32 s42, %[cc], 16\n\ts_mov_b32 s43, 0x43ff00\n\t"
                 R3_LOOP(R3_DMA, "", R3_CNT_LOAD, 3, R3_CNT_SIG, 2, "", "", R3_CNT_CHK, R3_CNT_OOL) DIAG_R3_END : R3_OUTS : R3_INS, [cc] "s"(cc) : R3_CLOB);
  } else {
    asm volatile("s_mov_b32 s67, %[anc]\n\t" R3_LOOP("", R3_TW_WAIT, "", 2, "", 1, R3_FWD R3_PREP, "", "", R3_TW_LATE) DIAG_R3_END : R3_OUTS : R3_INS, [anc] "s"(anchor) : R3_CLOB);
  }
  if (KIND == 2 && lane < 2) st4c_asm(flagY(ws, sel, NSEG - 2 + lane, v), 0u);
  RTS(3);
}
DEV void role_r3(unsigned char* __restrict__ ws, int v, int wave, int lane, unsigned sel TUNE_ARG) {
  if (wave == 0) r3_body<0>(ws, v, wave, lane, sel TUNE_PASS);
  else if (wave == 6) r3_body<2>(ws, v, wave, lane, sel TUNE_PASS);
  else if (wave == 7) r3_body<3>(ws, v, wave, lane, sel TUNE_PASS);
  else r3_body<1>(ws, v, wave, lane, sel TUNE_PASS);
}

__global__ void __launch_bounds__(512, 2)
main_kernel(const unsigned short* __restrict__ x, const unsigned short* __restrict__ b1, const unsigned short* __restrict__ b2,
            unsigned short* __restrict__ out, unsigned char* __restrict__ ws TUNE_ARG) {
  const int b = blockIdx.x, lane = threadIdx.x & 63;
  const int wave = __builtin_amdgcn_readfirstlane(threadIdx.x >> 6);
  RTS(0);
  const int xc = b & 7, slot = b >> 3;
  if (xc == 0 || xc == 2) {
    const unsigned sel = *(const unsigned*)(ws + O_SEL);
    role_r1(ws, (xc >> 1) * 32 + slot, wave, lane, sel TUNE_PASS);
  } else if (xc == 1) {
    if (slot < 28) {
      const unsigned sel = *(const unsigned*)(ws + O_SEL);
      role_r3(ws, slot, wave, lane, sel TUNE_PASS);
    }
  } else {
    const int k = slot * 5 + (xc - 3);
    if (k < 56) role_w1<true>(ws, k, wave, lane);
    else if (k < 112) role_w1<false>(ws, k - 56, wave, lane);
    else {
      if (k < 144) role_ffn(ws, x, b1, b2, k - 112, wave, lane);
      role_w3(ws, out, (159 - k) >> 2, k & 3, wave, lane);
    }
  }
}

extern "C" void kernel_launch(void* const* d_in, const int* in_sizes, int n_in, void* d_out, int out_size, void* d_ws,
                              size_t ws_size, hipStream_t stream) {
  unsigned char* ws = (unsigned char*)(((uintptr_t)d_ws + 2 * MB - 1) & ~(uintptr_t)(2 * MB - 1));
  const unsigned short* x = (const unsigned short*)d_in[0];
  constexpr int LDS_MAIN = 132 * 1024;
  (void)hipFuncSetAttribute((const void*)main_kernel, hipFuncAttributeMaxDynamicSharedMemorySize, LDS_MAIN);
  constexpr int LDS_PREP = 144 * 1024;
  (void)hipFuncSetAttribute((const void*)prep_kernel, hipFuncAttributeMaxDynamicSharedMemorySize, LDS_PREP);
  prep_kernel<<<256, 512, LDS_PREP, stream>>>(x, (const unsigned short*)d_in[1], (const unsigned short*)d_in[2], (const unsigned short*)d_in[3],
                                           (const unsigned short*)d_in[4], (const unsigned short*)d_in[5], (const unsigned short*)d_in[8],
                                           (const unsigned short*)d_in[10], ws);
  main_kernel<<<256, 512, LDS_MAIN, stream>>>(x, (const unsigned short*)d_in[9], (const unsigned short*)d_in[11], (unsigned short*)d_out, ws TUNE_PASS);
}
```

```cpp
#include <hip/hip_runtime.h>
#include <hip/hip_bf16.h>
#include <stdint.h>

typedef __bf16 bf2 __attribute__((ext_vector_type(2)));
typedef __bf16 bf8 __attribute__((ext_vector_type(8)));
typedef __bf16 bf16x __attribute__((ext_vector_type(16)));
typedef short s4v __attribute__((ext_vector_type(4)));
typedef float f2v __attribute__((ext_vector_type(2)));
typedef float f4v __attribute__((ext_vector_type(4)));
typedef float f16v __attribute__((ext_vector_type(16)));
typedef unsigned u2v __attribute__((ext_vector_type(2)));
typedef unsigned u4v __attribute__((ext_vector_type(4)));
typedef unsigned u8v __attribute__((ext_vector_type(8)));
#define LDS __attribute__((address_space(3)))
#define GLB __attribute__((address_space(1)))
#define DEV __device__ __forceinline__

constexpr int DM = 448, NH = 16, DH = 256, NSEG = 64, FF = 1792;
constexpr unsigned SENT = 0xFFFFFFFFu;

constexpr size_t MB = 1u << 20;
constexpr size_t O_HB = 0;
constexpr size_t O_FL = 4 * MB;
constexpr size_t O_SEL = O_FL + 256 * 1024;
constexpr size_t O_XN = 5 * MB;
constexpr size_t O_WA = 7 * MB;
constexpr size_t O_WB = 14 * MB;
constexpr size_t O_W1 = 21 * MB;
constexpr size_t O_W2 = 23 * MB;
constexpr size_t O_G = 25 * MB;
constexpr size_t O_U = 55 * MB;
constexpr size_t O_ST = 85 * MB;
constexpr size_t O_SM = 87 * MB;
constexpr size_t O_YG = 100 * MB;
constexpr size_t O_F = 111 * MB;
constexpr unsigned G_RUN = 64 * 14336 + 3 * 4096, U_RUN = 64 * 16384 + 5 * 4096, Y_RUN = 64 * 4096 + 4096;
constexpr size_t O_END = 115 * MB;

DEV unsigned pk2(float a, float b) { f2v v = {a, b}; return __builtin_bit_cast(unsigned, __builtin_convertvector(v, bf2)); }
DEV float bflo(unsigned u) { return __builtin_bit_cast(float, u << 16); }
DEV float bfhi(unsigned u) { return __builtin_bit_cast(float, u & 0xffff0000u); }
DEV float rbf(float a) { return bflo(pk2(a, 0.f) & 0xffffu); }
DEV bf8 asbf8(u4v v) { return __builtin_bit_cast(bf8, v); }
DEV u4v lds_r16(unsigned a) { return *(LDS u4v*)(size_t)a; }
DEV void lds_w16(unsigned a, u4v v) { *(LDS u4v*)(size_t)a = v; }
DEV u2v lds_tr(unsigned a) { return __builtin_bit_cast(u2v, __builtin_amdgcn_ds_read_tr16_b64_v4i16((LDS s4v*)(size_t)a)); }
DEV unsigned rdtime() { unsigned long long t; asm volatile("s_memtime %0\n\ts_waitcnt lgkmcnt(0)" : "=s"(t) :: "memory"); return (unsigned)t; }
DEV int swap13(int x) { return (x & 5) | ((x >> 2) & 2) | ((x << 2) & 8); }
DEV void dma16(const void* g, unsigned ldsaddr) {
  unsigned keep;
  asm volatile("s_mov_b32 %0, m0\n\ts_mov_b32 m0, %2\n\ts_nop 0\n\tglobal_load_lds_dwordx4 %1, off\n\ts_mov_b32 m0, %0" : "=&s"(keep) : "v"(g), "s"(ldsaddr) : "memory");
}
DEV void dma16c(const void* g, unsigned ldsaddr) {
  unsigned keep;
  asm volatile("s_mov_b32 %0, m0\n\ts_mov_b32 m0, %2\n\ts_nop 0\n\tglobal_load_lds_dwordx4 %1, off sc1\n\ts_mov_b32 m0, %0" : "=&s"(keep) : "v"(g), "s"(ldsaddr) : "memory");
}
DEV void vm0() { asm volatile("s_waitcnt vmcnt(0)" ::: "memory"); }
DEV void bar() { asm volatile("s_waitcnt lgkmcnt(0)\n\ts_barrier" ::: "memory"); }

DEV void stwt16(void* p, u4v v) { asm volatile("global_store_dwordx4 %0, %1, off sc1\n\ts_nop 1" :: "v"(p), "v"(v) : "memory"); }
DEV float wavesum(float v) {
  v += __builtin_bit_cast(float, __builtin_amdgcn_update_dpp(0, __builtin_bit_cast(int, v), 0xb1, 0xf, 0xf, true));
  v += __builtin_bit_cast(float, __builtin_amdgcn_update_dpp(0, __builtin_bit_cast(int, v), 0x4e, 0xf, 0xf, true));
  v += __builtin_bit_cast(float, __builtin_amdgcn_update_dpp(0, __builtin_bit_cast(int, v), 0x124, 0xf, 0xf, true));
  v += __builtin_bit_cast(float, __builtin_amdgcn_update_dpp(0, __builtin_bit_cast(int, v), 0x128, 0xf, 0xf, true));
  const unsigned u = __builtin_bit_cast(unsigned, v);
  const u2v a = __builtin_amdgcn_permlane16_swap(u, u, false, false);
  const unsigned ax = a.x, ay = a.y;
  const unsigned w = __builtin_bit_cast(unsigned, __builtin_bit_cast(float, ax) + __builtin_bit_cast(float, ay));
  const u2v c = __builtin_amdgcn_permlane32_swap(w, w, false, false);
  const unsigned cx = c.x, cy = c.y;
  return __builtin_bit_cast(float, cx) + __builtin_bit_cast(float, cy);
}
#define K0S(k)
DEV void ln_row(u4v xv, unsigned char* __restrict__ ws, int row, int lane) {
  float e[8];
  if (lane >= 56) xv = (u4v){0u, 0u, 0u, 0u};
#pragma unroll
  for (int j = 0; j < 4; ++j) { e[2 * j] = bflo(xv[j]); e[2 * j + 1] = bfhi(xv[j]); }
  float s = 0.f;
#pragma unroll
  for (int j = 0; j < 8; ++j) s += e[j];
  const float mean = wavesum(s) * (1.f / DM);
  float q = 0.f;
  if (lane < 56) {
#pragma unroll
    for (int j = 0; j < 8; ++j) { const float d = e[j] - mean; q += d * d; }
  }
  const float mb = rbf(mean), vb = rbf(wavesum(q) * (1.f / DM));
  const float den = rbf(__builtin_sqrtf(rbf(vb + 1e-5f)));
  unsigned o4[4];
#pragma unroll
  for (int j = 0; j < 4; ++j) o4[j] = pk2(rbf(e[2 * j] - mb) / den, rbf(e[2 * j + 1] - mb) / den);
  const bool odd = lane & 1;
  const unsigned r0 = __builtin_amdgcn_update_dpp(0u, odd ? o4[0] : o4[2], 0xb1, 0xf, 0xf, true);
  const unsigned r1 = __builtin_amdgcn_update_dpp(0u, odd ? o4[1] : o4[3], 0xb1, 0xf, 0xf, true);
  u4v p;
  if (odd) p = (u4v){r0, o4[2], r1, o4[3]}; else p = (u4v){o4[0], r0, o4[1], r1};
  if (lane < 56) stwt16(ws + O_XN + (size_t)(row >> 5) * 28672 + (lane >> 1) * 1024 + (lane & 1) * 512 + (row & 31) * 16, p);
}
DEV u4v srd(const void* p, unsigned bytes) { const unsigned long long a = (unsigned long long)p; return (u4v){(unsigned)a, (unsigned)(a >> 32), bytes, 0x00020000u}; }
DEV void bdma16(unsigned voff, u4v r, unsigned soff, unsigned ldsaddr) {
  asm volatile("s_mov_b32 m0, %3\n\ts_nop 0\n\tbuffer_load_dwordx4 %0, %1, %2 offen lds" :: "v"(voff), "s"(r), "s"(soff), "s"(ldsaddr) : "memory");
}
constexpr unsigned rowoff(int j) { return (16 * ((j >> 2) & 1) + 8 * (j & 1) + 4 * ((j >> 1) & 1) + 2 * (j >> 3)) * 8192u; }

template <bool ISA>
DEV void prep_gemm(const unsigned short* __restrict__ Rw, const unsigned short* __restrict__ Cw, const unsigned short* __restrict__ x,
                   unsigned char* __restrict__ ws, int b, int h, int cg, int wave, int lane) {
  const int g = lane >> 4, c16 = lane & 15, hl = lane >> 5, sl = lane & 31;
  const int row = b * 8 + wave;
  const unsigned xl = 147456 + wave * 1024;
  dma16(x + (size_t)row * DM + min(lane, 55) * 8, xl);
  if (ISA) {
    const u4v rc = srd(Cw + (size_t)64 * cg * 4096 + h * 256, 64 * 8192);
    const unsigned v0 = hl * 8192 + ((sl ^ hl ^ ((wave & 1) << 3)) << 4), s0 = ((wave >> 1) * 16 + (wave & 1) * 2) * 8192;
#pragma unroll
    for (int j = 0; j < 4; ++j) bdma16(v0 ^ (j << 5), rc, s0 + ((j & 1) * 8 + (j >> 1) * 4) * 8192, (wave * 4 + j) * 1024);
  } else {
    const u4v rc = srd(Cw + (size_t)h * 256 * DM + 64 * cg, 256 * 896);
    const unsigned v0 = (lane >> 3) * 896 + (((lane & 7) ^ (((lane >> 4) & 1) << 1)) << 4);
#pragma unroll
    for (int j = 0; j < 4; ++j) bdma16(v0 ^ ((j & 1) << 6), rc, (wave * 4 + j) * 8 * 896, (wave * 4 + j) * 1024);
  }
  if (wave == 7) {
    __builtin_amdgcn_sched_barrier(0);
    asm volatile("s_waitcnt vmcnt(0)\n\ts_barrier" ::: "memory");
    const u4v ones = {SENT, SENT, SENT, SENT}, zero = {0u, 0u, 0u, 0u};
#pragma unroll
    for (int j = 0; j < 16; ++j) if (b >= 2) stwt16(ws + O_HB + (size_t)b * 16384 + j * 1024 + lane * 16, ones);
    stwt16(ws + O_FL + b * 1024 + lane * 16, zero);
    ln_row(lds_r16(xl + lane * 16), ws, row, lane);
    return;
  }
  const float sc = ISA ? 0.0625f * 1.4426950408889634f : 1.f;
  unsigned char* dst = ws + (ISA ? O_WA : O_WB) + (size_t)(h * 28 + 4 * cg) * 14336 + 2 * wave * 1024 + lane * 16;
  const unsigned reg = 32768 + wave * 16384;
  const int rot = cg * 2;
  const unsigned srot = (16 * ((rot >> 2) & 1) + 8 * (rot & 1) + 4 * ((rot >> 1) & 1) + 2 * (rot >> 3)) * 8192u, mrot = reg | (rot << 10);
  const unsigned vr = hl * 8192 + (((sl ^ hl) << 4) ^ ((rot & 7) << 5));
  const u4v rr = srd(Rw + (size_t)(64 * wave) * 4096 + h * 256, 64 * 8192);
  const __amdgpu_buffer_rsrc_t rq = __builtin_amdgcn_make_buffer_rsrc((void*)(Rw + (size_t)(64 * wave + 32) * 4096 + h * 256), 0, 32 * 8192, 0x00020000);
  u4v nx[16];
#pragma unroll
  for (int j = 0; j < 2; ++j) bdma16(vr ^ ((j & 7) << 5), rr, srot ^ rowoff(j), mrot ^ (j << 10));
#pragma unroll
  for (int j = 0; j < 2; ++j) nx[j] = __builtin_amdgcn_raw_buffer_load_b128(rq, vr ^ ((j & 7) << 5), srot ^ rowoff(j), 0);
  __builtin_amdgcn_sched_barrier(0);
  asm volatile("s_waitcnt vmcnt(8)" ::: "memory");
  ln_row(lds_r16(xl + lane * 16), ws, row, lane);
  __builtin_amdgcn_sched_barrier(0);
  K0S(1);
  asm volatile("s_waitcnt vmcnt(5)\n\ts_barrier" ::: "memory");
  K0S(0);
#pragma unroll
  for (int j = 2; j < 16; ++j) bdma16(vr ^ ((j & 7) << 5), rr, srot ^ rowoff(j), mrot ^ (j << 10));
#pragma unroll
  for (int j = 2; j < 16; ++j) nx[j] = __builtin_amdgcn_raw_buffer_load_b128(rq, vr ^ ((j & 7) << 5), srot ^ rowoff(j), 0);
  __builtin_amdgcn_sched_barrier(0);
  asm volatile("s_waitcnt vmcnt(14)" ::: "memory");
  K0S(2);
  const int q = c16 >> 2, p = c16 & 3;
  const int xr = (q >> 1) | ((g & 1) << 1);
  auto bfrag = [&](int es, int ti) -> u4v {
    if (ISA) return lds_r16((ti * 16 + c16) * 512 + (((es * 4 + g) ^ c16) << 4));
    const unsigned ad = (32 * es + 8 * g + q) * 128 + ((ti ^ xr) << 5) + p * 8;
    const u2v lo = lds_tr(ad), hi = lds_tr(ad + 512);
    return (u4v){lo.x, lo.y, hi.x, hi.y};
  };
  auto afrag = [&](int es, int t) -> u4v { return lds_r16(reg + (t * 16 + c16) * 512 + (((es * 4 + g) ^ c16) << 4)); };
#pragma unroll
  for (int rb = 0; rb < 2; ++rb) {
    u4v bb[2][4], aa[2][2];
#pragma unroll
    for (int ti = 0; ti < 4; ++ti) bb[0][ti] = bfrag(0, ti);
    aa[0][0] = afrag(0, 0); aa[0][1] = afrag(0, 1);
    f4v acc[2][4];
#pragma unroll
    for (int t = 0; t < 2; ++t)
#pragma unroll
      for (int ti = 0; ti < 4; ++ti) acc[t][ti] = (f4v){0.f, 0.f, 0.f, 0.f};
#pragma unroll
    for (int es = 0; es < 8; ++es) {
      if (es < 7) {
        aa[(es + 1) & 1][0] = afrag(es + 1, 0); aa[(es + 1) & 1][1] = afrag(es + 1, 1);
#pragma unroll
        for (int ti = 0; ti < 4; ++ti) bb[(es + 1) & 1][ti] = bfrag(es + 1, ti);
      }
#pragma unroll
      for (int ti = 0; ti < 4; ++ti)
#pragma unroll
        for (int t = 0; t < 2; ++t)
          acc[t][ti] = __builtin_amdgcn_mfma_f32_16x16x32_bf16(asbf8(aa[es & 1][t]), asbf8(bb[es & 1][ti]), acc[t][ti], 0, 0, 0);
      __builtin_amdgcn_sched_barrier(0);
    }
    if (rb == 0) {
#pragma unroll
      for (int j = 0; j < 16; ++j) lds_w16((mrot ^ (j << 10)) + lane * 16, nx[j]);
    }
#pragma unroll
    for (int ti = 0; ti < 4; ++ti) {
      const u4v o = {pk2(acc[0][ti][0] * sc, acc[0][ti][1] * sc), pk2(acc[0][ti][2] * sc, acc[0][ti][3] * sc),
                     pk2(acc[1][ti][0] * sc, acc[1][ti][1] * sc), pk2(acc[1][ti][2] * sc, acc[1][ti][3] * sc)};
      stwt16(dst + (size_t)ti * 14336 + rb * 1024, o);
    }
  }
  K0S(3);
}

__global__ void __launch_bounds__(512, 2)
prep_kernel(const unsigned short* __restrict__ x, const unsigned short* __restrict__ mem0,
            const unsigned short* __restrict__ Wq, const unsigned short* __restrict__ Wk,
            const unsigned short* __restrict__ Wv, const unsigned short* __restrict__ Wo,
            const unsigned short* __restrict__ w1, const unsigned short* __restrict__ w2,
            unsigned char* __restrict__ ws) {
  const int b = blockIdx.x, tid = threadIdx.x, lane = tid & 63;
  const int wave = __builtin_amdgcn_readfirstlane(tid >> 6);
  const int xc = b & 7, slot = b >> 3;
  if (slot < 28) {
    const int m = xc * 4 + slot / 7, cg = slot % 7;
    if (m < 16) prep_gemm<true>(Wk, Wq, x, ws, b, m, cg, wave, lane);
    else prep_gemm<false>(Wv, Wo, x, ws, b, m - 16, cg, wave, lane);
  } else {
    const int row = b * 8 + wave;
    u4v xv = {0u, 0u, 0u, 0u};
    const int misc = (slot - 28) * 8 + xc;
    const int job = (misc - 1) * 8 + wave;
    const unsigned reg = wave * 14336;
    if (misc == 0) {
      if (wave == 7) {
        u4v m0[7];
#pragma unroll
        for (int j = 0; j < 7; ++j) { const int n = j * 64 + lane; m0[j] = *(const u4v*)(mem0 + ((n >> 1) & 7) * DM + (n >> 4) * 16 + (n & 1) * 8); }
        const u4v ones = {SENT, SENT, SENT, SENT};
        stwt16(ws + O_HB + (lane >> 4) * 8192 + (lane & 15) * 64, ones);
        stwt16(ws + O_HB + 2 * MB + (lane >> 4) * 8192 + (lane & 15) * 64, ones);
#pragma unroll
        for (int j = 0; j < 7; ++j) asm volatile("" : "+v"(m0[j]));
        vm0();
        auto timed = [&](const unsigned char* pc) -> unsigned {
          unsigned v;
          const unsigned c0 = rdtime();
          asm volatile("global_load_dword %0, %1, %2 sc1\n\ts_waitcnt vmcnt(0)" : "=&v"(v) : "v"(0u), "s"(pc) : "memory");
          return rdtime() - c0;
        };
        unsigned best[6];
#pragma unroll
        for (int c = 0; c < 6; ++c) best[c] = 0x7fffffff;
#pragma unroll 1
        for (int it = 0; it < 3; ++it) {
#pragma unroll
          for (int c = 0; c < 4; ++c) best[c] = min(best[c], timed(ws + O_HB + c * 8192 + it * 256));
        }
        unsigned sel = 0, bv = best[0];
#pragma unroll
        for (unsigned k = 1; k < 4; ++k) if (best[k] < bv) { bv = best[k]; sel = k; }
#pragma unroll 1
        for (int it = 0; it < 2; ++it) {
#pragma unroll
          for (int c = 0; c < 2; ++c) best[4 + c] = min(best[4 + c], timed(ws + O_HB + 2 * MB + (sel ^ c) * 8192 + it * 256));
        }
        sel |= (sel ^ (best[5] <= best[4] ? 1u : 0u)) << 2;
        if (lane == 0) *(unsigned*)(ws + O_SEL) = sel;
#pragma unroll
        for (int j = 0; j < 7; ++j) stwt16(ws + O_HB + (sel & 3u) * 8192 + j * 1024 + lane * 16, m0[j]);
#pragma unroll
        for (int j = 8; j < 32; ++j) stwt16(ws + O_HB + (((sel & 3u) * 8192) ^ (j * 1024)) + lane * 16, ones);
      }
    } else if (job < 224) {
      const bool first = job < 112;
      const int step = first ? job >> 2 : job - 112;
      const unsigned short* src = first ? w1 + (size_t)step * 16 * FF + (job & 3) * 448 : w2 + (size_t)step * 16 * DM;
      const int ld = first ? FF : DM;
#pragma unroll
      for (int j = 0; j < 14; ++j) { const int n = j * 64 + lane, r = n / 56; dma16(src + (size_t)r * ld + (n - r * 56) * 8, reg + j * 1024); }
    }
    if (lane < 56) xv = *(const u4v*)(x + (size_t)row * DM + lane * 8);
    asm volatile("" : "+v"(xv));
    vm0();
    if (misc > 0 && job < 224) {
      const bool first = job < 112;
      const int hi = lane >> 5, q = (lane & 15) >> 2;
      const int r0 = first ? 4 * hi + 8 * (q >> 1) + (q & 1) : 4 * hi + q, dr = first ? 2 : 8;
      const unsigned ad = reg + r0 * 896 + ((lane >> 4) & 1) * 32 + (lane & 3) * 8;
      unsigned char* dst = ws + (first ? O_W1 + (size_t)((job & 3) * 14 * 28 + (job >> 2)) * 1024 : O_W2 + (size_t)(job - 112) * 1024) + lane * 16;
      const size_t dstep = first ? 28672 : 114688;
#pragma unroll
      for (int t = 0; t < 14; ++t) {
        const u2v lo = lds_tr(ad + t * 64), hv = lds_tr(ad + t * 64 + dr * 896);
        stwt16(dst + t * dstep, (u4v){lo.x, lo.y, hv.x, hv.y});
      }
    }
    const u4v ones = {SENT, SENT, SENT, SENT}, zero = {0u, 0u, 0u, 0u};
    stwt16(ws + O_HB + (size_t)b * 16384 + tid * 16, ones);
    stwt16(ws + O_HB + (size_t)b * 16384 + 8192 + tid * 16, ones);
    if (wave == 0) stwt16(ws + O_FL + b * 1024 + lane * 16, zero);
    ln_row(xv, ws, row, lane);
  }
}
constexpr int C_W = 0  , C_F = 1856, C_ABORT = 1900, C_ANCHOR = 1920;
typedef __amdgpu_buffer_rsrc_t rsrc_t;
DEV rsrc_t mkrsrc(const void* p, unsigned bytes) { return __builtin_amdgcn_make_buffer_rsrc((void*)p, 0, bytes, 0x00020000); }
DEV u4v ldc16(rsrc_t r, unsigned off) { return __builtin_amdgcn_raw_buffer_load_b128(r, off, 0, 16); }
DEV u2v ldc8(rsrc_t r, unsigned off) { return __builtin_amdgcn_raw_buffer_load_b64(r, off, 0, 16); }
DEV void stc16(rsrc_t r, unsigned off, u4v v) { __builtin_amdgcn_raw_buffer_store_b128(v, r, off, 0, 16); }
DEV void stc4(rsrc_t r, unsigned off, unsigned v) { __builtin_amdgcn_raw_buffer_store_b32(v, r, off, 0, 16); }
DEV unsigned* cntp(unsigned char* ws, int idx) { return (unsigned*)(ws + O_FL) + idx * 16; }
DEV unsigned ld_cnt(const unsigned* p) { unsigned v; asm volatile("global_load_dword %0, %1, off sc1\n\ts_waitcnt vmcnt(0)" : "=&v"(v) : "v"(p) : "memory"); return v; }
DEV void add_cnt(unsigned* p, int lane) {
  if (lane == 0) { const unsigned one = 1; asm volatile("global_atomic_add %0, %1, off sc1" :: "v"(p), "v"(one) : "memory"); }
}
DEV void post(unsigned* p, unsigned v, int lane) { if (lane == 0) asm volatile("global_store_dword %0, %1, off sc1" :: "v"(p), "v"(v) : "memory"); }
DEV unsigned* doneG(unsigned char* ws, int j) { return cntp(ws, C_W + 2 * j); }
DEV unsigned* doneU(unsigned char* ws, int j) { return cntp(ws, C_W + 2 * (56 + j)); }
constexpr int SPIN_CAP = 1 << 17;
DEV void wait_cnt(unsigned char* ws, const unsigned* p, unsigned target, int nap = 6) {
  for (int it = 0;; ++it) {
    const unsigned v = ld_cnt(p);
    if (__all(v >= target)) return;
    if ((it & 63) == 63) {
      if (it >= SPIN_CAP) { const unsigned one = 1; asm volatile("global_store_dword %0, %1, off sc1" :: "v"(cntp(ws, C_ABORT)), "v"(one) : "memory"); return; }
      if (__any(ld_cnt(cntp(ws, C_ABORT)) != 0)) return;
    }
    if (nap > 6) __builtin_amdgcn_s_sleep(32); else __builtin_amdgcn_s_sleep(2);
  }
}
DEV unsigned hoff(unsigned sel, int gi, int kind) {
  const unsigned k = ((sel >> (2 * (gi >> 6))) & 3u) ^ ((((unsigned)gi >> 4) & 1u) << 1) ^ (unsigned)kind;
  return (unsigned)O_HB + (unsigned)gi * 32768u + k * 8192u;
}
DEV unsigned char* hblk(unsigned char* ws, unsigned sel, int gi, int kind) { return ws + hoff(sel, gi, kind); }
DEV unsigned char* flagS(unsigned char* ws, unsigned sel, int s, int id) { return hblk(ws, sel, s, 3) + id * 128; }
DEV unsigned char* flagY(unsigned char* ws, unsigned sel, int s, int v) { return hblk(ws, sel, 64 + s, 3) + v * 128; }
DEV unsigned char* flagT(unsigned char* ws, unsigned sel, int s, int h) { return hblk(ws, sel, 64 + s, 1) + h * 128; }
DEV void wait_flags(unsigned char* ws, const unsigned char* p) {
  for (int it = 0;; ++it) {
    if (!__any(ld_cnt((const unsigned*)p) == SENT)) return;
    if ((it & 63) == 63) {
      if (it >= SPIN_CAP) { const unsigned one = 1; asm volatile("global_store_dword %0, %1, off sc1" :: "v"(cntp(ws, C_ABORT)), "v"(one) : "memory"); return; }
      if (__any(ld_cnt(cntp(ws, C_ABORT)) != 0)) return;
    }
    __builtin_amdgcn_s_sleep(6);
  }
}
DEV f16v mfma32(u4v a, u4v b, f16v c) { return __builtin_amdgcn_mfma_f32_32x32x16_bf16(asbf8(a), asbf8(b), c, 0, 0, 0); }
DEV f4v mfma16(u4v a, u4v b, f4v c) { return __builtin_amdgcn_mfma_f32_16x16x32_bf16(asbf8(a), asbf8(b), c, 0, 0, 0); }
DEV void st16c_asm(void* p, u4v v) { asm volatile("global_store_dwordx4 %0, %1, off sc1\n\ts_nop 1" :: "v"(p), "v"(v) : "memory"); }

#define RTS(k)
template <bool ISG>
DEV void role_w1(unsigned char* __restrict__ ws, int j, int wave, int lane) {
  const int ct = wave & 3, rh = wave >> 2, hi = lane >> 5, c32 = lane & 31;
  int h, tile0;
  if (ISG) { const int hp = j / 7, ctile = (j % 7) * 4 + ct; h = 2 * hp + ctile / 14; tile0 = 2 * (ctile % 14); }
  else { const int V = j >> 2; h = 4 * (j & 3) + ct; tile0 = 2 * V; }
  const unsigned char* xn = ws + O_XN + wave * 7168 + lane * 16;
#pragma unroll
  for (int i = 0; i < 7; ++i) dma16(xn + i * 1024, wave * 7168 + i * 1024);
  u4v w[28];
  {
    const unsigned char* p = ws + (ISG ? O_WA : O_WB) + (size_t)(h * 28 + tile0 + (c32 >> 4)) * 14336 + hi * 256 + (c32 & 15) * 16;
#pragma unroll
    for (int s = 0; s < 28; ++s) w[s] = *(const u4v*)(p + (s >> 1) * 1024 + (s & 1) * 512);
  }
  constexpr int SEGB = ISG ? 14336 : 16384;
  unsigned char* out = (ISG ? ws + O_G + (size_t)(h * 2 + (c32 >> 4)) * G_RUN + (tile0 >> 1) * 1024
                            : ws + O_U + (size_t)(tile0 + (c32 >> 4)) * U_RUN + h * 1024) + rh * SEGB + hi * 256 + (c32 & 15) * 16;
  const int tok = ISG ? c32 : ((c32 & 19) | ((c32 >> 1) & 4) | ((c32 << 1) & 8));
  const unsigned rd = rh * 28672 + hi * 512 + tok * 16;
  unsigned* done = ISG ? doneG(ws, j) : doneU(ws, j);
#pragma unroll
  for (int s = 0; s < 28; ++s) asm volatile("" : "+v"(w[s]));
  vm0();
  RTS(1);
  for (int p = 0; p < 32; ++p) {
    asm volatile("s_waitcnt vmcnt(2)\n\ts_barrier" ::: "memory");
    if (wave == 7 && p >= 6) post(done, p - 1, lane);
    const unsigned buf = (p & 1) * 57344;
    const unsigned char* src = xn + (size_t)(p < 31 ? p + 1 : p) * 57344;
    const unsigned dstl = (57344 - buf) + wave * 7168;
    f16v acc;
#pragma unroll
    for (int i = 0; i < 16; ++i) acc[i] = 0.f;
    const LDS u4v* xp = (const LDS u4v*)(size_t)(buf + rd);
    u4v xr[5];
#pragma unroll
    for (int s = 0; s < 4; ++s) xr[s] = xp[s * 64];
#pragma unroll
    for (int s = 0; s < 28; ++s) {
      if (s + 4 < 28) xr[(s + 4) % 5] = xp[(s + 4) * 64];
      acc = ISG ? mfma32(w[s], xr[s % 5], acc) : mfma32(xr[s % 5], w[s], acc);
      __builtin_amdgcn_sched_barrier(0);
      if (s % 3 == 0 && s / 3 < 7) { dma16(src + (s / 3) * 1024, dstl + (s / 3) * 1024); __builtin_amdgcn_sched_barrier(0); }
      if (s == 16) {
        asm volatile("s_waitcnt vmcnt(6)" ::: "memory");
        __builtin_amdgcn_sched_barrier(0);
      }
    }
    const u4v o0 = {pk2(acc[0], acc[1]), pk2(acc[2], acc[3]), pk2(acc[4], acc[5]), pk2(acc[6], acc[7])};
    const u4v o1 = {pk2(acc[8], acc[9]), pk2(acc[10], acc[11]), pk2(acc[12], acc[13]), pk2(acc[14], acc[15])};
    unsigned char* o = out + p * 2 * SEGB;
    st16c_asm(o, o0); st16c_asm(o + 512, o1);
    if (p < 4) {
      RTS(4);
      asm volatile("s_waitcnt vmcnt(0)\n\ts_barrier" ::: "memory");
      if (wave == 7) post(done, p + 1, lane);
      RTS(2);
    }
  }
  asm volatile("s_waitcnt vmcnt(0)\n\ts_barrier" ::: "memory");
  if (wave == 7) post(done, 32, lane);
  RTS(3);
}

template <bool P2>
DEV void ffn_body(unsigned char* __restrict__ ws, const unsigned short* __restrict__ x, const unsigned short* __restrict__ b1,
                  const unsigned short* __restrict__ b2, int job, int wave, int lane) {
  const int hi = lane >> 5, c32 = lane & 31;
  constexpr int NL = P2 ? 60 : 28, RING = P2 ? 12 : 14, DIST = RING - 1;
  const u4v* w1p = (const u4v*)(ws + O_W1 + (size_t)wave * 28672) + lane;
  const u4v* w2p = (const u4v*)(ws + O_W2 + (size_t)(2 * wave) * 114688) + lane;
  auto wload = [&](int c, int i) -> u4v {
    if (i < 28) return w1p[(size_t)c * 14336 + i * 64];
    return w2p[(size_t)c * 1024 + ((i - 28) >> 1) * 64 + ((i - 28) & 1) * 7168];
  };
  u4v wr[RING];
#pragma unroll
  for (int i = 0; i < DIST; ++i) wr[i] = wload(0, i);
  {
    const unsigned char* xn = ws + O_XN + (size_t)job * 57344 + wave * 7168 + lane * 16;
#pragma unroll
    for (int i = 0; i < 7; ++i) dma16(xn + i * 1024, wave * 7168 + i * 1024);
  }
  f16v o[2][2];
#pragma unroll
  for (int a = 0; a < 2; ++a)
#pragma unroll
    for (int bq = 0; bq < 2; ++bq)
#pragma unroll
      for (int i = 0; i < 16; ++i) o[a][bq][i] = 0.f;
  vm0();
  bar();
  const LDS u4v* xp = (const LDS u4v*)(size_t)(lane * 16);
  for (int c = 0; c < 7; ++c) {
    const int cn = c < 6 ? c + 1 : 6;
    const unsigned hbuf = 57344 + (c & 1) * 32768;
    u2v bv[4];
#pragma unroll
    for (int q = 0; q < 4; ++q) bv[q] = *(const u2v*)(b1 + 32 * (8 * c + wave) + 4 * hi + 8 * q);
    f16v a0, a1;
#pragma unroll
    for (int i = 0; i < 16; ++i) { a0[i] = 0.f; a1[i] = 0.f; }
    u4v xa[3], xb[3];
#pragma unroll
    for (int s = 0; s < 2; ++s) { xa[s] = xp[s * 64]; xb[s] = xp[1792 + s * 64]; }
#pragma unroll
    for (int s = 0; s < 28; ++s) {
      wr[(s + DIST) % RING] = (s + DIST < NL) ? wload(c, s + DIST) : wload(cn, s + DIST - NL);
      if (s + 2 < 28) { xa[(s + 2) % 3] = xp[(s + 2) * 64]; xb[(s + 2) % 3] = xp[1792 + (s + 2) * 64]; }
      a0 = mfma32(wr[s % RING], xa[s % 3], a0);
      a1 = mfma32(wr[s % RING], xb[s % 3], a1);
      __builtin_amdgcn_sched_barrier(0);
    }
#pragma unroll
    for (int i = 0; i < 16; ++i) {
      const unsigned bw = bv[i >> 2][(i >> 1) & 1];
      const float bs = (i & 1) ? bfhi(bw) : bflo(bw);
      const float t0 = fmaxf(a0[i] + bs, 0.f), t1 = fmaxf(a1[i] + bs, 0.f);
      a0[i] = t0 * t0; a1[i] = t1 * t1;
    }
#pragma unroll
    for (int bq = 0; bq < 2; ++bq) {
      lds_w16(hbuf + ((wave * 2 + bq) * 2 + 0) * 1024 + lane * 16,
              (u4v){pk2(a0[8 * bq], a0[8 * bq + 1]), pk2(a0[8 * bq + 2], a0[8 * bq + 3]), pk2(a0[8 * bq + 4], a0[8 * bq + 5]), pk2(a0[8 * bq + 6], a0[8 * bq + 7])});
      lds_w16(hbuf + ((wave * 2 + bq) * 2 + 1) * 1024 + lane * 16,
              (u4v){pk2(a1[8 * bq], a1[8 * bq + 1]), pk2(a1[8 * bq + 2], a1[8 * bq + 3]), pk2(a1[8 * bq + 4], a1[8 * bq + 5]), pk2(a1[8 * bq + 6], a1[8 * bq + 7])});
    }
    bar();
    if (P2) {
      const LDS u4v* hp = (const LDS u4v*)(size_t)(hbuf + lane * 16);
      u4v h0[2], h1[2];
      h0[0] = hp[0]; h1[0] = hp[64];
#pragma unroll
      for (int s = 0; s < 16; ++s) {
        const int i = 28 + 2 * s;
        wr[(i + DIST) % RING] = (i + DIST < NL) ? wload(c, i + DIST) : wload(cn, i + DIST - NL);
        if (s + 1 < 16) { h0[(s + 1) & 1] = hp[(s + 1) * 128]; h1[(s + 1) & 1] = hp[(s + 1) * 128 + 64]; }
        o[0][0] = mfma32(wr[i % RING], h0[s & 1], o[0][0]); o[0][1] = mfma32(wr[i % RING], h1[s & 1], o[0][1]);
        __builtin_amdgcn_sched_barrier(0);
        wr[(i + 1 + DIST) % RING] = (i + 1 + DIST < NL) ? wload(c, i + 1 + DIST) : wload(cn, i + 1 + DIST - NL);
        o[1][0] = mfma32(wr[(i + 1) % RING], h0[s & 1], o[1][0]); o[1][1] = mfma32(wr[(i + 1) % RING], h1[s & 1], o[1][1]);
        __builtin_amdgcn_sched_barrier(0);
      }
    }
  }
  if (P2) {
    const rsrc_t rf = mkrsrc(ws + O_F, 4u << 20);
#pragma unroll
    for (int a = 0; a < 2; ++a)
#pragma unroll
      for (int sg = 0; sg < 2; ++sg)
#pragma unroll
        for (int Q = 0; Q < 4; ++Q) {
          const int n = 32 * (2 * wave + a) + 8 * Q + 4 * hi, seg = 2 * job + sg;
          const u2v xv = *(const u2v*)(x + (size_t)(seg * 32 + c32) * DM + n), bv = *(const u2v*)(b2 + n);
          const f4v r = {o[a][sg][4 * Q] + bflo(bv.x) + bflo(xv.x), o[a][sg][4 * Q + 1] + bfhi(bv.x) + bfhi(xv.x),
                         o[a][sg][4 * Q + 2] + bflo(bv.y) + bflo(xv.y), o[a][sg][4 * Q + 3] + bfhi(bv.y) + bfhi(xv.y)};
          const int v = 2 * (2 * wave + a) + (Q >> 1), g = 2 * (Q & 1) + hi;
          stc16(rf, ((seg * 28 + v) * 2 + (c32 >> 4)) * 1024 + (g * 16 + (c32 & 15)) * 16, __builtin_bit_cast(u4v, r));
        }
    vm0();
    add_cnt(cntp(ws, C_F + job), lane);
    RTS(3);
  }
}
DEV void role_ffn(unsigned char* __restrict__ ws, const unsigned short* __restrict__ x, const unsigned short* __restrict__ b1,
                  const unsigned short* __restrict__ b2, int job, int wave, int lane) {
  if (wave < 7) ffn_body<true>(ws, x, b1, b2, job, wave, lane);
  else ffn_body<false>(ws, x, b1, b2, job, wave, lane);
}

DEV float asf(unsigned u) { return __builtin_bit_cast(float, u); }
DEV unsigned asu(float f) { return __builtin_bit_cast(unsigned, f); }
DEV float xmax16_32(float v) {
  const u2v a = __builtin_amdgcn_permlane16_swap(asu(v), asu(v), false, false);
  v = fmaxf(asf(a.x), asf(a.y));
  const u2v c = __builtin_amdgcn_permlane32_swap(asu(v), asu(v), false, false);
  return fmaxf(asf(c.x), asf(c.y));
}
DEV float xsum16_32(float v) {
  const u2v a = __builtin_amdgcn_permlane16_swap(asu(v), asu(v), false, false);
  v = asf(a.x) + asf(a.y);
  const u2v c = __builtin_amdgcn_permlane32_swap(asu(v), asu(v), false, false);
  return asf(c.x) + asf(c.y);
}
DEV float halfsum(float a, float b) {
  const u2v s = __builtin_amdgcn_permlane32_swap(asu(a), asu(b), false, false);
  return asf(s.x) + asf(s.y);
}

DEV void role_stt(unsigned char* __restrict__ ws, int grp, int hg, int wave, int lane) {
  const unsigned sel = *(const unsigned*)(ws + O_SEL);
  const int g = lane >> 4, c16 = lane & 15, h = 8 * hg + wave;
  const rsrc_t rg = mkrsrc(ws + O_G, 30u << 20);
  const LDS u4v* xp = (const LDS u4v*)(size_t)((g >> 1) * 1024 + (g & 1) * 512 + c16 * 16);
  for (int s = grp; s < NSEG; s += 8) {
    if (wave == 7) wait_cnt(ws, doneG(ws, 28 * hg + min(lane, 27)), (s >> 1) + 1, 32);
    bar();
    if (wave < 7) {
#pragma unroll
      for (int i = 0; i < 4; ++i) dma16(ws + O_XN + (size_t)s * 28672 + (wave * 4 + i) * 1024 + lane * 16, (wave * 4 + i) * 1024);
    }
    u4v ga[28];
#pragma unroll
    for (int T = 0; T < 28; ++T) ga[T] = ldc16(rg, (h * 2 + T / 14) * G_RUN + s * 14336 + (T % 14) * 1024 + lane * 16);
    __builtin_amdgcn_sched_barrier(0);
    asm volatile("s_waitcnt vmcnt(28)\n\ts_barrier" ::: "memory");
    f4v acc[2][2];
#pragma unroll
    for (int q = 0; q < 2; ++q) { acc[0][q] = (f4v){0.f, 0.f, 0.f, 0.f}; acc[1][q] = acc[0][q]; }
#pragma unroll
    for (int T = 0; T < 14; ++T) {
      const u4v x0 = xp[T * 128], x1 = xp[T * 128 + 16];
      acc[0][0] = mfma16(ga[T], x0, acc[0][0]); acc[0][1] = mfma16(ga[T], x1, acc[0][1]);
      acc[1][0] = mfma16(ga[14 + T], x0, acc[1][0]); acc[1][1] = mfma16(ga[14 + T], x1, acc[1][1]);
    }
    unsigned char* o = ws + O_ST + (size_t)(s * 16 + h) * 2048 + c16 * 32 + g * 8;
#pragma unroll
    for (int q = 0; q < 2; ++q)
#pragma unroll
      for (int t = 0; t < 2; ++t) {
        const u2v val = {pk2(acc[t][q][0], acc[t][q][1]), pk2(acc[t][q][2], acc[t][q][3])};
        asm volatile("global_store_dwordx2 %0, %1, off sc1" :: "v"(o + q * 1024 + t * 512), "v"(val) : "memory");
      }
    vm0();
    if (lane == 0) { const unsigned zero = 0; asm volatile("global_store_dword %0, %1, off sc1" :: "v"(flagT(ws, sel, s, h)), "v"(zero) : "memory"); }
  }
  RTS(3);
}

constexpr int W3_GROUPS = 12;
DEV void role_w3(unsigned char* __restrict__ ws, unsigned short* __restrict__ out, int grp, int C, int wave, int lane) {
  const unsigned sel = *(const unsigned*)(ws + O_SEL);
  const int g = lane >> 4, c16 = lane & 15, v = 7 * C + (wave < 7 ? wave : 6);
  const rsrc_t ru = mkrsrc(ws + O_U, 30u << 20), rf = mkrsrc(ws + O_F, 4u << 20);
  constexpr unsigned L_P = 0, L_PM = 32768, L_ST = 40960, L_SM = L_ST + 32768, L_Y = L_SM + 32768;
  for (int s = grp; s < NSEG; s += W3_GROUPS) {
    if (wave == 7) {
      wait_cnt(ws, doneU(ws, 4 * ((7 * C) >> 1) + (lane & 15)), (s >> 1) + 1);
      wait_cnt(ws, cntp(ws, C_F + (s >> 1)), 7);
      wait_flags(ws, flagT(ws, sel, s, lane & 15));
    }
    bar();
    u4v uf[16], ff[2];
    if (wave < 7) {
#pragma unroll
      for (int hh = 0; hh < 16; ++hh) uf[hh] = ldc16(ru, v * U_RUN + (s * 16 + hh) * 1024 + lane * 16);
#pragma unroll
      for (int q = 0; q < 2; ++q) ff[q] = ldc16(rf, ((s * 28 + v) * 2 + q) * 1024 + lane * 16);
    } else {
      wait_flags(ws, flagY(ws, sel, s, 7 * C + min(lane, 6)));
      wait_flags(ws, flagS(ws, sel, s, lane));
    }
    __builtin_amdgcn_sched_barrier(0);
    bar();
#pragma unroll
    for (int it = 0; it < 4; ++it) {
      dma16c(ws + O_ST + (size_t)(s * 32 + wave * 4 + it) * 1024 + lane * 16, L_ST + (wave * 4 + it) * 1024);
      dma16c(ws + O_SM + (size_t)(s * 32 + wave * 4 + it) * 1024 + lane * 16, L_SM + (wave * 4 + it) * 1024);
    }
    if (wave < 7) {
#pragma unroll
      for (int H = 0; H < 4; ++H) dma16c(ws + O_YG + (size_t)v * Y_RUN + (s * 4 + H) * 1024 + lane * 16, L_Y + (wave * 4 + H) * 1024);
      asm volatile("s_waitcnt vmcnt(4)" ::: "memory");
    } else vm0();
#pragma unroll
    for (int it = 0; it < 4; ++it) {
      const int q = it & 1, hh = 2 * wave + (it >> 1), tq = 16 * q + c16;
      const u4v st = lds_r16(L_ST + (wave * 4 + it) * 1024 + (g >> 1) * 512 + c16 * 32 + (g & 1) * 16);
      const LDS unsigned* sm = (const LDS unsigned*)(size_t)(L_SM + (wave * 4 + it) * 1024 + lane * 4);
      const unsigned s0 = sm[0], s1 = sm[64], s2 = sm[128], s3 = sm[192];
      float e[8];
      float m0 = (bflo(s0) + bflo(s1)) + (bflo(s2) + bflo(s3));
      float m1 = (bfhi(s0) + bfhi(s1)) + (bfhi(s2) + bfhi(s3));
#pragma unroll
      for (int i = 0; i < 8; ++i) { const unsigned w = st[i >> 1]; e[i] = (8 * g + i <= tq) ? ((i & 1) ? bfhi(w) : bflo(w)) : -__builtin_inff(); }
      float mx = fmaxf(m0, m1);
#pragma unroll
      for (int i = 0; i < 8; ++i) mx = fmaxf(mx, e[i]);
      mx = xmax16_32(mx);
      m0 = __builtin_amdgcn_exp2f(m0 - mx); m1 = __builtin_amdgcn_exp2f(m1 - mx);
      float sum = m0 + m1;
#pragma unroll
      for (int i = 0; i < 8; ++i) { e[i] = __builtin_amdgcn_exp2f(e[i] - mx); sum += e[i]; }
      const float r = __builtin_amdgcn_rcpf(xsum16_32(sum));
      lds_w16(L_P + (q * 16 + hh) * 1024 + lane * 16,
              (u4v){pk2(e[0] * r, e[1] * r), pk2(e[2] * r, e[3] * r), pk2(e[4] * r, e[5] * r), pk2(e[6] * r, e[7] * r)});
      *(LDS unsigned*)(size_t)(L_PM + (q * 4 + (hh >> 2)) * 1024 + lane * 16 + (hh & 3) * 4) = pk2(m0 * r, m1 * r);
    }
    vm0();
    bar();
    if (wave < 7) {
      u4v yf[4];
#pragma unroll
      for (int H = 0; H < 4; ++H) {
        const u2v lo = *(const LDS u2v*)(size_t)(L_Y + (wave * 4 + H) * 1024 + lane * 8), hi = *(const LDS u2v*)(size_t)(L_Y + (wave * 4 + H) * 1024 + 512 + lane * 8);
        yf[H] = (u4v){lo.x, lo.y, hi.x, hi.y};
      }
      f4v a0 = __builtin_bit_cast(f4v, ff[0]), a1 = __builtin_bit_cast(f4v, ff[1]);
      const LDS u4v* pp = (const LDS u4v*)(size_t)(L_P + lane * 16);
      u4v p0[4], p1[4];
#pragma unroll
      for (int k = 0; k < 3; ++k) { p0[k] = pp[k * 64]; p1[k] = pp[(16 + k) * 64]; }
#pragma unroll
      for (int k = 0; k < 20; ++k) {
        const int n = k + 3;
        if (n < 16) { p0[n & 3] = pp[n * 64]; p1[n & 3] = pp[(16 + n) * 64]; }
        else if (n < 20) { p0[n & 3] = pp[(32 + n - 16) * 64]; p1[n & 3] = pp[(36 + n - 16) * 64]; }
        const u4v w = k < 16 ? uf[k] : yf[k - 16];
        a0 = mfma16(w, p0[k & 3], a0); a1 = mfma16(w, p1[k & 3], a1);
        __builtin_amdgcn_sched_barrier(0);
      }
      *(u2v*)(out + (size_t)(s * 32 + c16) * DM + v * 16 + g * 4) = (u2v){pk2(a0[0], a0[1]), pk2(a0[2], a0[3])};
      *(u2v*)(out + (size_t)(s * 32 + 16 + c16) * DM + v * 16 + g * 4) = (u2v){pk2(a1[0], a1[1]), pk2(a1[2], a1[3])};
    }
  }
  RTS(4);
}
#define TUNE_ARG
#define TUNE_PASS
#define K_PERIOD 760
#define K_DS 1560
#define K_MS 43
#define K_MM 50
#define K_LEAD 96
#define K_FIXED 0
#define K_WARM 0
constexpr int K_SMK = 2, K_DDE = 250;
constexpr int C_PT = 2048;
DEV unsigned* ptp(unsigned char* ws, int s) { return cntp(ws, C_PT + 2 * s); }
DEV unsigned ptword(int period, int ds) { return (unsigned)(period / 24) | ((unsigned)(period % 24) << 8) | ((unsigned)(ds - K_DDE - 80) << 16); }
DEV unsigned char* flagR(unsigned char* ws, unsigned sel, int id) { return hblk(ws, sel, 64 + (id >> 6), 0) + (id & 63) * 128; }
#define DBG(slot, v)
#define ST(n)
#define ARR(s_)
#define EV(code)
#define EV_OUT(base)
#define ST_CLOB
#define ST_OUT(base, ref)
constexpr int RET_E = 26;
constexpr int TRY_CAP = 1 << 16;

DEV int rt32() { unsigned long long t; asm volatile("s_memrealtime %0\n\ts_waitcnt lgkmcnt(0)" : "=s"(t) :: "memory"); return (int)(unsigned)t; }
struct Lat { int anchor, e, f; };
DEV void lat_fwd(Lat& L, int pe, int pf) { L.f += pf; const int c = L.f >= 24; L.f -= c ? 24 : 0; L.e += pe + c; }
DEV void lat_back(Lat& L, int pe, int pf) { L.f -= pf; const int c = L.f < 0; L.f += c ? 24 : 0; L.e -= pe + c; }
DEV void lat_prep(const Lat& L, int& tgt, int& jmp) { tgt = L.anchor + 4 * L.e; jmp = 108 - 4 * L.f; }
#define TPAIR(r, m) "s_memrealtime s[" #r ":" #r "+1]\n\ts_memtime s[" #m ":" #m "+1]\n\ts_nop 0\n\t"
#define TSEL(r, m) "s_cmp_lg_u32 s" #r ", s4\n\ts_cselect_b32 s58, s" #m ", s58\n\t"
#define TBURST \
  TPAIR(4, 6) TPAIR(8, 10) TPAIR(12, 14) TPAIR(16, 18) TPAIR(20, 22) TPAIR(24, 26) TPAIR(28, 30) TPAIR(52, 54) TPAIR(56, 58) \
  "s_waitcnt lgkmcnt(0)\n\t" TSEL(52, 54) TSEL(28, 30) TSEL(24, 26) TSEL(20, 22) TSEL(16, 18) TSEL(12, 14) TSEL(8, 10)
#define TBURST_CLOB "s4","s5","s6","s7","s8","s9","s10","s11","s12","s13","s14","s15","s16","s17","s18","s19","s20","s21","s22","s23", \
  "s24","s25","s26","s27","s28","s29","s30","s31","s52","s53","s54","s55","s56","s57","s58","s59"
DEV void lat_commit(Lat& L, int tgt, int jmp) {
  if (tgt == (int)0x80000000) return;
  int r = tgt - rt32();
  while (__builtin_expect(r < 0, 0)) { L.e += RET_E; tgt += 4 * RET_E; r += 4 * RET_E; }
  if (r > 0) {
    int d;
#define RT_STEP "s_memrealtime vcc\n\ts_waitcnt lgkmcnt(0)\n\ts_sub_i32 %0, %1, vcc_lo\n\ts_cmp_lt_i32 %0, 1\n\t"
    asm volatile("1:\n\t" RT_STEP "s_cbranch_scc1 2f\n\t" RT_STEP "s_cbranch_scc1 2f\n\t" RT_STEP "s_cbranch_scc1 2f\n\t"
                 RT_STEP "s_cbranch_scc0 1b\n\t"
                 "2:\n\t"
                 "s_getpc_b64 vcc\n\ts_add_u32 vcc_lo, vcc_lo, %2\n\ts_addc_u32 vcc_hi, vcc_hi, 0\n\ts_setpc_b64 vcc\n\t"
                 ".rept 24\n\ts_nop 0\n\t.endr"
                 : "=&s"(d) : "s"(tgt), "s"(jmp) : "scc", "vcc", "memory");
  }
}
DEV unsigned lat_wait(Lat& L) {
  int tgt, jmp;
  unsigned est;
  lat_prep(L, tgt, jmp);
  if (tgt - rt32() < 16) { lat_commit(L, tgt, jmp); return 0; }
  asm volatile("1:\n\ts_memrealtime vcc\n\ts_waitcnt lgkmcnt(0)\n\ts_sub_i32 s4, %1, vcc_lo\n\ts_cmp_gt_i32 s4, 16\n\ts_cbranch_scc1 1b\n\t"
               TBURST
               "s_sub_i32 s4, %1, s4\n\ts_mul_i32 s4, s4, 93\n\ts_lshr_b32 s4, s4, 2\n\t"
               "s_add_i32 s58, s58, s4\n\ts_add_i32 s58, s58, %2\n\ts_or_b32 %0, s58, 1\n\t"
               "s_memtime vcc\n\ts_waitcnt lgkmcnt(0)\n\t"
               "s_sub_i32 s4, s58, vcc_lo\n\ts_max_i32 s4, s4, 0\n\ts_min_i32 s4, s4, 512\n\ts_and_b32 s4, s4, -4\n\ts_sub_i32 s4, 524, s4\n\t"
               "s_getpc_b64 vcc\n\ts_add_u32 vcc_lo, vcc_lo, s4\n\ts_addc_u32 vcc_hi, vcc_hi, 0\n\ts_setpc_b64 vcc\n\t"
               ".rept 128\n\ts_nop 0\n\t.endr"
               : "=&s"(est) : "s"(tgt), "s"(4 * L.f - 93) : TBURST_CLOB, "scc", "vcc", "memory");
  return est;
}
DEV unsigned* anchorp(unsigned char* ws, int id) { return cntp(ws, C_ANCHOR + 2 * (id & 31)); }
DEV int get_anchor(unsigned char* ws, unsigned sel, int id, int lane) {
  post((unsigned*)flagR(ws, sel, id), 0u, lane);
  unsigned v = 0;
  for (int it = 0; it < SPIN_CAP && v == 0; ++it) v = __builtin_amdgcn_readfirstlane(ld_cnt(anchorp(ws, id)));
  const int n = rt32();
  const int d = (int)((v & ~1u) - ((unsigned)n << 1)) >> 1;
  return (d > 4096 || d < -(1 << 22)) ? n : n + d;
}
DEV f4v smf(u4v a, u8v b, f4v c, int idx) {
  return __builtin_amdgcn_smfmac_f32_16x16x64_bf16(asbf8(a), __builtin_bit_cast(bf16x, b), c, idx, 0, 0);
}
DEV unsigned umax4(u4v v) { return max(max(v.x, v.y), max(v.z, v.w)); }
DEV float dpp_ror8(float v) { return __builtin_bit_cast(float, __builtin_amdgcn_update_dpp(0, __builtin_bit_cast(int, v), 0x128, 0xf, 0xf, true)); }
DEV float dpp_xor1(float v) { return __builtin_bit_cast(float, __builtin_amdgcn_update_dpp(0, __builtin_bit_cast(int, v), 0xb1, 0xf, 0xf, true)); }
DEV void st4c_asm(void* p, unsigned v) { asm volatile("global_store_dword %0, %1, off sc1" :: "v"(p), "v"(v) : "memory"); }
DEV void st8c_asm(void* p, u2v v) { asm volatile("global_store_dwordx2 %0, %1, off sc1" :: "v"(p), "v"(v) : "memory"); }
DEV void ld16c_asm(u4v& v, const void* p) { asm volatile("global_load_dwordx4 %0, %1, off sc1" : "=&v"(v) : "v"(p) : "memory"); }
DEV void rawbar() { asm volatile("s_barrier" ::: "memory"); }
DEV unsigned lds_r4(unsigned a) { return *(volatile LDS unsigned*)(size_t)a; }
DEV void lds_w4(unsigned a, unsigned v) { *(volatile LDS unsigned*)(size_t)a = v; }
DEV u8v cat8(u4v a, u4v b) { return (u8v){a.x, a.y, a.z, a.w, b.x, b.y, b.z, b.w}; }
DEV void set_abort(unsigned char* ws) { st4c_asm(cntp(ws, C_ABORT), 1u); }

constexpr unsigned L_MEM = 0, L_KT = 8192, L_FLAG = 12288, L_RED = 16384;
constexpr unsigned L_EST = 12304, L_TS = 12308, L_CG = 12544, L_G = 32768, L_XN = 65536;
#define CLOB_V200_247 "v200","v201","v202","v203","v204","v205","v206","v207","v208","v209","v210","v211","v212","v213","v214","v215", \
  "v216","v217","v218","v219","v220","v221","v222","v223","v224","v225","v226","v227","v228","v229","v230","v231", \
  "v232","v233","v234","v235","v236","v237","v238","v239","v240","v241","v242","v243","v244","v245","v246","v247","v248"
#define SMF "v_smfmac_f32_16x16x64_bf16 "
#define R1_HEAD(o0, o1, o2, o3, o4, o5, o6, FIRST) \
  "v_mov_b32 v228, 0\n\tv_mov_b32 v229, 0\n\tv_mov_b32 v230, 0\n\tv_mov_b32 v231, 0\n\t" \
  "v_mov_b32 v232, 0\n\tv_mov_b32 v233, 0\n\tv_mov_b32 v234, 0\n\tv_mov_b32 v235, 0\n\t" \
  "v_mov_b32 v244, 0\n\tv_mov_b32 v245, 0\n\tv_mov_b32 v246, 0\n\tv_mov_b32 v247, 0\n\t" \
  "s_mov_b32 m0, %[m0v]\n\t" \
  "s_barrier\n\t" ST(80) \
  "global_load_lds_dwordx4 %[moff], %[src] sc1\n\t" \
  "s_waitcnt vmcnt(0)\n\t" \
  "ds_read_b128 v[200:203], %[rd] offset:" #o0 "*1024\n\t" \
  "s_barrier\n\t" ST(82) \
  "ds_read_b128 v[204:207], %[rd] offset:" #o1 "*1024\n\tds_read_b128 v[208:211], %[rd] offset:" #o2 "*1024\n\t" \
  "ds_read_b128 v[212:215], %[rd] offset:" #o3 "*1024\n\tds_read_b128 v[216:219], %[rd] offset:" #o4 "*1024\n\t" \
  "ds_read_b128 v[220:223], %[rd] offset:" #o5 "*1024\n\tds_read_b128 v[224:227], %[rd] offset:" #o6 "*1024\n\t"
#define R1_TWO(n6, n5, n4, n3, n2, n1, n0) \
  "s_waitcnt lgkmcnt(" #n6 ")\n\t" ST(84) SMF "v[228:231], v[200:203], %[a0], %[idx]\n\t" SMF "v[232:235], v[200:203], %[b0], %[idx]\n\t" \
  "s_waitcnt lgkmcnt(" #n5 ")\n\t" SMF "v[228:231], v[204:207], %[a1], %[idx]\n\t" SMF "v[232:235], v[204:207], %[b1], %[idx]\n\t" \
  "s_waitcnt lgkmcnt(" #n4 ")\n\t" SMF "v[228:231], v[208:211], %[a2], %[idx]\n\t" SMF "v[232:235], v[208:211], %[b2], %[idx]\n\t" \
  "s_waitcnt lgkmcnt(" #n3 ")\n\t" SMF "v[228:231], v[212:215], %[a3], %[idx]\n\t" SMF "v[232:235], v[212:215], %[b3], %[idx]\n\t" \
  "s_waitcnt lgkmcnt(" #n2 ")\n\t" SMF "v[228:231], v[216:219], %[a4], %[idx]\n\t" SMF "v[232:235], v[216:219], %[b4], %[idx]\n\t" \
  "s_waitcnt lgkmcnt(" #n1 ")\n\t" SMF "v[228:231], v[220:223], %[a5], %[idx]\n\t" \
  "s_waitcnt lgkmcnt(" #n0 ")\n\t" SMF "v[228:231], v[224:227], %[a6], %[idx]\n\t" \
  SMF "v[232:235], v[220:223], %[b5], %[idx]\n\t" SMF "v[232:235], v[224:227], %[b6], %[idx]\n\t" \
  ST(86) "s_nop 1\n\t" \
  "v_cvt_pk_bf16_f32 v228, v228, v229\n\tv_cvt_pk_bf16_f32 v229, v230, v231\n\t" \
  "ds_write_b64 %[ktw], v[228:229]\n\t" \
  "s_nop 2\n\t" \
  "v_cvt_pk_bf16_f32 v232, v232, v233\n\tv_cvt_pk_bf16_f32 v233, v234, v235\n\t" \
  "ds_write_b64 %[ktw], v[232:233] offset:512\n\t" \
  LDSWAIT ST(88) \
  "s_barrier\n\t" ST(90)
#define LDSWAIT
#define R1_WOPS [a0] "v"(w0[0]), [a1] "v"(w0[1]), [a2] "v"(w0[2]), [a3] "v"(w0[3]), [a4] "v"(w0[4]), [a5] "v"(w0[5]), [a6] "v"(w0[6])
#define R1_WOPS2 [b0] "v"(w1[0]), [b1] "v"(w1[1]), [b2] "v"(w1[2]), [b3] "v"(w1[3]), [b4] "v"(w1[4]), [b5] "v"(w1[5]), [b6] "v"(w1[6])
#define R1_COMMON [idx] "v"(idx), [rd] "v"(rd), [ktw] "v"(ktw), [moff] "v"(moff), [src] "s"(src), [m0v] "s"(m0v)

template <int W>
DEV void r1_mfma(unsigned char* __restrict__ ws, int h, int q, int lane, unsigned sel, int smk) {
  constexpr int wave = W, KIND = W == 0 ? 0 : W == 3 ? 2 : 1;
  const int g = lane >> 4, c16 = lane & 15;
  const int idx = (lane & 8) ? 0xEEEE : 0x4444;
  const unsigned moff = wave * 1024 + g * 256 + (lane & 7) * 32 + ((lane >> 3) & 1) * 16;
  u8v w0[7], w1[7];
  {
    const unsigned char* p = ws + O_WA + (size_t)(h * 28 + 7 * q + 2 * wave) * 14336 + lane * 16;
#pragma unroll
    for (int k = 0; k < 7; ++k) {
      const int o = ((k + W) % 7) * 2048;
      w0[k] = cat8(*(const u4v*)(p + o), *(const u4v*)(p + o + 1024));
      if (KIND != 2) w1[k] = cat8(*(const u4v*)(p + 14336 + o), *(const u4v*)(p + 14336 + o + 1024));
    }
#pragma unroll
    for (int k = 0; k < 7; ++k) { asm volatile("" : "+v"(w0[k])); if (KIND != 2) asm volatile("" : "+v"(w1[k])); }
  }
  RTS(1);
  const unsigned rd = L_MEM + lane * 16, ktw = L_KT + 2 * wave * 512 + c16 * 32 + g * 8, m0v = L_MEM + wave * 1024;
  const unsigned a0a = L_MEM + (7 * q + g) * 256 + c16 * 16, a1a = L_MEM + min(7 * q + 4 + g, 27) * 256 + c16 * 16;
  const unsigned trb = L_KT + (g >> 1) * 512 + (4 * (g & 1) + (c16 >> 2)) * 32 + (c16 & 3) * 8;
  const unsigned sto = h * 512 + q * 128 + (4 * (g & 1) + 2 * (g >> 1) + (c16 >> 3)) * 16 + (c16 & 7) * 2;
  const unsigned long long lo8 = 0x00FF00FF00FF00FFull;
  unsigned ep = 0;
  int budget = TRY_CAP;
  rawbar();
  rawbar();
  for (int s = 0; s < NSEG; ++s) {
    const unsigned char* src = hblk(ws, sel, s, 0);
    unsigned char* dst = hblk(ws, sel, 64 + s, smk);
    for (;;) {
      if (KIND == 0) {
        unsigned long long okm, tst;
        asm volatile(R1_HEAD(0, 1, 2, 3, 4, 5, 6, )
          "ds_read_b128 v[236:239], %[a0a]\n\tds_read_b128 v[240:243], %[a1a]\n\t"
          R1_TWO(8, 7, 6, 5, 4, 3, 2)
          "ds_read_b64_tr_b16 v[200:201], %[trb]\n\tds_read_b64_tr_b16 v[202:203], %[trb] offset:256\n\t"
          "ds_read_b64_tr_b16 v[204:205], %[trb] offset:1024\n\tds_read_b64_tr_b16 v[206:207], %[trb] offset:1280\n\t"
          "ds_read_b64_tr_b16 v[208:209], %[trb] offset:2048\n\tds_read_b64_tr_b16 v[210:211], %[trb] offset:2304\n\t"
          "ds_read_b64_tr_b16 v[212:213], %[trb] offset:3072\n\tds_read_b64_tr_b16 v[214:215], %[trb] offset:3328\n\t"
          "s_waitcnt lgkmcnt(4)\n\t" SMF "v[244:247], v[236:239], v[200:207], %[idx]\n\t"
          "s_waitcnt lgkmcnt(0)\n\t" SMF "v[244:247], v[240:243], v[208:215], %[idx]\n\t"
          "s_nop 7\n\t"
          "v_permlane32_swap_b32 v244, v246\n\tv_permlane32_swap_b32 v245, v247\n\t"
          "v_add_f32 v244, v244, v246\n\tv_add_f32 v245, v245, v247\n\t"
          "v_cndmask_b32 v246, v244, v245, %[lo8]\n\t"
          "v_cndmask_b32 v247, v245, v244, %[lo8]\n\t"
          "s_nop 1\n\t"
          "v_add_f32_dpp v247, v246, v247 row_ror:8 row_mask:0xf bank_mask:0xf\n\t"
          "v_cvt_pk_bf16_f32 v246, v247, v247\n\t"
          "v_cmpx_o_f32 vcc, v247, v247\n\t"
          "global_store_short %[sto], v246, %[dst] sc1\n\t" ST(92)
          "s_memtime %[tst]\n\t"
          "s_mov_b64 exec, -1\n\t"
          "s_mov_b64 %[okm], vcc\n\t"
          "s_waitcnt lgkmcnt(0)\n\t"
          : [okm] "=&s"(okm), [tst] "=&s"(tst)
          : R1_WOPS, R1_WOPS2, R1_COMMON, [a0a] "v"(a0a), [a1a] "v"(a1a), [trb] "v"(trb), [sto] "v"(sto), [dst] "s"(dst), [lo8] "s"(lo8)
          : CLOB_V200_247, "vcc", "memory" ST_CLOB);
        lds_w4(L_TS, (unsigned)tst);
        if (okm != ~0ull) {
          if (budget > 0) { if (lane == 0) lds_w4(L_FLAG, ep); }
          else asm volatile("global_store_short %0, %1, %2 sc1" :: "v"(sto), "v"(0x7FC0u), "s"(dst) : "memory");
        }
      } else if (KIND == 1) {
        if (W == 1) asm volatile(R1_HEAD(1, 2, 3, 4, 5, 6, 0, ) R1_TWO(6, 5, 4, 3, 2, 1, 0) :: R1_WOPS, R1_WOPS2, R1_COMMON : CLOB_V200_247, "memory" ST_CLOB);
        else asm volatile(R1_HEAD(2, 3, 4, 5, 6, 0, 1, ) R1_TWO(6, 5, 4, 3, 2, 1, 0) :: R1_WOPS, R1_WOPS2, R1_COMMON : CLOB_V200_247, "memory" ST_CLOB);
      } else {
        asm volatile(R1_HEAD(3, 4, 5, 6, 0, 1, 2, )
          "s_waitcnt lgkmcnt(6)\n\t" SMF "v[228:231], v[200:203], %[a0], %[idx]\n\t"
          "s_waitcnt lgkmcnt(5)\n\t" SMF "v[228:231], v[204:207], %[a1], %[idx]\n\t"
          "s_waitcnt lgkmcnt(4)\n\t" SMF "v[228:231], v[208:211], %[a2], %[idx]\n\t"
          "s_waitcnt lgkmcnt(3)\n\t" SMF "v[228:231], v[212:215], %[a3], %[idx]\n\t"
          "s_waitcnt lgkmcnt(2)\n\t" SMF "v[228:231], v[216:219], %[a4], %[idx]\n\t"
          "s_waitcnt lgkmcnt(1)\n\t" SMF "v[228:231], v[220:223], %[a5], %[idx]\n\t"
          "s_waitcnt lgkmcnt(0)\n\t" SMF "v[228:231], v[224:227], %[a6], %[idx]\n\t"
          "s_nop 7\n\t"
          "v_cvt_pk_bf16_f32 v228, v228, v229\n\tv_cvt_pk_bf16_f32 v229, v230, v231\n\t"
          "ds_write_b64 %[ktw], v[228:229]\n\t"
          LDSWAIT
          "s_barrier\n\t"
          :: R1_WOPS, R1_COMMON : CLOB_V200_247, "memory" ST_CLOB);
      }
      bar();
      const bool bad = __builtin_amdgcn_readfirstlane(lds_r4(L_FLAG)) == ep;
      ++ep;
      if (!bad) break;
      --budget;
      rawbar();
    }
    ARR(s);
    rawbar();
  }
}

typedef unsigned short h2v __attribute__((ext_vector_type(2)));
DEV unsigned pkmax(unsigned a, unsigned b) { return __builtin_bit_cast(unsigned, __builtin_elementwise_max(__builtin_bit_cast(h2v, a), __builtin_bit_cast(h2v, b))); }
DEV unsigned wavepkmax(unsigned v) {
  v = pkmax(v, __builtin_amdgcn_update_dpp(0u, v, 0xb1, 0xf, 0xf, true));
  v = pkmax(v, __builtin_amdgcn_update_dpp(0u, v, 0x4e, 0xf, 0xf, true));
  v = pkmax(v, __builtin_amdgcn_update_dpp(0u, v, 0x124, 0xf, 0xf, true));
  v = pkmax(v, __builtin_amdgcn_update_dpp(0u, v, 0x128, 0xf, 0xf, true));
  const u2v x = __builtin_amdgcn_permlane16_swap(v, v, false, false);
  v = pkmax(x.x, x.y);
  const u2v y = __builtin_amdgcn_permlane32_swap(v, v, false, false);
  return pkmax(y.x, y.y);
}
DEV void r1_boss(unsigned char* __restrict__ ws, int lane, unsigned sel TUNE_ARG) {
  constexpr unsigned L_FA = 13312, L_FY = 13568;
  int A = 0, V = 0, ms = 4 * K_MS, mm = 4 * K_MM;
  float rho = 0.f;
  int hist = K_PERIOD;
  bool prevf3 = false;
  unsigned ep = 0;
  if (lane == 1) hist += K_WARM;
  if (lane < 5) st4c_asm(ptp(ws, lane), ptword(hist, K_DS));
  rawbar();
  wait_flags(ws, flagR(ws, sel, lane));
  wait_flags(ws, flagR(ws, sel, 64 + min(lane, 27)));
  if (lane < 32) st4c_asm(anchorp(ws, lane), ((unsigned)(rt32() + K_LEAD) << 1) | 1u);
  for (int t = 0; t < NSEG; ++t) {
    const int s = max(t - 3, 0);
    const float pa = 4.f * (float)__builtin_amdgcn_readlane(hist, max(s - 1, 0)), py = 4.f * (float)__builtin_amdgcn_readlane(hist, s);
    rawbar(); rawbar();
    if (t >= 3) vm0();
    const unsigned a = lds_r4(L_FA + lane * 4), y = lds_r4(L_FY + lane * 4);
    const float da = (float)((a >> 12) & 0xffffu), dy = (float)((y >> 12) & 0xffffu);
    unsigned pk = min((unsigned)((float)(a & 0xfffu) * pa * __builtin_amdgcn_rcpf(da)), 65535u) |
                  (min((unsigned)((float)(y & 0xfffu) * py * __builtin_amdgcn_rcpf(dy)), 65535u) << 16);
    unsigned fl = (max(a, y) == SENT ? 1u : 0u) | ((a >> 28) != 0 ? 2u : 0u) | ((y >> 26) & 4u) |
                  (fabsf(da - 0.94f * pa) > 0.08f * pa ? 8u : 0u) | (fabsf(dy - 0.94f * py) > 0.08f * py ? 16u : 0u) | ((y >> 25) & 16u);
    float rn = dy * __builtin_amdgcn_rcpf(py);
    asm volatile("s_barrier" : "+v"(pk), "+v"(fl), "+v"(rn) :: "memory");
    pk = wavepkmax(pk);
    asm volatile("s_barrier\n\ts_barrier" : "+v"(pk) :: "memory");
    while (__builtin_amdgcn_readfirstlane(lds_r4(L_FLAG)) == ep++) { rawbar(); rawbar(); rawbar(); rawbar(); rawbar(); }
    if (t >= 3 && t + 2 < NSEG) {
      if (!__any(fl & 1)) {
        const unsigned m = __builtin_amdgcn_readfirstlane(pk);
        const bool f1 = __any(fl & 2), f3 = __any(fl & 4);
        if (!__any(fl & 10)) A = A == 0 ? (int)(m & 0xffffu) : A + (((int)(m & 0xffffu) - A) >> 2);
        if (!__any(fl & 20)) { V = V == 0 ? (int)(m >> 16) : V + (((int)(m >> 16) - V) >> 2); rho = rho == 0.f ? rn : rho + 0.25f * (rn - rho); }
        if (s >= 4) {
          if (f1 && !prevf3) mm = min(mm + 20, 4 * K_MM + 240);
          if (f3 && !f1) ms = min(ms + 20, 4 * K_MS + 240);
        }
        prevf3 = f3;
      }
      int P = K_PERIOD, DS = K_DS;
      if (A > 0 && V > 0 && !K_FIXED) {
        DS = min(max((int)((float)(A + ms) * rho), 1200), 3000);
        P = min(max((A + ms + V - 84 + mm) >> 2, 600), 1500);
      }
      if (lane == t + 2) hist = P;
      post(ptp(ws, t + 2), ptword(P, DS), lane);
    }
    if (t >= 2) {
      asm volatile("s_mov_b32 m0, %2\n\ts_nop 0\n\tglobal_load_lds_dword %0, off sc1\n\ts_mov_b32 m0, %3\n\ts_nop 0\n\tglobal_load_lds_dword %1, off sc1"
                   :: "v"(flagS(ws, sel, t - 2, lane)), "v"(flagY(ws, sel, t - 2, min(lane, 27))), "s"(L_FA), "s"(L_FY) : "memory");
    }
  }
  vm0();
  rawbar();
}

DEV void role_r1(unsigned char* __restrict__ ws, int id, int wave, int lane, unsigned sel TUNE_ARG) {
  const int h = id >> 2, q = id & 3;
  if (wave == 0) { r1_mfma<0>(ws, h, q, lane, sel, K_SMK); return; }
  if (wave == 1) { r1_mfma<1>(ws, h, q, lane, sel, K_SMK); return; }
  if (wave == 2) { r1_mfma<2>(ws, h, q, lane, sel, K_SMK); return; }
  if (wave == 3) { r1_mfma<3>(ws, h, q, lane, sel, K_SMK); return; }
  if (wave == 7 && id == 3) { r1_boss(ws, lane, sel TUNE_PASS); return; }
  const int g = lane >> 4, c16 = lane & 15;
  const bool act = q < 2;
  const int fr = wave - 1;
  const unsigned moff = fr * 1024 + g * 256 + (lane & 7) * 32 + ((lane >> 3) & 1) * 16, m0v = L_MEM + fr * 1024;
  const unsigned xo = L_XN + (g >> 1) * 1024 + (g & 1) * 512 + c16 * 16;
  unsigned ep = 0;
  if (wave == 7) {
    const int idx = (lane & 8) ? 0xEEEE : 0x4444;
    u8v w0[7];
    const unsigned rd = lane * 16;
    const unsigned sto = h * 512 + (q & 1) * 256 + (4 * (g & 1) + 2 * (g >> 1) + (lane & 1)) * 32 + (c16 >> 1) * 4;
    const unsigned long long odd = 0xAAAAAAAAAAAAAAAAull;
    __builtin_amdgcn_s_setprio(3);
    rawbar();
    for (int s = 0; s < NSEG; ++s) {
      unsigned char* dst = hblk(ws, sel, s, 2);
      unsigned char* raw = hblk(ws, sel, s, 1);
      const bool go = act && s < NSEG - 1;
      if (go) {
#pragma unroll
        for (int k = 0; k < 7; ++k) w0[k] = cat8(lds_r16(L_G + (s & 1) * 14336 + rd + k * 2048), lds_r16(L_G + (s & 1) * 14336 + rd + k * 2048 + 1024));
      }
      for (;;) {
        if (go) {
          asm volatile(
            "v_mov_b32 v228, 0\n\tv_mov_b32 v229, 0\n\tv_mov_b32 v230, 0\n\tv_mov_b32 v231, 0\n\t"
            "s_barrier\n\ts_barrier\n\ts_barrier\n\t" ST(80)
            "ds_read_b128 v[200:203], %[rd]\n\tds_read_b128 v[204:207], %[rd] offset:1024\n\tds_read_b128 v[208:211], %[rd] offset:2048\n\t"
            "ds_read_b128 v[212:215], %[rd] offset:3072\n\tds_read_b128 v[216:219], %[rd] offset:4096\n\tds_read_b128 v[220:223], %[rd] offset:5120\n\t"
            "ds_read_b128 v[224:227], %[rd] offset:6144\n\t"
            "s_waitcnt lgkmcnt(6)\n\t" SMF "v[228:231], v[200:203], %[a0], %[idx]\n\t"
            "s_waitcnt lgkmcnt(5)\n\t" SMF "v[228:231], v[204:207], %[a1], %[idx]\n\t"
            "s_waitcnt lgkmcnt(4)\n\t" SMF "v[228:231], v[208:211], %[a2], %[idx]\n\t"
            "s_waitcnt lgkmcnt(3)\n\t" SMF "v[228:231], v[212:215], %[a3], %[idx]\n\t"
            "s_waitcnt lgkmcnt(2)\n\t" SMF "v[228:231], v[216:219], %[a4], %[idx]\n\t"
            "s_waitcnt lgkmcnt(1)\n\t" SMF "v[228:231], v[220:223], %[a5], %[idx]\n\t"
            "s_waitcnt lgkmcnt(0)\n\t" SMF "v[228:231], v[224:227], %[a6], %[idx]\n\t"
            "s_nop 7\n\t"
            "v_permlane32_swap_b32 v228, v230\n\tv_permlane32_swap_b32 v229, v231\n\t"
            "v_add_f32 v228, v228, v230\n\tv_add_f32 v229, v229, v231\n\t"
            "v_cndmask_b32 v230, v229, v228, %[odd]\n\t"
            "s_nop 1\n\t"
            "v_mov_b32_dpp v230, v230 quad_perm:[1,0,3,2] row_mask:0xf bank_mask:0xf\n\t"
            "v_cndmask_b32 v228, v228, v230, %[odd]\n\tv_cndmask_b32 v229, v230, v229, %[odd]\n\t"
            "v_exp_f32 v230, v228\n\tv_exp_f32 v231, v229\n\t"
            "v_cvt_pk_bf16_f32 v232, v228, v229\n\t"
            "v_cvt_pk_bf16_f32 v233, v230, v231\n\t"
            "v_cmpx_o_f32 vcc, v228, v229\n\t"
            "global_store_dword %[sto], v233, %[dst] sc1\n\t" ST(82)
            "global_store_dword %[sto], v232, %[raw] sc1\n\t"
            "s_mov_b64 exec, -1\n\t"
            "s_barrier\n\t"
            :: R1_WOPS, [idx] "v"(idx), [rd] "v"(rd), [sto] "v"(sto), [dst] "s"(dst), [raw] "s"(raw), [odd] "s"(odd)
            : CLOB_V200_247, "vcc", "memory" ST_CLOB);
          rawbar();
        } else {
          rawbar(); rawbar(); rawbar(); rawbar(); rawbar();
        }
        const bool bad = __builtin_amdgcn_readfirstlane(lds_r4(L_FLAG)) == ep;
        ++ep;
        if (!bad) break;
      }
    }
    rawbar();
  } else if (wave == 4) {
    const unsigned trb = L_KT + (g >> 1) * 512 + (4 * (g & 1) + (c16 >> 2)) * 32 + (c16 & 3) * 8;
    unsigned char* so = ws + O_SM + (size_t)h * 2048 + q * 256 + lane * 4;
    const unsigned x3 = xo, xlast = g < 2 ? 6144 : 5120;
#pragma unroll
    for (int u = 0; u < 8; ++u) *(LDS u2v*)(size_t)(L_KT + u * 512 + lane * 8) = u < 7 ? (u2v){SENT, SENT} : (u2v){0u, 0u};
    if (lane == 0) lds_w4(L_FLAG, SENT);
    rawbar();
    (void)ld_cnt((const unsigned*)(lane < 32 ? so : lane < 48 ? so + 63 * 32768 : hblk(ws, sel, 70, 0)));
    unsigned pay = 0, eprev = 0;
    for (int s = 0; s < NSEG; ++s) {
      bool sig = s > 0;
      const unsigned ep0 = ep;
      for (;;) {
        bar(); rawbar(); rawbar();
        if (sig) { vm0(); if (lane == 0) st4c_asm(flagS(ws, sel, s - 1, id), pay); sig = false; }
        rawbar(); rawbar();
        const bool bad = __builtin_amdgcn_readfirstlane(lds_r4(L_FLAG)) == ep;
        ++ep;
        if (!bad) break;
      }
      {
        const unsigned est = lds_r4(L_EST), tst = lds_r4(L_TS);
        pay = (est ? min(tst - est, 4095u) : 0u) | ((est && eprev) ? min(est - eprev, 65535u) << 12 : 0u);
        pay |= min(ep - ep0 - 1, 7u) << 28;
        eprev = est;
      }
      f4v c0 = {0.f, 0.f, 0.f, 0.f}, c1 = c0;
      u4v ka[4], xa[4], xb[4];
#pragma unroll
      for (int u = 0; u < 4; ++u) {
        const u2v lo = lds_tr(trb + u * 1024), hi = lds_tr(trb + u * 1024 + 256);
        ka[u] = (u4v){lo.x, lo.y, hi.x, hi.y};
        const unsigned ad = x3 + (s & 1) * 8192 + (u < 3 ? u * 2048 : xlast);
        xa[u] = lds_r16(ad); xb[u] = lds_r16(ad + 256);
      }
      __builtin_amdgcn_sched_barrier(0);
#pragma unroll
      for (int u = 0; u < 4; ++u) { c0 = mfma16(ka[u], xa[u], c0); c1 = mfma16(ka[u], xb[u], c1); }
#pragma unroll
      for (int u = 0; u < 7; ++u) *(LDS u2v*)(size_t)(L_KT + u * 512 + lane * 8) = (u2v){SENT, SENT};
      st4c_asm(so + (size_t)s * 32768, pk2(halfsum(c0[0], c0[1]), halfsum(c0[2], c0[3])));
      st4c_asm(so + (size_t)s * 32768 + 1024, pk2(halfsum(c1[0], c1[1]), halfsum(c1[2], c1[3])));
      ARR(s);
    }
    vm0();
    rawbar();
    if (lane == 0) st4c_asm(flagS(ws, sel, NSEG - 1, id), pay);
  } else if (wave == 5) {
    const unsigned char* gp = ws + O_G + (size_t)(h * 2 + (q & 1)) * G_RUN;
    const unsigned v0 = lane * 16, v1 = v0 + 4096, v2 = v0 + 8192, v3 = v0 + 12288;
    auto fetch = [&](int t) {
#define GL4(v) "global_load_lds_dwordx4 " v ", %4 sc1\n\t" "global_load_lds_dwordx4 " v ", %4 offset:1024 sc1\n\t"
#define GL8(v) GL4(v) "global_load_lds_dwordx4 " v ", %4 offset:2048 sc1\n\t" "global_load_lds_dwordx4 " v ", %4 offset:3072 sc1\n\t"
      asm volatile("s_mov_b32 m0, %5\n\ts_nop 0\n\t" GL8("%0") "s_add_u32 m0, m0, 4096\n\ts_nop 0\n\t" GL8("%1")
                   "s_add_u32 m0, m0, 4096\n\ts_nop 0\n\t" GL8("%2") "s_add_u32 m0, m0, 4096\n\ts_nop 0\n\t" GL4("%3")
                   :: "v"(v0), "v"(v1), "v"(v2), "v"(v3), "s"(gp + t * 14336), "s"(L_G + (t & 1) * 14336) : "scc", "memory");
    };
    auto fetchx = [&](int t) {
      asm volatile("s_mov_b32 m0, %3\n\ts_nop 0\n\t"
                   "global_load_lds_dwordx4 %0, %2\n\tglobal_load_lds_dwordx4 %0, %2 offset:1024\n\tglobal_load_lds_dwordx4 %0, %2 offset:2048\n\t"
                   "global_load_lds_dwordx4 %0, %2 offset:3072\n\ts_add_u32 m0, m0, 4096\n\ts_nop 0\n\t"
                   "global_load_lds_dwordx4 %1, %2\n\tglobal_load_lds_dwordx4 %1, %2 offset:1024\n\tglobal_load_lds_dwordx4 %1, %2 offset:2048"
                   :: "v"(v0), "v"(v1), "s"(ws + O_XN + (size_t)t * 28672 + q * 7168), "s"(L_XN + (t & 1) * 8192) : "scc", "memory");
    };
    const unsigned* gdone = doneG(ws, 7 * (h >> 1) + min(lane, 6));
    auto peek = [&](int t) {
      unsigned keep;
      asm volatile("s_mov_b32 %0, m0\n\ts_mov_b32 m0, %2\n\ts_nop 0\n\tglobal_load_lds_dword %1, off sc1\n\ts_mov_b32 m0, %0"
                   : "=&s"(keep) : "v"(lane == 63 ? ptp(ws, t) : gdone), "s"(L_CG) : "memory");
    };
    if (act) {
      wait_cnt(ws, gdone, 1);
      fetch(0); fetch(1);
    }
    fetchx(0);
    vm0();
    RTS(1);
    rawbar();
    for (int s = 0; s < NSEG; ++s) {
      const unsigned char* src = hblk(ws, sel, s, 0) + moff;
      bool todo = act && s + 2 < NSEG - 1, todox = s + 1 < NSEG;
      for (;;) {
        rawbar();
        if (s == 0) peek(0);
        rawbar();
        dma16c(src, m0v);
        vm0();
        rawbar();
        const bool there = __all(lds_r4(L_CG + (lane & 7) * 4) >= (unsigned)(s >> 1) + 2);
        rawbar();
        rawbar();
        if (todo && there) { fetch(s + 2); todo = false; }
        if (todox) { fetchx(s + 1); peek(s + 1); todox = false; }
        const bool bad = __builtin_amdgcn_readfirstlane(lds_r4(L_FLAG)) == ep;
        ++ep;
        if (!bad) break;
      }
      if (todo) {
        wait_cnt(ws, gdone, (s >> 1) + 2);
        fetch(s + 2);
      }
      ARR(s);
    }
    vm0();
    bar();
  } else {
      { Lat W = {rt32(), 6, 0}; (void)lat_wait(W); }
    rawbar();
    Lat L = {get_anchor(ws, sel, id, lane), 0, 0};
    for (int s = 0; s < NSEG; ++s) {
      const unsigned char* src = hblk(ws, sel, s, 0) + moff;
      unsigned pw = 0;
      for (;;) {
        bar();
        const unsigned est = lat_wait(L);
        rawbar();
        asm volatile("s_mov_b32 m0, %1\n\ts_nop 0\n\tglobal_load_lds_dwordx4 %0, off sc1\n\tglobal_load_lds_dwordx4 %0, off offset:1024 sc1\n\ts_waitcnt vmcnt(0)"
                     :: "v"(src), "s"(m0v) : "memory");
        rawbar();
        lds_w4(L_EST, est);
        if (pw == 0) pw = __builtin_amdgcn_readfirstlane(lds_r4(L_CG + 252)) | 0x80000000u;
        bar(); rawbar();
        const bool bad = __builtin_amdgcn_readfirstlane(lds_r4(L_FLAG)) == ep;
        ++ep;
        EV(bad ? 1 : 0);
        if (!bad) break;
        L.e += RET_E;
      }
      if (pw == 0x80000000u && s < NSEG - 1) { wait_cnt(ws, ptp(ws, s), 1); pw = __builtin_amdgcn_readfirstlane(ld_cnt(ptp(ws, s))); }
      lat_fwd(L, pw & 255, (pw >> 8) & 255);
      ARR(s);
    }
    bar();
    EV_OUT(id);
  }
}

#define XRED(op, v, t) \
  "v_mov_b32 " t ", " v "\n\ts_nop 1\n\tv_permlane16_swap_b32 " v ", " t "\n\t" op " " v ", " v ", " t "\n\t" \
  "v_mov_b32 " t ", " v "\n\ts_nop 1\n\tv_permlane32_swap_b32 " v ", " t "\n\t" op " " v ", " v ", " t "\n\t"
#define MF16 "v_mfma_f32_16x16x32_bf16 "
#define WAIT_CYC(T) \
  "s_memtime vcc\n\ts_waitcnt lgkmcnt(0)\n\t" \
  "s_sub_i32 s70, " T ", vcc_lo\n\ts_max_i32 s70, s70, 0\n\ts_mul_i32 s70, s70, 13\n\ts_lshr_b32 s70, s70, 6\n\t" \
  "s_min_i32 s70, s70, 128\n\ts_lshl_b32 s70, s70, 2\n\ts_sub_i32 s70, 524, s70\n\t" \
  "s_getpc_b64 vcc\n\ts_add_u32 vcc_lo, vcc_lo, s70\n\ts_addc_u32 vcc_hi, vcc_hi, 0\n\ts_setpc_b64 vcc\n\t" \
  ".rept 128\n\ts_nop 0\n\t.endr\n\t"
#define R3_Y_A \
  "ds_read_b128 v[200:203], %[rd]\n\tds_read_b128 v[204:207], %[rd] offset:1024\n\tds_read_b128 v[208:211], %[rd] offset:2048\n\t" \
  "ds_read_b128 v[212:215], %[rd] offset:3072\n\tds_read_b128 v[216:219], %[rd] offset:4096\n\tds_read_b128 v[220:223], %[rd] offset:5120\n\t" \
  "ds_read_b128 v[224:227], %[rd] offset:6144\n\t" \
  "s_waitcnt lgkmcnt(6)\n\t" SMF "v[228:231], v[200:203], %[a0], %[idx]\n\t" SMF "v[232:235], v[200:203], %[b0], %[idx]\n\t" \
  "s_waitcnt lgkmcnt(5)\n\t" SMF "v[228:231], v[204:207], %[a1], %[idx]\n\t" SMF "v[232:235], v[204:207], %[b1], %[idx]\n\t" \
  "s_waitcnt lgkmcnt(4)\n\t" SMF "v[228:231], v[208:211], %[a2], %[idx]\n\t" SMF "v[232:235], v[208:211], %[b2], %[idx]\n\t" \
  "s_waitcnt lgkmcnt(3)\n\t" SMF "v[228:231], v[212:215], %[a3], %[idx]\n\t" SMF "v[232:235], v[212:215], %[b3], %[idx]\n\t" \
    \
  "s_waitcnt lgkmcnt(0)\n\t" \
  "v_readfirstlane_b32 s68, v244\n\t" \
  "v_max3_u32 v244, v216, v217, v218\n\tv_max3_u32 v245, v219, v220, v221\n\tv_max3_u32 v244, v244, v222, v223\n\t" \
  "v_max3_u32 v245, v245, v224, v225\n\tv_max3_u32 v244, v244, v226, v227\n\tv_max_u32 v244, v244, v245\n\t" \
  "v_cmp_eq_u32 s[74:75], -1, v244\n\t" \
  "v_cmp_u_f32 vcc, v228, v229\n\t" \
  "s_or_b64 s[74:75], s[74:75], vcc\n\t" \
  "v_cmp_u_f32 vcc, v234, v235\n\t" \
  "s_or_b64 vcc, s[74:75], vcc\n\t"
#define R3_Y_B \
  SMF "v[228:231], v[216:219], %[a4], %[idx]\n\t" SMF "v[232:235], v[216:219], %[b4], %[idx]\n\t" \
  SMF "v[228:231], v[220:223], %[a5], %[idx]\n\t" SMF "v[228:231], v[224:227], %[a6], %[idx]\n\t" \
  SMF "v[232:235], v[220:223], %[b5], %[idx]\n\t" SMF "v[232:235], v[224:227], %[b6], %[idx]\n\t" \
  "s_nop 1\n\t" \
  "v_permlane32_swap_b32 v228, v229\n\tv_permlane32_swap_b32 v230, v231\n\t" \
  "v_add_f32 v228, v228, v229\n\tv_add_f32 v230, v230, v231\n\t" \
  "v_cvt_pk_bf16_f32 v164, v228, v230\n\t" \
  "s_nop 0\n\t" \
  "v_permlane32_swap_b32 v232, v233\n\tv_permlane32_swap_b32 v234, v235\n\t" \
  "v_add_f32 v232, v232, v233\n\tv_add_f32 v234, v234, v235\n\t" \
  "v_cvt_pk_bf16_f32 v165, v232, v234\n\t" \
  ST(82)
#define R3_POLL \
  WAIT_CYC("s68") \
  "global_load_dwordx4 v[236:239], %[to], s[60:61] sc1\n\t" \
  WAIT_CYC("s69") \
  "global_load_dwordx4 v[240:243], %[tm], s[60:61] sc1\n\t" ST(86)
#define R3_POLL2   \
  "55:\n\ts_memtime vcc\n\ts_waitcnt lgkmcnt(0)\n\ts_sub_i32 s70, s69, vcc_lo\n\ts_cmp_gt_i32 s70, 0\n\ts_cbranch_scc1 55b\n\t" \
  "global_load_dwordx4 v[236:239], %[to], s[60:61] sc1\n\tglobal_load_dwordx4 v[240:243], %[tm], s[60:61] sc1\n\t"
#define R3_S_PHASE(SIG, NS) \
  "s_waitcnt vmcnt(1)\n\t" ST(84) \
  "v_cndmask_b32 v208, 0, v236, s[76:77]\n\tv_cndmask_b32 v209, 0, v237, s[76:77]\n\tv_cndmask_b32 v210, 0, v238, s[76:77]\n\tv_cndmask_b32 v211, 0, v239, s[76:77]\n\t" \
  MF16 "v[220:223], v[160:163], v[236:239], 0\n\t" \
  "v_cndmask_b32 v212, v236, 0, s[76:77]\n\tv_cndmask_b32 v213, v237, 0, s[76:77]\n\tv_cndmask_b32 v214, v238, 0, s[76:77]\n\tv_cndmask_b32 v215, v239, 0, s[76:77]\n\t" \
  MF16 "v[216:219], v[192:195], v[208:211], 0\n\t" \
  "s_nop 0\n\t" \
  MF16 "v[216:219], v[196:199], v[212:215], v[216:219]\n\t" \
  "global_store_dwordx2 %[yo], v[164:165], s[60:61] sc1\n\t"        \
  SIG "s_waitcnt vmcnt(" #NS ")\n\t" ST(88) \
  MF16 "v[232:235], %[selA], v[240:243], 0\n\t" \
  "s_nop 7\n\t" \
  "v_exp_f32 v234, v232\n\tv_exp_f32 v235, v233\n\t" \
  "s_nop 0\n\t" \
  "v_cvt_pk_bf16_f32 v240, v234, v235\n\t" \
  "v_cndmask_b32 v168, 0, v240, s[76:77]\n\tv_cndmask_b32 v169, v240, 0, s[76:77]\n\t" \
  "s_nop 1\n\t" \
  MF16 "v[220:223], v[160:163], v[168:171], v[220:223]\n\t" \
  MF16 "v[244:247], v[164:167], v[168:171], v[216:219]\n\t" \
  "s_nop 6\n\t" \
  "v_rcp_f32 v242, v220\n\t" \
  "v_med3_f32 v241, v220, v172, v173\n\t" \
  "v_cmp_lg_f32 vcc, v241, v220\n\t" \
  "s_cbranch_vccnz 12f\n\t" \
  "11:\n\t" \
  "v_pk_mul_f32 v[244:245], v[244:245], v[242:243] op_sel_hi:[1,0]\n\t" \
  "v_pk_mul_f32 v[246:247], v[246:247], v[242:243] op_sel_hi:[1,0]\n\t" \
  "s_nop 0\n\t" \
  "v_add_f32_dpp v244, v244, v244 row_ror:8 row_mask:0xf bank_mask:0xf\n\tv_add_f32_dpp v245, v245, v245 row_ror:8 row_mask:0xf bank_mask:0xf\n\t" \
  "v_add_f32_dpp v246, v246, v246 row_ror:8 row_mask:0xf bank_mask:0xf\n\tv_add_f32_dpp v247, v247, v247 row_ror:8 row_mask:0xf bank_mask:0xf\n\t" \
  ST(90) "ds_write_b128 %[redw], v[244:247]\n\t" \
  LDSWAIT \
  "s_barrier\n\t" ST(92)
#define R3_S_RARE \
    "12:\n\t" \
  "v_xor_b32 v241, 0x6000, %[to]\n\t" \
  "global_load_dwordx4 v[200:203], v241, s[60:61] sc1\n\t" \
  "s_waitcnt vmcnt(0)\n\t" \
  "v_lshlrev_b32 v208, 16, v200\n\tv_and_b32 v209, 0xffff0000, v200\n\tv_lshlrev_b32 v210, 16, v201\n\tv_and_b32 v211, 0xffff0000, v201\n\t" \
  "v_lshlrev_b32 v212, 16, v202\n\tv_and_b32 v213, 0xffff0000, v202\n\tv_lshlrev_b32 v214, 16, v203\n\tv_and_b32 v215, 0xffff0000, v203\n\t" \
  "v_max3_f32 v240, v208, v209, v210\n\tv_max3_f32 v241, v211, v212, v213\n\tv_max3_f32 v240, v240, v214, v215\n\t" \
  "v_max3_f32 v241, v241, v232, v233\n\tv_max_f32 v240, v240, v241\n\t" \
  XRED("v_max_f32", "v240", "v241") \
  "v_sub_f32 v208, v208, v240\n\tv_sub_f32 v209, v209, v240\n\tv_sub_f32 v210, v210, v240\n\tv_sub_f32 v211, v211, v240\n\t" \
  "v_sub_f32 v212, v212, v240\n\tv_sub_f32 v213, v213, v240\n\tv_sub_f32 v214, v214, v240\n\tv_sub_f32 v215, v215, v240\n\t" \
  "v_sub_f32 v234, v232, v240\n\tv_sub_f32 v235, v233, v240\n\t" \
  "v_exp_f32 v208, v208\n\tv_exp_f32 v209, v209\n\tv_exp_f32 v210, v210\n\tv_exp_f32 v211, v211\n\t" \
  "v_exp_f32 v212, v212\n\tv_exp_f32 v213, v213\n\tv_exp_f32 v214, v214\n\tv_exp_f32 v215, v215\n\t" \
  "v_exp_f32 v234, v234\n\tv_exp_f32 v235, v235\n\ts_nop 1\n\t" \
  "v_cvt_pk_bf16_f32 v200, v208, v209\n\tv_cvt_pk_bf16_f32 v201, v210, v211\n\tv_cvt_pk_bf16_f32 v202, v212, v213\n\tv_cvt_pk_bf16_f32 v203, v214, v215\n\t" \
  "v_cvt_pk_bf16_f32 v240, v234, v235\n\t" \
  "v_cndmask_b32 v208, 0, v200, s[76:77]\n\tv_cndmask_b32 v209, 0, v201, s[76:77]\n\tv_cndmask_b32 v210, 0, v202, s[76:77]\n\tv_cndmask_b32 v211, 0, v203, s[76:77]\n\t" \
  "v_cndmask_b32 v212, v200, 0, s[76:77]\n\tv_cndmask_b32 v213, v201, 0, s[76:77]\n\tv_cndmask_b32 v214, v202, 0, s[76:77]\n\tv_cndmask_b32 v215, v203, 0, s[76:77]\n\t" \
  "v_cndmask_b32 v168, 0, v240, s[76:77]\n\tv_cndmask_b32 v169, v240, 0, s[76:77]\n\ts_nop 1\n\t" \
  MF16 "v[220:223], v[160:163], v[200:203], 0\n\t" \
  MF16 "v[220:223], v[160:163], v[168:171], v[220:223]\n\t" \
  MF16 "v[244:247], v[192:195], v[208:211], 0\n\t" \
  MF16 "v[244:247], v[196:199], v[212:215], v[244:247]\n\t" \
  MF16 "v[244:247], v[164:167], v[168:171], v[244:247]\n\t" \
  "s_nop 7\n\t" \
  "v_rcp_f32 v242, v220\n\ts_nop 0\n\t" \
  "s_branch 11b\n\t"
#define R3_PREP "s_lshl_b32 s44, s65, 2\n\ts_add_i32 s44, s44, s67\n\ts_lshl_b32 s45, s66, 2\n\ts_sub_i32 s45, 108, s45\n\t"
#define R3_FWD "s_add_i32 s66, s66, s47\n\ts_add_i32 s65, s65, s46\n\ts_cmp_ge_i32 s66, 24\n\ts_cselect_b32 s70, 24, 0\n\ts_addc_u32 s65, s65, 0\n\ts_sub_i32 s66, s66, s70\n\t"
#define R3_RT "s_memrealtime vcc\n\ts_waitcnt lgkmcnt(0)\n\ts_sub_i32 s70, s44, vcc_lo\n\ts_cmp_lt_i32 s70, 1\n\t"
#define R3_TW_WAIT \
  R3_RT "s_cbranch_scc1 40f\n\t" \
  "20:\n\t" R3_RT "s_cbranch_scc1 21f\n\t" R3_RT "s_cbranch_scc1 21f\n\t" R3_RT "s_cbranch_scc1 21f\n\t" R3_RT "s_cbranch_scc0 20b\n\t" \
  "21:\n\t" \
  "s_getpc_b64 vcc\n\ts_add_u32 vcc_lo, vcc_lo, s45\n\ts_addc_u32 vcc_hi, vcc_hi, 0\n\ts_setpc_b64 vcc\n\t" \
  ".rept 24\n\ts_nop 0\n\t.endr\n\t" \
  "22:\n\t"
#define R3_TW_LATE \
  "40:\n\t" \
  "s_cmp_eq_u32 s70, 0\n\ts_cbranch_scc1 22b\n\t" \
  "41:\n\ts_add_i32 s65, s65, 26\n\ts_add_i32 s44, s44, 4*26\n\ts_add_i32 s70, s70, 4*26\n\ts_cmp_lt_i32 s70, 0\n\ts_cbranch_scc1 41b\n\t" \
  "s_branch 20b\n\t"
#define R3_STEP(op, uop)   \
  op " %[moff], 0x8000, %[moff]\n\t" op " %[to], 0x8000, %[to]\n\t" op " %[tm], 0x8000, %[tm]\n\t" op " %[sto], 0x8000, %[sto]\n\t" \
  uop " %[uo], 0x4000, %[uo]\n\t" uop " %[yo], 0x1000, %[yo]\n\t"
#define R3_FLIP \
  "s_and_b32 s70, s62, 15\n\ts_cselect_b32 s70, 0, 0x4000\n\ts_add_i32 s71, s62, 1\n\ts_and_b32 s71, s71, 15\n\ts_cselect_b32 s71, 0, 0x4000\n\t" \
  "v_xor_b32 %[moff], s70, %[moff]\n\tv_xor_b32 %[to], s70, %[to]\n\tv_xor_b32 %[tm], s70, %[tm]\n\tv_xor_b32 %[sto], s71, %[sto]\n\t"
#define ULOADS "global_load_dwordx4 v[192:195], %[uo], s[60:61] sc1\n\tglobal_load_dwordx4 v[196:199], %[uo], s[60:61] offset:1024 sc1\n\t"
#define R3_DMA "global_load_lds_dwordx4 %[moff], s[60:61] sc1\n\t"
#define R3_TW_PT "s_lshl_b32 s70, s62, 7\n\ts_add_i32 s70, s70, 0x420080\n\tv_mov_b32 v178, s70\n\tglobal_load_dword v177, v178, s[60:61] sc1\n\t"
#define R3_TW_NEXT "v_readfirstlane_b32 s70, v177\n\ts_cmp_eq_u32 s70, 0\n\ts_cbranch_scc1 45f\n\t" \
  "19:\n\ts_and_b32 s46, s70, 0xff\n\ts_bfe_u32 s47, s70, 0x80008\n\ts_lshr_b32 s48, s70, 16\n\t"
#define R3_TW_OOL        \
  "45:\n\ts_mov_b32 s71, 0x40000\n\t" \
  "46:\n\tglobal_load_dword v177, v178, s[60:61] sc1\n\ts_waitcnt vmcnt(0)\n\tv_readfirstlane_b32 s70, v177\n\t" \
  "s_cmp_eq_u32 s70, 0\n\ts_cbranch_scc0 19b\n\ts_sub_i32 s71, s71, 1\n\ts_cmp_gt_i32 s71, 0\n\ts_cbranch_scc1 46b\n\ts_movk_i32 s70, 40\n\ts_branch 19b\n\t"
#define R3_TW_TIME \
  TBURST \
  "s_sub_i32 s4, s4, s44\n\ts_add_i32 s4, s4, 4\n\ts_mul_i32 s4, s4, 93\n\ts_ashr_i32 s4, s4, 2\n\ts_sub_i32 s58, s58, s4\n\t" \
  "s_lshl_b32 s4, s66, 2\n\ts_add_i32 s58, s58, s4\n\ts_sub_i32 s4, s58, s51\n\ts_mov_b32 s51, s58\n\ts_add_i32 s58, s58, s48\n\t" \
  "v_mov_b32 v200, s58\n\tv_mov_b32 v201, s4\n\tds_write_b64 v188, v[200:201] offset:12296\n\ts_waitcnt lgkmcnt(0)\n\t"
#define R3_WARM(beg, len) \
  "s_getpc_b64 vcc\n\t9:\n\ts_add_u32 vcc_lo, vcc_lo, " beg "-9b\n\ts_addc_u32 vcc_hi, vcc_hi, 0\n\tv_lshlrev_b32 v200, 3, %[rd]\n\tv_min_u32 v200, " len "-64, v200\n\t" \
  "global_load_dword v201, v200, vcc\n\tglobal_load_dword v202, v200, vcc offset:64\n\ts_waitcnt vmcnt(0)\n\t"
#define R3_LOOP(WARM, DMA, TWWAIT, CNTLOAD, CNTPAY, CNTPACK, TWTIME, NW, CNTSIG, NS, TWFWD, FIN, CNTCHK, OUTOFLINE) \
  "s_mov_b64 s[60:61], %[wsp]\n\ts_mov_b32 s62, 0\n\ts_mov_b32 s63, 0\n\ts_mov_b32 s64, 0x10000\n\ts_mov_b32 s78, 0\n\t" \
  "s_mov_b32 s76, 0x00ff00ff\n\ts_mov_b32 s77, s76\n\t" \
  "s_and_b32 s46, %[p0], 0xff\n\ts_bfe_u32 s47, %[p0], 0x80008\n\ts_lshr_b32 s48, %[p0], 16\n\ts_mov_b32 s65, 0\n\ts_mov_b32 s66, 0\n\t" \
  "s_movk_i32 s49, 250\n\ts_lshl_b32 s50, %[p1], 10\n\t" \
  "v_mov_b32 v188, 0\n\tv_mov_b32 v160, 0x3f803f80\n\tv_mov_b32 v161, v160\n\tv_mov_b32 v162, v160\n\tv_mov_b32 v163, v160\n\t" \
  "v_mov_b32 v166, 0\n\tv_mov_b32 v167, 0\n\tv_mov_b32 v170, 0\n\tv_mov_b32 v171, 0\n\tv_mov_b32 v172, 0x1f800000\n\tv_mov_b32 v173, 0x71800000\n\t" \
  R3_PREP \
  WARM \
  "10:\n\t" \
  TWWAIT \
  "s_mov_b32 m0, s50\n\t" \
  "s_waitcnt lgkmcnt(0)\n\ts_barrier\n\t" \
  "s_memtime s[72:73]\n\t" \
  DMA \
  "ds_read_b32 v200, v188 offset:12288\n\t" \
  "s_waitcnt lgkmcnt(0)\n\t" \
  "v_readfirstlane_b32 s74, v200\n\t" \
  "s_cmp_eq_u32 s74, s63\n\ts_cbranch_scc1 50f\n\t" \
  "s_add_i32 s63, s63, 1\n\t" \
  "v_mov_b32 v228, 0\n\tv_mov_b32 v229, 0\n\tv_mov_b32 v230, 0\n\tv_mov_b32 v231, 0\n\t" \
  "v_mov_b32 v232, 0\n\tv_mov_b32 v233, 0\n\tv_mov_b32 v234, 0\n\tv_mov_b32 v235, 0\n\t" \
  ULOADS \
  CNTLOAD TWTIME \
  "s_waitcnt vmcnt(" #NW ")\n\t" \
  "s_barrier\n\t" ST(80) \
  CNTPAY "ds_read_b32 v244, v188 offset:12296\n\t" \
  R3_Y_A \
  "s_cbranch_vccnz 60f\n\t" \
  "14:\n\t" \
  CNTPACK "s_add_i32 s69, s68, s49\n\t" \
  "s_cmp_eq_u32 s62, 63\n\ts_cbranch_scc1 98f\n\t" \
  R3_POLL R3_Y_B \
  TWFWD \
  "15:\n\t" \
  R3_S_PHASE(CNTSIG, NS) FIN CNTCHK \
  "s_add_i32 s62, s62, 1\n\t" \
  R3_STEP("v_add_u32", "v_add_u32") \
  R3_FLIP DIAG_R3 \
  "s_branch 10b\n\t" \
  R3_S_RARE \
    \
  "50:\n\t" \
  "s_sub_i32 s64, s64, 1\n\ts_add_i32 s63, s63, 1\n\ts_bitset1_b32 s78, 28\n\t" \
  R3_FLIP R3_STEP("v_subrev_u32", "v_subrev_u32") \
  "s_sub_i32 s62, s62, 1\n\t" \
  "s_memtime vcc\n\ts_waitcnt lgkmcnt(0)\n\t" \
  "s_sub_i32 s70, s69, s68\n\ts_cmp_eq_u32 s70, s49\n\ts_cselect_b32 s70, 256, 0\n\ts_sub_i32 s69, s69, s70\n\t"       \
  "53:\n\ts_add_i32 s68, s68, 93*26\n\ts_add_i32 s69, s69, 93*26\n\ts_add_i32 s65, s65, 26\n\t" \
  "s_sub_i32 s70, s69, vcc_lo\n\ts_cmp_lt_i32 s70, 0\n\ts_cbranch_scc1 53b\n\t" \
  R3_PREP R3_POLL2 \
  "s_branch 15b\n\t" \
    \
  "60:\n\ts_bitset1_b32 s78, 29\n\ts_sub_i32 s64, s64, 1\n\ts_cmp_gt_i32 s64, 0\n\ts_cbranch_scc0 14b\n\t" \
  "s_add_i32 s65, s65, 26\n\t" R3_PREP "s_branch 10b\n\t" \
  OUTOFLINE \
  "98:\n\t" R3_Y_B \
  "global_store_dwordx2 %[yo], v[164:165], s[60:61] sc1\n\t" \
  "s_waitcnt vmcnt(0)\n\ts_barrier\n\t"
#define R3_FIN \
  "ds_read_b128 v[200:203], %[redr]\n\tds_read_b128 v[204:207], %[redr] offset:512\n\t" \
  "ds_read_b128 v[208:211], %[redr] offset:1024\n\tds_read_b128 v[212:215], %[redr] offset:1536\n\t" \
  "s_waitcnt lgkmcnt(2)\n\t" \
  "v_pk_add_f32 v[200:201], v[200:201], v[204:205]\n\tv_pk_add_f32 v[202:203], v[202:203], v[206:207]\n\t" \
  "s_waitcnt lgkmcnt(1)\n\t" \
  "v_pk_add_f32 v[200:201], v[200:201], v[208:209]\n\tv_pk_add_f32 v[202:203], v[202:203], v[210:211]\n\t" \
  "s_waitcnt lgkmcnt(0)\n\t" \
  "v_pk_add_f32 v[200:201], v[200:201], v[212:213]\n\tv_pk_add_f32 v[202:203], v[202:203], v[214:215]\n\t" \
  "s_nop 1\n\t" \
  "v_permlane32_swap_b32 v200, v202\n\tv_permlane32_swap_b32 v201, v203\n\t" \
  "v_add_f32 v200, v200, v202\n\tv_add_f32 v201, v201, v203\n\t" \
  "v_cvt_pk_bf16_f32 v202, v200, v201\n\t" \
  "v_cmpx_o_f32 vcc, v200, v201\n\t" \
  "global_store_dword %[sto], v202, s[60:61] sc1\n\t" ST(94) \
  "s_memtime s[74:75]\n\t" \
  "s_mov_b64 exec, -1\n\t" \
  "v_mov_b32 v200, -1\n\tv_mov_b32 v201, -1\n\tv_mov_b32 v202, -1\n\tv_mov_b32 v203, -1\n\t" \
  "ds_write_b128 %[redr], v[200:203]\n\tds_write_b128 %[redr], v[200:203] offset:512\n\t" \
  "ds_write_b128 %[redr], v[200:203] offset:1024\n\tds_write_b128 %[redr], v[200:203] offset:1536\n\t" \
  "s_waitcnt lgkmcnt(0)\n\ts_sub_u32 s70, s74, s69\n\tv_mov_b32 v200, s70\n\tds_write_b32 v188, v200 offset:12308\n\t" \
  "s_cmp_eq_u64 vcc, -1\n\ts_cbranch_scc0 80f\n\t" \
  "16:\n\t"
#define R3_FIN_OOL \
  "80:\n\ts_cmp_gt_i32 s64, 0\n\ts_cbranch_scc0 81f\n\t" \
  "v_mov_b32 v200, s63\n\tds_write_b32 v188, v200 offset:12288\n\ts_branch 16b\n\t" \
  "81:\n\tv_mov_b32 v200, 0x7fc07fc0\n\tglobal_store_dword %[sto], v200, s[60:61] sc1\n\ts_branch 16b\n\t"
#define R3_CNT_PAY "ds_read_b32 v174, v188 offset:12300\n\tds_read_b32 v175, v188 offset:12308\n\t"
#define R3_CNT_PACK \
  "v_min_u32 v175, 0xfff, v175\n\tv_min_u32 v174, 0xffff, v174\n\tv_lshl_or_b32 v176, v174, 12, v175\n\tv_or_b32 v176, s78, v176\n\ts_mov_b32 s78, 0\n\t"
#define R3_CNT_SIG "global_store_dword v187, v176, s[60:61] sc1\n\t"
#define R3_CNT_LOAD "v_mov_b32 v187, s43\n\ts_mov_b64 s[38:39], s[40:41]\n\tv_cndmask_b32 v144, %[uo], v186, s[40:41]\n\tglobal_load_dword v145, v144, s[60:61] sc1\n\t"
#define R3_CNT_CHK "v_readfirstlane_b32 s43, %[tm]\n\ts_and_b32 s43, s43, 0xffffe000\n\ts_xor_b32 s43, s43, s42\n\t" \
  "v_cndmask_b32 v189, v189, v145, s[38:39]\n\ts_mov_b64 s[38:39], 0\n\t" \
  "s_add_i32 s70, s62, 3\n\ts_lshr_b32 s70, s70, 1\n\tv_cmp_gt_u32 vcc, s70, v189\n\ts_cbranch_vccnz 85f\n\t17:\n\t" \
  "s_add_i32 s70, s70, 1\n\ts_min_u32 s70, s70, 32\n\tv_cmp_gt_u32 vcc, s70, v189\n\ts_cmp_lg_u64 vcc, 0\n\ts_cselect_b64 s[40:41], -1, 0\n\t"
#define R3_CNT_OOL        \
  "85:\n\ts_mov_b32 s71, 0x40000\n\t" \
  "86:\n\tglobal_load_dword v189, v186, s[60:61] sc1\n\ts_waitcnt vmcnt(0)\n\t" \
  "v_cmp_gt_u32 vcc, s70, v189\n\ts_cbranch_vccz 17b\n\ts_sleep 2\n\ts_sub_i32 s71, s71, 1\n\ts_cmp_gt_i32 s71, 0\n\ts_cbranch_scc1 86b\n\ts_branch 17b\n\t"
#define DIAG_R3
#define DIAG_R3_END
#define DIAG_R3_OUT
#define R3_OUTS [moff] "+v"(moff), [to] "+v"(to), [tm] "+v"(tm), [sto] "+v"(sto), [uo] "+v"(uo), [yo] "+v"(yo) DIAG_R3_OUT
#define R3_INS R1_WOPS, R1_WOPS2, [idx] "v"(idx), [rd] "v"(rd), [redw] "v"(redw), [selA] "v"(selA), [wsp] "s"(ws), [p0] "s"(p0), [p1] "s"(p1)
#define R3_CLOB CLOB_V200_247, "v144","v145","v146","v147","v148","v149","v150","v151", "v160","v161","v162","v163","v164","v165","v166","v167","v168","v169","v170","v171","v172","v173","v174","v175","v176","v177","v178", "v179","v180","v181","v182","v183","s96","s97","v184","v185","v186","v187","v188","v189","v190","v191","v192","v193","v194","v195","v196","v197","v198","v199", \
  "s38","s39","s40","s41","s42","s43","s44","s45","s46","s47","s48","s49","s50","s51","s60","s61","s62","s63","s64","s65","s66","s67","s68","s69","s70","s71", \
  "s72","s73","s74","s75","s76","s77","s78","vcc","scc","memory" ST_CLOB
#define STR2(x) #x
#define STR(x) STR2(x)

template <int KIND>
DEV void r3_body(unsigned char* __restrict__ ws, int v, int wave, int lane, unsigned sel TUNE_ARG) {
  const int g = lane >> 4, hs = (lane >> 3) & 1, i8 = lane & 7;
  const int idx = (lane & 8) ? 0xEEEE : 0x4444;
  const int dw = wave < 7 ? wave : 6;
  const unsigned rd = lane * 16;
  u8v w0[7], w1[7];
  {
    const unsigned char* p = ws + O_WB + (size_t)(2 * wave * 28 + v) * 14336 + lane * 16;
#pragma unroll
    for (int k = 0; k < 7; ++k) {
      w0[k] = cat8(*(const u4v*)(p + k * 2048), *(const u4v*)(p + k * 2048 + 1024));
      w1[k] = cat8(*(const u4v*)(p + 28 * 14336 + k * 2048), *(const u4v*)(p + 28 * 14336 + k * 2048 + 1024));
    }
#pragma unroll
    for (int k = 0; k < 7; ++k) asm volatile("" : "+v"(w0[k]), "+v"(w1[k]));
  }
  RTS(1);
  if (KIND == 3 && lane == 0) lds_w4(L_FLAG, SENT);
  if (KIND == 2) wait_cnt(ws, doneU(ws, 4 * (v >> 1) + (lane & 3)), 1);
  *(LDS u2v*)(size_t)(L_RED + wave * 512 + lane * 8) = (u2v){SENT, SENT};
  RTS(2);
  unsigned moff = hoff(sel, 0, 0) + dw * 1024 + g * 256 + i8 * 32 + hs * 16;
  unsigned to = hoff(sel, 0, 2) + (2 * wave + hs) * 512 + (g >> 1) * 256 + i8 * 32 + (g & 1) * 16;
  unsigned tm = hoff(sel, 64, K_SMK) + (2 * wave + hs) * 512 + g * 128 + i8 * 16;
  u4v selA = {0u, 0u, 0u, 0u};
  if (((lane & 3) >> 1) == 0) {
    const int gq = (lane >> 2) & 3, j = 4 * (gq & 1) + (gq >> 1) + 2 * (lane & 1);
    const unsigned one = (j & 1) ? 0x3f800000u : 0x00003f80u;
    selA = (u4v){(j >> 1) == 0 ? one : 0u, (j >> 1) == 1 ? one : 0u, (j >> 1) == 2 ? one : 0u, (j >> 1) == 3 ? one : 0u};
  }
  unsigned sto = hoff(sel, 1, 0) + v * 256 + i8 * 32 + ((lane >> 3) & 3) * 8 + (lane >> 5) * 4;
  unsigned uo = (unsigned)O_U + v * U_RUN + 2 * wave * 1024 + lane * 16;
  unsigned yo = (unsigned)O_YG + v * Y_RUN + wave * 512 + lane * 8;
  const unsigned redw = L_RED + hs * 4096 + wave * 512 + (g * 8 + i8) * 16;
  const unsigned redr = L_RED + (lane >> 5) * 2048 + (lane & 31) * 16;
  const unsigned p1 = dw;
  {
    const unsigned o = lane < 8 ? yo : lane < 16 ? yo + 63 * 4096 : lane < 24 ? uo : lane < 32 ? uo + 63 * 16384 : lane < 40 ? hoff(sel, 70, 0) : lane < 48 ? hoff(sel, 1, 0) + 7168 : (unsigned)O_FL + 0x3ff00;
    (void)ld_cnt((const unsigned*)(ws + (o & ~3u)));
  }
  int anchor = 0;
  unsigned p0 = 0;
  if (KIND == 3) asm volatile(R3_WARM(".Lr3tw_beg", ".Lr3tw_end-.Lr3tw_beg") :: [rd] "v"(rd) : "v200", "v201", "v202", "vcc", "scc", "memory");
  if (KIND == 3) { wait_cnt(ws, ptp(ws, 0), 1); p0 = __builtin_amdgcn_readfirstlane(ld_cnt(ptp(ws, 0))); }
  bar();
  if (KIND == 3) anchor = get_anchor(ws, sel, 64 + v, lane);
  if (KIND == 0) {
    asm volatile(R3_LOOP(R3_WARM("10f", "98f-10f"), R3_DMA, "", "", "", "", "", 2, "", 1, "", R3_FIN, "", R3_FIN_OOL) DIAG_R3_END : R3_OUTS : R3_INS, [redr] "v"(redr) : R3_CLOB);
  } else if (KIND == 1) {
    asm volatile(R3_LOOP(R3_WARM("10f", "98f-10f"), R3_DMA, "", "", "", "", "", 2, "", 1, "", "", "", "") DIAG_R3_END : R3_OUTS : R3_INS : R3_CLOB);
  } else if (KIND == 2) {
    const unsigned cc = ((unsigned)(3 - K_SMK) << 13) | (unsigned)(v * 128);
    const unsigned uc = (unsigned)O_FL + (C_W + 2 * (56 + 4 * (v >> 1) + (lane & 3))) * 64;
    asm volatile("s_mov_b64 s[40:41], -1\n\ts_mov_b32 s42, %[cc]\n\ts_and_b32 s43, s42, 0x1fff\n\ts_add_i32 s43, s43, 0x43e000\n\tv_mov_b32 v186, %[uc]\n\t"
                 R3_LOOP(R3_WARM("10f", "98f-10f"), R3_DMA, "", R3_CNT_LOAD, R3_CNT_PAY, R3_CNT_PACK, "", 3, R3_CNT_SIG, 2, "", "", R3_CNT_CHK, R3_CNT_OOL) DIAG_R3_END : R3_OUTS : R3_INS, [cc] "s"(cc), [uc] "v"(uc) : R3_CLOB);
  } else {
    asm volatile("s_mov_b32 s67, %[anc]\n\t" R3_LOOP(".Lr3tw_beg:\n\t", "", R3_TW_WAIT, R3_TW_PT, "", "", R3_TW_TIME, 3, "", 1, R3_FWD R3_PREP, "", R3_TW_NEXT, R3_TW_LATE R3_TW_OOL ".Lr3tw_end:\n\t") DIAG_R3_END : R3_OUTS : R3_INS, [anc] "s"(anchor) : R3_CLOB, TBURST_CLOB);
  }
  if (KIND == 2 && lane < 2) st4c_asm(flagY(ws, sel, NSEG - 2 + lane, v), 0u);
  RTS(3);
}
DEV void role_r3(unsigned char* __restrict__ ws, int v, int wave, int lane, unsigned sel TUNE_ARG) {
  if (wave == 0) r3_body<0>(ws, v, wave, lane, sel TUNE_PASS);
  else if (wave == 6) r3_body<2>(ws, v, wave, lane, sel TUNE_PASS);
  else if (wave == 7) r3_body<3>(ws, v, wave, lane, sel TUNE_PASS);
  else r3_body<1>(ws, v, wave, lane, sel TUNE_PASS);
}

#define PF_LAG 4
DEV void role_pf(unsigned char* __restrict__ ws, int k, int wave, int lane, unsigned sel) {
  constexpr int LAG = PF_LAG;
  const rsrc_t ru = mkrsrc(ws + O_U, 30u << 20);
  unsigned have = 0;
  for (int t = 1; t < NSEG - 1; ++t) {
    if (wave == 0) {
      unsigned skip = 0;
      const unsigned need = (t >> 1) + 1;
      if (__any(have < need)) { wait_cnt(ws, doneU(ws, min(lane, 55)), need); have = ld_cnt(doneU(ws, min(lane, 55))); }
      if (t >= LAG) wait_flags(ws, flagY(ws, sel, t - LAG, k));
      else if (t >= LAG - 2) {
        wait_cnt(ws, anchorp(ws, 28 + k), 1);
        const unsigned at = (__builtin_amdgcn_readfirstlane(ld_cnt(anchorp(ws, 28 + k))) & ~1u) + (t - LAG + 2) * 264;
        skip = (int)(((unsigned)rt32() << 1) - at) > 40;
        for (int it = 0; it < 4096 && (int)(at - ((unsigned)rt32() << 1)) > 0; ++it) __builtin_amdgcn_s_sleep(1);
      }
      lds_w4((t & 1) * 4, skip);
    }
    bar();
    if (__builtin_amdgcn_readfirstlane(lds_r4((t & 1) * 4))) continue;
    u4v r[14];
#pragma unroll
    for (int i = 0; i < 14; ++i) { const int n = (k * 8 + wave) * 14 + i; r[i] = ldc16(ru, (n >> 4) * U_RUN + (t * 16 + (n & 15)) * 1024 + lane * 16); }
#pragma unroll
    for (int i = 0; i < 14; ++i) asm volatile("" :: "v"(r[i]));
  }
}

__global__ void __launch_bounds__(512, 2)
main_kernel(const unsigned short* __restrict__ x, const unsigned short* __restrict__ b1, const unsigned short* __restrict__ b2,
            unsigned short* __restrict__ out, unsigned char* __restrict__ ws TUNE_ARG) {
  const int b = blockIdx.x, lane = threadIdx.x & 63;
  const int wave = __builtin_amdgcn_readfirstlane(threadIdx.x >> 6);
  RTS(0);
  const int xc = b & 7, slot = b >> 3;
  if (xc == 0 || xc == 2) {
    const unsigned sel = *(const unsigned*)(ws + O_SEL);
    role_r1(ws, (xc >> 1) * 32 + slot, wave, lane, sel TUNE_PASS);
  } else if (xc == 1) {
    const unsigned sel = *(const unsigned*)(ws + O_SEL);
    if (slot < 28) role_r3(ws, slot, wave, lane, sel TUNE_PASS);
    else role_pf(ws, slot - 28, wave, lane, sel);
  } else {
    const int k = slot * 5 + (xc - 3);
    if (k < 56) role_w1<true>(ws, k, wave, lane);
    else if (k < 112) role_w1<false>(ws, k - 56, wave, lane);
    else {
      if (k < 144) role_ffn(ws, x, b1, b2, k - 112, wave, lane);
      else role_stt(ws, (k - 144) >> 1, k & 1, wave, lane);
      role_w3(ws, out, (159 - k) >> 2, k & 3, wave, lane);
    }
  }
}

extern "C" void kernel_launch(void* const* d_in, const int* in_sizes, int n_in, void* d_out, int out_size, void* d_ws,
                              size_t ws_size, hipStream_t stream) {
  unsigned char* ws = (unsigned char*)(((uintptr_t)d_ws + 2 * MB - 1) & ~(uintptr_t)(2 * MB - 1));
  const unsigned short* x = (const unsigned short*)d_in[0];
  constexpr int LDS_MAIN = 132 * 1024;
  (void)hipFuncSetAttribute((const void*)main_kernel, hipFuncAttributeMaxDynamicSharedMemorySize, LDS_MAIN);
  constexpr int LDS_PREP = 152 * 1024;
  (void)hipFuncSetAttribute((const void*)prep_kernel, hipFuncAttributeMaxDynamicSharedMemorySize, LDS_PREP);
  prep_kernel<<<256, 512, LDS_PREP, stream>>>(x, (const unsigned short*)d_in[1], (const unsigned short*)d_in[2], (const unsigned short*)d_in[3],
                                           (const unsigned short*)d_in[4], (const unsigned short*)d_in[5], (const unsigned short*)d_in[8],
                                           (const unsigned short*)d_in[10], ws);
  main_kernel<<<256, 512, LDS_MAIN, stream>>>(x, (const unsigned short*)d_in[9], (const unsigned short*)d_in[11], (unsigned short*)d_out, ws TUNE_PASS);
}
```

```cpp
#include <hip/hip_runtime.h>
#include <hip/hip_bf16.h>
#include <stdint.h>
#include <stdio.h>

typedef __bf16 bf2 __attribute__((ext_vector_type(2)));
typedef __bf16 bf8 __attribute__((ext_vector_type(8)));
typedef __bf16 bf16x __attribute__((ext_vector_type(16)));
typedef short s4v __attribute__((ext_vector_type(4)));
typedef float f2v __attribute__((ext_vector_type(2)));
typedef float f4v __attribute__((ext_vector_type(4)));
typedef float f16v __attribute__((ext_vector_type(16)));
typedef unsigned u2v __attribute__((ext_vector_type(2)));
typedef unsigned u4v __attribute__((ext_vector_type(4)));
typedef unsigned u8v __attribute__((ext_vector_type(8)));
#define LDS __attribute__((address_space(3)))
#define GLB __attribute__((address_space(1)))
#define DEV __device__ __forceinline__

constexpr int DM = 448, NH = 16, DH = 256, NSEG = 64, FF = 1792;
constexpr unsigned SENT = 0xFFFFFFFFu;

constexpr size_t MB = 1u << 20;
constexpr size_t O_HB = 0;
constexpr size_t O_FL = 4 * MB;
constexpr size_t O_SEL = O_FL + 256 * 1024;
constexpr size_t O_XN = 5 * MB;
constexpr size_t O_WA = 7 * MB;
constexpr size_t O_WB = 14 * MB;
constexpr size_t O_W1 = 21 * MB;
constexpr size_t O_W2 = 23 * MB;
constexpr size_t O_G = 25 * MB;
constexpr size_t O_U = 55 * MB;
constexpr size_t O_ST = 85 * MB;
constexpr size_t O_SM = 87 * MB;
constexpr size_t O_YG = 100 * MB;
constexpr size_t O_F = 111 * MB;
constexpr unsigned G_RUN = 64 * 14336 + 3 * 4096, U_RUN = 64 * 16384 + 5 * 4096, Y_RUN = 64 * 4096 + 4096;
constexpr size_t O_END = 115 * MB;

DEV unsigned pk2(float a, float b) { f2v v = {a, b}; return __builtin_bit_cast(unsigned, __builtin_convertvector(v, bf2)); }
DEV float bflo(unsigned u) { return __builtin_bit_cast(float, u << 16); }
DEV float bfhi(unsigned u) { return __builtin_bit_cast(float, u & 0xffff0000u); }
DEV float rbf(float a) { return bflo(pk2(a, 0.f) & 0xffffu); }
DEV bf8 asbf8(u4v v) { return __builtin_bit_cast(bf8, v); }
DEV u4v lds_r16(unsigned a) { return *(LDS u4v*)(size_t)a; }
DEV void lds_w16(unsigned a, u4v v) { *(LDS u4v*)(size_t)a = v; }
DEV u2v lds_tr(unsigned a) { return __builtin_bit_cast(u2v, __builtin_amdgcn_ds_read_tr16_b64_v4i16((LDS s4v*)(size_t)a)); }
DEV unsigned rdtime() { unsigned long long t; asm volatile("s_memtime %0\n\ts_waitcnt lgkmcnt(0)" : "=s"(t) :: "memory"); return (unsigned)t; }
DEV int swap13(int x) { return (x & 5) | ((x >> 2) & 2) | ((x << 2) & 8); }
DEV void dma16(const void* g, unsigned ldsaddr) {
  unsigned keep;
  asm volatile("s_mov_b32 %0, m0\n\ts_mov_b32 m0, %2\n\ts_nop 0\n\tglobal_load_lds_dwordx4 %1, off\n\ts_mov_b32 m0, %0" : "=&s"(keep) : "v"(g), "s"(ldsaddr) : "memory");
}
DEV void dma16c(const void* g, unsigned ldsaddr) {
  unsigned keep;
  asm volatile("s_mov_b32 %0, m0\n\ts_mov_b32 m0, %2\n\ts_nop 0\n\tglobal_load_lds_dwordx4 %1, off sc1\n\ts_mov_b32 m0, %0" : "=&s"(keep) : "v"(g), "s"(ldsaddr) : "memory");
}
DEV void vm0() { asm volatile("s_waitcnt vmcnt(0)" ::: "memory"); }
DEV void bar() { asm volatile("s_waitcnt lgkmcnt(0)\n\ts_barrier" ::: "memory"); }

DEV void stwt16(void* p, u4v v) { asm volatile("global_store_dwordx4 %0, %1, off sc1\n\ts_nop 1" :: "v"(p), "v"(v) : "memory"); }
DEV float wavesum(float v) {
  v += __builtin_bit_cast(float, __builtin_amdgcn_update_dpp(0, __builtin_bit_cast(int, v), 0xb1, 0xf, 0xf, true));
  v += __builtin_bit_cast(float, __builtin_amdgcn_update_dpp(0, __builtin_bit_cast(int, v), 0x4e, 0xf, 0xf, true));
  v += __builtin_bit_cast(float, __builtin_amdgcn_update_dpp(0, __builtin_bit_cast(int, v), 0x124, 0xf, 0xf, true));
  v += __builtin_bit_cast(float, __builtin_amdgcn_update_dpp(0, __builtin_bit_cast(int, v), 0x128, 0xf, 0xf, true));
  const unsigned u = __builtin_bit_cast(unsigned, v);
  const u2v a = __builtin_amdgcn_permlane16_swap(u, u, false, false);
  const unsigned ax = a.x, ay = a.y;
  const unsigned w = __builtin_bit_cast(unsigned, __builtin_bit_cast(float, ax) + __builtin_bit_cast(float, ay));
  const u2v c = __builtin_amdgcn_permlane32_swap(w, w, false, false);
  const unsigned cx = c.x, cy = c.y;
  return __builtin_bit_cast(float, cx) + __builtin_bit_cast(float, cy);
}
#define K0S(k)
DEV void ln_row(u4v xv, unsigned char* __restrict__ ws, int row, int lane) {
  float e[8];
  if (lane >= 56) xv = (u4v){0u, 0u, 0u, 0u};
#pragma unroll
  for (int j = 0; j < 4; ++j) { e[2 * j] = bflo(xv[j]); e[2 * j + 1] = bfhi(xv[j]); }
  float s = 0.f;
#pragma unroll
  for (int j = 0; j < 8; ++j) s += e[j];
  const float mean = wavesum(s) * (1.f / DM);
  float q = 0.f;
  if (lane < 56) {
#pragma unroll
    for (int j = 0; j < 8; ++j) { const float d = e[j] - mean; q += d * d; }
  }
  const float mb = rbf(mean), vb = rbf(wavesum(q) * (1.f / DM));
  const float den = rbf(__builtin_sqrtf(rbf(vb + 1e-5f)));
  unsigned o4[4];
#pragma unroll
  for (int j = 0; j < 4; ++j) o4[j] = pk2(rbf(e[2 * j] - mb) / den, rbf(e[2 * j + 1] - mb) / den);
  const bool odd = lane & 1;
  const unsigned r0 = __builtin_amdgcn_update_dpp(0u, odd ? o4[0] : o4[2], 0xb1, 0xf, 0xf, true);
  const unsigned r1 = __builtin_amdgcn_update_dpp(0u, odd ? o4[1] : o4[3], 0xb1, 0xf, 0xf, true);
  u4v p;
  if (odd) p = (u4v){r0, o4[2], r1, o4[3]}; else p = (u4v){o4[0], r0, o4[1], r1};
  if (lane < 56) stwt16(ws + O_XN + (size_t)(row >> 5) * 28672 + (lane >> 1) * 1024 + (lane & 1) * 512 + (row & 31) * 16, p);
}
DEV u4v srd(const void* p, unsigned bytes) { const unsigned long long a = (unsigned long long)p; return (u4v){(unsigned)a, (unsigned)(a >> 32), bytes, 0x00020000u}; }
DEV void bdma16(unsigned voff, u4v r, unsigned soff, unsigned ldsaddr) {
  asm volatile("s_mov_b32 m0, %3\n\ts_nop 0\n\tbuffer_load_dwordx4 %0, %1, %2 offen lds" :: "v"(voff), "s"(r), "s"(soff), "s"(ldsaddr) : "memory");
}
constexpr unsigned rowoff(int j) { return (16 * ((j >> 2) & 1) + 8 * (j & 1) + 4 * ((j >> 1) & 1) + 2 * (j >> 3)) * 8192u; }

template <bool ISA>
DEV void prep_gemm(const unsigned short* __restrict__ Rw, const unsigned short* __restrict__ Cw, const unsigned short* __restrict__ x,
                   unsigned char* __restrict__ ws, int b, int h, int cg, int wave, int lane) {
  const int g = lane >> 4, c16 = lane & 15, hl = lane >> 5, sl = lane & 31;
  const int row = b * 8 + wave;
  const unsigned xl = 147456 + wave * 1024;
  dma16(x + (size_t)row * DM + min(lane, 55) * 8, xl);
  if (ISA) {
    const u4v rc = srd(Cw + (size_t)64 * cg * 4096 + h * 256, 64 * 8192);
    const unsigned v0 = hl * 8192 + ((sl ^ hl ^ ((wave & 1) << 3)) << 4), s0 = ((wave >> 1) * 16 + (wave & 1) * 2) * 8192;
#pragma unroll
    for (int j = 0; j < 4; ++j) bdma16(v0 ^ (j << 5), rc, s0 + ((j & 1) * 8 + (j >> 1) * 4) * 8192, (wave * 4 + j) * 1024);
  } else {
    const u4v rc = srd(Cw + (size_t)h * 256 * DM + 64 * cg, 256 * 896);
    const unsigned v0 = (lane >> 3) * 896 + (((lane & 7) ^ (((lane >> 4) & 1) << 1)) << 4);
#pragma unroll
    for (int j = 0; j < 4; ++j) bdma16(v0 ^ ((j & 1) << 6), rc, (wave * 4 + j) * 8 * 896, (wave * 4 + j) * 1024);
  }
  if (wave == 7) {
    __builtin_amdgcn_sched_barrier(0);
    asm volatile("s_waitcnt vmcnt(0)\n\ts_barrier" ::: "memory");
    const u4v ones = {SENT, SENT, SENT, SENT}, zero = {0u, 0u, 0u, 0u};
#pragma unroll
    for (int j = 0; j < 16; ++j) if (b >= 2) stwt16(ws + O_HB + (size_t)b * 16384 + j * 1024 + lane * 16, ones);
    stwt16(ws + O_FL + b * 1024 + lane * 16, zero);
    ln_row(lds_r16(xl + lane * 16), ws, row, lane);
    return;
  }
  const float sc = ISA ? 0.0625f * 1.4426950408889634f : 1.f;
  unsigned char* dst = ws + (ISA ? O_WA : O_WB) + (size_t)(h * 28 + 4 * cg) * 14336 + 2 * wave * 1024 + lane * 16;
  const unsigned reg = 32768 + wave * 16384;
  const int rot = cg * 2;
  const unsigned srot = (16 * ((rot >> 2) & 1) + 8 * (rot & 1) + 4 * ((rot >> 1) & 1) + 2 * (rot >> 3)) * 8192u, mrot = reg | (rot << 10);
  const unsigned vr = hl * 8192 + (((sl ^ hl) << 4) ^ ((rot & 7) << 5));
  const u4v rr = srd(Rw + (size_t)(64 * wave) * 4096 + h * 256, 64 * 8192);
  const __amdgpu_buffer_rsrc_t rq = __builtin_amdgcn_make_buffer_rsrc((void*)(Rw + (size_t)(64 * wave + 32) * 4096 + h * 256), 0, 32 * 8192, 0x00020000);
  u4v nx[16];
#pragma unroll
  for (int j = 0; j < 2; ++j) bdma16(vr ^ ((j & 7) << 5), rr, srot ^ rowoff(j), mrot ^ (j << 10));
#pragma unroll
  for (int j = 0; j < 2; ++j) nx[j] = __builtin_amdgcn_raw_buffer_load_b128(rq, vr ^ ((j & 7) << 5), srot ^ rowoff(j), 0);
  __builtin_amdgcn_sched_barrier(0);
  asm volatile("s_waitcnt vmcnt(8)" ::: "memory");
  ln_row(lds_r16(xl + lane * 16), ws, row, lane);
  __builtin_amdgcn_sched_barrier(0);
  K0S(1);
  asm volatile("s_waitcnt vmcnt(5)\n\ts_barrier" ::: "memory");
  K0S(0);
#pragma unroll
  for (int j = 2; j < 16; ++j) bdma16(vr ^ ((j & 7) << 5), rr, srot ^ rowoff(j), mrot ^ (j << 10));
#pragma unroll
  for (int j = 2; j < 16; ++j) nx[j] = __builtin_amdgcn_raw_buffer_load_b128(rq, vr ^ ((j & 7) << 5), srot ^ rowoff(j), 0);
  __builtin_amdgcn_sched_barrier(0);
  asm volatile("s_waitcnt vmcnt(14)" ::: "memory");
  K0S(2);
  const int q = c16 >> 2, p = c16 & 3;
  const int xr = (q >> 1) | ((g & 1) << 1);
  auto bfrag = [&](int es, int ti) -> u4v {
    if (ISA) return lds_r16((ti * 16 + c16) * 512 + (((es * 4 + g) ^ c16) << 4));
    const unsigned ad = (32 * es + 8 * g + q) * 128 + ((ti ^ xr) << 5) + p * 8;
    const u2v lo = lds_tr(ad), hi = lds_tr(ad + 512);
    return (u4v){lo.x, lo.y, hi.x, hi.y};
  };
  auto afrag = [&](int es, int t) -> u4v { return lds_r16(reg + (t * 16 + c16) * 512 + (((es * 4 + g) ^ c16) << 4)); };
#pragma unroll
  for (int rb = 0; rb < 2; ++rb) {
    u4v bb[2][4], aa[2][2];
#pragma unroll
    for (int ti = 0; ti < 4; ++ti) bb[0][ti] = bfrag(0, ti);
    aa[0][0] = afrag(0, 0); aa[0][1] = afrag(0, 1);
    f4v acc[2][4];
#pragma unroll
    for (int t = 0; t < 2; ++t)
#pragma unroll
      for (int ti = 0; ti < 4; ++ti) acc[t][ti] = (f4v){0.f, 0.f, 0.f, 0.f};
#pragma unroll
    for (int es = 0; es < 8; ++es) {
      if (es < 7) {
        aa[(es + 1) & 1][0] = afrag(es + 1, 0); aa[(es + 1) & 1][1] = afrag(es + 1, 1);
#pragma unroll
        for (int ti = 0; ti < 4; ++ti) bb[(es + 1) & 1][ti] = bfrag(es + 1, ti);
      }
#pragma unroll
      for (int ti = 0; ti < 4; ++ti)
#pragma unroll
        for (int t = 0; t < 2; ++t)
          acc[t][ti] = __builtin_amdgcn_mfma_f32_16x16x32_bf16(asbf8(aa[es & 1][t]), asbf8(bb[es & 1][ti]), acc[t][ti], 0, 0, 0);
      __builtin_amdgcn_sched_barrier(0);
    }
    if (rb == 0) {
#pragma unroll
      for (int j = 0; j < 16; ++j) lds_w16((mrot ^ (j << 10)) + lane * 16, nx[j]);
    }
#pragma unroll
    for (int ti = 0; ti < 4; ++ti) {
      const u4v o = {pk2(acc[0][ti][0] * sc, acc[0][ti][1] * sc), pk2(acc[0][ti][2] * sc, acc[0][ti][3] * sc),
                     pk2(acc[1][ti][0] * sc, acc[1][ti][1] * sc), pk2(acc[1][ti][2] * sc, acc[1][ti][3] * sc)};
      stwt16(dst + (size_t)ti * 14336 + rb * 1024, o);
    }
  }
  K0S(3);
}

__global__ void __launch_bounds__(512, 2)
prep_kernel(const unsigned short* __restrict__ x, const unsigned short* __restrict__ mem0,
            const unsigned short* __restrict__ Wq, const unsigned short* __restrict__ Wk,
            const unsigned short* __restrict__ Wv, const unsigned short* __restrict__ Wo,
            const unsigned short* __restrict__ w1, const unsigned short* __restrict__ w2,
            unsigned char* __restrict__ ws) {
  const int b = blockIdx.x, tid = threadIdx.x, lane = tid & 63;
  const int wave = __builtin_amdgcn_readfirstlane(tid >> 6);
  const int xc = b & 7, slot = b >> 3;
  if (slot < 28) {
    const int m = xc * 4 + slot / 7, cg = slot % 7;
    if (m < 16) prep_gemm<true>(Wk, Wq, x, ws, b, m, cg, wave, lane);
    else prep_gemm<false>(Wv, Wo, x, ws, b, m - 16, cg, wave, lane);
  } else {
    const int row = b * 8 + wave;
    u4v xv = {0u, 0u, 0u, 0u};
    const int misc = (slot - 28) * 8 + xc;
    const int job = (misc - 1) * 8 + wave;
    const unsigned reg = wave * 14336;
    if (misc == 0) {
      if (wave == 7) {
        u4v m0[7];
#pragma unroll
        for (int j = 0; j < 7; ++j) { const int n = j * 64 + lane; m0[j] = *(const u4v*)(mem0 + ((n >> 1) & 7) * DM + (n >> 4) * 16 + (n & 1) * 8); }
        const u4v ones = {SENT, SENT, SENT, SENT};
        stwt16(ws + O_HB + (lane >> 4) * 8192 + (lane & 15) * 64, ones);
        stwt16(ws + O_HB + 2 * MB + (lane >> 4) * 8192 + (lane & 15) * 64, ones);
#pragma unroll
        for (int j = 0; j < 7; ++j) asm volatile("" : "+v"(m0[j]));
        vm0();
        auto timed = [&](const unsigned char* pc) -> unsigned {
          unsigned v;
          const unsigned c0 = rdtime();
          asm volatile("global_load_dword %0, %1, %2 sc1\n\ts_waitcnt vmcnt(0)" : "=&v"(v) : "v"(0u), "s"(pc) : "memory");
          return rdtime() - c0;
        };
        unsigned best[6];
#pragma unroll
        for (int c = 0; c < 6; ++c) best[c] = 0x7fffffff;
#pragma unroll 1
        for (int it = 0; it < 3; ++it) {
#pragma unroll
          for (int c = 0; c < 4; ++c) best[c] = min(best[c], timed(ws + O_HB + c * 8192 + it * 256));
        }
        unsigned sel = 0, bv = best[0];
#pragma unroll
        for (unsigned k = 1; k < 4; ++k) if (best[k] < bv) { bv = best[k]; sel = k; }
#pragma unroll 1
        for (int it = 0; it < 2; ++it) {
#pragma unroll
          for (int c = 0; c < 2; ++c) best[4 + c] = min(best[4 + c], timed(ws + O_HB + 2 * MB + (sel ^ c) * 8192 + it * 256));
        }
        sel |= (sel ^ (best[5] <= best[4] ? 1u : 0u)) << 2;
        if (lane == 0) *(unsigned*)(ws + O_SEL) = sel;
        if (lane == 0) printf("probe %u %u %u %u | %u %u\n", best[0], best[1], best[2], best[3], best[4], best[5]);
#pragma unroll
        for (int j = 0; j < 7; ++j) stwt16(ws + O_HB + (sel & 3u) * 8192 + j * 1024 + lane * 16, m0[j]);
#pragma unroll
        for (int j = 8; j < 32; ++j) stwt16(ws + O_HB + (((sel & 3u) * 8192) ^ (j * 1024)) + lane * 16, ones);
      }
    } else if (job < 224) {
      const bool first = job < 112;
      const int step = first ? job >> 2 : job - 112;
      const unsigned short* src = first ? w1 + (size_t)step * 16 * FF + (job & 3) * 448 : w2 + (size_t)step * 16 * DM;
      const int ld = first ? FF : DM;
#pragma unroll
      for (int j = 0; j < 14; ++j) { const int n = j * 64 + lane, r = n / 56; dma16(src + (size_t)r * ld + (n - r * 56) * 8, reg + j * 1024); }
    }
    if (lane < 56) xv = *(const u4v*)(x + (size_t)row * DM + lane * 8);
    asm volatile("" : "+v"(xv));
    vm0();
    if (misc > 0 && job < 224) {
      const bool first = job < 112;
      const int hi = lane >> 5, q = (lane & 15) >> 2;
      const int r0 = first ? 4 * hi + 8 * (q >> 1) + (q & 1) : 4 * hi + q, dr = first ? 2 : 8;
      const unsigned ad = reg + r0 * 896 + ((lane >> 4) & 1) * 32 + (lane & 3) * 8;
      unsigned char* dst = ws + (first ? O_W1 + (size_t)((job & 3) * 14 * 28 + (job >> 2)) * 1024 : O_W2 + (size_t)(job - 112) * 1024) + lane * 16;
      const size_t dstep = first ? 28672 : 114688;
#pragma unroll
      for (int t = 0; t < 14; ++t) {
        const u2v lo = lds_tr(ad + t * 64), hv = lds_tr(ad + t * 64 + dr * 896);
        stwt16(dst + t * dstep, (u4v){lo.x, lo.y, hv.x, hv.y});
      }
    }
    const u4v ones = {SENT, SENT, SENT, SENT}, zero = {0u, 0u, 0u, 0u};
    stwt16(ws + O_HB + (size_t)b * 16384 + tid * 16, ones);
    stwt16(ws + O_HB + (size_t)b * 16384 + 8192 + tid * 16, ones);
    if (wave == 0) stwt16(ws + O_FL + b * 1024 + lane * 16, zero);
    ln_row(xv, ws, row, lane);
  }
}
constexpr int C_W = 0  , C_F = 1856, C_ABORT = 1900, C_ANCHOR = 1920;
typedef __amdgpu_buffer_rsrc_t rsrc_t;
DEV rsrc_t mkrsrc(const void* p, unsigned bytes) { return __builtin_amdgcn_make_buffer_rsrc((void*)p, 0, bytes, 0x00020000); }
DEV u4v ldc16(rsrc_t r, unsigned off) { return __builtin_amdgcn_raw_buffer_load_b128(r, off, 0, 16); }
DEV u2v ldc8(rsrc_t r, unsigned off) { return __builtin_amdgcn_raw_buffer_load_b64(r, off, 0, 16); }
DEV void stc16(rsrc_t r, unsigned off, u4v v) { __builtin_amdgcn_raw_buffer_store_b128(v, r, off, 0, 16); }
DEV void stc4(rsrc_t r, unsigned off, unsigned v) { __builtin_amdgcn_raw_buffer_store_b32(v, r, off, 0, 16); }
DEV unsigned* cntp(unsigned char* ws, int idx) { return (unsigned*)(ws + O_FL) + idx * 16; }
DEV unsigned ld_cnt(const unsigned* p) { unsigned v; asm volatile("global_load_dword %0, %1, off sc1\n\ts_waitcnt vmcnt(0)" : "=&v"(v) : "v"(p) : "memory"); return v; }
DEV void add_cnt(unsigned* p, int lane) {
  if (lane == 0) { const unsigned one = 1; asm volatile("global_atomic_add %0, %1, off sc1" :: "v"(p), "v"(one) : "memory"); }
}
DEV void post(unsigned* p, unsigned v, int lane) { if (lane == 0) asm volatile("global_store_dword %0, %1, off sc1" :: "v"(p), "v"(v) : "memory"); }
DEV unsigned* doneG(unsigned char* ws, int j) { return cntp(ws, C_W + 2 * j); }
DEV unsigned* doneU(unsigned char* ws, int j) { return cntp(ws, C_W + 2 * (56 + j)); }
constexpr int SPIN_CAP = 1 << 17;
DEV void wait_cnt(unsigned char* ws, const unsigned* p, unsigned target, int nap = 6) {
  for (int it = 0;; ++it) {
    const unsigned v = ld_cnt(p);
    if (__all(v >= target)) return;
    if ((it & 63) == 63) {
      if (it >= SPIN_CAP) { const unsigned one = 1; asm volatile("global_store_dword %0, %1, off sc1" :: "v"(cntp(ws, C_ABORT)), "v"(one) : "memory"); return; }
      if (__any(ld_cnt(cntp(ws, C_ABORT)) != 0)) return;
    }
    if (nap > 6) __builtin_amdgcn_s_sleep(32); else __builtin_amdgcn_s_sleep(2);
  }
}
DEV unsigned hoff(unsigned sel, int gi, int kind) {
  const unsigned k = ((sel >> (2 * (gi >> 6))) & 3u) ^ ((((unsigned)gi >> 4) & 1u) << 1) ^ (unsigned)kind;
  return (unsigned)O_HB + (unsigned)gi * 32768u + k * 8192u;
}
DEV unsigned char* hblk(unsigned char* ws, unsigned sel, int gi, int kind) { return ws + hoff(sel, gi, kind); }
DEV unsigned char* flagS(unsigned char* ws, unsigned sel, int s, int id) { return hblk(ws, sel, s, 3) + id * 128; }
DEV unsigned char* flagY(unsigned char* ws, unsigned sel, int s, int v) { return hblk(ws, sel, 64 + s, 3) + v * 128; }
DEV unsigned char* flagT(unsigned char* ws, unsigned sel, int s, int h) { return hblk(ws, sel, 64 + s, 1) + h * 128; }
DEV void wait_flags(unsigned char* ws, const unsigned char* p) {
  for (int it = 0;; ++it) {
    if (!__any(ld_cnt((const unsigned*)p) == SENT)) return;
    if ((it & 63) == 63) {
      if (it >= SPIN_CAP) { const unsigned one = 1; asm volatile("global_store_dword %0, %1, off sc1" :: "v"(cntp(ws, C_ABORT)), "v"(one) : "memory"); return; }
      if (__any(ld_cnt(cntp(ws, C_ABORT)) != 0)) return;
    }
    __builtin_amdgcn_s_sleep(6);
  }
}
DEV f16v mfma32(u4v a, u4v b, f16v c) { return __builtin_amdgcn_mfma_f32_32x32x16_bf16(asbf8(a), asbf8(b), c, 0, 0, 0); }
DEV f4v mfma16(u4v a, u4v b, f4v c) { return __builtin_amdgcn_mfma_f32_16x16x32_bf16(asbf8(a), asbf8(b), c, 0, 0, 0); }
DEV void st16c_asm(void* p, u4v v) { asm volatile("global_store_dwordx4 %0, %1, off sc1\n\ts_nop 1" :: "v"(p), "v"(v) : "memory"); }

#define RTS(k)
template <bool ISG>
DEV void role_w1(unsigned char* __restrict__ ws, int j, int wave, int lane) {
  const int ct = wave & 3, rh = wave >> 2, hi = lane >> 5, c32 = lane & 31;
  int h, tile0;
  if (ISG) { const int hp = j / 7, ctile = (j % 7) * 4 + ct; h = 2 * hp + ctile / 14; tile0 = 2 * (ctile % 14); }
  else { const int V = j >> 2; h = 4 * (j & 3) + ct; tile0 = 2 * V; }
  const unsigned char* xn = ws + O_XN + wave * 7168 + lane * 16;
#pragma unroll
  for (int i = 0; i < 7; ++i) dma16(xn + i * 1024, wave * 7168 + i * 1024);
  u4v w[28];
  {
    const unsigned char* p = ws + (ISG ? O_WA : O_WB) + (size_t)(h * 28 + tile0 + (c32 >> 4)) * 14336 + hi * 256 + (c32 & 15) * 16;
#pragma unroll
    for (int s = 0; s < 28; ++s) w[s] = *(const u4v*)(p + (s >> 1) * 1024 + (s & 1) * 512);
  }
  constexpr int SEGB = ISG ? 14336 : 16384;
  unsigned char* out = (ISG ? ws + O_G + (size_t)(h * 2 + (c32 >> 4)) * G_RUN + (tile0 >> 1) * 1024
                            : ws + O_U + (size_t)(tile0 + (c32 >> 4)) * U_RUN + h * 1024) + rh * SEGB + hi * 256 + (c32 & 15) * 16;
  const int tok = ISG ? c32 : ((c32 & 19) | ((c32 >> 1) & 4) | ((c32 << 1) & 8));
  const unsigned rd = rh * 28672 + hi * 512 + tok * 16;
  unsigned* done = ISG ? doneG(ws, j) : doneU(ws, j);
#pragma unroll
  for (int s = 0; s < 28; ++s) asm volatile("" : "+v"(w[s]));
  vm0();
  RTS(1);
  for (int p = 0; p < 32; ++p) {
    asm volatile("s_waitcnt vmcnt(2)\n\ts_barrier" ::: "memory");
    if (wave == 7 && p >= 6) post(done, p - 1, lane);
    const unsigned buf = (p & 1) * 57344;
    const unsigned char* src = xn + (size_t)(p < 31 ? p + 1 : p) * 57344;
    const unsigned dstl = (57344 - buf) + wave * 7168;
    f16v acc;
#pragma unroll
    for (int i = 0; i < 16; ++i) acc[i] = 0.f;
    const LDS u4v* xp = (const LDS u4v*)(size_t)(buf + rd);
    u4v xr[5];
#pragma unroll
    for (int s = 0; s < 4; ++s) xr[s] = xp[s * 64];
#pragma unroll
    for (int s = 0; s < 28; ++s) {
      if (s + 4 < 28) xr[(s + 4) % 5] = xp[(s + 4) * 64];
      acc = ISG ? mfma32(w[s], xr[s % 5], acc) : mfma32(xr[s % 5], w[s], acc);
      __builtin_amdgcn_sched_barrier(0);
      if (s % 3 == 0 && s / 3 < 7) { dma16(src + (s / 3) * 1024, dstl + (s / 3) * 1024); __builtin_amdgcn_sched_barrier(0); }
      if (s == 16) {
        asm volatile("s_waitcnt vmcnt(6)" ::: "memory");
        __builtin_amdgcn_sched_barrier(0);
      }
    }
    const u4v o0 = {pk2(acc[0], acc[1]), pk2(acc[2], acc[3]), pk2(acc[4], acc[5]), pk2(acc[6], acc[7])};
    const u4v o1 = {pk2(acc[8], acc[9]), pk2(acc[10], acc[11]), pk2(acc[12], acc[13]), pk2(acc[14], acc[15])};
    unsigned char* o = out + p * 2 * SEGB;
    st16c_asm(o, o0); st16c_asm(o + 512, o1);
    if (p < 4) {
      RTS(4);
      asm volatile("s_waitcnt vmcnt(0)\n\ts_barrier" ::: "memory");
      if (wave == 7) post(done, p + 1, lane);
      RTS(2);
    }
  }
  asm volatile("s_waitcnt vmcnt(0)\n\ts_barrier" ::: "memory");
  if (wave == 7) post(done, 32, lane);
  RTS(3);
}

template <bool P2>
DEV void ffn_body(unsigned char* __restrict__ ws, const unsigned short* __restrict__ x, const unsigned short* __restrict__ b1,
                  const unsigned short* __restrict__ b2, int job, int wave, int lane) {
  const int hi = lane >> 5, c32 = lane & 31;
  constexpr int NL = P2 ? 60 : 28, RING = P2 ? 12 : 14, DIST = RING - 1;
  const u4v* w1p = (const u4v*)(ws + O_W1 + (size_t)wave * 28672) + lane;
  const u4v* w2p = (const u4v*)(ws + O_W2 + (size_t)(2 * wave) * 114688) + lane;
  auto wload = [&](int c, int i) -> u4v {
    if (i < 28) return w1p[(size_t)c * 14336 + i * 64];
    return w2p[(size_t)c * 1024 + ((i - 28) >> 1) * 64 + ((i - 28) & 1) * 7168];
  };
  u4v wr[RING];
#pragma unroll
  for (int i = 0; i < DIST; ++i) wr[i] = wload(0, i);
  {
    const unsigned char* xn = ws + O_XN + (size_t)job * 57344 + wave * 7168 + lane * 16;
#pragma unroll
    for (int i = 0; i < 7; ++i) dma16(xn + i * 1024, wave * 7168 + i * 1024);
  }
  f16v o[2][2];
#pragma unroll
  for (int a = 0; a < 2; ++a)
#pragma unroll
    for (int bq = 0; bq < 2; ++bq)
#pragma unroll
      for (int i = 0; i < 16; ++i) o[a][bq][i] = 0.f;
  vm0();
  bar();
  const LDS u4v* xp = (const LDS u4v*)(size_t)(lane * 16);
  for (int c = 0; c < 7; ++c) {
    const int cn = c < 6 ? c + 1 : 6;
    const unsigned hbuf = 57344 + (c & 1) * 32768;
    u2v bv[4];
#pragma unroll
    for (int q = 0; q < 4; ++q) bv[q] = *(const u2v*)(b1 + 32 * (8 * c + wave) + 4 * hi + 8 * q);
    f16v a0, a1;
#pragma unroll
    for (int i = 0; i < 16; ++i) { a0[i] = 0.f; a1[i] = 0.f; }
    u4v xa[3], xb[3];
#pragma unroll
    for (int s = 0; s < 2; ++s) { xa[s] = xp[s * 64]; xb[s] = xp[1792 + s * 64]; }
#pragma unroll
    for (int s = 0; s < 28; ++s) {
      wr[(s + DIST) % RING] = (s + DIST < NL) ? wload(c, s + DIST) : wload(cn, s + DIST - NL);
      if (s + 2 < 28) { xa[(s + 2) % 3] = xp[(s + 2) * 64]; xb[(s + 2) % 3] = xp[1792 + (s + 2) * 64]; }
      a0 = mfma32(wr[s % RING], xa[s % 3], a0);
      a1 = mfma32(wr[s % RING], xb[s % 3], a1);
      __builtin_amdgcn_sched_barrier(0);
    }
#pragma unroll
    for (int i = 0; i < 16; ++i) {
      const unsigned bw = bv[i >> 2][(i >> 1) & 1];
      const float bs = (i & 1) ? bfhi(bw) : bflo(bw);
      const float t0 = fmaxf(a0[i] + bs, 0.f), t1 = fmaxf(a1[i] + bs, 0.f);
      a0[i] = t0 * t0; a1[i] = t1 * t1;
    }
#pragma unroll
    for (int bq = 0; bq < 2; ++bq) {
      lds_w16(hbuf + ((wave * 2 + bq) * 2 + 0) * 1024 + lane * 16,
              (u4v){pk2(a0[8 * bq], a0[8 * bq + 1]), pk2(a0[8 * bq + 2], a0[8 * bq + 3]), pk2(a0[8 * bq + 4], a0[8 * bq + 5]), pk2(a0[8 * bq + 6], a0[8 * bq + 7])});
      lds_w16(hbuf + ((wave * 2 + bq) * 2 + 1) * 1024 + lane * 16,
              (u4v){pk2(a1[8 * bq], a1[8 * bq + 1]), pk2(a1[8 * bq + 2], a1[8 * bq + 3]), pk2(a1[8 * bq + 4], a1[8 * bq + 5]), pk2(a1[8 * bq + 6], a1[8 * bq + 7])});
    }
    bar();
    if (P2) {
      const LDS u4v* hp = (const LDS u4v*)(size_t)(hbuf + lane * 16);
      u4v h0[2], h1[2];
      h0[0] = hp[0]; h1[0] = hp[64];
#pragma unroll
      for (int s = 0; s < 16; ++s) {
        const int i = 28 + 2 * s;
        wr[(i + DIST) % RING] = (i + DIST < NL) ? wload(c, i + DIST) : wload(cn, i + DIST - NL);
        if (s + 1 < 16) { h0[(s + 1) & 1] = hp[(s + 1) * 128]; h1[(s + 1) & 1] = hp[(s + 1) * 128 + 64]; }
        o[0][0] = mfma32(wr[i % RING], h0[s & 1], o[0][0]); o[0][1] = mfma32(wr[i % RING], h1[s & 1], o[0][1]);
        __builtin_amdgcn_sched_barrier(0);
        wr[(i + 1 + DIST) % RING] = (i + 1 + DIST < NL) ? wload(c, i + 1 + DIST) : wload(cn, i + 1 + DIST - NL);
        o[1][0] = mfma32(wr[(i + 1) % RING], h0[s & 1], o[1][0]); o[1][1] = mfma32(wr[(i + 1) % RING], h1[s & 1], o[1][1]);
        __builtin_amdgcn_sched_barrier(0);
      }
    }
  }
  if (P2) {
    const rsrc_t rf = mkrsrc(ws + O_F, 4u << 20);
#pragma unroll
    for (int a = 0; a < 2; ++a)
#pragma unroll
      for (int sg = 0; sg < 2; ++sg)
#pragma unroll
        for (int Q = 0; Q < 4; ++Q) {
          const int n = 32 * (2 * wave + a) + 8 * Q + 4 * hi, seg = 2 * job + sg;
          const u2v xv = *(const u2v*)(x + (size_t)(seg * 32 + c32) * DM + n), bv = *(const u2v*)(b2 + n);
          const f4v r = {o[a][sg][4 * Q] + bflo(bv.x) + bflo(xv.x), o[a][sg][4 * Q + 1] + bfhi(bv.x) + bfhi(xv.x),
                         o[a][sg][4 * Q + 2] + bflo(bv.y) + bflo(xv.y), o[a][sg][4 * Q + 3] + bfhi(bv.y) + bfhi(xv.y)};
          const int v = 2 * (2 * wave + a) + (Q >> 1), g = 2 * (Q & 1) + hi;
          stc16(rf, ((seg * 28 + v) * 2 + (c32 >> 4)) * 1024 + (g * 16 + (c32 & 15)) * 16, __builtin_bit_cast(u4v, r));
        }
    vm0();
    add_cnt(cntp(ws, C_F + job), lane);
    RTS(3);
  }
}
DEV void role_ffn(unsigned char* __restrict__ ws, const unsigned short* __restrict__ x, const unsigned short* __restrict__ b1,
                  const unsigned short* __restrict__ b2, int job, int wave, int lane) {
  if (wave < 7) ffn_body<true>(ws, x, b1, b2, job, wave, lane);
  else ffn_body<false>(ws, x, b1, b2, job, wave, lane);
}

DEV float asf(unsigned u) { return __builtin_bit_cast(float, u); }
DEV unsigned asu(float f) { return __builtin_bit_cast(unsigned, f); }
DEV float xmax16_32(float v) {
  const u2v a = __builtin_amdgcn_permlane16_swap(asu(v), asu(v), false, false);
  v = fmaxf(asf(a.x), asf(a.y));
  const u2v c = __builtin_amdgcn_permlane32_swap(asu(v), asu(v), false, false);
  return fmaxf(asf(c.x), asf(c.y));
}
DEV float xsum16_32(float v) {
  const u2v a = __builtin_amdgcn_permlane16_swap(asu(v), asu(v), false, false);
  v = asf(a.x) + asf(a.y);
  const u2v c = __builtin_amdgcn_permlane32_swap(asu(v), asu(v), false, false);
  return asf(c.x) + asf(c.y);
}
DEV float halfsum(float a, float b) {
  const u2v s = __builtin_amdgcn_permlane32_swap(asu(a), asu(b), false, false);
  return asf(s.x) + asf(s.y);
}

DEV void role_stt(unsigned char* __restrict__ ws, int grp, int hg, int wave, int lane) {
  const unsigned sel = *(const unsigned*)(ws + O_SEL);
  const int g = lane >> 4, c16 = lane & 15, h = 8 * hg + wave;
  const rsrc_t rg = mkrsrc(ws + O_G, 30u << 20);
  const LDS u4v* xp = (const LDS u4v*)(size_t)((g >> 1) * 1024 + (g & 1) * 512 + c16 * 16);
  for (int s = grp; s < NSEG; s += 8) {
    if (wave == 7) wait_cnt(ws, doneG(ws, 28 * hg + min(lane, 27)), (s >> 1) + 1, 32);
    bar();
    if (wave < 7) {
#pragma unroll
      for (int i = 0; i < 4; ++i) dma16(ws + O_XN + (size_t)s * 28672 + (wave * 4 + i) * 1024 + lane * 16, (wave * 4 + i) * 1024);
    }
    u4v ga[28];
#pragma unroll
    for (int T = 0; T < 28; ++T) ga[T] = ldc16(rg, (h * 2 + T / 14) * G_RUN + s * 14336 + (T % 14) * 1024 + lane * 16);
    __builtin_amdgcn_sched_barrier(0);
    asm volatile("s_waitcnt vmcnt(28)\n\ts_barrier" ::: "memory");
    f4v acc[2][2];
#pragma unroll
    for (int q = 0; q < 2; ++q) { acc[0][q] = (f4v){0.f, 0.f, 0.f, 0.f}; acc[1][q] = acc[0][q]; }
#pragma unroll
    for (int T = 0; T < 14; ++T) {
      const u4v x0 = xp[T * 128], x1 = xp[T * 128 + 16];
      acc[0][0] = mfma16(ga[T], x0, acc[0][0]); acc[0][1] = mfma16(ga[T], x1, acc[0][1]);
      acc[1][0] = mfma16(ga[14 + T], x0, acc[1][0]); acc[1][1] = mfma16(ga[14 + T], x1, acc[1][1]);
    }
    unsigned char* o = ws + O_ST + (size_t)(s * 16 + h) * 2048 + c16 * 32 + g * 8;
#pragma unroll
    for (int q = 0; q < 2; ++q)
#pragma unroll
      for (int t = 0; t < 2; ++t) {
        const u2v val = {pk2(acc[t][q][0], acc[t][q][1]), pk2(acc[t][q][2], acc[t][q][3])};
        asm volatile("global_store_dwordx2 %0, %1, off sc1" :: "v"(o + q * 1024 + t * 512), "v"(val) : "memory");
      }
    vm0();
    if (lane == 0) { const unsigned zero = 0; asm volatile("global_store_dword %0, %1, off sc1" :: "v"(flagT(ws, sel, s, h)), "v"(zero) : "memory"); }
  }
  RTS(3);
}

constexpr int W3_GROUPS = 12;
DEV void role_w3(unsigned char* __restrict__ ws, unsigned short* __restrict__ out, int grp, int C, int wave, int lane) {
  const unsigned sel = *(const unsigned*)(ws + O_SEL);
  const int g = lane >> 4, c16 = lane & 15, v = 7 * C + (wave < 7 ? wave : 6);
  const rsrc_t ru = mkrsrc(ws + O_U, 30u << 20), rf = mkrsrc(ws + O_F, 4u << 20);
  constexpr unsigned L_P = 0, L_PM = 32768, L_ST = 40960, L_SM = L_ST + 32768, L_Y = L_SM + 32768;
  for (int s = grp; s < NSEG; s += W3_GROUPS) {
    if (wave == 7) {
      wait_cnt(ws, doneU(ws, 4 * ((7 * C) >> 1) + (lane & 15)), (s >> 1) + 1);
      wait_cnt(ws, cntp(ws, C_F + (s >> 1)), 7);
      wait_flags(ws, flagT(ws, sel, s, lane & 15));
    }
    bar();
    u4v uf[16], ff[2];
    if (wave < 7) {
#pragma unroll
      for (int hh = 0; hh < 16; ++hh) uf[hh] = ldc16(ru, v * U_RUN + (s * 16 + hh) * 1024 + lane * 16);
#pragma unroll
      for (int q = 0; q < 2; ++q) ff[q] = ldc16(rf, ((s * 28 + v) * 2 + q) * 1024 + lane * 16);
    } else {
      wait_flags(ws, flagY(ws, sel, s, 7 * C + min(lane, 6)));
      wait_flags(ws, flagS(ws, sel, s, lane));
    }
    __builtin_amdgcn_sched_barrier(0);
    bar();
#pragma unroll
    for (int it = 0; it < 4; ++it) {
      dma16c(ws + O_ST + (size_t)(s * 32 + wave * 4 + it) * 1024 + lane * 16, L_ST + (wave * 4 + it) * 1024);
      dma16c(ws + O_SM + (size_t)(s * 32 + wave * 4 + it) * 1024 + lane * 16, L_SM + (wave * 4 + it) * 1024);
    }
    if (wave < 7) {
#pragma unroll
      for (int H = 0; H < 4; ++H) dma16c(ws + O_YG + (size_t)v * Y_RUN + (s * 4 + H) * 1024 + lane * 16, L_Y + (wave * 4 + H) * 1024);
      asm volatile("s_waitcnt vmcnt(4)" ::: "memory");
    } else vm0();
#pragma unroll
    for (int it = 0; it < 4; ++it) {
      const int q = it & 1, hh = 2 * wave + (it >> 1), tq = 16 * q + c16;
      const u4v st = lds_r16(L_ST + (wave * 4 + it) * 1024 + (g >> 1) * 512 + c16 * 32 + (g & 1) * 16);
      const LDS unsigned* sm = (const LDS unsigned*)(size_t)(L_SM + (wave * 4 + it) * 1024 + lane * 4);
      const unsigned s0 = sm[0], s1 = sm[64], s2 = sm[128], s3 = sm[192];
      float e[8];
      float m0 = (bflo(s0) + bflo(s1)) + (bflo(s2) + bflo(s3));
      float m1 = (bfhi(s0) + bfhi(s1)) + (bfhi(s2) + bfhi(s3));
#pragma unroll
      for (int i = 0; i < 8; ++i) { const unsigned w = st[i >> 1]; e[i] = (8 * g + i <= tq) ? ((i & 1) ? bfhi(w) : bflo(w)) : -__builtin_inff(); }
      float mx = fmaxf(m0, m1);
#pragma unroll
      for (int i = 0; i < 8; ++i) mx = fmaxf(mx, e[i]);
      mx = xmax16_32(mx);
      m0 = __builtin_amdgcn_exp2f(m0 - mx); m1 = __builtin_amdgcn_exp2f(m1 - mx);
      float sum = m0 + m1;
#pragma unroll
      for (int i = 0; i < 8; ++i) { e[i] = __builtin_amdgcn_exp2f(e[i] - mx); sum += e[i]; }
      const float r = __builtin_amdgcn_rcpf(xsum16_32(sum));
      lds_w16(L_P + (q * 16 + hh) * 1024 + lane * 16,
              (u4v){pk2(e[0] * r, e[1] * r), pk2(e[2] * r, e[3] * r), pk2(e[4] * r, e[5] * r), pk2(e[6] * r, e[7] * r)});
      *(LDS unsigned*)(size_t)(L_PM + (q * 4 + (hh >> 2)) * 1024 + lane * 16 + (hh & 3) * 4) = pk2(m0 * r, m1 * r);
    }
    vm0();
    bar();
    if (wave < 7) {
      u4v yf[4];
#pragma unroll
      for (int H = 0; H < 4; ++H) {
        const u2v lo = *(const LDS u2v*)(size_t)(L_Y + (wave * 4 + H) * 1024 + lane * 8), hi = *(const LDS u2v*)(size_t)(L_Y + (wave * 4 + H) * 1024 + 512 + lane * 8);
        yf[H] = (u4v){lo.x, lo.y, hi.x, hi.y};
      }
      f4v a0 = __builtin_bit_cast(f4v, ff[0]), a1 = __builtin_bit_cast(f4v, ff[1]);
      const LDS u4v* pp = (const LDS u4v*)(size_t)(L_P + lane * 16);
      u4v p0[4], p1[4];
#pragma unroll
      for (int k = 0; k < 3; ++k) { p0[k] = pp[k * 64]; p1[k] = pp[(16 + k) * 64]; }
#pragma unroll
      for (int k = 0; k < 20; ++k) {
        const int n = k + 3;
        if (n < 16) { p0[n & 3] = pp[n * 64]; p1[n & 3] = pp[(16 + n) * 64]; }
        else if (n < 20) { p0[n & 3] = pp[(32 + n - 16) * 64]; p1[n & 3] = pp[(36 + n - 16) * 64]; }
        const u4v w = k < 16 ? uf[k] : yf[k - 16];
        a0 = mfma16(w, p0[k & 3], a0); a1 = mfma16(w, p1[k & 3], a1);
        __builtin_amdgcn_sched_barrier(0);
      }
      *(u2v*)(out + (size_t)(s * 32 + c16) * DM + v * 16 + g * 4) = (u2v){pk2(a0[0], a0[1]), pk2(a0[2], a0[3])};
      *(u2v*)(out + (size_t)(s * 32 + 16 + c16) * DM + v * 16 + g * 4) = (u2v){pk2(a1[0], a1[1]), pk2(a1[2], a1[3])};
    }
  }
  RTS(4);
}
#define TUNE_ARG
#define TUNE_PASS
#define K_PERIOD 760
#define K_DS 1560
#define K_MS 43
#define K_MM 50
#define K_LEAD 96
#define K_FIXED 0
#define K_WARM 0
constexpr int K_SMK = 2, K_DDE = 250;
constexpr int C_PT = 2048;
DEV unsigned* ptp(unsigned char* ws, int s) { return cntp(ws, C_PT + 2 * s); }
DEV unsigned ptword(int period, int ds) { return (unsigned)(period / 24) | ((unsigned)(period % 24) << 8) | ((unsigned)(ds - K_DDE - 80) << 16); }
DEV unsigned char* flagR(unsigned char* ws, unsigned sel, int id) { return hblk(ws, sel, 64 + (id >> 6), 0) + (id & 63) * 128; }
#define DBG(slot, v)
#define ST(n)
#define ARR(s_)
#define EV(code)
#define EV_OUT(base)
#define ST_CLOB
#define ST_OUT(base, ref)
constexpr int RET_E = 26;
constexpr int TRY_CAP = 1 << 16;

DEV int rt32() { unsigned long long t; asm volatile("s_memrealtime %0\n\ts_waitcnt lgkmcnt(0)" : "=s"(t) :: "memory"); return (int)(unsigned)t; }
struct Lat { int anchor, e, f; };
DEV void lat_fwd(Lat& L, int pe, int pf) { L.f += pf; const int c = L.f >= 24; L.f -= c ? 24 : 0; L.e += pe + c; }
DEV void lat_back(Lat& L, int pe, int pf) { L.f -= pf; const int c = L.f < 0; L.f += c ? 24 : 0; L.e -= pe + c; }
DEV void lat_prep(const Lat& L, int& tgt, int& jmp) { tgt = L.anchor + 4 * L.e; jmp = 108 - 4 * L.f; }
#define TPAIR(r, m) "s_memrealtime s[" #r ":" #r "+1]\n\ts_memtime s[" #m ":" #m "+1]\n\ts_nop 0\n\t"
#define TSEL(r, m) "s_cmp_lg_u32 s" #r ", s4\n\ts_cselect_b32 s58, s" #m ", s58\n\t"
#define TBURST \
  TPAIR(4, 6) TPAIR(8, 10) TPAIR(12, 14) TPAIR(16, 18) TPAIR(20, 22) TPAIR(24, 26) TPAIR(28, 30) TPAIR(52, 54) TPAIR(56, 58) \
  "s_waitcnt lgkmcnt(0)\n\t" TSEL(52, 54) TSEL(28, 30) TSEL(24, 26) TSEL(20, 22) TSEL(16, 18) TSEL(12, 14) TSEL(8, 10)
#define TBURST_CLOB "s4","s5","s6","s7","s8","s9","s10","s11","s12","s13","s14","s15","s16","s17","s18","s19","s20","s21","s22","s23", \
  "s24","s25","s26","s27","s28","s29","s30","s31","s52","s53","s54","s55","s56","s57","s58","s59"
DEV void lat_commit(Lat& L, int tgt, int jmp) {
  if (tgt == (int)0x80000000) return;
  int r = tgt - rt32();
  while (__builtin_expect(r < 0, 0)) { L.e += RET_E; tgt += 4 * RET_E; r += 4 * RET_E; }
  if (r > 0) {
    int d;
#define RT_STEP "s_memrealtime vcc\n\ts_waitcnt lgkmcnt(0)\n\ts_sub_i32 %0, %1, vcc_lo\n\ts_cmp_lt_i32 %0, 1\n\t"
    asm volatile("1:\n\t" RT_STEP "s_cbranch_scc1 2f\n\t" RT_STEP "s_cbranch_scc1 2f\n\t" RT_STEP "s_cbranch_scc1 2f\n\t"
                 RT_STEP "s_cbranch_scc0 1b\n\t"
                 "2:\n\t"
                 "s_getpc_b64 vcc\n\ts_add_u32 vcc_lo, vcc_lo, %2\n\ts_addc_u32 vcc_hi, vcc_hi, 0\n\ts_setpc_b64 vcc\n\t"
                 ".rept 24\n\ts_nop 0\n\t.endr"
                 : "=&s"(d) : "s"(tgt), "s"(jmp) : "scc", "vcc", "memory");
  }
}
DEV unsigned lat_wait(Lat& L) {
  int tgt, jmp;
  unsigned est;
  lat_prep(L, tgt, jmp);
  if (tgt - rt32() < 16) { lat_commit(L, tgt, jmp); return 0; }
  asm volatile("1:\n\ts_memrealtime vcc\n\ts_waitcnt lgkmcnt(0)\n\ts_sub_i32 s4, %1, vcc_lo\n\ts_cmp_gt_i32 s4, 16\n\ts_cbranch_scc1 1b\n\t"
               TBURST
               "s_sub_i32 s4, %1, s4\n\ts_mul_i32 s4, s4, 93\n\ts_lshr_b32 s4, s4, 2\n\t"
               "s_add_i32 s58, s58, s4\n\ts_add_i32 s58, s58, %2\n\ts_or_b32 %0, s58, 1\n\t"
               "s_memtime vcc\n\ts_waitcnt lgkmcnt(0)\n\t"
               "s_sub_i32 s4, s58, vcc_lo\n\ts_max_i32 s4, s4, 0\n\ts_min_i32 s4, s4, 512\n\ts_and_b32 s4, s4, -4\n\ts_sub_i32 s4, 524, s4\n\t"
               "s_getpc_b64 vcc\n\ts_add_u32 vcc_lo, vcc_lo, s4\n\ts_addc_u32 vcc_hi, vcc_hi, 0\n\ts_setpc_b64 vcc\n\t"
               ".rept 128\n\ts_nop 0\n\t.endr"
               : "=&s"(est) : "s"(tgt), "s"(4 * L.f - 93) : TBURST_CLOB, "scc", "vcc", "memory");
  return est;
}
DEV unsigned* anchorp(unsigned char* ws, int id) { return cntp(ws, C_ANCHOR + 2 * (id & 31)); }
DEV int get_anchor(unsigned char* ws, unsigned sel, int id, int lane) {
  post((unsigned*)flagR(ws, sel, id), 0u, lane);
  unsigned v = 0;
  for (int it = 0; it < SPIN_CAP && v == 0; ++it) v = __builtin_amdgcn_readfirstlane(ld_cnt(anchorp(ws, id)));
  const int n = rt32();
  const int d = (int)((v & ~1u) - ((unsigned)n << 1)) >> 1;
  return (d > 4096 || d < -(1 << 22)) ? n : n + d;
}
DEV f4v smf(u4v a, u8v b, f4v c, int idx) {
  return __builtin_amdgcn_smfmac_f32_16x16x64_bf16(asbf8(a), __builtin_bit_cast(bf16x, b), c, idx, 0, 0);
}
DEV unsigned umax4(u4v v) { return max(max(v.x, v.y), max(v.z, v.w)); }
DEV float dpp_ror8(float v) { return __builtin_bit_cast(float, __builtin_amdgcn_update_dpp(0, __builtin_bit_cast(int, v), 0x128, 0xf, 0xf, true)); }
DEV float dpp_xor1(float v) { return __builtin_bit_cast(float, __builtin_amdgcn_update_dpp(0, __builtin_bit_cast(int, v), 0xb1, 0xf, 0xf, true)); }
DEV void st4c_asm(void* p, unsigned v) { asm volatile("global_store_dword %0, %1, off sc1" :: "v"(p), "v"(v) : "memory"); }
DEV void st8c_asm(void* p, u2v v) { asm volatile("global_store_dwordx2 %0, %1, off sc1" :: "v"(p), "v"(v) : "memory"); }
DEV void ld16c_asm(u4v& v, const void* p) { asm volatile("global_load_dwordx4 %0, %1, off sc1" : "=&v"(v) : "v"(p) : "memory"); }
DEV void rawbar() { asm volatile("s_barrier" ::: "memory"); }
DEV unsigned lds_r4(unsigned a) { return *(volatile LDS unsigned*)(size_t)a; }
DEV void lds_w4(unsigned a, unsigned v) { *(volatile LDS unsigned*)(size_t)a = v; }
DEV u8v cat8(u4v a, u4v b) { return (u8v){a.x, a.y, a.z, a.w, b.x, b.y, b.z, b.w}; }
DEV void set_abort(unsigned char* ws) { st4c_asm(cntp(ws, C_ABORT), 1u); }

constexpr unsigned L_MEM = 0, L_KT = 8192, L_FLAG = 12288, L_RED = 16384;
constexpr unsigned L_EST = 12304, L_TS = 12308, L_CG = 12544, L_G = 32768, L_XN = 65536;
#define CLOB_V200_247 "v200","v201","v202","v203","v204","v205","v206","v207","v208","v209","v210","v211","v212","v213","v214","v215", \
  "v216","v217","v218","v219","v220","v221","v222","v223","v224","v225","v226","v227","v228","v229","v230","v231", \
  "v232","v233","v234","v235","v236","v237","v238","v239","v240","v241","v242","v243","v244","v245","v246","v247","v248"
#define SMF "v_smfmac_f32_16x16x64_bf16 "
#define R1_HEAD(o0, o1, o2, o3, o4, o5, o6, FIRST) \
  "v_mov_b32 v228, 0\n\tv_mov_b32 v229, 0\n\tv_mov_b32 v230, 0\n\tv_mov_b32 v231, 0\n\t" \
  "v_mov_b32 v232, 0\n\tv_mov_b32 v233, 0\n\tv_mov_b32 v234, 0\n\tv_mov_b32 v235, 0\n\t" \
  "v_mov_b32 v244, 0\n\tv_mov_b32 v245, 0\n\tv_mov_b32 v246, 0\n\tv_mov_b32 v247, 0\n\t" \
  "s_mov_b32 m0, %[m0v]\n\t" \
  "s_barrier\n\t" ST(80) \
  "global_load_lds_dwordx4 %[moff], %[src] sc1\n\t" \
  "s_waitcnt vmcnt(0)\n\t" \
  "ds_read_b128 v[200:203], %[rd] offset:" #o0 "*1024\n\t" \
  "s_barrier\n\t" ST(82) \
  "ds_read_b128 v[204:207], %[rd] offset:" #o1 "*1024\n\tds_read_b128 v[208:211], %[rd] offset:" #o2 "*1024\n\t" \
  "ds_read_b128 v[212:215], %[rd] offset:" #o3 "*1024\n\tds_read_b128 v[216:219], %[rd] offset:" #o4 "*1024\n\t" \
  "ds_read_b128 v[220:223], %[rd] offset:" #o5 "*1024\n\tds_read_b128 v[224:227], %[rd] offset:" #o6 "*1024\n\t"
#define R1_TWO(n6, n5, n4, n3, n2, n1, n0) \
  "s_waitcnt lgkmcnt(" #n6 ")\n\t" ST(84) SMF "v[228:231], v[200:203], %[a0], %[idx]\n\t" SMF "v[232:235], v[200:203], %[b0], %[idx]\n\t" \
  "s_waitcnt lgkmcnt(" #n5 ")\n\t" SMF "v[228:231], v[204:207], %[a1], %[idx]\n\t" SMF "v[232:235], v[204:207], %[b1], %[idx]\n\t" \
  "s_waitcnt lgkmcnt(" #n4 ")\n\t" SMF "v[228:231], v[208:211], %[a2], %[idx]\n\t" SMF "v[232:235], v[208:211], %[b2], %[idx]\n\t" \
  "s_waitcnt lgkmcnt(" #n3 ")\n\t" SMF "v[228:231], v[212:215], %[a3], %[idx]\n\t" SMF "v[232:235], v[212:215], %[b3], %[idx]\n\t" \
  "s_waitcnt lgkmcnt(" #n2 ")\n\t" SMF "v[228:231], v[216:219], %[a4], %[idx]\n\t" SMF "v[232:235], v[216:219], %[b4], %[idx]\n\t" \
  "s_waitcnt lgkmcnt(" #n1 ")\n\t" SMF "v[228:231], v[220:223], %[a5], %[idx]\n\t" \
  "s_waitcnt lgkmcnt(" #n0 ")\n\t" SMF "v[228:231], v[224:227], %[a6], %[idx]\n\t" \
  SMF "v[232:235], v[220:223], %[b5], %[idx]\n\t" SMF "v[232:235], v[224:227], %[b6], %[idx]\n\t" \
  ST(86) "s_nop 1\n\t" \
  "v_cvt_pk_bf16_f32 v228, v228, v229\n\tv_cvt_pk_bf16_f32 v229, v230, v231\n\t" \
  "ds_write_b64 %[ktw], v[228:229]\n\t" \
  "s_nop 2\n\t" \
  "v_cvt_pk_bf16_f32 v232, v232, v233\n\tv_cvt_pk_bf16_f32 v233, v234, v235\n\t" \
  "ds_write_b64 %[ktw], v[232:233] offset:512\n\t" \
  LDSWAIT ST(88) \
  "s_barrier\n\t" ST(90)
#define LDSWAIT
#define R1_WOPS [a0] "v"(w0[0]), [a1] "v"(w0[1]), [a2] "v"(w0[2]), [a3] "v"(w0[3]), [a4] "v"(w0[4]), [a5] "v"(w0[5]), [a6] "v"(w0[6])
#define R1_WOPS2 [b0] "v"(w1[0]), [b1] "v"(w1[1]), [b2] "v"(w1[2]), [b3] "v"(w1[3]), [b4] "v"(w1[4]), [b5] "v"(w1[5]), [b6] "v"(w1[6])
#define R1_COMMON [idx] "v"(idx), [rd] "v"(rd), [ktw] "v"(ktw), [moff] "v"(moff), [src] "s"(src), [m0v] "s"(m0v)

template <int W>
DEV void r1_mfma(unsigned char* __restrict__ ws, int h, int q, int lane, unsigned sel, int smk) {
  constexpr int wave = W, KIND = W == 0 ? 0 : W == 3 ? 2 : 1;
  const int g = lane >> 4, c16 = lane & 15;
  const int idx = (lane & 8) ? 0xEEEE : 0x4444;
  const unsigned moff = wave * 1024 + g * 256 + (lane & 7) * 32 + ((lane >> 3) & 1) * 16;
  u8v w0[7], w1[7];
  {
    const unsigned char* p = ws + O_WA + (size_t)(h * 28 + 7 * q + 2 * wave) * 14336 + lane * 16;
#pragma unroll
    for (int k = 0; k < 7; ++k) {
      const int o = ((k + W) % 7) * 2048;
      w0[k] = cat8(*(const u4v*)(p + o), *(const u4v*)(p + o + 1024));
      if (KIND != 2) w1[k] = cat8(*(const u4v*)(p + 14336 + o), *(const u4v*)(p + 14336 + o + 1024));
    }
#pragma unroll
    for (int k = 0; k < 7; ++k) { asm volatile("" : "+v"(w0[k])); if (KIND != 2) asm volatile("" : "+v"(w1[k])); }
  }
  RTS(1);
  const unsigned rd = L_MEM + lane * 16, ktw = L_KT + 2 * wave * 512 + c16 * 32 + g * 8, m0v = L_MEM + wave * 1024;
  const unsigned a0a = L_MEM + (7 * q + g) * 256 + c16 * 16, a1a = L_MEM + min(7 * q + 4 + g, 27) * 256 + c16 * 16;
  const unsigned trb = L_KT + (g >> 1) * 512 + (4 * (g & 1) + (c16 >> 2)) * 32 + (c16 & 3) * 8;
  const unsigned sto = h * 512 + q * 128 + (4 * (g & 1) + 2 * (g >> 1) + (c16 >> 3)) * 16 + (c16 & 7) * 2;
  const unsigned long long lo8 = 0x00FF00FF00FF00FFull;
  unsigned ep = 0;
  int budget = TRY_CAP;
  rawbar();
  rawbar();
  for (int s = 0; s < NSEG; ++s) {
    const unsigned char* src = hblk(ws, sel, s, 0);
    unsigned char* dst = hblk(ws, sel, 64 + s, smk);
    for (;;) {
      if (KIND == 0) {
        unsigned long long okm, tst;
        asm volatile(R1_HEAD(0, 1, 2, 3, 4, 5, 6, )
          "ds_read_b128 v[236:239], %[a0a]\n\tds_read_b128 v[240:243], %[a1a]\n\t"
          R1_TWO(8, 7, 6, 5, 4, 3, 2)
          "ds_read_b64_tr_b16 v[200:201], %[trb]\n\tds_read_b64_tr_b16 v[202:203], %[trb] offset:256\n\t"
          "ds_read_b64_tr_b16 v[204:205], %[trb] offset:1024\n\tds_read_b64_tr_b16 v[206:207], %[trb] offset:1280\n\t"
          "ds_read_b64_tr_b16 v[208:209], %[trb] offset:2048\n\tds_read_b64_tr_b16 v[210:211], %[trb] offset:2304\n\t"
          "ds_read_b64_tr_b16 v[212:213], %[trb] offset:3072\n\tds_read_b64_tr_b16 v[214:215], %[trb] offset:3328\n\t"
          "s_waitcnt lgkmcnt(4)\n\t" SMF "v[244:247], v[236:239], v[200:207], %[idx]\n\t"
          "s_waitcnt lgkmcnt(0)\n\t" SMF "v[244:247], v[240:243], v[208:215], %[idx]\n\t"
          "s_nop 7\n\t"
          "v_permlane32_swap_b32 v244, v246\n\tv_permlane32_swap_b32 v245, v247\n\t"
          "v_add_f32 v244, v244, v246\n\tv_add_f32 v245, v245, v247\n\t"
          "v_cndmask_b32 v246, v244, v245, %[lo8]\n\t"
          "v_cndmask_b32 v247, v245, v244, %[lo8]\n\t"
          "s_nop 1\n\t"
          "v_add_f32_dpp v247, v246, v247 row_ror:8 row_mask:0xf bank_mask:0xf\n\t"
          "v_cvt_pk_bf16_f32 v246, v247, v247\n\t"
          "v_cmpx_o_f32 vcc, v247, v247\n\t"
          "global_store_short %[sto], v246, %[dst] sc1\n\t" ST(92)
          "s_memtime %[tst]\n\t"
          "s_mov_b64 exec, -1\n\t"
          "s_mov_b64 %[okm], vcc\n\t"
          "s_waitcnt lgkmcnt(0)\n\t"
          : [okm] "=&s"(okm), [tst] "=&s"(tst)
          : R1_WOPS, R1_WOPS2, R1_COMMON, [a0a] "v"(a0a), [a1a] "v"(a1a), [trb] "v"(trb), [sto] "v"(sto), [dst] "s"(dst), [lo8] "s"(lo8)
          : CLOB_V200_247, "vcc", "memory" ST_CLOB);
        lds_w4(L_TS, (unsigned)tst);
        if (okm != ~0ull) {
          if (budget > 0) { if (lane == 0) lds_w4(L_FLAG, ep); }
          else asm volatile("global_store_short %0, %1, %2 sc1" :: "v"(sto), "v"(0x7FC0u), "s"(dst) : "memory");
        }
      } else if (KIND == 1) {
        if (W == 1) asm volatile(R1_HEAD(1, 2, 3, 4, 5, 6, 0, ) R1_TWO(6, 5, 4, 3, 2, 1, 0) :: R1_WOPS, R1_WOPS2, R1_COMMON : CLOB_V200_247, "memory" ST_CLOB);
        else asm volatile(R1_HEAD(2, 3, 4, 5, 6, 0, 1, ) R1_TWO(6, 5, 4, 3, 2, 1, 0) :: R1_WOPS, R1_WOPS2, R1_COMMON : CLOB_V200_247, "memory" ST_CLOB);
      } else {
        asm volatile(R1_HEAD(3, 4, 5, 6, 0, 1, 2, )
          "s_waitcnt lgkmcnt(6)\n\t" SMF "v[228:231], v[200:203], %[a0], %[idx]\n\t"
          "s_waitcnt lgkmcnt(5)\n\t" SMF "v[228:231], v[204:207], %[a1], %[idx]\n\t"
          "s_waitcnt lgkmcnt(4)\n\t" SMF "v[228:231], v[208:211], %[a2], %[idx]\n\t"
          "s_waitcnt lgkmcnt(3)\n\t" SMF "v[228:231], v[212:215], %[a3], %[idx]\n\t"
          "s_waitcnt lgkmcnt(2)\n\t" SMF "v[228:231], v[216:219], %[a4], %[idx]\n\t"
          "s_waitcnt lgkmcnt(1)\n\t" SMF "v[228:231], v[220:223], %[a5], %[idx]\n\t"
          "s_waitcnt lgkmcnt(0)\n\t" SMF "v[228:231], v[224:227], %[a6], %[idx]\n\t"
          "s_nop 7\n\t"
          "v_cvt_pk_bf16_f32 v228, v228, v229\n\tv_cvt_pk_bf16_f32 v229, v230, v231\n\t"
          "ds_write_b64 %[ktw], v[228:229]\n\t"
          LDSWAIT
          "s_barrier\n\t"
          :: R1_WOPS, R1_COMMON : CLOB_V200_247, "memory" ST_CLOB);
      }
      bar();
      const bool bad = __builtin_amdgcn_readfirstlane(lds_r4(L_FLAG)) == ep;
      ++ep;
      if (!bad) break;
      --budget;
      rawbar();
    }
    ARR(s);
    rawbar();
  }
}

typedef unsigned short h2v __attribute__((ext_vector_type(2)));
DEV unsigned pkmax(unsigned a, unsigned b) { return __builtin_bit_cast(unsigned, __builtin_elementwise_max(__builtin_bit_cast(h2v, a), __builtin_bit_cast(h2v, b))); }
DEV unsigned wavepkmax(unsigned v) {
  v = pkmax(v, __builtin_amdgcn_update_dpp(0u, v, 0xb1, 0xf, 0xf, true));
  v = pkmax(v, __builtin_amdgcn_update_dpp(0u, v, 0x4e, 0xf, 0xf, true));
  v = pkmax(v, __builtin_amdgcn_update_dpp(0u, v, 0x124, 0xf, 0xf, true));
  v = pkmax(v, __builtin_amdgcn_update_dpp(0u, v, 0x128, 0xf, 0xf, true));
  const u2v x = __builtin_amdgcn_permlane16_swap(v, v, false, false);
  v = pkmax(x.x, x.y);
  const u2v y = __builtin_amdgcn_permlane32_swap(v, v, false, false);
  return pkmax(y.x, y.y);
}
DEV void r1_boss(unsigned char* __restrict__ ws, int lane, unsigned sel TUNE_ARG) {
  constexpr unsigned L_FA = 13312, L_FY = 13568;
  int A = 0, V = 0, ms = 4 * K_MS, mm = 4 * K_MM;
  float rho = 0.f;
  int hist = K_PERIOD;
  bool prevf3 = false;
  unsigned ep = 0;
  const int tstart = rt32();
  if (lane == 1) hist += K_WARM;
  if (lane < 5) st4c_asm(ptp(ws, lane), ptword(hist, K_DS));
  rawbar();
  wait_flags(ws, flagR(ws, sel, lane));
  wait_flags(ws, flagR(ws, sel, 64 + min(lane, 27)));
  const int tanch = rt32();
  if (lane < 32) st4c_asm(anchorp(ws, lane), ((unsigned)(rt32() + K_LEAD) << 1) | 1u);
  for (int t = 0; t < NSEG; ++t) {
    const int s = max(t - 3, 0);
    const float pa = 4.f * (float)__builtin_amdgcn_readlane(hist, max(s - 1, 0)), py = 4.f * (float)__builtin_amdgcn_readlane(hist, s);
    rawbar(); rawbar();
    if (t >= 3) vm0();
    const unsigned a = lds_r4(L_FA + lane * 4), y = lds_r4(L_FY + lane * 4);
    const float da = (float)((a >> 12) & 0xffffu), dy = (float)((y >> 12) & 0xffffu);
    unsigned pk = min((unsigned)((float)(a & 0xfffu) * pa * __builtin_amdgcn_rcpf(da)), 65535u) |
                  (min((unsigned)((float)(y & 0xfffu) * py * __builtin_amdgcn_rcpf(dy)), 65535u) << 16);
    unsigned fl = (max(a, y) == SENT ? 1u : 0u) | ((a >> 28) != 0 ? 2u : 0u) | ((y >> 26) & 4u) |
                  (fabsf(da - 0.94f * pa) > 0.08f * pa ? 8u : 0u) | (fabsf(dy - 0.94f * py) > 0.08f * py ? 16u : 0u) | ((y >> 25) & 16u);
    float rn = dy * __builtin_amdgcn_rcpf(py);
    asm volatile("s_barrier" : "+v"(pk), "+v"(fl), "+v"(rn) :: "memory");
    pk = wavepkmax(pk);
    asm volatile("s_barrier\n\ts_barrier" : "+v"(pk) :: "memory");
    while (__builtin_amdgcn_readfirstlane(lds_r4(L_FLAG)) == ep++) { rawbar(); rawbar(); rawbar(); rawbar(); rawbar(); }
    if (t >= 3 && t + 2 < NSEG) {
      if (!__any(fl & 1)) {
        const unsigned m = __builtin_amdgcn_readfirstlane(pk);
        const bool f1 = __any(fl & 2), f3 = __any(fl & 4);
        if (!__any(fl & 10)) A = A == 0 ? (int)(m & 0xffffu) : A + (((int)(m & 0xffffu) - A) >> 2);
        if (!__any(fl & 20)) { V = V == 0 ? (int)(m >> 16) : V + (((int)(m >> 16) - V) >> 2); rho = rho == 0.f ? rn : rho + 0.25f * (rn - rho); }
        if (s >= 4) {
          if (f1 && !prevf3) mm = min(mm + 20, 4 * K_MM + 240);
          if (f3 && !f1) ms = min(ms + 20, 4 * K_MS + 240);
        }
        prevf3 = f3;
      }
      int P = K_PERIOD, DS = K_DS;
      if (A > 0 && V > 0 && !K_FIXED) {
        DS = min(max((int)((float)(A + ms) * rho), 1200), 3000);
        P = min(max((A + ms + V - 84 + mm) >> 2, 600), 1500);
      }
      if (lane == t + 2) hist = P;
      post(ptp(ws, t + 2), ptword(P, DS), lane);
    }
    if (t >= 2) {
      asm volatile("s_mov_b32 m0, %2\n\ts_nop 0\n\tglobal_load_lds_dword %0, off sc1\n\ts_mov_b32 m0, %3\n\ts_nop 0\n\tglobal_load_lds_dword %1, off sc1"
                   :: "v"(flagS(ws, sel, t - 2, lane)), "v"(flagY(ws, sel, t - 2, min(lane, 27))), "s"(L_FA), "s"(L_FY) : "memory");
    }
  }
  vm0();
  rawbar();
  { const int tend = rt32();
    if (lane == 0) printf("ws %p sel %u A %d V %d rho %f ms %d mm %d tries %u t_anchor_to_end %d start_to_anchor %d\n", ws, sel, A, V, rho, ms, mm, ep, tend - tanch, tanch - tstart);
    if ((lane & 3) == 0) printf("P[%d]=%d ", lane, hist); }
}

DEV void role_r1(unsigned char* __restrict__ ws, int id, int wave, int lane, unsigned sel TUNE_ARG) {
  const int h = id >> 2, q = id & 3;
  if (wave == 0) { r1_mfma<0>(ws, h, q, lane, sel, K_SMK); return; }
  if (wave == 1) { r1_mfma<1>(ws, h, q, lane, sel, K_SMK); return; }
  if (wave == 2) { r1_mfma<2>(ws, h, q, lane, sel, K_SMK); return; }
  if (wave == 3) { r1_mfma<3>(ws, h, q, lane, sel, K_SMK); return; }
  if (wave == 7 && id == 3) { r1_boss(ws, lane, sel TUNE_PASS); return; }
  const int g = lane >> 4, c16 = lane & 15;
  const bool act = q < 2;
  const int fr = wave - 1;
  const unsigned moff = fr * 1024 + g * 256 + (lane & 7) * 32 + ((lane >> 3) & 1) * 16, m0v = L_MEM + fr * 1024;
  const unsigned xo = L_XN + (g >> 1) * 1024 + (g & 1) * 512 + c16 * 16;
  unsigned ep = 0;
  if (wave == 7) {
    const int idx = (lane & 8) ? 0xEEEE : 0x4444;
    u8v w0[7];
    const unsigned rd = lane * 16;
    const unsigned sto = h * 512 + (q & 1) * 256 + (4 * (g & 1) + 2 * (g >> 1) + (lane & 1)) * 32 + (c16 >> 1) * 4;
    const unsigned long long odd = 0xAAAAAAAAAAAAAAAAull;
    __builtin_amdgcn_s_setprio(3);
    rawbar();
    for (int s = 0; s < NSEG; ++s) {
      unsigned char* dst = hblk(ws, sel, s, 2);
      unsigned char* raw = hblk(ws, sel, s, 1);
      const bool go = act && s < NSEG - 1;
      if (go) {
#pragma unroll
        for (int k = 0; k < 7; ++k) w0[k] = cat8(lds_r16(L_G + (s & 1) * 14336 + rd + k * 2048), lds_r16(L_G + (s & 1) * 14336 + rd + k * 2048 + 1024));
      }
      for (;;) {
        if (go) {
          asm volatile(
            "v_mov_b32 v228, 0\n\tv_mov_b32 v229, 0\n\tv_mov_b32 v230, 0\n\tv_mov_b32 v231, 0\n\t"
            "s_barrier\n\ts_barrier\n\ts_barrier\n\t" ST(80)
            "ds_read_b128 v[200:203], %[rd]\n\tds_read_b128 v[204:207], %[rd] offset:1024\n\tds_read_b128 v[208:211], %[rd] offset:2048\n\t"
            "ds_read_b128 v[212:215], %[rd] offset:3072\n\tds_read_b128 v[216:219], %[rd] offset:4096\n\tds_read_b128 v[220:223], %[rd] offset:5120\n\t"
            "ds_read_b128 v[224:227], %[rd] offset:6144\n\t"
            "s_waitcnt lgkmcnt(6)\n\t" SMF "v[228:231], v[200:203], %[a0], %[idx]\n\t"
            "s_waitcnt lgkmcnt(5)\n\t" SMF "v[228:231], v[204:207], %[a1], %[idx]\n\t"
            "s_waitcnt lgkmcnt(4)\n\t" SMF "v[228:231], v[208:211], %[a2], %[idx]\n\t"
            "s_waitcnt lgkmcnt(3)\n\t" SMF "v[228:231], v[212:215], %[a3], %[idx]\n\t"
            "s_waitcnt lgkmcnt(2)\n\t" SMF "v[228:231], v[216:219], %[a4], %[idx]\n\t"
            "s_waitcnt lgkmcnt(1)\n\t" SMF "v[228:231], v[220:223], %[a5], %[idx]\n\t"
            "s_waitcnt lgkmcnt(0)\n\t" SMF "v[228:231], v[224:227], %[a6], %[idx]\n\t"
            "s_nop 7\n\t"
            "v_permlane32_swap_b32 v228, v230\n\tv_permlane32_swap_b32 v229, v231\n\t"
            "v_add_f32 v228, v228, v230\n\tv_add_f32 v229, v229, v231\n\t"
            "v_cndmask_b32 v230, v229, v228, %[odd]\n\t"
            "s_nop 1\n\t"
            "v_mov_b32_dpp v230, v230 quad_perm:[1,0,3,2] row_mask:0xf bank_mask:0xf\n\t"
            "v_cndmask_b32 v228, v228, v230, %[odd]\n\tv_cndmask_b32 v229, v230, v229, %[odd]\n\t"
            "v_exp_f32 v230, v228\n\tv_exp_f32 v231, v229\n\t"
            "v_cvt_pk_bf16_f32 v232, v228, v229\n\t"
            "v_cvt_pk_bf16_f32 v233, v230, v231\n\t"
            "v_cmpx_o_f32 vcc, v228, v229\n\t"
            "global_store_dword %[sto], v233, %[dst] sc1\n\t" ST(82)
            "global_store_dword %[sto], v232, %[raw] sc1\n\t"
            "s_mov_b64 exec, -1\n\t"
            "s_barrier\n\t"
            :: R1_WOPS, [idx] "v"(idx), [rd] "v"(rd), [sto] "v"(sto), [dst] "s"(dst), [raw] "s"(raw), [odd] "s"(odd)
            : CLOB_V200_247, "vcc", "memory" ST_CLOB);
          rawbar();
        } else {
          rawbar(); rawbar(); rawbar(); rawbar(); rawbar();
        }
        const bool bad = __builtin_amdgcn_readfirstlane(lds_r4(L_FLAG)) == ep;
        ++ep;
        if (!bad) break;
      }
    }
    rawbar();
  } else if (wave == 4) {
    const unsigned trb = L_KT + (g >> 1) * 512 + (4 * (g & 1) + (c16 >> 2)) * 32 + (c16 & 3) * 8;
    unsigned char* so = ws + O_SM + (size_t)h * 2048 + q * 256 + lane * 4;
    const unsigned x3 = xo, xlast = g < 2 ? 6144 : 5120;
#pragma unroll
    for (int u = 0; u < 8; ++u) *(LDS u2v*)(size_t)(L_KT + u * 512 + lane * 8) = u < 7 ? (u2v){SENT, SENT} : (u2v){0u, 0u};
    if (lane == 0) lds_w4(L_FLAG, SENT);
    rawbar();
    (void)ld_cnt((const unsigned*)(lane < 32 ? so : lane < 48 ? so + 63 * 32768 : hblk(ws, sel, 70, 0)));
    unsigned pay = 0, eprev = 0;
    for (int s = 0; s < NSEG; ++s) {
      bool sig = s > 0;
      const unsigned ep0 = ep;
      for (;;) {
        bar(); rawbar(); rawbar();
        if (sig) { vm0(); if (lane == 0) st4c_asm(flagS(ws, sel, s - 1, id), pay); sig = false; }
        rawbar(); rawbar();
        const bool bad = __builtin_amdgcn_readfirstlane(lds_r4(L_FLAG)) == ep;
        ++ep;
        if (!bad) break;
      }
      {
        const unsigned est = lds_r4(L_EST), tst = lds_r4(L_TS);
        pay = (est ? min(tst - est, 4095u) : 0u) | ((est && eprev) ? min(est - eprev, 65535u) << 12 : 0u);
        pay |= min(ep - ep0 - 1, 7u) << 28;
        eprev = est;
      }
      f4v c0 = {0.f, 0.f, 0.f, 0.f}, c1 = c0;
      u4v ka[4], xa[4], xb[4];
#pragma unroll
      for (int u = 0; u < 4; ++u) {
        const u2v lo = lds_tr(trb + u * 1024), hi = lds_tr(trb + u * 1024 + 256);
        ka[u] = (u4v){lo.x, lo.y, hi.x, hi.y};
        const unsigned ad = x3 + (s & 1) * 8192 + (u < 3 ? u * 2048 : xlast);
        xa[u] = lds_r16(ad); xb[u] = lds_r16(ad + 256);
      }
      __builtin_amdgcn_sched_barrier(0);
#pragma unroll
      for (int u = 0; u < 4; ++u) { c0 = mfma16(ka[u], xa[u], c0); c1 = mfma16(ka[u], xb[u], c1); }
#pragma unroll
      for (int u = 0; u < 7; ++u) *(LDS u2v*)(size_t)(L_KT + u * 512 + lane * 8) = (u2v){SENT, SENT};
      st4c_asm(so + (size_t)s * 32768, pk2(halfsum(c0[0], c0[1]), halfsum(c0[2], c0[3])));
      st4c_asm(so + (size_t)s * 32768 + 1024, pk2(halfsum(c1[0], c1[1]), halfsum(c1[2], c1[3])));
      ARR(s);
    }
    vm0();
    rawbar();
    if (lane == 0) st4c_asm(flagS(ws, sel, NSEG - 1, id), pay);
  } else if (wave == 5) {
    const unsigned char* gp = ws + O_G + (size_t)(h * 2 + (q & 1)) * G_RUN;
    const unsigned v0 = lane * 16, v1 = v0 + 4096, v2 = v0 + 8192, v3 = v0 + 12288;
    auto fetch = [&](int t) {
#define GL4(v) "global_load_lds_dwordx4 " v ", %4 sc1\n\t" "global_load_lds_dwordx4 " v ", %4 offset:1024 sc1\n\t"
#define GL8(v) GL4(v) "global_load_lds_dwordx4 " v ", %4 offset:2048 sc1\n\t" "global_load_lds_dwordx4 " v ", %4 offset:3072 sc1\n\t"
      asm volatile("s_mov_b32 m0, %5\n\ts_nop 0\n\t" GL8("%0") "s_add_u32 m0, m0, 4096\n\ts_nop 0\n\t" GL8("%1")
                   "s_add_u32 m0, m0, 4096\n\ts_nop 0\n\t" GL8("%2") "s_add_u32 m0, m0, 4096\n\ts_nop 0\n\t" GL4("%3")
                   :: "v"(v0), "v"(v1), "v"(v2), "v"(v3), "s"(gp + t * 14336), "s"(L_G + (t & 1) * 14336) : "scc", "memory");
    };
    auto fetchx = [&](int t) {
      asm volatile("s_mov_b32 m0, %3\n\ts_nop 0\n\t"
                   "global_load_lds_dwordx4 %0, %2\n\tglobal_load_lds_dwordx4 %0, %2 offset:1024\n\tglobal_load_lds_dwordx4 %0, %2 offset:2048\n\t"
                   "global_load_lds_dwordx4 %0, %2 offset:3072\n\ts_add_u32 m0, m0, 4096\n\ts_nop 0\n\t"
                   "global_load_lds_dwordx4 %1, %2\n\tglobal_load_lds_dwordx4 %1, %2 offset:1024\n\tglobal_load_lds_dwordx4 %1, %2 offset:2048"
                   :: "v"(v0), "v"(v1), "s"(ws + O_XN + (size_t)t * 28672 + q * 7168), "s"(L_XN + (t & 1) * 8192) : "scc", "memory");
    };
    const unsigned* gdone = doneG(ws, 7 * (h >> 1) + min(lane, 6));
    auto peek = [&](int t) {
      unsigned keep;
      asm volatile("s_mov_b32 %0, m0\n\ts_mov_b32 m0, %2\n\ts_nop 0\n\tglobal_load_lds_dword %1, off sc1\n\ts_mov_b32 m0, %0"
                   : "=&s"(keep) : "v"(lane == 63 ? ptp(ws, t) : gdone), "s"(L_CG) : "memory");
    };
    if (act) {
      wait_cnt(ws, gdone, 1);
      fetch(0); fetch(1);
    }
    fetchx(0);
    vm0();
    RTS(1);
    rawbar();
    for (int s = 0; s < NSEG; ++s) {
      const unsigned char* src = hblk(ws, sel, s, 0) + moff;
      bool todo = act && s + 2 < NSEG - 1, todox = s + 1 < NSEG;
      for (;;) {
        rawbar();
        if (s == 0) peek(0);
        rawbar();
        dma16c(src, m0v);
        vm0();
        rawbar();
        const bool there = __all(lds_r4(L_CG + (lane & 7) * 4) >= (unsigned)(s >> 1) + 2);
        rawbar();
        rawbar();
        if (todo && there) { fetch(s + 2); todo = false; }
        if (todox) { fetchx(s + 1); peek(s + 1); todox = false; }
        const bool bad = __builtin_amdgcn_readfirstlane(lds_r4(L_FLAG)) == ep;
        ++ep;
        if (!bad) break;
      }
      if (todo) {
        wait_cnt(ws, gdone, (s >> 1) + 2);
        fetch(s + 2);
      }
      ARR(s);
    }
    vm0();
    bar();
  } else {
      { Lat W = {rt32(), 6, 0}; (void)lat_wait(W); }
    rawbar();
    Lat L = {get_anchor(ws, sel, id, lane), 0, 0};
    for (int s = 0; s < NSEG; ++s) {
      const unsigned char* src = hblk(ws, sel, s, 0) + moff;
      unsigned pw = 0;
      for (;;) {
        bar();
        const unsigned est = lat_wait(L);
        rawbar();
        asm volatile("s_mov_b32 m0, %1\n\ts_nop 0\n\tglobal_load_lds_dwordx4 %0, off sc1\n\tglobal_load_lds_dwordx4 %0, off offset:1024 sc1\n\ts_waitcnt vmcnt(0)"
                     :: "v"(src), "s"(m0v) : "memory");
        rawbar();
        lds_w4(L_EST, est);
        if (pw == 0) pw = __builtin_amdgcn_readfirstlane(lds_r4(L_CG + 252)) | 0x80000000u;
        bar(); rawbar();
        const bool bad = __builtin_amdgcn_readfirstlane(lds_r4(L_FLAG)) == ep;
        ++ep;
        EV(bad ? 1 : 0);
        if (!bad) break;
        L.e += RET_E;
      }
      if (pw == 0x80000000u && s < NSEG - 1) { wait_cnt(ws, ptp(ws, s), 1); pw = __builtin_amdgcn_readfirstlane(ld_cnt(ptp(ws, s))); }
      lat_fwd(L, pw & 255, (pw >> 8) & 255);
      ARR(s);
    }
    bar();
    EV_OUT(id);
  }
}

#define XRED(op, v, t) \
  "v_mov_b32 " t ", " v "\n\ts_nop 1\n\tv_permlane16_swap_b32 " v ", " t "\n\t" op " " v ", " v ", " t "\n\t" \
  "v_mov_b32 " t ", " v "\n\ts_nop 1\n\tv_permlane32_swap_b32 " v ", " t "\n\t" op " " v ", " v ", " t "\n\t"
#define MF16 "v_mfma_f32_16x16x32_bf16 "
#define WAIT_CYC(T) \
  "s_memtime vcc\n\ts_waitcnt lgkmcnt(0)\n\t" \
  "s_sub_i32 s70, " T ", vcc_lo\n\ts_max_i32 s70, s70, 0\n\ts_mul_i32 s70, s70, 13\n\ts_lshr_b32 s70, s70, 6\n\t" \
  "s_min_i32 s70, s70, 128\n\ts_lshl_b32 s70, s70, 2\n\ts_sub_i32 s70, 524, s70\n\t" \
  "s_getpc_b64 vcc\n\ts_add_u32 vcc_lo, vcc_lo, s70\n\ts_addc_u32 vcc_hi, vcc_hi, 0\n\ts_setpc_b64 vcc\n\t" \
  ".rept 128\n\ts_nop 0\n\t.endr\n\t"
#define R3_Y_A \
  "ds_read_b128 v[200:203], %[rd]\n\tds_read_b128 v[204:207], %[rd] offset:1024\n\tds_read_b128 v[208:211], %[rd] offset:2048\n\t" \
  "ds_read_b128 v[212:215], %[rd] offset:3072\n\tds_read_b128 v[216:219], %[rd] offset:4096\n\tds_read_b128 v[220:223], %[rd] offset:5120\n\t" \
  "ds_read_b128 v[224:227], %[rd] offset:6144\n\t" \
  "s_waitcnt lgkmcnt(6)\n\t" SMF "v[228:231], v[200:203], %[a0], %[idx]\n\t" SMF "v[232:235], v[200:203], %[b0], %[idx]\n\t" \
  "s_waitcnt lgkmcnt(5)\n\t" SMF "v[228:231], v[204:207], %[a1], %[idx]\n\t" SMF "v[232:235], v[204:207], %[b1], %[idx]\n\t" \
  "s_waitcnt lgkmcnt(4)\n\t" SMF "v[228:231], v[208:211], %[a2], %[idx]\n\t" SMF "v[232:235], v[208:211], %[b2], %[idx]\n\t" \
  "s_waitcnt lgkmcnt(3)\n\t" SMF "v[228:231], v[212:215], %[a3], %[idx]\n\t" SMF "v[232:235], v[212:215], %[b3], %[idx]\n\t" \
    \
  "s_waitcnt lgkmcnt(0)\n\t" \
  "v_readfirstlane_b32 s68, v244\n\t" \
  "v_max3_u32 v244, v216, v217, v218\n\tv_max3_u32 v245, v219, v220, v221\n\tv_max3_u32 v244, v244, v222, v223\n\t" \
  "v_max3_u32 v245, v245, v224, v225\n\tv_max3_u32 v244, v244, v226, v227\n\tv_max_u32 v244, v244, v245\n\t" \
  "v_cmp_eq_u32 s[74:75], -1, v244\n\t" \
  "v_cmp_u_f32 vcc, v228, v229\n\t" \
  "s_or_b64 s[74:75], s[74:75], vcc\n\t" \
  "v_cmp_u_f32 vcc, v234, v235\n\t" \
  "s_or_b64 vcc, s[74:75], vcc\n\t"
#define R3_Y_B \
  SMF "v[228:231], v[216:219], %[a4], %[idx]\n\t" SMF "v[232:235], v[216:219], %[b4], %[idx]\n\t" \
  SMF "v[228:231], v[220:223], %[a5], %[idx]\n\t" SMF "v[228:231], v[224:227], %[a6], %[idx]\n\t" \
  SMF "v[232:235], v[220:223], %[b5], %[idx]\n\t" SMF "v[232:235], v[224:227], %[b6], %[idx]\n\t" \
  "s_nop 1\n\t" \
  "v_permlane32_swap_b32 v228, v229\n\tv_permlane32_swap_b32 v230, v231\n\t" \
  "v_add_f32 v228, v228, v229\n\tv_add_f32 v230, v230, v231\n\t" \
  "v_cvt_pk_bf16_f32 v164, v228, v230\n\t" \
  "s_nop 0\n\t" \
  "v_permlane32_swap_b32 v232, v233\n\tv_permlane32_swap_b32 v234, v235\n\t" \
  "v_add_f32 v232, v232, v233\n\tv_add_f32 v234, v234, v235\n\t" \
  "v_cvt_pk_bf16_f32 v165, v232, v234\n\t" \
  ST(82)
#define R3_POLL \
  WAIT_CYC("s68") \
  "global_load_dwordx4 v[236:239], %[to], s[60:61] sc1\n\t" \
  WAIT_CYC("s69") \
  "global_load_dwordx4 v[240:243], %[tm], s[60:61] sc1\n\t" ST(86)
#define R3_POLL2   \
  "55:\n\ts_memtime vcc\n\ts_waitcnt lgkmcnt(0)\n\ts_sub_i32 s70, s69, vcc_lo\n\ts_cmp_gt_i32 s70, 0\n\ts_cbranch_scc1 55b\n\t" \
  "global_load_dwordx4 v[236:239], %[to], s[60:61] sc1\n\tglobal_load_dwordx4 v[240:243], %[tm], s[60:61] sc1\n\t"
#define R3_S_PHASE(SIG, NS) \
  "s_waitcnt vmcnt(1)\n\t" ST(84) \
  "v_cndmask_b32 v208, 0, v236, s[76:77]\n\tv_cndmask_b32 v209, 0, v237, s[76:77]\n\tv_cndmask_b32 v210, 0, v238, s[76:77]\n\tv_cndmask_b32 v211, 0, v239, s[76:77]\n\t" \
  MF16 "v[220:223], v[160:163], v[236:239], 0\n\t" \
  "v_cndmask_b32 v212, v236, 0, s[76:77]\n\tv_cndmask_b32 v213, v237, 0, s[76:77]\n\tv_cndmask_b32 v214, v238, 0, s[76:77]\n\tv_cndmask_b32 v215, v239, 0, s[76:77]\n\t" \
  MF16 "v[216:219], v[192:195], v[208:211], 0\n\t" \
  "s_nop 0\n\t" \
  MF16 "v[216:219], v[196:199], v[212:215], v[216:219]\n\t" \
  "global_store_dwordx2 %[yo], v[164:165], s[60:61] sc1\n\t"        \
  SIG "s_waitcnt vmcnt(" #NS ")\n\t" ST(88) \
  MF16 "v[232:235], %[selA], v[240:243], 0\n\t" \
  "s_nop 7\n\t" \
  "v_exp_f32 v234, v232\n\tv_exp_f32 v235, v233\n\t" \
  "s_nop 0\n\t" \
  "v_cvt_pk_bf16_f32 v240, v234, v235\n\t" \
  "v_cndmask_b32 v168, 0, v240, s[76:77]\n\tv_cndmask_b32 v169, v240, 0, s[76:77]\n\t" \
  "s_nop 1\n\t" \
  MF16 "v[220:223], v[160:163], v[168:171], v[220:223]\n\t" \
  MF16 "v[244:247], v[164:167], v[168:171], v[216:219]\n\t" \
  "s_nop 6\n\t" \
  "v_rcp_f32 v242, v220\n\t" \
  "v_med3_f32 v241, v220, v172, v173\n\t" \
  "v_cmp_lg_f32 vcc, v241, v220\n\t" \
  "s_cbranch_vccnz 12f\n\t" \
  "11:\n\t" \
  "v_pk_mul_f32 v[244:245], v[244:245], v[242:243] op_sel_hi:[1,0]\n\t" \
  "v_pk_mul_f32 v[246:247], v[246:247], v[242:243] op_sel_hi:[1,0]\n\t" \
  "s_nop 0\n\t" \
  "v_add_f32_dpp v244, v244, v244 row_ror:8 row_mask:0xf bank_mask:0xf\n\tv_add_f32_dpp v245, v245, v245 row_ror:8 row_mask:0xf bank_mask:0xf\n\t" \
  "v_add_f32_dpp v246, v246, v246 row_ror:8 row_mask:0xf bank_mask:0xf\n\tv_add_f32_dpp v247, v247, v247 row_ror:8 row_mask:0xf bank_mask:0xf\n\t" \
  ST(90) "ds_write_b128 %[redw], v[244:247]\n\t" \
  LDSWAIT \
  "s_barrier\n\t" ST(92)
#define R3_S_RARE \
    "12:\n\t" \
  "v_xor_b32 v241, 0x6000, %[to]\n\t" \
  "global_load_dwordx4 v[200:203], v241, s[60:61] sc1\n\t" \
  "s_waitcnt vmcnt(0)\n\t" \
  "v_lshlrev_b32 v208, 16, v200\n\tv_and_b32 v209, 0xffff0000, v200\n\tv_lshlrev_b32 v210, 16, v201\n\tv_and_b32 v211, 0xffff0000, v201\n\t" \
  "v_lshlrev_b32 v212, 16, v202\n\tv_and_b32 v213, 0xffff0000, v202\n\tv_lshlrev_b32 v214, 16, v203\n\tv_and_b32 v215, 0xffff0000, v203\n\t" \
  "v_max3_f32 v240, v208, v209, v210\n\tv_max3_f32 v241, v211, v212, v213\n\tv_max3_f32 v240, v240, v214, v215\n\t" \
  "v_max3_f32 v241, v241, v232, v233\n\tv_max_f32 v240, v240, v241\n\t" \
  XRED("v_max_f32", "v240", "v241") \
  "v_sub_f32 v208, v208, v240\n\tv_sub_f32 v209, v209, v240\n\tv_sub_f32 v210, v210, v240\n\tv_sub_f32 v211, v211, v240\n\t" \
  "v_sub_f32 v212, v212, v240\n\tv_sub_f32 v213, v213, v240\n\tv_sub_f32 v214, v214, v240\n\tv_sub_f32 v215, v215, v240\n\t" \
  "v_sub_f32 v234, v232, v240\n\tv_sub_f32 v235, v233, v240\n\t" \
  "v_exp_f32 v208, v208\n\tv_exp_f32 v209, v209\n\tv_exp_f32 v210, v210\n\tv_exp_f32 v211, v211\n\t" \
  "v_exp_f32 v212, v212\n\tv_exp_f32 v213, v213\n\tv_exp_f32 v214, v214\n\tv_exp_f32 v215, v215\n\t" \
  "v_exp_f32 v234, v234\n\tv_exp_f32 v235, v235\n\ts_nop 1\n\t" \
  "v_cvt_pk_bf16_f32 v200, v208, v209\n\tv_cvt_pk_bf16_f32 v201, v210, v211\n\tv_cvt_pk_bf16_f32 v202, v212, v213\n\tv_cvt_pk_bf16_f32 v203, v214, v215\n\t" \
  "v_cvt_pk_bf16_f32 v240, v234, v235\n\t" \
  "v_cndmask_b32 v208, 0, v200, s[76:77]\n\tv_cndmask_b32 v209, 0, v201, s[76:77]\n\tv_cndmask_b32 v210, 0, v202, s[76:77]\n\tv_cndmask_b32 v211, 0, v203, s[76:77]\n\t" \
  "v_cndmask_b32 v212, v200, 0, s[76:77]\n\tv_cndmask_b32 v213, v201, 0, s[76:77]\n\tv_cndmask_b32 v214, v202, 0, s[76:77]\n\tv_cndmask_b32 v215, v203, 0, s[76:77]\n\t" \
  "v_cndmask_b32 v168, 0, v240, s[76:77]\n\tv_cndmask_b32 v169, v240, 0, s[76:77]\n\ts_nop 1\n\t" \
  MF16 "v[220:223], v[160:163], v[200:203], 0\n\t" \
  MF16 "v[220:223], v[160:163], v[168:171], v[220:223]\n\t" \
  MF16 "v[244:247], v[192:195], v[208:211], 0\n\t" \
  MF16 "v[244:247], v[196:199], v[212:215], v[244:247]\n\t" \
  MF16 "v[244:247], v[164:167], v[168:171], v[244:247]\n\t" \
  "s_nop 7\n\t" \
  "v_rcp_f32 v242, v220\n\ts_nop 0\n\t" \
  "s_branch 11b\n\t"
#define R3_PREP "s_lshl_b32 s44, s65, 2\n\ts_add_i32 s44, s44, s67\n\ts_lshl_b32 s45, s66, 2\n\ts_sub_i32 s45, 108, s45\n\t"
#define R3_FWD "s_add_i32 s66, s66, s47\n\ts_add_i32 s65, s65, s46\n\ts_cmp_ge_i32 s66, 24\n\ts_cselect_b32 s70, 24, 0\n\ts_addc_u32 s65, s65, 0\n\ts_sub_i32 s66, s66, s70\n\t"
#define R3_RT "s_memrealtime vcc\n\ts_waitcnt lgkmcnt(0)\n\ts_sub_i32 s70, s44, vcc_lo\n\ts_cmp_lt_i32 s70, 1\n\t"
#define R3_TW_WAIT \
  R3_RT "s_cbranch_scc1 40f\n\t" \
  "20:\n\t" R3_RT "s_cbranch_scc1 21f\n\t" R3_RT "s_cbranch_scc1 21f\n\t" R3_RT "s_cbranch_scc1 21f\n\t" R3_RT "s_cbranch_scc0 20b\n\t" \
  "21:\n\t" \
  "s_getpc_b64 vcc\n\ts_add_u32 vcc_lo, vcc_lo, s45\n\ts_addc_u32 vcc_hi, vcc_hi, 0\n\ts_setpc_b64 vcc\n\t" \
  ".rept 24\n\ts_nop 0\n\t.endr\n\t" \
  "22:\n\t"
#define R3_TW_LATE \
  "40:\n\t" \
  "s_cmp_eq_u32 s70, 0\n\ts_cbranch_scc1 22b\n\t" \
  "41:\n\ts_add_i32 s65, s65, 26\n\ts_add_i32 s44, s44, 4*26\n\ts_add_i32 s70, s70, 4*26\n\ts_cmp_lt_i32 s70, 0\n\ts_cbranch_scc1 41b\n\t" \
  "s_branch 20b\n\t"
#define R3_STEP(op, uop)   \
  op " %[moff], 0x8000, %[moff]\n\t" op " %[to], 0x8000, %[to]\n\t" op " %[tm], 0x8000, %[tm]\n\t" op " %[sto], 0x8000, %[sto]\n\t" \
  uop " %[uo], 0x4000, %[uo]\n\t" uop " %[yo], 0x1000, %[yo]\n\t"
#define R3_FLIP \
  "s_and_b32 s70, s62, 15\n\ts_cselect_b32 s70, 0, 0x4000\n\ts_add_i32 s71, s62, 1\n\ts_and_b32 s71, s71, 15\n\ts_cselect_b32 s71, 0, 0x4000\n\t" \
  "v_xor_b32 %[moff], s70, %[moff]\n\tv_xor_b32 %[to], s70, %[to]\n\tv_xor_b32 %[tm], s70, %[tm]\n\tv_xor_b32 %[sto], s71, %[sto]\n\t"
#define ULOADS "global_load_dwordx4 v[192:195], %[uo], s[60:61] sc1\n\tglobal_load_dwordx4 v[196:199], %[uo], s[60:61] offset:1024 sc1\n\t"
#define R3_DMA "global_load_lds_dwordx4 %[moff], s[60:61] sc1\n\t"
#define R3_TW_PT "s_lshl_b32 s70, s62, 7\n\ts_add_i32 s70, s70, 0x420080\n\tv_mov_b32 v178, s70\n\tglobal_load_dword v177, v178, s[60:61] sc1\n\t"
#define R3_TW_NEXT "v_readfirstlane_b32 s70, v177\n\ts_cmp_eq_u32 s70, 0\n\ts_cbranch_scc1 45f\n\t" \
  "19:\n\ts_and_b32 s46, s70, 0xff\n\ts_bfe_u32 s47, s70, 0x80008\n\ts_lshr_b32 s48, s70, 16\n\t"
#define R3_TW_OOL        \
  "45:\n\ts_mov_b32 s71, 0x40000\n\t" \
  "46:\n\tglobal_load_dword v177, v178, s[60:61] sc1\n\ts_waitcnt vmcnt(0)\n\tv_readfirstlane_b32 s70, v177\n\t" \
  "s_cmp_eq_u32 s70, 0\n\ts_cbranch_scc0 19b\n\ts_sub_i32 s71, s71, 1\n\ts_cmp_gt_i32 s71, 0\n\ts_cbranch_scc1 46b\n\ts_movk_i32 s70, 40\n\ts_branch 19b\n\t"
#define R3_TW_TIME \
  TBURST \
  "s_sub_i32 s4, s4, s44\n\ts_add_i32 s4, s4, 4\n\ts_mul_i32 s4, s4, 93\n\ts_ashr_i32 s4, s4, 2\n\ts_sub_i32 s58, s58, s4\n\t" \
  "s_lshl_b32 s4, s66, 2\n\ts_add_i32 s58, s58, s4\n\ts_sub_i32 s4, s58, s51\n\ts_mov_b32 s51, s58\n\ts_add_i32 s58, s58, s48\n\t" \
  "v_mov_b32 v200, s58\n\tv_mov_b32 v201, s4\n\tds_write_b64 v188, v[200:201] offset:12296\n\ts_waitcnt lgkmcnt(0)\n\t"
#define R3_WARM(beg, len) \
  "s_getpc_b64 vcc\n\t9:\n\ts_add_u32 vcc_lo, vcc_lo, " beg "-9b\n\ts_addc_u32 vcc_hi, vcc_hi, 0\n\tv_lshlrev_b32 v200, 3, %[rd]\n\tv_min_u32 v200, " len "-64, v200\n\t" \
  "global_load_dword v201, v200, vcc\n\tglobal_load_dword v202, v200, vcc offset:64\n\ts_waitcnt vmcnt(0)\n\t"
#define R3_LOOP(WARM, DMA, TWWAIT, CNTLOAD, CNTPAY, CNTPACK, TWTIME, NW, CNTSIG, NS, TWFWD, FIN, CNTCHK, OUTOFLINE) \
  "s_mov_b64 s[60:61], %[wsp]\n\ts_mov_b32 s62, 0\n\ts_mov_b32 s63, 0\n\ts_mov_b32 s64, 0x10000\n\ts_mov_b32 s78, 0\n\t" \
  "s_mov_b32 s76, 0x00ff00ff\n\ts_mov_b32 s77, s76\n\t" \
  "s_and_b32 s46, %[p0], 0xff\n\ts_bfe_u32 s47, %[p0], 0x80008\n\ts_lshr_b32 s48, %[p0], 16\n\ts_mov_b32 s65, 0\n\ts_mov_b32 s66, 0\n\t" \
  "s_movk_i32 s49, 250\n\ts_lshl_b32 s50, %[p1], 10\n\t" \
  "v_mov_b32 v188, 0\n\tv_mov_b32 v160, 0x3f803f80\n\tv_mov_b32 v161, v160\n\tv_mov_b32 v162, v160\n\tv_mov_b32 v163, v160\n\t" \
  "v_mov_b32 v166, 0\n\tv_mov_b32 v167, 0\n\tv_mov_b32 v170, 0\n\tv_mov_b32 v171, 0\n\tv_mov_b32 v172, 0x1f800000\n\tv_mov_b32 v173, 0x71800000\n\t" \
  R3_PREP \
  WARM \
  "10:\n\t" \
  TWWAIT \
  "s_mov_b32 m0, s50\n\t" \
  "s_waitcnt lgkmcnt(0)\n\ts_barrier\n\t" \
  "s_memtime s[72:73]\n\t" \
  DMA \
  "ds_read_b32 v200, v188 offset:12288\n\t" \
  "s_waitcnt lgkmcnt(0)\n\t" \
  "v_readfirstlane_b32 s74, v200\n\t" \
  "s_cmp_eq_u32 s74, s63\n\ts_cbranch_scc1 50f\n\t" \
  "s_add_i32 s63, s63, 1\n\t" \
  "v_mov_b32 v228, 0\n\tv_mov_b32 v229, 0\n\tv_mov_b32 v230, 0\n\tv_mov_b32 v231, 0\n\t" \
  "v_mov_b32 v232, 0\n\tv_mov_b32 v233, 0\n\tv_mov_b32 v234, 0\n\tv_mov_b32 v235, 0\n\t" \
  ULOADS \
  CNTLOAD TWTIME \
  "s_waitcnt vmcnt(" #NW ")\n\t" \
  "s_barrier\n\t" ST(80) \
  CNTPAY "ds_read_b32 v244, v188 offset:12296\n\t" \
  R3_Y_A \
  "s_cbranch_vccnz 60f\n\t" \
  "14:\n\t" \
  CNTPACK "s_add_i32 s69, s68, s49\n\t" \
  "s_cmp_eq_u32 s62, 63\n\ts_cbranch_scc1 98f\n\t" \
  R3_POLL R3_Y_B \
  TWFWD \
  "15:\n\t" \
  R3_S_PHASE(CNTSIG, NS) FIN CNTCHK \
  "s_add_i32 s62, s62, 1\n\t" \
  R3_STEP("v_add_u32", "v_add_u32") \
  R3_FLIP DIAG_R3 \
  "s_branch 10b\n\t" \
  R3_S_RARE \
    \
  "50:\n\t" \
  "s_sub_i32 s64, s64, 1\n\ts_add_i32 s63, s63, 1\n\ts_bitset1_b32 s78, 28\n\t" \
  R3_FLIP R3_STEP("v_subrev_u32", "v_subrev_u32") \
  "s_sub_i32 s62, s62, 1\n\t" \
  "s_memtime vcc\n\ts_waitcnt lgkmcnt(0)\n\t" \
  "s_sub_i32 s70, s69, s68\n\ts_cmp_eq_u32 s70, s49\n\ts_cselect_b32 s70, 256, 0\n\ts_sub_i32 s69, s69, s70\n\t"       \
  "53:\n\ts_add_i32 s68, s68, 93*26\n\ts_add_i32 s69, s69, 93*26\n\ts_add_i32 s65, s65, 26\n\t" \
  "s_sub_i32 s70, s69, vcc_lo\n\ts_cmp_lt_i32 s70, 0\n\ts_cbranch_scc1 53b\n\t" \
  R3_PREP R3_POLL2 \
  "s_branch 15b\n\t" \
    \
  "60:\n\ts_bitset1_b32 s78, 29\n\ts_sub_i32 s64, s64, 1\n\ts_cmp_gt_i32 s64, 0\n\ts_cbranch_scc0 14b\n\t" \
  "s_add_i32 s65, s65, 26\n\t" R3_PREP "s_branch 10b\n\t" \
  OUTOFLINE \
  "98:\n\t" R3_Y_B \
  "global_store_dwordx2 %[yo], v[164:165], s[60:61] sc1\n\t" \
  "s_waitcnt vmcnt(0)\n\ts_barrier\n\t"
#define R3_FIN \
  "ds_read_b128 v[200:203], %[redr]\n\tds_read_b128 v[204:207], %[redr] offset:512\n\t" \
  "ds_read_b128 v[208:211], %[redr] offset:1024\n\tds_read_b128 v[212:215], %[redr] offset:1536\n\t" \
  "s_waitcnt lgkmcnt(2)\n\t" \
  "v_pk_add_f32 v[200:201], v[200:201], v[204:205]\n\tv_pk_add_f32 v[202:203], v[202:203], v[206:207]\n\t" \
  "s_waitcnt lgkmcnt(1)\n\t" \
  "v_pk_add_f32 v[200:201], v[200:201], v[208:209]\n\tv_pk_add_f32 v[202:203], v[202:203], v[210:211]\n\t" \
  "s_waitcnt lgkmcnt(0)\n\t" \
  "v_pk_add_f32 v[200:201], v[200:201], v[212:213]\n\tv_pk_add_f32 v[202:203], v[202:203], v[214:215]\n\t" \
  "s_nop 1\n\t" \
  "v_permlane32_swap_b32 v200, v202\n\tv_permlane32_swap_b32 v201, v203\n\t" \
  "v_add_f32 v200, v200, v202\n\tv_add_f32 v201, v201, v203\n\t" \
  "v_cvt_pk_bf16_f32 v202, v200, v201\n\t" \
  "v_cmpx_o_f32 vcc, v200, v201\n\t" \
  "global_store_dword %[sto], v202, s[60:61] sc1\n\t" ST(94) \
  "s_memtime s[74:75]\n\t" \
  "s_mov_b64 exec, -1\n\t" \
  "v_mov_b32 v200, -1\n\tv_mov_b32 v201, -1\n\tv_mov_b32 v202, -1\n\tv_mov_b32 v203, -1\n\t" \
  "ds_write_b128 %[redr], v[200:203]\n\tds_write_b128 %[redr], v[200:203] offset:512\n\t" \
  "ds_write_b128 %[redr], v[200:203] offset:1024\n\tds_write_b128 %[redr], v[200:203] offset:1536\n\t" \
  "s_waitcnt lgkmcnt(0)\n\ts_sub_u32 s70, s74, s69\n\tv_mov_b32 v200, s70\n\tds_write_b32 v188, v200 offset:12308\n\t" \
  "s_cmp_eq_u64 vcc, -1\n\ts_cbranch_scc0 80f\n\t" \
  "16:\n\t"
#define R3_FIN_OOL \
  "80:\n\ts_cmp_gt_i32 s64, 0\n\ts_cbranch_scc0 81f\n\t" \
  "v_mov_b32 v200, s63\n\tds_write_b32 v188, v200 offset:12288\n\ts_branch 16b\n\t" \
  "81:\n\tv_mov_b32 v200, 0x7fc07fc0\n\tglobal_store_dword %[sto], v200, s[60:61] sc1\n\ts_branch 16b\n\t"
#define R3_CNT_PAY "ds_read_b32 v174, v188 offset:12300\n\tds_read_b32 v175, v188 offset:12308\n\t"
#define R3_CNT_PACK \
  "v_min_u32 v175, 0xfff, v175\n\tv_min_u32 v174, 0xffff, v174\n\tv_lshl_or_b32 v176, v174, 12, v175\n\tv_or_b32 v176, s78, v176\n\ts_mov_b32 s78, 0\n\t"
#define R3_CNT_SIG "global_store_dword v187, v176, s[60:61] sc1\n\t"
#define R3_CNT_LOAD "v_mov_b32 v187, s43\n\ts_mov_b64 s[38:39], s[40:41]\n\tv_cndmask_b32 v144, %[uo], v186, s[40:41]\n\tglobal_load_dword v145, v144, s[60:61] sc1\n\t"
#define R3_CNT_CHK "v_readfirstlane_b32 s43, %[tm]\n\ts_and_b32 s43, s43, 0xffffe000\n\ts_xor_b32 s43, s43, s42\n\t" \
  "v_cndmask_b32 v189, v189, v145, s[38:39]\n\ts_mov_b64 s[38:39], 0\n\t" \
  "s_add_i32 s70, s62, 3\n\ts_lshr_b32 s70, s70, 1\n\tv_cmp_gt_u32 vcc, s70, v189\n\ts_cbranch_vccnz 85f\n\t17:\n\t" \
  "s_add_i32 s70, s70, 1\n\ts_min_u32 s70, s70, 32\n\tv_cmp_gt_u32 vcc, s70, v189\n\ts_cmp_lg_u64 vcc, 0\n\ts_cselect_b64 s[40:41], -1, 0\n\t"
#define R3_CNT_OOL        \
  "85:\n\ts_mov_b32 s71, 0x40000\n\t" \
  "86:\n\tglobal_load_dword v189, v186, s[60:61] sc1\n\ts_waitcnt vmcnt(0)\n\t" \
  "v_cmp_gt_u32 vcc, s70, v189\n\ts_cbranch_vccz 17b\n\ts_sleep 2\n\ts_sub_i32 s71, s71, 1\n\ts_cmp_gt_i32 s71, 0\n\ts_cbranch_scc1 86b\n\ts_branch 17b\n\t"
#define DIAG_R3
#define DIAG_R3_END
#define DIAG_R3_OUT
#define R3_OUTS [moff] "+v"(moff), [to] "+v"(to), [tm] "+v"(tm), [sto] "+v"(sto), [uo] "+v"(uo), [yo] "+v"(yo) DIAG_R3_OUT
#define R3_INS R1_WOPS, R1_WOPS2, [idx] "v"(idx), [rd] "v"(rd), [redw] "v"(redw), [selA] "v"(selA), [wsp] "s"(ws), [p0] "s"(p0), [p1] "s"(p1)
#define R3_CLOB CLOB_V200_247, "v144","v145","v146","v147","v148","v149","v150","v151", "v160","v161","v162","v163","v164","v165","v166","v167","v168","v169","v170","v171","v172","v173","v174","v175","v176","v177","v178", "v179","v180","v181","v182","v183","s96","s97","v184","v185","v186","v187","v188","v189","v190","v191","v192","v193","v194","v195","v196","v197","v198","v199", \
  "s38","s39","s40","s41","s42","s43","s44","s45","s46","s47","s48","s49","s50","s51","s60","s61","s62","s63","s64","s65","s66","s67","s68","s69","s70","s71", \
  "s72","s73","s74","s75","s76","s77","s78","vcc","scc","memory" ST_CLOB
#define STR2(x) #x
#define STR(x) STR2(x)

template <int KIND>
DEV void r3_body(unsigned char* __restrict__ ws, int v, int wave, int lane, unsigned sel TUNE_ARG) {
  const int g = lane >> 4, hs = (lane >> 3) & 1, i8 = lane & 7;
  const int idx = (lane & 8) ? 0xEEEE : 0x4444;
  const int dw = wave < 7 ? wave : 6;
  const unsigned rd = lane * 16;
  u8v w0[7], w1[7];
  {
    const unsigned char* p = ws + O_WB + (size_t)(2 * wave * 28 + v) * 14336 + lane * 16;
#pragma unroll
    for (int k = 0; k < 7; ++k) {
      w0[k] = cat8(*(const u4v*)(p + k * 2048), *(const u4v*)(p + k * 2048 + 1024));
      w1[k] = cat8(*(const u4v*)(p + 28 * 14336 + k * 2048), *(const u4v*)(p + 28 * 14336 + k * 2048 + 1024));
    }
#pragma unroll
    for (int k = 0; k < 7; ++k) asm volatile("" : "+v"(w0[k]), "+v"(w1[k]));
  }
  RTS(1);
  if (KIND == 3 && lane == 0) lds_w4(L_FLAG, SENT);
  if (KIND == 2) wait_cnt(ws, doneU(ws, 4 * (v >> 1) + (lane & 3)), 1);
  *(LDS u2v*)(size_t)(L_RED + wave * 512 + lane * 8) = (u2v){SENT, SENT};
  RTS(2);
  unsigned moff = hoff(sel, 0, 0) + dw * 1024 + g * 256 + i8 * 32 + hs * 16;
  unsigned to = hoff(sel, 0, 2) + (2 * wave + hs) * 512 + (g >> 1) * 256 + i8 * 32 + (g & 1) * 16;
  unsigned tm = hoff(sel, 64, K_SMK) + (2 * wave + hs) * 512 + g * 128 + i8 * 16;
  u4v selA = {0u, 0u, 0u, 0u};
  if (((lane & 3) >> 1) == 0) {
    const int gq = (lane >> 2) & 3, j = 4 * (gq & 1) + (gq >> 1) + 2 * (lane & 1);
    const unsigned one = (j & 1) ? 0x3f800000u : 0x00003f80u;
    selA = (u4v){(j >> 1) == 0 ? one : 0u, (j >> 1) == 1 ? one : 0u, (j >> 1) == 2 ? one : 0u, (j >> 1) == 3 ? one : 0u};
  }
  unsigned sto = hoff(sel, 1, 0) + v * 256 + i8 * 32 + ((lane >> 3) & 3) * 8 + (lane >> 5) * 4;
  unsigned uo = (unsigned)O_U + v * U_RUN + 2 * wave * 1024 + lane * 16;
  unsigned yo = (unsigned)O_YG + v * Y_RUN + wave * 512 + lane * 8;
  const unsigned redw = L_RED + hs * 4096 + wave * 512 + (g * 8 + i8) * 16;
  const unsigned redr = L_RED + (lane >> 5) * 2048 + (lane & 31) * 16;
  const unsigned p1 = dw;
  {
    const unsigned o = lane < 8 ? yo : lane < 16 ? yo + 63 * 4096 : lane < 24 ? uo : lane < 32 ? uo + 63 * 16384 : lane < 40 ? hoff(sel, 70, 0) : lane < 48 ? hoff(sel, 1, 0) + 7168 : (unsigned)O_FL + 0x3ff00;
    (void)ld_cnt((const unsigned*)(ws + (o & ~3u)));
  }
  int anchor = 0;
  unsigned p0 = 0;
  if (KIND == 3) asm volatile(R3_WARM(".Lr3tw_beg", ".Lr3tw_end-.Lr3tw_beg") :: [rd] "v"(rd) : "v200", "v201", "v202", "vcc", "scc", "memory");
  if (KIND == 3) { wait_cnt(ws, ptp(ws, 0), 1); p0 = __builtin_amdgcn_readfirstlane(ld_cnt(ptp(ws, 0))); }
  bar();
  if (KIND == 3) anchor = get_anchor(ws, sel, 64 + v, lane);
  if (KIND == 0) {
    asm volatile(R3_LOOP(R3_WARM("10f", "98f-10f"), R3_DMA, "", "", "", "", "", 2, "", 1, "", R3_FIN, "", R3_FIN_OOL) DIAG_R3_END : R3_OUTS : R3_INS, [redr] "v"(redr) : R3_CLOB);
  } else if (KIND == 1) {
    asm volatile(R3_LOOP(R3_WARM("10f", "98f-10f"), R3_DMA, "", "", "", "", "", 2, "", 1, "", "", "", "") DIAG_R3_END : R3_OUTS : R3_INS : R3_CLOB);
  } else if (KIND == 2) {
    const unsigned cc = ((unsigned)(3 - K_SMK) << 13) | (unsigned)(v * 128);
    const unsigned uc = (unsigned)O_FL + (C_W + 2 * (56 + 4 * (v >> 1) + (lane & 3))) * 64;
    asm volatile("s_mov_b64 s[40:41], -1\n\ts_mov_b32 s42, %[cc]\n\ts_and_b32 s43, s42, 0x1fff\n\ts_add_i32 s43, s43, 0x43e000\n\tv_mov_b32 v186, %[uc]\n\t"
                 R3_LOOP(R3_WARM("10f", "98f-10f"), R3_DMA, "", R3_CNT_LOAD, R3_CNT_PAY, R3_CNT_PACK, "", 3, R3_CNT_SIG, 2, "", "", R3_CNT_CHK, R3_CNT_OOL) DIAG_R3_END : R3_OUTS : R3_INS, [cc] "s"(cc), [uc] "v"(uc) : R3_CLOB);
  } else {
    asm volatile("s_mov_b32 s67, %[anc]\n\t" R3_LOOP(".Lr3tw_beg:\n\t", "", R3_TW_WAIT, R3_TW_PT, "", "", R3_TW_TIME, 3, "", 1, R3_FWD R3_PREP, "", R3_TW_NEXT, R3_TW_LATE R3_TW_OOL ".Lr3tw_end:\n\t") DIAG_R3_END : R3_OUTS : R3_INS, [anc] "s"(anchor) : R3_CLOB, TBURST_CLOB);
  }
  if (KIND == 2 && lane < 2) st4c_asm(flagY(ws, sel, NSEG - 2 + lane, v), 0u);
  RTS(3);
}
DEV void role_r3(unsigned char* __restrict__ ws, int v, int wave, int lane, unsigned sel TUNE_ARG) {
  if (wave == 0) r3_body<0>(ws, v, wave, lane, sel TUNE_PASS);
  else if (wave == 6) r3_body<2>(ws, v, wave, lane, sel TUNE_PASS);
  else if (wave == 7) r3_body<3>(ws, v, wave, lane, sel TUNE_PASS);
  else r3_body<1>(ws, v, wave, lane, sel TUNE_PASS);
}

#define PF_LAG 4
DEV void role_pf(unsigned char* __restrict__ ws, int k, int wave, int lane, unsigned sel) {
  constexpr int LAG = PF_LAG;
  const rsrc_t ru = mkrsrc(ws + O_U, 30u << 20);
  unsigned have = 0;
  for (int t = 1; t < NSEG - 1; ++t) {
    if (wave == 0) {
      unsigned skip = 0;
      const unsigned need = (t >> 1) + 1;
      if (__any(have < need)) { wait_cnt(ws, doneU(ws, min(lane, 55)), need); have = ld_cnt(doneU(ws, min(lane, 55))); }
      if (t >= LAG) wait_flags(ws, flagY(ws, sel, t - LAG, k));
      else if (t >= LAG - 2) {
        wait_cnt(ws, anchorp(ws, 28 + k), 1);
        const unsigned at = (__builtin_amdgcn_readfirstlane(ld_cnt(anchorp(ws, 28 + k))) & ~1u) + (t - LAG + 2) * 264;
        skip = (int)(((unsigned)rt32() << 1) - at) > 40;
        for (int it = 0; it < 4096 && (int)(at - ((unsigned)rt32() << 1)) > 0; ++it) __builtin_amdgcn_s_sleep(1);
      }
      lds_w4((t & 1) * 4, skip);
    }
    bar();
    if (__builtin_amdgcn_readfirstlane(lds_r4((t & 1) * 4))) continue;
    u4v r[14];
#pragma unroll
    for (int i = 0; i < 14; ++i) { const int n = (k * 8 + wave) * 14 + i; r[i] = ldc16(ru, (n >> 4) * U_RUN + (t * 16 + (n & 15)) * 1024 + lane * 16); }
#pragma unroll
    for (int i = 0; i < 14; ++i) asm volatile("" :: "v"(r[i]));
  }
}

__global__ void __launch_bounds__(512, 2)
main_kernel(const unsigned short* __restrict__ x, const unsigned short* __restrict__ b1, const unsigned short* __restrict__ b2,
            unsigned short* __restrict__ out, unsigned char* __restrict__ ws TUNE_ARG) {
  const int b = blockIdx.x, lane = threadIdx.x & 63;
  const int wave = __builtin_amdgcn_readfirstlane(threadIdx.x >> 6);
  RTS(0);
  const int xc = b & 7, slot = b >> 3;
  if (xc == 0 || xc == 2) {
    const unsigned sel = *(const unsigned*)(ws + O_SEL);
    role_r1(ws, (xc >> 1) * 32 + slot, wave, lane, sel TUNE_PASS);
  } else if (xc == 1) {
    const unsigned sel = *(const unsigned*)(ws + O_SEL);
    if (slot < 28) role_r3(ws, slot, wave, lane, sel TUNE_PASS);
    else role_pf(ws, slot - 28, wave, lane, sel);
  } else {
    const int k = slot * 5 + (xc - 3);
    if (k < 56) role_w1<true>(ws, k, wave, lane);
    else if (k < 112) role_w1<false>(ws, k - 56, wave, lane);
    else {
      if (k < 144) role_ffn(ws, x, b1, b2, k - 112, wave, lane);
      else role_stt(ws, (k - 144) >> 1, k & 1, wave, lane);
      role_w3(ws, out, (159 - k) >> 2, k & 3, wave, lane);
    }
  }
}

extern "C" void kernel_launch(void* const* d_in, const int* in_sizes, int n_in, void* d_out, int out_size, void* d_ws,
                              size_t ws_size, hipStream_t stream) {
  unsigned char* ws = (unsigned char*)(((uintptr_t)d_ws + 2 * MB - 1) & ~(uintptr_t)(2 * MB - 1));
  const unsigned short* x = (const unsigned short*)d_in[0];
  constexpr int LDS_MAIN = 132 * 1024;
  (void)hipFuncSetAttribute((const void*)main_kernel, hipFuncAttributeMaxDynamicSharedMemorySize, LDS_MAIN);
  constexpr int LDS_PREP = 152 * 1024;
  (void)hipFuncSetAttribute((const void*)prep_kernel, hipFuncAttributeMaxDynamicSharedMemorySize, LDS_PREP);
  prep_kernel<<<256, 512, LDS_PREP, stream>>>(x, (const unsigned short*)d_in[1], (const unsigned short*)d_in[2], (const unsigned short*)d_in[3],
                                           (const unsigned short*)d_in[4], (const unsigned short*)d_in[5], (const unsigned short*)d_in[8],
                                           (const unsigned short*)d_in[10], ws);
  main_kernel<<<256, 512, LDS_MAIN, stream>>>(x, (const unsigned short*)d_in[9], (const unsigned short*)d_in[11], (unsigned short*)d_out, ws TUNE_PASS);
}
```

```cpp
#include <hip/hip_runtime.h>
#include <hip/hip_bf16.h>
#include <stdint.h>

typedef __bf16 bf2 __attribute__((ext_vector_type(2)));
typedef __bf16 bf8 __attribute__((ext_vector_type(8)));
typedef __bf16 bf16x __attribute__((ext_vector_type(16)));
typedef short s4v __attribute__((ext_vector_type(4)));
typedef float f2v __attribute__((ext_vector_type(2)));
typedef float f4v __attribute__((ext_vector_type(4)));
typedef float f16v __attribute__((ext_vector_type(16)));
typedef unsigned u2v __attribute__((ext_vector_type(2)));
typedef unsigned u4v __attribute__((ext_vector_type(4)));
typedef unsigned u8v __attribute__((ext_vector_type(8)));
#define LDS __attribute__((address_space(3)))
#define GLB __attribute__((address_space(1)))
#define DEV __device__ __forceinline__

constexpr int DM = 448, NH = 16, DH = 256, NSEG = 64, FF = 1792;
constexpr unsigned SENT = 0xFFFFFFFFu;

constexpr size_t MB = 1u << 20;
constexpr size_t O_HB = 0;
constexpr size_t O_FL = 4 * MB;
constexpr size_t O_SEL = O_FL + 256 * 1024;
constexpr size_t O_XN = 5 * MB;
constexpr size_t O_WA = 7 * MB;
constexpr size_t O_WB = 14 * MB;
constexpr size_t O_W1 = 21 * MB;
constexpr size_t O_W2 = 23 * MB;
constexpr size_t O_G = 25 * MB;
constexpr size_t O_U = 55 * MB;
constexpr size_t O_ST = 85 * MB;
constexpr size_t O_SM = 87 * MB;
constexpr size_t O_YG = 100 * MB;
constexpr size_t O_F = 111 * MB;
constexpr unsigned G_RUN = 64 * 14336 + 3 * 4096, U_RUN = 64 * 16384 + 5 * 4096, Y_RUN = 64 * 4096 + 4096;
constexpr size_t O_END = 115 * MB;

DEV unsigned pk2(float a, float b) { f2v v = {a, b}; return __builtin_bit_cast(unsigned, __builtin_convertvector(v, bf2)); }
DEV float bflo(unsigned u) { return __builtin_bit_cast(float, u << 16); }
DEV float bfhi(unsigned u) { return __builtin_bit_cast(float, u & 0xffff0000u); }
DEV float rbf(float a) { return bflo(pk2(a, 0.f) & 0xffffu); }
DEV bf8 asbf8(u4v v) { return __builtin_bit_cast(bf8, v); }
DEV u4v lds_r16(unsigned a) { return *(LDS u4v*)(size_t)a; }
DEV void lds_w16(unsigned a, u4v v) { *(LDS u4v*)(size_t)a = v; }
DEV u2v lds_tr(unsigned a) { return __builtin_bit_cast(u2v, __builtin_amdgcn_ds_read_tr16_b64_v4i16((LDS s4v*)(size_t)a)); }
DEV unsigned rdtime() { unsigned long long t; asm volatile("s_memtime %0\n\ts_waitcnt lgkmcnt(0)" : "=s"(t) :: "memory"); return (unsigned)t; }
DEV int swap13(int x) { return (x & 5) | ((x >> 2) & 2) | ((x << 2) & 8); }
DEV void dma16(const void* g, unsigned ldsaddr) {
  unsigned keep;
  asm volatile("s_mov_b32 %0, m0\n\ts_mov_b32 m0, %2\n\ts_nop 0\n\tglobal_load_lds_dwordx4 %1, off\n\ts_mov_b32 m0, %0" : "=&s"(keep) : "v"(g), "s"(ldsaddr) : "memory");
}
DEV void dma16c(const void* g, unsigned ldsaddr) {
  unsigned keep;
  asm volatile("s_mov_b32 %0, m0\n\ts_mov_b32 m0, %2\n\ts_nop 0\n\tglobal_load_lds_dwordx4 %1, off sc1\n\ts_mov_b32 m0, %0" : "=&s"(keep) : "v"(g), "s"(ldsaddr) : "memory");
}
DEV void vm0() { asm volatile("s_waitcnt vmcnt(0)" ::: "memory"); }
DEV void bar() { asm volatile("s_waitcnt lgkmcnt(0)\n\ts_barrier" ::: "memory"); }

DEV void stwt16(void* p, u4v v) { asm volatile("global_store_dwordx4 %0, %1, off sc1\n\ts_nop 1" :: "v"(p), "v"(v) : "memory"); }
DEV float wavesum(float v) {
  v += __builtin_bit_cast(float, __builtin_amdgcn_update_dpp(0, __builtin_bit_cast(int, v), 0xb1, 0xf, 0xf, true));
  v += __builtin_bit_cast(float, __builtin_amdgcn_update_dpp(0, __builtin_bit_cast(int, v), 0x4e, 0xf, 0xf, true));
  v += __builtin_bit_cast(float, __builtin_amdgcn_update_dpp(0, __builtin_bit_cast(int, v), 0x124, 0xf, 0xf, true));
  v += __builtin_bit_cast(float, __builtin_amdgcn_update_dpp(0, __builtin_bit_cast(int, v), 0x128, 0xf, 0xf, true));
  const unsigned u = __builtin_bit_cast(unsigned, v);
  const u2v a = __builtin_amdgcn_permlane16_swap(u, u, false, false);
  const unsigned ax = a.x, ay = a.y;
  const unsigned w = __builtin_bit_cast(unsigned, __builtin_bit_cast(float, ax) + __builtin_bit_cast(float, ay));
  const u2v c = __builtin_amdgcn_permlane32_swap(w, w, false, false);
  const unsigned cx = c.x, cy = c.y;
  return __builtin_bit_cast(float, cx) + __builtin_bit_cast(float, cy);
}
#define K0S(k)
DEV void ln_row(u4v xv, unsigned char* __restrict__ ws, int row, int lane) {
  float e[8];
  if (lane >= 56) xv = (u4v){0u, 0u, 0u, 0u};
#pragma unroll
  for (int j = 0; j < 4; ++j) { e[2 * j] = bflo(xv[j]); e[2 * j + 1] = bfhi(xv[j]); }
  float s = 0.f;
#pragma unroll
  for (int j = 0; j < 8; ++j) s += e[j];
  const float mean = wavesum(s) * (1.f / DM);
  float q = 0.f;
  if (lane < 56) {
#pragma unroll
    for (int j = 0; j < 8; ++j) { const float d = e[j] - mean; q += d * d; }
  }
  const float mb = rbf(mean), vb = rbf(wavesum(q) * (1.f / DM));
  const float den = rbf(__builtin_sqrtf(rbf(vb + 1e-5f)));
  unsigned o4[4];
#pragma unroll
  for (int j = 0; j < 4; ++j) o4[j] = pk2(rbf(e[2 * j] - mb) / den, rbf(e[2 * j + 1] - mb) / den);
  const bool odd = lane & 1;
  const unsigned r0 = __builtin_amdgcn_update_dpp(0u, odd ? o4[0] : o4[2], 0xb1, 0xf, 0xf, true);
  const unsigned r1 = __builtin_amdgcn_update_dpp(0u, odd ? o4[1] : o4[3], 0xb1, 0xf, 0xf, true);
  u4v p;
  if (odd) p = (u4v){r0, o4[2], r1, o4[3]}; else p = (u4v){o4[0], r0, o4[1], r1};
  if (lane < 56) stwt16(ws + O_XN + (size_t)(row >> 5) * 28672 + (lane >> 1) * 1024 + (lane & 1) * 512 + (row & 31) * 16, p);
}
DEV u4v srd(const void* p, unsigned bytes) { const unsigned long long a = (unsigned long long)p; return (u4v){(unsigned)a, (unsigned)(a >> 32), bytes, 0x00020000u}; }
DEV void bdma16(unsigned voff, u4v r, unsigned soff, unsigned ldsaddr) {
  asm volatile("s_mov_b32 m0, %3\n\ts_nop 0\n\tbuffer_load_dwordx4 %0, %1, %2 offen lds" :: "v"(voff), "s"(r), "s"(soff), "s"(ldsaddr) : "memory");
}
constexpr unsigned rowoff(int j) { return (16 * ((j >> 2) & 1) + 8 * (j & 1) + 4 * ((j >> 1) & 1) + 2 * (j >> 3)) * 8192u; }

template <bool ISA>
DEV void prep_gemm(const unsigned short* __restrict__ Rw, const unsigned short* __restrict__ Cw, const unsigned short* __restrict__ x,
                   unsigned char* __restrict__ ws, int b, int h, int cg, int wave, int lane) {
  const int g = lane >> 4, c16 = lane & 15, hl = lane >> 5, sl = lane & 31;
  const int row = b * 8 + wave;
  const unsigned xl = 147456 + wave * 1024;
  dma16(x + (size_t)row * DM + min(lane, 55) * 8, xl);
  if (ISA) {
    const u4v rc = srd(Cw + (size_t)64 * cg * 4096 + h * 256, 64 * 8192);
    const unsigned v0 = hl * 8192 + ((sl ^ hl ^ ((wave & 1) << 3)) << 4), s0 = ((wave >> 1) * 16 + (wave & 1) * 2) * 8192;
#pragma unroll
    for (int j = 0; j < 4; ++j) bdma16(v0 ^ (j << 5), rc, s0 + ((j & 1) * 8 + (j >> 1) * 4) * 8192, (wave * 4 + j) * 1024);
  } else {
    const u4v rc = srd(Cw + (size_t)h * 256 * DM + 64 * cg, 256 * 896);
    const unsigned v0 = (lane >> 3) * 896 + (((lane & 7) ^ (((lane >> 4) & 1) << 1)) << 4);
#pragma unroll
    for (int j = 0; j < 4; ++j) bdma16(v0 ^ ((j & 1) << 6), rc, (wave * 4 + j) * 8 * 896, (wave * 4 + j) * 1024);
  }
  if (wave == 7) {
    __builtin_amdgcn_sched_barrier(0);
    asm volatile("s_waitcnt vmcnt(0)\n\ts_barrier" ::: "memory");
    const u4v ones = {SENT, SENT, SENT, SENT}, zero = {0u, 0u, 0u, 0u};
#pragma unroll
    for (int j = 0; j < 16; ++j) if (b >= 2) stwt16(ws + O_HB + (size_t)b * 16384 + j * 1024 + lane * 16, ones);
    stwt16(ws + O_FL + b * 1024 + lane * 16, zero);
    ln_row(lds_r16(xl + lane * 16), ws, row, lane);
    return;
  }
  const float sc = ISA ? 0.0625f * 1.4426950408889634f : 1.f;
  unsigned char* dst = ws + (ISA ? O_WA : O_WB) + (size_t)(h * 28 + 4 * cg) * 14336 + 2 * wave * 1024 + lane * 16;
  const unsigned reg = 32768 + wave * 16384;
  const int rot = cg * 2;
  const unsigned srot = (16 * ((rot >> 2) & 1) + 8 * (rot & 1) + 4 * ((rot >> 1) & 1) + 2 * (rot >> 3)) * 8192u, mrot = reg | (rot << 10);
  const unsigned vr = hl * 8192 + (((sl ^ hl) << 4) ^ ((rot & 7) << 5));
  const u4v rr = srd(Rw + (size_t)(64 * wave) * 4096 + h * 256, 64 * 8192);
  const __amdgpu_buffer_rsrc_t rq = __builtin_amdgcn_make_buffer_rsrc((void*)(Rw + (size_t)(64 * wave + 32) * 4096 + h * 256), 0, 32 * 8192, 0x00020000);
  u4v nx[16];
#pragma unroll
  for (int j = 0; j < 2; ++j) bdma16(vr ^ ((j & 7) << 5), rr, srot ^ rowoff(j), mrot ^ (j << 10));
#pragma unroll
  for (int j = 0; j < 2; ++j) nx[j] = __builtin_amdgcn_raw_buffer_load_b128(rq, vr ^ ((j & 7) << 5), srot ^ rowoff(j), 0);
  __builtin_amdgcn_sched_barrier(0);
  asm volatile("s_waitcnt vmcnt(8)" ::: "memory");
  ln_row(lds_r16(xl + lane * 16), ws, row, lane);
  __builtin_amdgcn_sched_barrier(0);
  K0S(1);
  asm volatile("s_waitcnt vmcnt(5)\n\ts_barrier" ::: "memory");
  K0S(0);
#pragma unroll
  for (int j = 2; j < 16; ++j) bdma16(vr ^ ((j & 7) << 5), rr, srot ^ rowoff(j), mrot ^ (j << 10));
#pragma unroll
  for (int j = 2; j < 16; ++j) nx[j] = __builtin_amdgcn_raw_buffer_load_b128(rq, vr ^ ((j & 7) << 5), srot ^ rowoff(j), 0);
  __builtin_amdgcn_sched_barrier(0);
  asm volatile("s_waitcnt vmcnt(14)" ::: "memory");
  K0S(2);
  const int q = c16 >> 2, p = c16 & 3;
  const int xr = (q >> 1) | ((g & 1) << 1);
  auto bfrag = [&](int es, int ti) -> u4v {
    if (ISA) return lds_r16((ti * 16 + c16) * 512 + (((es * 4 + g) ^ c16) << 4));
    const unsigned ad = (32 * es + 8 * g + q) * 128 + ((ti ^ xr) << 5) + p * 8;
    const u2v lo = lds_tr(ad), hi = lds_tr(ad + 512);
    return (u4v){lo.x, lo.y, hi.x, hi.y};
  };
  auto afrag = [&](int es, int t) -> u4v { return lds_r16(reg + (t * 16 + c16) * 512 + (((es * 4 + g) ^ c16) << 4)); };
#pragma unroll
  for (int rb = 0; rb < 2; ++rb) {
    u4v bb[2][4], aa[2][2];
#pragma unroll
    for (int ti = 0; ti < 4; ++ti) bb[0][ti] = bfrag(0, ti);
    aa[0][0] = afrag(0, 0); aa[0][1] = afrag(0, 1);
    f4v acc[2][4];
#pragma unroll
    for (int t = 0; t < 2; ++t)
#pragma unroll
      for (int ti = 0; ti < 4; ++ti) acc[t][ti] = (f4v){0.f, 0.f, 0.f, 0.f};
#pragma unroll
    for (int es = 0; es < 8; ++es) {
      if (es < 7) {
        aa[(es + 1) & 1][0] = afrag(es + 1, 0); aa[(es + 1) & 1][1] = afrag(es + 1, 1);
#pragma unroll
        for (int ti = 0; ti < 4; ++ti) bb[(es + 1) & 1][ti] = bfrag(es + 1, ti);
      }
#pragma unroll
      for (int ti = 0; ti < 4; ++ti)
#pragma unroll
        for (int t = 0; t < 2; ++t)
          acc[t][ti] = __builtin_amdgcn_mfma_f32_16x16x32_bf16(asbf8(aa[es & 1][t]), asbf8(bb[es & 1][ti]), acc[t][ti], 0, 0, 0);
      __builtin_amdgcn_sched_barrier(0);
    }
    if (rb == 0) {
#pragma unroll
      for (int j = 0; j < 16; ++j) lds_w16((mrot ^ (j << 10)) + lane * 16, nx[j]);
    }
#pragma unroll
    for (int ti = 0; ti < 4; ++ti) {
      const u4v o = {pk2(acc[0][ti][0] * sc, acc[0][ti][1] * sc), pk2(acc[0][ti][2] * sc, acc[0][ti][3] * sc),
                     pk2(acc[1][ti][0] * sc, acc[1][ti][1] * sc), pk2(acc[1][ti][2] * sc, acc[1][ti][3] * sc)};
      stwt16(dst + (size_t)ti * 14336 + rb * 1024, o);
    }
  }
  K0S(3);
}

__global__ void __launch_bounds__(512, 2)
prep_kernel(const unsigned short* __restrict__ x, const unsigned short* __restrict__ mem0,
            const unsigned short* __restrict__ Wq, const unsigned short* __restrict__ Wk,
            const unsigned short* __restrict__ Wv, const unsigned short* __restrict__ Wo,
            const unsigned short* __restrict__ w1, const unsigned short* __restrict__ w2,
            unsigned char* __restrict__ ws) {
  const int b = blockIdx.x, tid = threadIdx.x, lane = tid & 63;
  const int wave = __builtin_amdgcn_readfirstlane(tid >> 6);
  const int xc = b & 7, slot = b >> 3;
  if (slot < 28) {
    const int m = xc * 4 + slot / 7, cg = slot % 7;
    if (m < 16) prep_gemm<true>(Wk, Wq, x, ws, b, m, cg, wave, lane);
    else prep_gemm<false>(Wv, Wo, x, ws, b, m - 16, cg, wave, lane);
  } else {
    const int row = b * 8 + wave;
    u4v xv = {0u, 0u, 0u, 0u};
    const int misc = (slot - 28) * 8 + xc;
    const int job = (misc - 1) * 8 + wave;
    const unsigned reg = wave * 14336;
    if (misc == 0) {
      if (wave == 7) {
        u4v m0[7];
#pragma unroll
        for (int j = 0; j < 7; ++j) { const int n = j * 64 + lane; m0[j] = *(const u4v*)(mem0 + ((n >> 1) & 7) * DM + (n >> 4) * 16 + (n & 1) * 8); }
        const u4v ones = {SENT, SENT, SENT, SENT};
        stwt16(ws + O_HB + (lane >> 4) * 8192 + (lane & 15) * 64, ones);
        stwt16(ws + O_HB + 2 * MB + (lane >> 4) * 8192 + (lane & 15) * 64, ones);
#pragma unroll
        for (int j = 0; j < 7; ++j) asm volatile("" : "+v"(m0[j]));
        vm0();
        auto timed = [&](const unsigned char* pc) -> unsigned {
          unsigned v;
          const unsigned c0 = rdtime();
          asm volatile("global_load_dword %0, %1, %2 sc1\n\ts_waitcnt vmcnt(0)" : "=&v"(v) : "v"(0u), "s"(pc) : "memory");
          return rdtime() - c0;
        };
        unsigned best[6];
#pragma unroll
        for (int c = 0; c < 6; ++c) best[c] = 0x7fffffff;
#pragma unroll 1
        for (int it = 0; it < 3; ++it) {
#pragma unroll
          for (int c = 0; c < 4; ++c) best[c] = min(best[c], timed(ws + O_HB + c * 8192 + it * 256));
        }
        unsigned sel = 0, bv = best[0];
#pragma unroll
        for (unsigned k = 1; k < 4; ++k) if (best[k] < bv) { bv = best[k]; sel = k; }
#pragma unroll 1
        for (int it = 0; it < 2; ++it) {
#pragma unroll
          for (int c = 0; c < 2; ++c) best[4 + c] = min(best[4 + c], timed(ws + O_HB + 2 * MB + (sel ^ c) * 8192 + it * 256));
        }
        sel |= (sel ^ (best[5] <= best[4] ? 1u : 0u)) << 2;
        if (lane == 0) *(unsigned*)(ws + O_SEL) = sel;
#pragma unroll
        for (int j = 0; j < 7; ++j) stwt16(ws + O_HB + (sel & 3u) * 8192 + j * 1024 + lane * 16, m0[j]);
#pragma unroll
        for (int j = 8; j < 32; ++j) stwt16(ws + O_HB + (((sel & 3u) * 8192) ^ (j * 1024)) + lane * 16, ones);
      }
    } else if (job < 224) {
      const bool first = job < 112;
      const int step = first ? job >> 2 : job - 112;
      const unsigned short* src = first ? w1 + (size_t)step * 16 * FF + (job & 3) * 448 : w2 + (size_t)step * 16 * DM;
      const int ld = first ? FF : DM;
#pragma unroll
      for (int j = 0; j < 14; ++j) { const int n = j * 64 + lane, r = n / 56; dma16(src + (size_t)r * ld + (n - r * 56) * 8, reg + j * 1024); }
    }
    if (lane < 56) xv = *(const u4v*)(x + (size_t)row * DM + lane * 8);
    asm volatile("" : "+v"(xv));
    vm0();
    if (misc > 0 && job < 224) {
      const bool first = job < 112;
      const int hi = lane >> 5, q = (lane & 15) >> 2;
      const int r0 = first ? 4 * hi + 8 * (q >> 1) + (q & 1) : 4 * hi + q, dr = first ? 2 : 8;
      const unsigned ad = reg + r0 * 896 + ((lane >> 4) & 1) * 32 + (lane & 3) * 8;
      unsigned char* dst = ws + (first ? O_W1 + (size_t)((job & 3) * 14 * 28 + (job >> 2)) * 1024 : O_W2 + (size_t)(job - 112) * 1024) + lane * 16;
      const size_t dstep = first ? 28672 : 114688;
#pragma unroll
      for (int t = 0; t < 14; ++t) {
        const u2v lo = lds_tr(ad + t * 64), hv = lds_tr(ad + t * 64 + dr * 896);
        stwt16(dst + t * dstep, (u4v){lo.x, lo.y, hv.x, hv.y});
      }
    }
    const u4v ones = {SENT, SENT, SENT, SENT}, zero = {0u, 0u, 0u, 0u};
    stwt16(ws + O_HB + (size_t)b * 16384 + tid * 16, ones);
    stwt16(ws + O_HB + (size_t)b * 16384 + 8192 + tid * 16, ones);
    if (wave == 0) stwt16(ws + O_FL + b * 1024 + lane * 16, zero);
    ln_row(xv, ws, row, lane);
  }
}
constexpr int C_W = 0  , C_F = 1856, C_ABORT = 1900, C_ANCHOR = 1920;
typedef __amdgpu_buffer_rsrc_t rsrc_t;
DEV rsrc_t mkrsrc(const void* p, unsigned bytes) { return __builtin_amdgcn_make_buffer_rsrc((void*)p, 0, bytes, 0x00020000); }
DEV u4v ldc16(rsrc_t r, unsigned off) { return __builtin_amdgcn_raw_buffer_load_b128(r, off, 0, 16); }
DEV u2v ldc8(rsrc_t r, unsigned off) { return __builtin_amdgcn_raw_buffer_load_b64(r, off, 0, 16); }
DEV void stc16(rsrc_t r, unsigned off, u4v v) { __builtin_amdgcn_raw_buffer_store_b128(v, r, off, 0, 16); }
DEV void stc4(rsrc_t r, unsigned off, unsigned v) { __builtin_amdgcn_raw_buffer_store_b32(v, r, off, 0, 16); }
DEV unsigned* cntp(unsigned char* ws, int idx) { return (unsigned*)(ws + O_FL) + idx * 16; }
DEV unsigned ld_cnt(const unsigned* p) { unsigned v; asm volatile("global_load_dword %0, %1, off sc1\n\ts_waitcnt vmcnt(0)" : "=&v"(v) : "v"(p) : "memory"); return v; }
DEV void add_cnt(unsigned* p, int lane) {
  if (lane == 0) { const unsigned one = 1; asm volatile("global_atomic_add %0, %1, off sc1" :: "v"(p), "v"(one) : "memory"); }
}
DEV void post(unsigned* p, unsigned v, int lane) { if (lane == 0) asm volatile("global_store_dword %0, %1, off sc1" :: "v"(p), "v"(v) : "memory"); }
DEV unsigned* doneG(unsigned char* ws, int j) { return cntp(ws, C_W + 2 * j); }
DEV unsigned* doneU(unsigned char* ws, int j) { return cntp(ws, C_W + 2 * (56 + j)); }
constexpr int SPIN_CAP = 1 << 17;
DEV void wait_cnt(unsigned char* ws, const unsigned* p, unsigned target, int nap = 6) {
  for (int it = 0;; ++it) {
    const unsigned v = ld_cnt(p);
    if (__all(v >= target)) return;
    if ((it & 63) == 63) {
      if (it >= SPIN_CAP) { const unsigned one = 1; asm volatile("global_store_dword %0, %1, off sc1" :: "v"(cntp(ws, C_ABORT)), "v"(one) : "memory"); return; }
      if (__any(ld_cnt(cntp(ws, C_ABORT)) != 0)) return;
    }
    if (nap > 6) __builtin_amdgcn_s_sleep(32); else __builtin_amdgcn_s_sleep(2);
  }
}
DEV unsigned hoff(unsigned sel, int gi, int kind) {
  const unsigned k = ((sel >> (2 * (gi >> 6))) & 3u) ^ ((((unsigned)gi >> 4) & 1u) << 1) ^ (unsigned)kind;
  return (unsigned)O_HB + (unsigned)gi * 32768u + k * 8192u;
}
DEV unsigned char* hblk(unsigned char* ws, unsigned sel, int gi, int kind) { return ws + hoff(sel, gi, kind); }
DEV unsigned char* flagS(unsigned char* ws, unsigned sel, int s, int id) { return hblk(ws, sel, s, 3) + id * 128; }
DEV unsigned char* flagY(unsigned char* ws, unsigned sel, int s, int v) { return hblk(ws, sel, 64 + s, 3) + v * 128; }
DEV unsigned char* flagT(unsigned char* ws, unsigned sel, int s, int h) { return hblk(ws, sel, 64 + s, 1) + h * 128; }
DEV void wait_flags(unsigned char* ws, const unsigned char* p) {
  for (int it = 0;; ++it) {
    if (!__any(ld_cnt((const unsigned*)p) == SENT)) return;
    if ((it & 63) == 63) {
      if (it >= SPIN_CAP) { const unsigned one = 1; asm volatile("global_store_dword %0, %1, off sc1" :: "v"(cntp(ws, C_ABORT)), "v"(one) : "memory"); return; }
      if (__any(ld_cnt(cntp(ws, C_ABORT)) != 0)) return;
    }
    __builtin_amdgcn_s_sleep(6);
  }
}
DEV f16v mfma32(u4v a, u4v b, f16v c) { return __builtin_amdgcn_mfma_f32_32x32x16_bf16(asbf8(a), asbf8(b), c, 0, 0, 0); }
DEV f4v mfma16(u4v a, u4v b, f4v c) { return __builtin_amdgcn_mfma_f32_16x16x32_bf16(asbf8(a), asbf8(b), c, 0, 0, 0); }
DEV void st16c_asm(void* p, u4v v) { asm volatile("global_store_dwordx4 %0, %1, off sc1\n\ts_nop 1" :: "v"(p), "v"(v) : "memory"); }

#define RTS(k)
template <bool ISG>
DEV void role_w1(unsigned char* __restrict__ ws, int j, int wave, int lane) {
  const int ct = wave & 3, rh = wave >> 2, hi = lane >> 5, c32 = lane & 31;
  int h, tile0;
  if (ISG) { const int hp = j / 7, ctile = (j % 7) * 4 + ct; h = 2 * hp + ctile / 14; tile0 = 2 * (ctile % 14); }
  else { const int V = j >> 2; h = 4 * (j & 3) + ct; tile0 = 2 * V; }
  const unsigned char* xn = ws + O_XN + wave * 7168 + lane * 16;
#pragma unroll
  for (int i = 0; i < 7; ++i) dma16(xn + i * 1024, wave * 7168 + i * 1024);
  u4v w[28];
  {
    const unsigned char* p = ws + (ISG ? O_WA : O_WB) + (size_t)(h * 28 + tile0 + (c32 >> 4)) * 14336 + hi * 256 + (c32 & 15) * 16;
#pragma unroll
    for (int s = 0; s < 28; ++s) w[s] = *(const u4v*)(p + (s >> 1) * 1024 + (s & 1) * 512);
  }
  constexpr int SEGB = ISG ? 14336 : 16384;
  unsigned char* out = (ISG ? ws + O_G + (size_t)(h * 2 + (c32 >> 4)) * G_RUN + (tile0 >> 1) * 1024
                            : ws + O_U + (size_t)(tile0 + (c32 >> 4)) * U_RUN + h * 1024) + rh * SEGB + hi * 256 + (c32 & 15) * 16;
  const int tok = ISG ? c32 : ((c32 & 19) | ((c32 >> 1) & 4) | ((c32 << 1) & 8));
  const unsigned rd = rh * 28672 + hi * 512 + tok * 16;
  unsigned* done = ISG ? doneG(ws, j) : doneU(ws, j);
#pragma unroll
  for (int s = 0; s < 28; ++s) asm volatile("" : "+v"(w[s]));
  vm0();
  RTS(1);
  for (int p = 0; p < 32; ++p) {
    asm volatile("s_waitcnt vmcnt(2)\n\ts_barrier" ::: "memory");
    if (wave == 7 && p >= 6) post(done, p - 1, lane);
    const unsigned buf = (p & 1) * 57344;
    const unsigned char* src = xn + (size_t)(p < 31 ? p + 1 : p) * 57344;
    const unsigned dstl = (57344 - buf) + wave * 7168;
    f16v acc;
#pragma unroll
    for (int i = 0; i < 16; ++i) acc[i] = 0.f;
    const LDS u4v* xp = (const LDS u4v*)(size_t)(buf + rd);
    u4v xr[5];
#pragma unroll
    for (int s = 0; s < 4; ++s) xr[s] = xp[s * 64];
#pragma unroll
    for (int s = 0; s < 28; ++s) {
      if (s + 4 < 28) xr[(s + 4) % 5] = xp[(s + 4) * 64];
      acc = ISG ? mfma32(w[s], xr[s % 5], acc) : mfma32(xr[s % 5], w[s], acc);
      __builtin_amdgcn_sched_barrier(0);
      if (s % 3 == 0 && s / 3 < 7) { dma16(src + (s / 3) * 1024, dstl + (s / 3) * 1024); __builtin_amdgcn_sched_barrier(0); }
      if (s == 16) {
        asm volatile("s_waitcnt vmcnt(6)" ::: "memory");
        __builtin_amdgcn_sched_barrier(0);
      }
    }
    const u4v o0 = {pk2(acc[0], acc[1]), pk2(acc[2], acc[3]), pk2(acc[4], acc[5]), pk2(acc[6], acc[7])};
    const u4v o1 = {pk2(acc[8], acc[9]), pk2(acc[10], acc[11]), pk2(acc[12], acc[13]), pk2(acc[14], acc[15])};
    unsigned char* o = out + p * 2 * SEGB;
    st16c_asm(o, o0); st16c_asm(o + 512, o1);
    if (p < 4) {
      RTS(4);
      asm volatile("s_waitcnt vmcnt(0)\n\ts_barrier" ::: "memory");
      if (wave == 7) post(done, p + 1, lane);
      RTS(2);
    }
  }
  asm volatile("s_waitcnt vmcnt(0)\n\ts_barrier" ::: "memory");
  if (wave == 7) post(done, 32, lane);
  RTS(3);
}

template <bool P2>
DEV void ffn_body(unsigned char* __restrict__ ws, const unsigned short* __restrict__ x, const unsigned short* __restrict__ b1,
                  const unsigned short* __restrict__ b2, int job, int wave, int lane) {
  const int hi = lane >> 5, c32 = lane & 31;
  constexpr int NL = P2 ? 60 : 28, RING = P2 ? 12 : 14, DIST = RING - 1;
  const u4v* w1p = (const u4v*)(ws + O_W1 + (size_t)wave * 28672) + lane;
  const u4v* w2p = (const u4v*)(ws + O_W2 + (size_t)(2 * wave) * 114688) + lane;
  auto wload = [&](int c, int i) -> u4v {
    if (i < 28) return w1p[(size_t)c * 14336 + i * 64];
    return w2p[(size_t)c * 1024 + ((i - 28) >> 1) * 64 + ((i - 28) & 1) * 7168];
  };
  u4v wr[RING];
#pragma unroll
  for (int i = 0; i < DIST; ++i) wr[i] = wload(0, i);
  {
    const unsigned char* xn = ws + O_XN + (size_t)job * 57344 + wave * 7168 + lane * 16;
#pragma unroll
    for (int i = 0; i < 7; ++i) dma16(xn + i * 1024, wave * 7168 + i * 1024);
  }
  f16v o[2][2];
#pragma unroll
  for (int a = 0; a < 2; ++a)
#pragma unroll
    for (int bq = 0; bq < 2; ++bq)
#pragma unroll
      for (int i = 0; i < 16; ++i) o[a][bq][i] = 0.f;
  vm0();
  bar();
  const LDS u4v* xp = (const LDS u4v*)(size_t)(lane * 16);
  for (int c = 0; c < 7; ++c) {
    const int cn = c < 6 ? c + 1 : 6;
    const unsigned hbuf = 57344 + (c & 1) * 32768;
    u2v bv[4];
#pragma unroll
    for (int q = 0; q < 4; ++q) bv[q] = *(const u2v*)(b1 + 32 * (8 * c + wave) + 4 * hi + 8 * q);
    f16v a0, a1;
#pragma unroll
    for (int i = 0; i < 16; ++i) { a0[i] = 0.f; a1[i] = 0.f; }
    u4v xa[3], xb[3];
#pragma unroll
    for (int s = 0; s < 2; ++s) { xa[s] = xp[s * 64]; xb[s] = xp[1792 + s * 64]; }
#pragma unroll
    for (int s = 0; s < 28; ++s) {
      wr[(s + DIST) % RING] = (s + DIST < NL) ? wload(c, s + DIST) : wload(cn, s + DIST - NL);
      if (s + 2 < 28) { xa[(s + 2) % 3] = xp[(s + 2) * 64]; xb[(s + 2) % 3] = xp[1792 + (s + 2) * 64]; }
      a0 = mfma32(wr[s % RING], xa[s % 3], a0);
      a1 = mfma32(wr[s % RING], xb[s % 3], a1);
      __builtin_amdgcn_sched_barrier(0);
    }
#pragma unroll
    for (int i = 0; i < 16; ++i) {
      const unsigned bw = bv[i >> 2][(i >> 1) & 1];
      const float bs = (i & 1) ? bfhi(bw) : bflo(bw);
      const float t0 = fmaxf(a0[i] + bs, 0.f), t1 = fmaxf(a1[i] + bs, 0.f);
      a0[i] = t0 * t0; a1[i] = t1 * t1;
    }
#pragma unroll
    for (int bq = 0; bq < 2; ++bq) {
      lds_w16(hbuf + ((wave * 2 + bq) * 2 + 0) * 1024 + lane * 16,
              (u4v){pk2(a0[8 * bq], a0[8 * bq + 1]), pk2(a0[8 * bq + 2], a0[8 * bq + 3]), pk2(a0[8 * bq + 4], a0[8 * bq + 5]), pk2(a0[8 * bq + 6], a0[8 * bq + 7])});
      lds_w16(hbuf + ((wave * 2 + bq) * 2 + 1) * 1024 + lane * 16,
              (u4v){pk2(a1[8 * bq], a1[8 * bq + 1]), pk2(a1[8 * bq + 2], a1[8 * bq + 3]), pk2(a1[8 * bq + 4], a1[8 * bq + 5]), pk2(a1[8 * bq + 6], a1[8 * bq + 7])});
    }
    bar();
    if (P2) {
      const LDS u4v* hp = (const LDS u4v*)(size_t)(hbuf + lane * 16);
      u4v h0[2], h1[2];
      h0[0] = hp[0]; h1[0] = hp[64];
#pragma unroll
      for (int s = 0; s < 16; ++s) {
        const int i = 28 + 2 * s;
        wr[(i + DIST) % RING] = (i + DIST < NL) ? wload(c, i + DIST) : wload(cn, i + DIST - NL);
        if (s + 1 < 16) { h0[(s + 1) & 1] = hp[(s + 1) * 128]; h1[(s + 1) & 1] = hp[(s + 1) * 128 + 64]; }
        o[0][0] = mfma32(wr[i % RING], h0[s & 1], o[0][0]); o[0][1] = mfma32(wr[i % RING], h1[s & 1], o[0][1]);
        __builtin_amdgcn_sched_barrier(0);
        wr[(i + 1 + DIST) % RING] = (i + 1 + DIST < NL) ? wload(c, i + 1 + DIST) : wload(cn, i + 1 + DIST - NL);
        o[1][0] = mfma32(wr[(i + 1) % RING], h0[s & 1], o[1][0]); o[1][1] = mfma32(wr[(i + 1) % RING], h1[s & 1], o[1][1]);
        __builtin_amdgcn_sched_barrier(0);
      }
    }
  }
  if (P2) {
    const rsrc_t rf = mkrsrc(ws + O_F, 4u << 20);
#pragma unroll
    for (int a = 0; a < 2; ++a)
#pragma unroll
      for (int sg = 0; sg < 2; ++sg)
#pragma unroll
        for (int Q = 0; Q < 4; ++Q) {
          const int n = 32 * (2 * wave + a) + 8 * Q + 4 * hi, seg = 2 * job + sg;
          const u2v xv = *(const u2v*)(x + (size_t)(seg * 32 + c32) * DM + n), bv = *(const u2v*)(b2 + n);
          const f4v r = {o[a][sg][4 * Q] + bflo(bv.x) + bflo(xv.x), o[a][sg][4 * Q + 1] + bfhi(bv.x) + bfhi(xv.x),
                         o[a][sg][4 * Q + 2] + bflo(bv.y) + bflo(xv.y), o[a][sg][4 * Q + 3] + bfhi(bv.y) + bfhi(xv.y)};
          const int v = 2 * (2 * wave + a) + (Q >> 1), g = 2 * (Q & 1) + hi;
          stc16(rf, ((seg * 28 + v) * 2 + (c32 >> 4)) * 1024 + (g * 16 + (c32 & 15)) * 16, __builtin_bit_cast(u4v, r));
        }
    vm0();
    add_cnt(cntp(ws, C_F + job), lane);
    RTS(3);
  }
}
DEV void role_ffn(unsigned char* __restrict__ ws, const unsigned short* __restrict__ x, const unsigned short* __restrict__ b1,
                  const unsigned short* __restrict__ b2, int job, int wave, int lane) {
  if (wave < 7) ffn_body<true>(ws, x, b1, b2, job, wave, lane);
  else ffn_body<false>(ws, x, b1, b2, job, wave, lane);
}

DEV float asf(unsigned u) { return __builtin_bit_cast(float, u); }
DEV unsigned asu(float f) { return __builtin_bit_cast(unsigned, f); }
DEV float xmax16_32(float v) {
  const u2v a = __builtin_amdgcn_permlane16_swap(asu(v), asu(v), false, false);
  v = fmaxf(asf(a.x), asf(a.y));
  const u2v c = __builtin_amdgcn_permlane32_swap(asu(v), asu(v), false, false);
  return fmaxf(asf(c.x), asf(c.y));
}
DEV float xsum16_32(float v) {
  const u2v a = __builtin_amdgcn_permlane16_swap(asu(v), asu(v), false, false);
  v = asf(a.x) + asf(a.y);
  const u2v c = __builtin_amdgcn_permlane32_swap(asu(v), asu(v), false, false);
  return asf(c.x) + asf(c.y);
}
DEV float halfsum(float a, float b) {
  const u2v s = __builtin_amdgcn_permlane32_swap(asu(a), asu(b), false, false);
  return asf(s.x) + asf(s.y);
}

DEV void role_stt(unsigned char* __restrict__ ws, int grp, int hg, int wave, int lane) {
  const unsigned sel = *(const unsigned*)(ws + O_SEL);
  const int g = lane >> 4, c16 = lane & 15, h = 8 * hg + wave;
  const rsrc_t rg = mkrsrc(ws + O_G, 30u << 20);
  const LDS u4v* xp = (const LDS u4v*)(size_t)((g >> 1) * 1024 + (g & 1) * 512 + c16 * 16);
  for (int s = grp; s < NSEG; s += 8) {
    if (wave == 7) wait_cnt(ws, doneG(ws, 28 * hg + min(lane, 27)), (s >> 1) + 1, 32);
    bar();
    if (wave < 7) {
#pragma unroll
      for (int i = 0; i < 4; ++i) dma16(ws + O_XN + (size_t)s * 28672 + (wave * 4 + i) * 1024 + lane * 16, (wave * 4 + i) * 1024);
    }
    u4v ga[28];
#pragma unroll
    for (int T = 0; T < 28; ++T) ga[T] = ldc16(rg, (h * 2 + T / 14) * G_RUN + s * 14336 + (T % 14) * 1024 + lane * 16);
    __builtin_amdgcn_sched_barrier(0);
    asm volatile("s_waitcnt vmcnt(28)\n\ts_barrier" ::: "memory");
    f4v acc[2][2];
#pragma unroll
    for (int q = 0; q < 2; ++q) { acc[0][q] = (f4v){0.f, 0.f, 0.f, 0.f}; acc[1][q] = acc[0][q]; }
#pragma unroll
    for (int T = 0; T < 14; ++T) {
      const u4v x0 = xp[T * 128], x1 = xp[T * 128 + 16];
      acc[0][0] = mfma16(ga[T], x0, acc[0][0]); acc[0][1] = mfma16(ga[T], x1, acc[0][1]);
      acc[1][0] = mfma16(ga[14 + T], x0, acc[1][0]); acc[1][1] = mfma16(ga[14 + T], x1, acc[1][1]);
    }
    unsigned char* o = ws + O_ST + (size_t)(s * 16 + h) * 2048 + c16 * 32 + g * 8;
#pragma unroll
    for (int q = 0; q < 2; ++q)
#pragma unroll
      for (int t = 0; t < 2; ++t) {
        const u2v val = {pk2(acc[t][q][0], acc[t][q][1]), pk2(acc[t][q][2], acc[t][q][3])};
        asm volatile("global_store_dwordx2 %0, %1, off sc1" :: "v"(o + q * 1024 + t * 512), "v"(val) : "memory");
      }
    vm0();
    if (lane == 0) { const unsigned zero = 0; asm volatile("global_store_dword %0, %1, off sc1" :: "v"(flagT(ws, sel, s, h)), "v"(zero) : "memory"); }
  }
  RTS(3);
}

constexpr int W3_GROUPS = 12;
DEV void role_w3(unsigned char* __restrict__ ws, unsigned short* __restrict__ out, int grp, int C, int wave, int lane) {
  const unsigned sel = *(const unsigned*)(ws + O_SEL);
  const int g = lane >> 4, c16 = lane & 15, v = 7 * C + (wave < 7 ? wave : 6);
  const rsrc_t ru = mkrsrc(ws + O_U, 30u << 20), rf = mkrsrc(ws + O_F, 4u << 20);
  constexpr unsigned L_P = 0, L_PM = 32768, L_ST = 40960, L_SM = L_ST + 32768, L_Y = L_SM + 32768;
  for (int s = grp; s < NSEG; s += W3_GROUPS) {
    if (wave == 7) {
      wait_cnt(ws, doneU(ws, 4 * ((7 * C) >> 1) + (lane & 15)), (s >> 1) + 1);
      wait_cnt(ws, cntp(ws, C_F + (s >> 1)), 7);
      wait_flags(ws, flagT(ws, sel, s, lane & 15));
    }
    bar();
    u4v uf[16], ff[2];
    if (wave < 7) {
#pragma unroll
      for (int hh = 0; hh < 16; ++hh) uf[hh] = ldc16(ru, v * U_RUN + (s * 16 + hh) * 1024 + lane * 16);
#pragma unroll
      for (int q = 0; q < 2; ++q) ff[q] = ldc16(rf, ((s * 28 + v) * 2 + q) * 1024 + lane * 16);
    } else {
      wait_flags(ws, flagY(ws, sel, s, 7 * C + min(lane, 6)));
      wait_flags(ws, flagS(ws, sel, s, lane));
    }
    __builtin_amdgcn_sched_barrier(0);
    bar();
#pragma unroll
    for (int it = 0; it < 4; ++it) {
      dma16c(ws + O_ST + (size_t)(s * 32 + wave * 4 + it) * 1024 + lane * 16, L_ST + (wave * 4 + it) * 1024);
      dma16c(ws + O_SM + (size_t)(s * 32 + wave * 4 + it) * 1024 + lane * 16, L_SM + (wave * 4 + it) * 1024);
    }
    if (wave < 7) {
#pragma unroll
      for (int H = 0; H < 4; ++H) dma16c(ws + O_YG + (size_t)v * Y_RUN + (s * 4 + H) * 1024 + lane * 16, L_Y + (wave * 4 + H) * 1024);
      asm volatile("s_waitcnt vmcnt(4)" ::: "memory");
    } else vm0();
#pragma unroll
    for (int it = 0; it < 4; ++it) {
      const int q = it & 1, hh = 2 * wave + (it >> 1), tq = 16 * q + c16;
      const u4v st = lds_r16(L_ST + (wave * 4 + it) * 1024 + (g >> 1) * 512 + c16 * 32 + (g & 1) * 16);
      const LDS unsigned* sm = (const LDS unsigned*)(size_t)(L_SM + (wave * 4 + it) * 1024 + lane * 4);
      const unsigned s0 = sm[0], s1 = sm[64], s2 = sm[128], s3 = sm[192];
      float e[8];
      float m0 = (bflo(s0) + bflo(s1)) + (bflo(s2) + bflo(s3));
      float m1 = (bfhi(s0) + bfhi(s1)) + (bfhi(s2) + bfhi(s3));
#pragma unroll
      for (int i = 0; i < 8; ++i) { const unsigned w = st[i >> 1]; e[i] = (8 * g + i <= tq) ? ((i & 1) ? bfhi(w) : bflo(w)) : -__builtin_inff(); }
      float mx = fmaxf(m0, m1);
#pragma unroll
      for (int i = 0; i < 8; ++i) mx = fmaxf(mx, e[i]);
      mx = xmax16_32(mx);
      m0 = __builtin_amdgcn_exp2f(m0 - mx); m1 = __builtin_amdgcn_exp2f(m1 - mx);
      float sum = m0 + m1;
#pragma unroll
      for (int i = 0; i < 8; ++i) { e[i] = __builtin_amdgcn_exp2f(e[i] - mx); sum += e[i]; }
      const float r = __builtin_amdgcn_rcpf(xsum16_32(sum));
      lds_w16(L_P + (q * 16 + hh) * 1024 + lane * 16,
              (u4v){pk2(e[0] * r, e[1] * r), pk2(e[2] * r, e[3] * r), pk2(e[4] * r, e[5] * r), pk2(e[6] * r, e[7] * r)});
      *(LDS unsigned*)(size_t)(L_PM + (q * 4 + (hh >> 2)) * 1024 + lane * 16 + (hh & 3) * 4) = pk2(m0 * r, m1 * r);
    }
    vm0();
    bar();
    if (wave < 7) {
      u4v yf[4];
#pragma unroll
      for (int H = 0; H < 4; ++H) {
        const u2v lo = *(const LDS u2v*)(size_t)(L_Y + (wave * 4 + H) * 1024 + lane * 8), hi = *(const LDS u2v*)(size_t)(L_Y + (wave * 4 + H) * 1024 + 512 + lane * 8);
        yf[H] = (u4v){lo.x, lo.y, hi.x, hi.y};
      }
      f4v a0 = __builtin_bit_cast(f4v, ff[0]), a1 = __builtin_bit_cast(f4v, ff[1]);
      const LDS u4v* pp = (const LDS u4v*)(size_t)(L_P + lane * 16);
      u4v p0[4], p1[4];
#pragma unroll
      for (int k = 0; k < 3; ++k) { p0[k] = pp[k * 64]; p1[k] = pp[(16 + k) * 64]; }
#pragma unroll
      for (int k = 0; k < 20; ++k) {
        const int n = k + 3;
        if (n < 16) { p0[n & 3] = pp[n * 64]; p1[n & 3] = pp[(16 + n) * 64]; }
        else if (n < 20) { p0[n & 3] = pp[(32 + n - 16) * 64]; p1[n & 3] = pp[(36 + n - 16) * 64]; }
        const u4v w = k < 16 ? uf[k] : yf[k - 16];
        a0 = mfma16(w, p0[k & 3], a0); a1 = mfma16(w, p1[k & 3], a1);
        __builtin_amdgcn_sched_barrier(0);
      }
      *(u2v*)(out + (size_t)(s * 32 + c16) * DM + v * 16 + g * 4) = (u2v){pk2(a0[0], a0[1]), pk2(a0[2], a0[3])};
      *(u2v*)(out + (size_t)(s * 32 + 16 + c16) * DM + v * 16 + g * 4) = (u2v){pk2(a1[0], a1[1]), pk2(a1[2], a1[3])};
    }
  }
  RTS(4);
}
#define TUNE_ARG
#define TUNE_PASS
#define K_PERIOD 760
#define K_DS 1560
#define K_MS 43
#define K_MM 50
#define K_LEAD 96
#define K_FIXED 0
constexpr int K_SMK = 2, K_DDE = 250;
constexpr int C_PT = 2048;
DEV unsigned* ptp(unsigned char* ws, int s) { return cntp(ws, C_PT + 2 * s); }
DEV unsigned ptword(int period, int ds) { return (unsigned)(period / 24) | ((unsigned)(period % 24) << 8) | ((unsigned)(ds - K_DDE - 80) << 16); }
DEV unsigned char* flagR(unsigned char* ws, unsigned sel, int id) { return hblk(ws, sel, 64 + (id >> 6), 0) + (id & 63) * 128; }
#define DBG(slot, v)
#define ST(n)
#define ARR(s_)
#define EV(code)
#define EV_OUT(base)
#define ST_CLOB
#define ST_OUT(base, ref)
constexpr int RET_E = 26;
constexpr int TRY_CAP = 1 << 16;

DEV int rt32() { unsigned long long t; asm volatile("s_memrealtime %0\n\ts_waitcnt lgkmcnt(0)" : "=s"(t) :: "memory"); return (int)(unsigned)t; }
struct Lat { int anchor, e, f; };
DEV void lat_fwd(Lat& L, int pe, int pf) { L.f += pf; const int c = L.f >= 24; L.f -= c ? 24 : 0; L.e += pe + c; }
DEV void lat_back(Lat& L, int pe, int pf) { L.f -= pf; const int c = L.f < 0; L.f += c ? 24 : 0; L.e -= pe + c; }
DEV void lat_prep(const Lat& L, int& tgt, int& jmp) { tgt = L.anchor + 4 * L.e; jmp = 108 - 4 * L.f; }
#define TPAIR(r, m) "s_memrealtime s[" #r ":" #r "+1]\n\ts_memtime s[" #m ":" #m "+1]\n\ts_nop 0\n\t"
#define TSEL(r, m) "s_cmp_lg_u32 s" #r ", s4\n\ts_cselect_b32 s58, s" #m ", s58\n\t"
#define TBURST \
  TPAIR(4, 6) TPAIR(8, 10) TPAIR(12, 14) TPAIR(16, 18) TPAIR(20, 22) TPAIR(24, 26) TPAIR(28, 30) TPAIR(52, 54) TPAIR(56, 58) \
  "s_waitcnt lgkmcnt(0)\n\t" TSEL(52, 54) TSEL(28, 30) TSEL(24, 26) TSEL(20, 22) TSEL(16, 18) TSEL(12, 14) TSEL(8, 10)
#define TBURST_CLOB "s4","s5","s6","s7","s8","s9","s10","s11","s12","s13","s14","s15","s16","s17","s18","s19","s20","s21","s22","s23", \
  "s24","s25","s26","s27","s28","s29","s30","s31","s52","s53","s54","s55","s56","s57","s58","s59"
DEV void lat_commit(Lat& L, int tgt, int jmp) {
  if (tgt == (int)0x80000000) return;
  int r = tgt - rt32();
  while (__builtin_expect(r < 0, 0)) { L.e += RET_E; tgt += 4 * RET_E; r += 4 * RET_E; }
  if (r > 0) {
    int d;
#define RT_STEP "s_memrealtime vcc\n\ts_waitcnt lgkmcnt(0)\n\ts_sub_i32 %0, %1, vcc_lo\n\ts_cmp_lt_i32 %0, 1\n\t"
    asm volatile("1:\n\t" RT_STEP "s_cbranch_scc1 2f\n\t" RT_STEP "s_cbranch_scc1 2f\n\t" RT_STEP "s_cbranch_scc1 2f\n\t"
                 RT_STEP "s_cbranch_scc0 1b\n\t"
                 "2:\n\t"
                 "s_getpc_b64 vcc\n\ts_add_u32 vcc_lo, vcc_lo, %2\n\ts_addc_u32 vcc_hi, vcc_hi, 0\n\ts_setpc_b64 vcc\n\t"
                 ".rept 24\n\ts_nop 0\n\t.endr"
                 : "=&s"(d) : "s"(tgt), "s"(jmp) : "scc", "vcc", "memory");
  }
}
DEV unsigned lat_wait(Lat& L) {
  int tgt, jmp;
  unsigned est;
  lat_prep(L, tgt, jmp);
  if (tgt - rt32() < 16) { lat_commit(L, tgt, jmp); return 0; }
  asm volatile("1:\n\ts_memrealtime vcc\n\ts_waitcnt lgkmcnt(0)\n\ts_sub_i32 s4, %1, vcc_lo\n\ts_cmp_gt_i32 s4, 16\n\ts_cbranch_scc1 1b\n\t"
               TBURST
               "s_sub_i32 s4, %1, s4\n\ts_mul_i32 s4, s4, 93\n\ts_lshr_b32 s4, s4, 2\n\t"
               "s_add_i32 s58, s58, s4\n\ts_add_i32 s58, s58, %2\n\ts_or_b32 %0, s58, 1\n\t"
               "s_memtime vcc\n\ts_waitcnt lgkmcnt(0)\n\t"
               "s_sub_i32 s4, s58, vcc_lo\n\ts_max_i32 s4, s4, 0\n\ts_min_i32 s4, s4, 512\n\ts_and_b32 s4, s4, -4\n\ts_sub_i32 s4, 524, s4\n\t"
               "s_getpc_b64 vcc\n\ts_add_u32 vcc_lo, vcc_lo, s4\n\ts_addc_u32 vcc_hi, vcc_hi, 0\n\ts_setpc_b64 vcc\n\t"
               ".rept 128\n\ts_nop 0\n\t.endr"
               : "=&s"(est) : "s"(tgt), "s"(4 * L.f - 93) : TBURST_CLOB, "scc", "vcc", "memory");
  return est;
}
DEV unsigned* anchorp(unsigned char* ws, int id) { return cntp(ws, C_ANCHOR + 2 * (id & 31)); }
DEV int get_anchor(unsigned char* ws, unsigned sel, int id, int lane) {
  post((unsigned*)flagR(ws, sel, id), 0u, lane);
  unsigned v = 0;
  for (int it = 0; it < SPIN_CAP && v == 0; ++it) v = __builtin_amdgcn_readfirstlane(ld_cnt(anchorp(ws, id)));
  const int n = rt32();
  const int d = (int)((v & ~1u) - ((unsigned)n << 1)) >> 1;
  return (d > 4096 || d < -(1 << 22)) ? n : n + d;
}
DEV f4v smf(u4v a, u8v b, f4v c, int idx) {
  return __builtin_amdgcn_smfmac_f32_16x16x64_bf16(asbf8(a), __builtin_bit_cast(bf16x, b), c, idx, 0, 0);
}
DEV unsigned umax4(u4v v) { return max(max(v.x, v.y), max(v.z, v.w)); }
DEV float dpp_ror8(float v) { return __builtin_bit_cast(float, __builtin_amdgcn_update_dpp(0, __builtin_bit_cast(int, v), 0x128, 0xf, 0xf, true)); }
DEV float dpp_xor1(float v) { return __builtin_bit_cast(float, __builtin_amdgcn_update_dpp(0, __builtin_bit_cast(int, v), 0xb1, 0xf, 0xf, true)); }
DEV void st4c_asm(void* p, unsigned v) { asm volatile("global_store_dword %0, %1, off sc1" :: "v"(p), "v"(v) : "memory"); }
DEV void st8c_asm(void* p, u2v v) { asm volatile("global_store_dwordx2 %0, %1, off sc1" :: "v"(p), "v"(v) : "memory"); }
DEV void ld16c_asm(u4v& v, const void* p) { asm volatile("global_load_dwordx4 %0, %1, off sc1" : "=&v"(v) : "v"(p) : "memory"); }
DEV void rawbar() { asm volatile("s_barrier" ::: "memory"); }
DEV unsigned lds_r4(unsigned a) { return *(volatile LDS unsigned*)(size_t)a; }
DEV void lds_w4(unsigned a, unsigned v) { *(volatile LDS unsigned*)(size_t)a = v; }
DEV u8v cat8(u4v a, u4v b) { return (u8v){a.x, a.y, a.z, a.w, b.x, b.y, b.z, b.w}; }
DEV void set_abort(unsigned char* ws) { st4c_asm(cntp(ws, C_ABORT), 1u); }

constexpr unsigned L_MEM = 0, L_KT = 8192, L_FLAG = 12288, L_RED = 16384;
constexpr unsigned L_EST = 12304, L_TS = 12308, L_CG = 12544, L_G = 32768, L_XN = 65536;
#define CLOB_V200_247 "v200","v201","v202","v203","v204","v205","v206","v207","v208","v209","v210","v211","v212","v213","v214","v215", \
  "v216","v217","v218","v219","v220","v221","v222","v223","v224","v225","v226","v227","v228","v229","v230","v231", \
  "v232","v233","v234","v235","v236","v237","v238","v239","v240","v241","v242","v243","v244","v245","v246","v247","v248"
#define SMF "v_smfmac_f32_16x16x64_bf16 "
#define R1_HEAD(o0, o1, o2, o3, o4, o5, o6, FIRST) \
  "v_mov_b32 v228, 0\n\tv_mov_b32 v229, 0\n\tv_mov_b32 v230, 0\n\tv_mov_b32 v231, 0\n\t" \
  "v_mov_b32 v232, 0\n\tv_mov_b32 v233, 0\n\tv_mov_b32 v234, 0\n\tv_mov_b32 v235, 0\n\t" \
  "v_mov_b32 v244, 0\n\tv_mov_b32 v245, 0\n\tv_mov_b32 v246, 0\n\tv_mov_b32 v247, 0\n\t" \
  "s_mov_b32 m0, %[m0v]\n\t" \
  "s_barrier\n\t" ST(80) \
  "global_load_lds_dwordx4 %[moff], %[src] sc1\n\t" \
  "s_waitcnt vmcnt(0)\n\t" \
  "ds_read_b128 v[200:203], %[rd] offset:" #o0 "*1024\n\t" \
  "s_barrier\n\t" ST(82) \
  "ds_read_b128 v[204:207], %[rd] offset:" #o1 "*1024\n\tds_read_b128 v[208:211], %[rd] offset:" #o2 "*1024\n\t" \
  "ds_read_b128 v[212:215], %[rd] offset:" #o3 "*1024\n\tds_read_b128 v[216:219], %[rd] offset:" #o4 "*1024\n\t" \
  "ds_read_b128 v[220:223], %[rd] offset:" #o5 "*1024\n\tds_read_b128 v[224:227], %[rd] offset:" #o6 "*1024\n\t"
#define R1_TWO(n6, n5, n4, n3, n2, n1, n0) \
  "s_waitcnt lgkmcnt(" #n6 ")\n\t" ST(84) SMF "v[228:231], v[200:203], %[a0], %[idx]\n\t" SMF "v[232:235], v[200:203], %[b0], %[idx]\n\t" \
  "s_waitcnt lgkmcnt(" #n5 ")\n\t" SMF "v[228:231], v[204:207], %[a1], %[idx]\n\t" SMF "v[232:235], v[204:207], %[b1], %[idx]\n\t" \
  "s_waitcnt lgkmcnt(" #n4 ")\n\t" SMF "v[228:231], v[208:211], %[a2], %[idx]\n\t" SMF "v[232:235], v[208:211], %[b2], %[idx]\n\t" \
  "s_waitcnt lgkmcnt(" #n3 ")\n\t" SMF "v[228:231], v[212:215], %[a3], %[idx]\n\t" SMF "v[232:235], v[212:215], %[b3], %[idx]\n\t" \
  "s_waitcnt lgkmcnt(" #n2 ")\n\t" SMF "v[228:231], v[216:219], %[a4], %[idx]\n\t" SMF "v[232:235], v[216:219], %[b4], %[idx]\n\t" \
  "s_waitcnt lgkmcnt(" #n1 ")\n\t" SMF "v[228:231], v[220:223], %[a5], %[idx]\n\t" \
  "s_waitcnt lgkmcnt(" #n0 ")\n\t" SMF "v[228:231], v[224:227], %[a6], %[idx]\n\t" \
  SMF "v[232:235], v[220:223], %[b5], %[idx]\n\t" SMF "v[232:235], v[224:227], %[b6], %[idx]\n\t" \
  ST(86) "s_nop 1\n\t" \
  "v_cvt_pk_bf16_f32 v228, v228, v229\n\tv_cvt_pk_bf16_f32 v229, v230, v231\n\t" \
  "ds_write_b64 %[ktw], v[228:229]\n\t" \
  "s_nop 2\n\t" \
  "v_cvt_pk_bf16_f32 v232, v232, v233\n\tv_cvt_pk_bf16_f32 v233, v234, v235\n\t" \
  "ds_write_b64 %[ktw], v[232:233] offset:512\n\t" \
  LDSWAIT ST(88) \
  "s_barrier\n\t" ST(90)
#define LDSWAIT
#define R1_WOPS [a0] "v"(w0[0]), [a1] "v"(w0[1]), [a2] "v"(w0[2]), [a3] "v"(w0[3]), [a4] "v"(w0[4]), [a5] "v"(w0[5]), [a6] "v"(w0[6])
#define R1_WOPS2 [b0] "v"(w1[0]), [b1] "v"(w1[1]), [b2] "v"(w1[2]), [b3] "v"(w1[3]), [b4] "v"(w1[4]), [b5] "v"(w1[5]), [b6] "v"(w1[6])
#define R1_COMMON [idx] "v"(idx), [rd] "v"(rd), [ktw] "v"(ktw), [moff] "v"(moff), [src] "s"(src), [m0v] "s"(m0v)

template <int W>
DEV void r1_mfma(unsigned char* __restrict__ ws, int h, int q, int lane, unsigned sel, int smk) {
  constexpr int wave = W, KIND = W == 0 ? 0 : W == 3 ? 2 : 1;
  const int g = lane >> 4, c16 = lane & 15;
  const int idx = (lane & 8) ? 0xEEEE : 0x4444;
  const unsigned moff = wave * 1024 + g * 256 + (lane & 7) * 32 + ((lane >> 3) & 1) * 16;
  u8v w0[7], w1[7];
  {
    const unsigned char* p = ws + O_WA + (size_t)(h * 28 + 7 * q + 2 * wave) * 14336 + lane * 16;
#pragma unroll
    for (int k = 0; k < 7; ++k) {
      const int o = ((k + W) % 7) * 2048;
      w0[k] = cat8(*(const u4v*)(p + o), *(const u4v*)(p + o + 1024));
      if (KIND != 2) w1[k] = cat8(*(const u4v*)(p + 14336 + o), *(const u4v*)(p + 14336 + o + 1024));
    }
#pragma unroll
    for (int k = 0; k < 7; ++k) { asm volatile("" : "+v"(w0[k])); if (KIND != 2) asm volatile("" : "+v"(w1[k])); }
  }
  RTS(1);
  const unsigned rd = L_MEM + lane * 16, ktw = L_KT + 2 * wave * 512 + c16 * 32 + g * 8, m0v = L_MEM + wave * 1024;
  const unsigned a0a = L_MEM + (7 * q + g) * 256 + c16 * 16, a1a = L_MEM + min(7 * q + 4 + g, 27) * 256 + c16 * 16;
  const unsigned trb = L_KT + (g >> 1) * 512 + (4 * (g & 1) + (c16 >> 2)) * 32 + (c16 & 3) * 8;
  const unsigned sto = h * 512 + q * 128 + (4 * (g & 1) + 2 * (g >> 1) + (c16 >> 3)) * 16 + (c16 & 7) * 2;
  const unsigned long long lo8 = 0x00FF00FF00FF00FFull;
  unsigned ep = 0;
  int budget = TRY_CAP;
  rawbar();
  rawbar();
  for (int s = 0; s < NSEG; ++s) {
    const unsigned char* src = hblk(ws, sel, s, 0);
    unsigned char* dst = hblk(ws, sel, 64 + s, smk);
    for (;;) {
      if (KIND == 0) {
        unsigned long long okm, tst;
        asm volatile(R1_HEAD(0, 1, 2, 3, 4, 5, 6, )
          "ds_read_b128 v[236:239], %[a0a]\n\tds_read_b128 v[240:243], %[a1a]\n\t"
          R1_TWO(8, 7, 6, 5, 4, 3, 2)
          "ds_read_b64_tr_b16 v[200:201], %[trb]\n\tds_read_b64_tr_b16 v[202:203], %[trb] offset:256\n\t"
          "ds_read_b64_tr_b16 v[204:205], %[trb] offset:1024\n\tds_read_b64_tr_b16 v[206:207], %[trb] offset:1280\n\t"
          "ds_read_b64_tr_b16 v[208:209], %[trb] offset:2048\n\tds_read_b64_tr_b16 v[210:211], %[trb] offset:2304\n\t"
          "ds_read_b64_tr_b16 v[212:213], %[trb] offset:3072\n\tds_read_b64_tr_b16 v[214:215], %[trb] offset:3328\n\t"
          "s_waitcnt lgkmcnt(4)\n\t" SMF "v[244:247], v[236:239], v[200:207], %[idx]\n\t"
          "s_waitcnt lgkmcnt(0)\n\t" SMF "v[244:247], v[240:243], v[208:215], %[idx]\n\t"
          "s_nop 7\n\t"
          "v_permlane32_swap_b32 v244, v246\n\tv_permlane32_swap_b32 v245, v247\n\t"
          "v_add_f32 v244, v244, v246\n\tv_add_f32 v245, v245, v247\n\t"
          "v_cndmask_b32 v246, v244, v245, %[lo8]\n\t"
          "v_cndmask_b32 v247, v245, v244, %[lo8]\n\t"
          "s_nop 1\n\t"
          "v_add_f32_dpp v247, v246, v247 row_ror:8 row_mask:0xf bank_mask:0xf\n\t"
          "v_cvt_pk_bf16_f32 v246, v247, v247\n\t"
          "v_cmpx_o_f32 vcc, v247, v247\n\t"
          "global_store_short %[sto], v246, %[dst] sc1\n\t" ST(92)
          "s_memtime %[tst]\n\t"
          "s_mov_b64 exec, -1\n\t"
          "s_mov_b64 %[okm], vcc\n\t"
          "s_waitcnt lgkmcnt(0)\n\t"
          : [okm] "=&s"(okm), [tst] "=&s"(tst)
          : R1_WOPS, R1_WOPS2, R1_COMMON, [a0a] "v"(a0a), [a1a] "v"(a1a), [trb] "v"(trb), [sto] "v"(sto), [dst] "s"(dst), [lo8] "s"(lo8)
          : CLOB_V200_247, "vcc", "memory" ST_CLOB);
        lds_w4(L_TS, (unsigned)tst);
        if (okm != ~0ull) {
          if (budget > 0) { if (lane == 0) lds_w4(L_FLAG, ep); }
          else asm volatile("global_store_short %0, %1, %2 sc1" :: "v"(sto), "v"(0x7FC0u), "s"(dst) : "memory");
        }
      } else if (KIND == 1) {
        if (W == 1) asm volatile(R1_HEAD(1, 2, 3, 4, 5, 6, 0, ) R1_TWO(6, 5, 4, 3, 2, 1, 0) :: R1_WOPS, R1_WOPS2, R1_COMMON : CLOB_V200_247, "memory" ST_CLOB);
        else asm volatile(R1_HEAD(2, 3, 4, 5, 6, 0, 1, ) R1_TWO(6, 5, 4, 3, 2, 1, 0) :: R1_WOPS, R1_WOPS2, R1_COMMON : CLOB_V200_247, "memory" ST_CLOB);
      } else {
        asm volatile(R1_HEAD(3, 4, 5, 6, 0, 1, 2, )
          "s_waitcnt lgkmcnt(6)\n\t" SMF "v[228:231], v[200:203], %[a0], %[idx]\n\t"
          "s_waitcnt lgkmcnt(5)\n\t" SMF "v[228:231], v[204:207], %[a1], %[idx]\n\t"
          "s_waitcnt lgkmcnt(4)\n\t" SMF "v[228:231], v[208:211], %[a2], %[idx]\n\t"
          "s_waitcnt lgkmcnt(3)\n\t" SMF "v[228:231], v[212:215], %[a3], %[idx]\n\t"
          "s_waitcnt lgkmcnt(2)\n\t" SMF "v[228:231], v[216:219], %[a4], %[idx]\n\t"
          "s_waitcnt lgkmcnt(1)\n\t" SMF "v[228:231], v[220:223], %[a5], %[idx]\n\t"
          "s_waitcnt lgkmcnt(0)\n\t" SMF "v[228:231], v[224:227], %[a6], %[idx]\n\t"
          "s_nop 7\n\t"
          "v_cvt_pk_bf16_f32 v228, v228, v229\n\tv_cvt_pk_bf16_f32 v229, v230, v231\n\t"
          "ds_write_b64 %[ktw], v[228:229]\n\t"
          LDSWAIT
          "s_barrier\n\t"
          :: R1_WOPS, R1_COMMON : CLOB_V200_247, "memory" ST_CLOB);
      }
      bar();
      const bool bad = __builtin_amdgcn_readfirstlane(lds_r4(L_FLAG)) == ep;
      ++ep;
      if (!bad) break;
      --budget;
      rawbar();
    }
    ARR(s);
    rawbar();
  }
}

typedef unsigned short h2v __attribute__((ext_vector_type(2)));
DEV unsigned pkmax(unsigned a, unsigned b) { return __builtin_bit_cast(unsigned, __builtin_elementwise_max(__builtin_bit_cast(h2v, a), __builtin_bit_cast(h2v, b))); }
DEV unsigned wavepkmax(unsigned v) {
  v = pkmax(v, __builtin_amdgcn_update_dpp(0u, v, 0xb1, 0xf, 0xf, true));
  v = pkmax(v, __builtin_amdgcn_update_dpp(0u, v, 0x4e, 0xf, 0xf, true));
  v = pkmax(v, __builtin_amdgcn_update_dpp(0u, v, 0x124, 0xf, 0xf, true));
  v = pkmax(v, __builtin_amdgcn_update_dpp(0u, v, 0x128, 0xf, 0xf, true));
  const u2v x = __builtin_amdgcn_permlane16_swap(v, v, false, false);
  v = pkmax(x.x, x.y);
  const u2v y = __builtin_amdgcn_permlane32_swap(v, v, false, false);
  return pkmax(y.x, y.y);
}
DEV void r1_boss(unsigned char* __restrict__ ws, int lane, unsigned sel TUNE_ARG) {
  constexpr unsigned L_FA = 13312, L_FY = 13568;
  int A = 0, V = 0, ms = 4 * K_MS, mm = 4 * K_MM;
  float rho = 0.f;
  bool prevf3 = false;
  int run = 0;
  unsigned ep = 0;
  const unsigned c0 = rdtime();
  const int r0 = rt32();
  rawbar();
  const int P0 = (int)((float)K_PERIOD * (0.3f + 0.7f * fmaxf(23.4f * (float)(rt32() - r0) * __builtin_amdgcn_rcpf((float)(rdtime() - c0)), 1.f)));
  int hist = P0;
  if (lane < 5) st4c_asm(ptp(ws, lane), ptword(P0, K_DS));
  for (int it = 0; it < SPIN_CAP; ++it) {
    unsigned x, y;
    asm volatile("global_load_dword %0, %2, off sc1\n\tglobal_load_dword %1, %3, off sc1\n\ts_waitcnt vmcnt(0)"
                 : "=&v"(x), "=&v"(y) : "v"(flagR(ws, sel, lane)), "v"(flagR(ws, sel, 64 + min(lane, 27))) : "memory");
    if (!__any(max(x, y) == SENT)) break;
  }
  if (lane < 32) st4c_asm(anchorp(ws, lane), ((unsigned)(rt32() + K_LEAD) << 1) | 1u);
  for (int t = 0; t < NSEG; ++t) {
    const int s = max(t - 3, 0);
    const float pa = 4.f * (float)__builtin_amdgcn_readlane(hist, max(s - 1, 0)), py = 4.f * (float)__builtin_amdgcn_readlane(hist, s);
    rawbar(); rawbar();
    if (t >= 3) vm0();
    const unsigned a = lds_r4(L_FA + lane * 4), y = lds_r4(L_FY + lane * 4);
    const float da = (float)((a >> 12) & 0xffffu), dy = (float)((y >> 12) & 0xffffu);
    unsigned pk = min((unsigned)((float)(a & 0xfffu) * pa * __builtin_amdgcn_rcpf(da)), 65535u) |
                  (min((unsigned)((float)(y & 0xfffu) * py * __builtin_amdgcn_rcpf(dy)), 65535u) << 16);
    unsigned fl = (max(a, y) == SENT ? 1u : 0u) | ((a >> 28) != 0 ? 2u : 0u) | ((y >> 26) & 4u) |
                  (fabsf(da - 0.86f * pa) > 0.18f * pa ? 8u : 0u) | (fabsf(dy - 0.86f * py) > 0.18f * py ? 16u : 0u) | ((y >> 25) & 16u);
    float rn = dy * __builtin_amdgcn_rcpf(py);
    asm volatile("s_barrier" : "+v"(pk), "+v"(fl), "+v"(rn) :: "memory");
    pk = wavepkmax(pk);
    asm volatile("s_barrier\n\ts_barrier" : "+v"(pk) :: "memory");
    while (__builtin_amdgcn_readfirstlane(lds_r4(L_FLAG)) == ep++) { rawbar(); rawbar(); rawbar(); rawbar(); rawbar(); }
    if (t >= 3 && t + 2 < NSEG) {
      if (!__any(fl & 1)) {
        const unsigned m = __builtin_amdgcn_readfirstlane(pk);
        const bool f1 = __any(fl & 2), f3 = __any(fl & 4);
        if (!__any(fl & 10)) A = A == 0 ? (int)(m & 0xffffu) : A + (((int)(m & 0xffffu) - A) >> 2);
        if (!__any(fl & 20)) { V = V == 0 ? (int)(m >> 16) : V + (((int)(m >> 16) - V) >> 2); rho = rho == 0.f ? rn : rho + 0.25f * (rn - rho); }
        run = (f1 || f3) ? run + 1 : 0;
        if ((f1 && !prevf3) || run > 3) mm = min(mm + 20, 4 * K_MM + 800);
        if ((f3 && !f1) || run > 3) ms = min(ms + 20, 4 * K_MS + 800);
        prevf3 = f3;
      }
      int P = P0 + ((ms + mm) >> 2) - K_MS - K_MM, DS = K_DS + ms - 4 * K_MS;
      if (A > 0 && V > 0 && !K_FIXED) {
        DS = min(max((int)((float)(A + ms) * rho), 1200), 3000);
        P = min(max((A + ms + V - 84 + mm) >> 2, 600), 1500);
      }
      if (lane == t + 2) hist = P;
      post(ptp(ws, t + 2), ptword(P, DS), lane);
    }
    if (t >= 2) {
      asm volatile("s_mov_b32 m0, %2\n\ts_nop 0\n\tglobal_load_lds_dword %0, off sc1\n\ts_mov_b32 m0, %3\n\ts_nop 0\n\tglobal_load_lds_dword %1, off sc1"
                   :: "v"(flagS(ws, sel, t - 2, lane)), "v"(flagY(ws, sel, t - 2, min(lane, 27))), "s"(L_FA), "s"(L_FY) : "memory");
    }
  }
  vm0();
  rawbar();
}

DEV void role_r1(unsigned char* __restrict__ ws, int id, int wave, int lane, unsigned sel TUNE_ARG) {
  const int h = id >> 2, q = id & 3;
  if (wave == 0) { r1_mfma<0>(ws, h, q, lane, sel, K_SMK); return; }
  if (wave == 1) { r1_mfma<1>(ws, h, q, lane, sel, K_SMK); return; }
  if (wave == 2) { r1_mfma<2>(ws, h, q, lane, sel, K_SMK); return; }
  if (wave == 3) { r1_mfma<3>(ws, h, q, lane, sel, K_SMK); return; }
  if (wave == 7 && id == 3) { r1_boss(ws, lane, sel TUNE_PASS); return; }
  const int g = lane >> 4, c16 = lane & 15;
  const bool act = q < 2;
  const int fr = wave - 1;
  const unsigned moff = fr * 1024 + g * 256 + (lane & 7) * 32 + ((lane >> 3) & 1) * 16, m0v = L_MEM + fr * 1024;
  const unsigned xo = L_XN + (g >> 1) * 1024 + (g & 1) * 512 + c16 * 16;
  unsigned ep = 0;
  if (wave == 7) {
    const int idx = (lane & 8) ? 0xEEEE : 0x4444;
    u8v w0[7];
    const unsigned rd = lane * 16;
    const unsigned sto = h * 512 + (q & 1) * 256 + (4 * (g & 1) + 2 * (g >> 1) + (lane & 1)) * 32 + (c16 >> 1) * 4;
    const unsigned long long odd = 0xAAAAAAAAAAAAAAAAull;
    __builtin_amdgcn_s_setprio(3);
    rawbar();
    for (int s = 0; s < NSEG; ++s) {
      unsigned char* dst = hblk(ws, sel, s, 2);
      unsigned char* raw = hblk(ws, sel, s, 1);
      const bool go = act && s < NSEG - 1;
      if (go) {
#pragma unroll
        for (int k = 0; k < 7; ++k) w0[k] = cat8(lds_r16(L_G + (s & 1) * 14336 + rd + k * 2048), lds_r16(L_G + (s & 1) * 14336 + rd + k * 2048 + 1024));
      }
      for (;;) {
        if (go) {
          asm volatile(
            "v_mov_b32 v228, 0\n\tv_mov_b32 v229, 0\n\tv_mov_b32 v230, 0\n\tv_mov_b32 v231, 0\n\t"
            "s_barrier\n\ts_barrier\n\ts_barrier\n\t" ST(80)
            "ds_read_b128 v[200:203], %[rd]\n\tds_read_b128 v[204:207], %[rd] offset:1024\n\tds_read_b128 v[208:211], %[rd] offset:2048\n\t"
            "ds_read_b128 v[212:215], %[rd] offset:3072\n\tds_read_b128 v[216:219], %[rd] offset:4096\n\tds_read_b128 v[220:223], %[rd] offset:5120\n\t"
            "ds_read_b128 v[224:227], %[rd] offset:6144\n\t"
            "s_waitcnt lgkmcnt(6)\n\t" SMF "v[228:231], v[200:203], %[a0], %[idx]\n\t"
            "s_waitcnt lgkmcnt(5)\n\t" SMF "v[228:231], v[204:207], %[a1], %[idx]\n\t"
            "s_waitcnt lgkmcnt(4)\n\t" SMF "v[228:231], v[208:211], %[a2], %[idx]\n\t"
            "s_waitcnt lgkmcnt(3)\n\t" SMF "v[228:231], v[212:215], %[a3], %[idx]\n\t"
            "s_waitcnt lgkmcnt(2)\n\t" SMF "v[228:231], v[216:219], %[a4], %[idx]\n\t"
            "s_waitcnt lgkmcnt(1)\n\t" SMF "v[228:231], v[220:223], %[a5], %[idx]\n\t"
            "s_waitcnt lgkmcnt(0)\n\t" SMF "v[228:231], v[224:227], %[a6], %[idx]\n\t"
            "s_nop 7\n\t"
            "v_permlane32_swap_b32 v228, v230\n\tv_permlane32_swap_b32 v229, v231\n\t"
            "v_add_f32 v228, v228, v230\n\tv_add_f32 v229, v229, v231\n\t"
            "v_cndmask_b32 v230, v229, v228, %[odd]\n\t"
            "s_nop 1\n\t"
            "v_mov_b32_dpp v230, v230 quad_perm:[1,0,3,2] row_mask:0xf bank_mask:0xf\n\t"
            "v_cndmask_b32 v228, v228, v230, %[odd]\n\tv_cndmask_b32 v229, v230, v229, %[odd]\n\t"
            "v_exp_f32 v230, v228\n\tv_exp_f32 v231, v229\n\t"
            "v_cvt_pk_bf16_f32 v232, v228, v229\n\t"
            "v_cvt_pk_bf16_f32 v233, v230, v231\n\t"
            "v_cmpx_o_f32 vcc, v228, v229\n\t"
            "global_store_dword %[sto], v233, %[dst] sc1\n\t" ST(82)
            "global_store_dword %[sto], v232, %[raw] sc1\n\t"
            "s_mov_b64 exec, -1\n\t"
            "s_barrier\n\t"
            :: R1_WOPS, [idx] "v"(idx), [rd] "v"(rd), [sto] "v"(sto), [dst] "s"(dst), [raw] "s"(raw), [odd] "s"(odd)
            : CLOB_V200_247, "vcc", "memory" ST_CLOB);
          rawbar();
        } else {
          rawbar(); rawbar(); rawbar(); rawbar(); rawbar();
        }
        const bool bad = __builtin_amdgcn_readfirstlane(lds_r4(L_FLAG)) == ep;
        ++ep;
        if (!bad) break;
      }
    }
    rawbar();
  } else if (wave == 4) {
    const unsigned trb = L_KT + (g >> 1) * 512 + (4 * (g & 1) + (c16 >> 2)) * 32 + (c16 & 3) * 8;
    unsigned char* so = ws + O_SM + (size_t)h * 2048 + q * 256 + lane * 4;
    const unsigned x3 = xo, xlast = g < 2 ? 6144 : 5120;
#pragma unroll
    for (int u = 0; u < 8; ++u) *(LDS u2v*)(size_t)(L_KT + u * 512 + lane * 8) = u < 7 ? (u2v){SENT, SENT} : (u2v){0u, 0u};
    if (lane == 0) lds_w4(L_FLAG, SENT);
    rawbar();
    (void)ld_cnt((const unsigned*)(lane < 32 ? so : lane < 48 ? so + 63 * 32768 : hblk(ws, sel, 70, 0)));
    unsigned pay = 0, eprev = 0;
    for (int s = 0; s < NSEG; ++s) {
      bool sig = s > 0;
      const unsigned ep0 = ep;
      for (;;) {
        bar(); rawbar(); rawbar();
        if (sig) { vm0(); if (lane == 0) st4c_asm(flagS(ws, sel, s - 1, id), pay); sig = false; }
        rawbar(); rawbar();
        const bool bad = __builtin_amdgcn_readfirstlane(lds_r4(L_FLAG)) == ep;
        ++ep;
        if (!bad) break;
      }
      {
        const unsigned est = lds_r4(L_EST), tst = lds_r4(L_TS);
        pay = (est ? min(tst - est, 4095u) : 0u) | ((est && eprev) ? min(est - eprev, 65535u) << 12 : 0u);
        pay |= min(ep - ep0 - 1, 7u) << 28;
        eprev = est;
      }
      f4v c0 = {0.f, 0.f, 0.f, 0.f}, c1 = c0;
      u4v ka[4], xa[4], xb[4];
#pragma unroll
      for (int u = 0; u < 4; ++u) {
        const u2v lo = lds_tr(trb + u * 1024), hi = lds_tr(trb + u * 1024 + 256);
        ka[u] = (u4v){lo.x, lo.y, hi.x, hi.y};
        const unsigned ad = x3 + (s & 1) * 8192 + (u < 3 ? u * 2048 : xlast);
        xa[u] = lds_r16(ad); xb[u] = lds_r16(ad + 256);
      }
      __builtin_amdgcn_sched_barrier(0);
#pragma unroll
      for (int u = 0; u < 4; ++u) { c0 = mfma16(ka[u], xa[u], c0); c1 = mfma16(ka[u], xb[u], c1); }
#pragma unroll
      for (int u = 0; u < 7; ++u) *(LDS u2v*)(size_t)(L_KT + u * 512 + lane * 8) = (u2v){SENT, SENT};
      st4c_asm(so + (size_t)s * 32768, pk2(halfsum(c0[0], c0[1]), halfsum(c0[2], c0[3])));
      st4c_asm(so + (size_t)s * 32768 + 1024, pk2(halfsum(c1[0], c1[1]), halfsum(c1[2], c1[3])));
      ARR(s);
    }
    vm0();
    rawbar();
    if (lane == 0) st4c_asm(flagS(ws, sel, NSEG - 1, id), pay);
  } else if (wave == 5) {
    const unsigned char* gp = ws + O_G + (size_t)(h * 2 + (q & 1)) * G_RUN;
    const unsigned v0 = lane * 16, v1 = v0 + 4096, v2 = v0 + 8192, v3 = v0 + 12288;
    auto fetch = [&](int t) {
#define GL4(v) "global_load_lds_dwordx4 " v ", %4 sc1\n\t" "global_load_lds_dwordx4 " v ", %4 offset:1024 sc1\n\t"
#define GL8(v) GL4(v) "global_load_lds_dwordx4 " v ", %4 offset:2048 sc1\n\t" "global_load_lds_dwordx4 " v ", %4 offset:3072 sc1\n\t"
      asm volatile("s_mov_b32 m0, %5\n\ts_nop 0\n\t" GL8("%0") "s_add_u32 m0, m0, 4096\n\ts_nop 0\n\t" GL8("%1")
                   "s_add_u32 m0, m0, 4096\n\ts_nop 0\n\t" GL8("%2") "s_add_u32 m0, m0, 4096\n\ts_nop 0\n\t" GL4("%3")
                   :: "v"(v0), "v"(v1), "v"(v2), "v"(v3), "s"(gp + t * 14336), "s"(L_G + (t & 1) * 14336) : "scc", "memory");
    };
    auto fetchx = [&](int t) {
      asm volatile("s_mov_b32 m0, %3\n\ts_nop 0\n\t"
                   "global_load_lds_dwordx4 %0, %2\n\tglobal_load_lds_dwordx4 %0, %2 offset:1024\n\tglobal_load_lds_dwordx4 %0, %2 offset:2048\n\t"
                   "global_load_lds_dwordx4 %0, %2 offset:3072\n\ts_add_u32 m0, m0, 4096\n\ts_nop 0\n\t"
                   "global_load_lds_dwordx4 %1, %2\n\tglobal_load_lds_dwordx4 %1, %2 offset:1024\n\tglobal_load_lds_dwordx4 %1, %2 offset:2048"
                   :: "v"(v0), "v"(v1), "s"(ws + O_XN + (size_t)t * 28672 + q * 7168), "s"(L_XN + (t & 1) * 8192) : "scc", "memory");
    };
    const unsigned* gdone = doneG(ws, 7 * (h >> 1) + min(lane, 6));
    auto peek = [&](int t) {
      unsigned keep;
      asm volatile("s_mov_b32 %0, m0\n\ts_mov_b32 m0, %2\n\ts_nop 0\n\tglobal_load_lds_dword %1, off sc1\n\ts_mov_b32 m0, %0"
                   : "=&s"(keep) : "v"(lane == 63 ? ptp(ws, t) : gdone), "s"(L_CG) : "memory");
    };
    if (act) {
      wait_cnt(ws, gdone, 1);
      fetch(0); fetch(1);
    }
    fetchx(0);
    vm0();
    RTS(1);
    rawbar();
    for (int s = 0; s < NSEG; ++s) {
      const unsigned char* src = hblk(ws, sel, s, 0) + moff;
      bool todo = act && s + 2 < NSEG - 1, todox = s + 1 < NSEG;
      for (;;) {
        rawbar();
        if (s == 0) peek(0);
        rawbar();
        dma16c(src, m0v);
        vm0();
        rawbar();
        const bool there = __all(lds_r4(L_CG + (lane & 7) * 4) >= (unsigned)(s >> 1) + 2);
        rawbar();
        rawbar();
        if (todo && there) { fetch(s + 2); todo = false; }
        if (todox) { fetchx(s + 1); peek(s + 1); todox = false; }
        const bool bad = __builtin_amdgcn_readfirstlane(lds_r4(L_FLAG)) == ep;
        ++ep;
        if (!bad) break;
      }
      if (todo) {
        wait_cnt(ws, gdone, (s >> 1) + 2);
        fetch(s + 2);
      }
      ARR(s);
    }
    vm0();
    bar();
  } else {
      { Lat W = {rt32(), 6, 0}; (void)lat_wait(W); }
    rawbar();
    Lat L = {get_anchor(ws, sel, id, lane), 0, 0};
    for (int s = 0; s < NSEG; ++s) {
      const unsigned char* src = hblk(ws, sel, s, 0) + moff;
      unsigned pw = 0;
      for (;;) {
        bar();
        const unsigned est = lat_wait(L);
        rawbar();
        asm volatile("s_mov_b32 m0, %1\n\ts_nop 0\n\tglobal_load_lds_dwordx4 %0, off sc1\n\tglobal_load_lds_dwordx4 %0, off offset:1024 sc1\n\ts_waitcnt vmcnt(0)"
                     :: "v"(src), "s"(m0v) : "memory");
        rawbar();
        lds_w4(L_EST, est);
        if (pw == 0) pw = __builtin_amdgcn_readfirstlane(lds_r4(L_CG + 252)) | 0x80000000u;
        bar(); rawbar();
        const bool bad = __builtin_amdgcn_readfirstlane(lds_r4(L_FLAG)) == ep;
        ++ep;
        EV(bad ? 1 : 0);
        if (!bad) break;
        L.e += RET_E;
      }
      if (pw == 0x80000000u && s < NSEG - 1) { wait_cnt(ws, ptp(ws, s), 1); pw = __builtin_amdgcn_readfirstlane(ld_cnt(ptp(ws, s))); }
      lat_fwd(L, pw & 255, (pw >> 8) & 255);
      ARR(s);
    }
    bar();
    EV_OUT(id);
  }
}

#define XRED(op, v, t) \
  "v_mov_b32 " t ", " v "\n\ts_nop 1\n\tv_permlane16_swap_b32 " v ", " t "\n\t" op " " v ", " v ", " t "\n\t" \
  "v_mov_b32 " t ", " v "\n\ts_nop 1\n\tv_permlane32_swap_b32 " v ", " t "\n\t" op " " v ", " v ", " t "\n\t"
#define MF16 "v_mfma_f32_16x16x32_bf16 "
#define WAIT_CYC(T) \
  "s_memtime vcc\n\ts_waitcnt lgkmcnt(0)\n\t" \
  "s_sub_i32 s70, " T ", vcc_lo\n\ts_max_i32 s70, s70, 0\n\ts_mul_i32 s70, s70, 13\n\ts_lshr_b32 s70, s70, 6\n\t" \
  "s_min_i32 s70, s70, 128\n\ts_lshl_b32 s70, s70, 2\n\ts_sub_i32 s70, 524, s70\n\t" \
  "s_getpc_b64 vcc\n\ts_add_u32 vcc_lo, vcc_lo, s70\n\ts_addc_u32 vcc_hi, vcc_hi, 0\n\ts_setpc_b64 vcc\n\t" \
  ".rept 128\n\ts_nop 0\n\t.endr\n\t"
#define R3_Y_A \
  "ds_read_b128 v[200:203], %[rd]\n\tds_read_b128 v[204:207], %[rd] offset:1024\n\tds_read_b128 v[208:211], %[rd] offset:2048\n\t" \
  "ds_read_b128 v[212:215], %[rd] offset:3072\n\tds_read_b128 v[216:219], %[rd] offset:4096\n\tds_read_b128 v[220:223], %[rd] offset:5120\n\t" \
  "ds_read_b128 v[224:227], %[rd] offset:6144\n\t" \
  "s_waitcnt lgkmcnt(6)\n\t" SMF "v[228:231], v[200:203], %[a0], %[idx]\n\t" SMF "v[232:235], v[200:203], %[b0], %[idx]\n\t" \
  "s_waitcnt lgkmcnt(5)\n\t" SMF "v[228:231], v[204:207], %[a1], %[idx]\n\t" SMF "v[232:235], v[204:207], %[b1], %[idx]\n\t" \
  "s_waitcnt lgkmcnt(4)\n\t" SMF "v[228:231], v[208:211], %[a2], %[idx]\n\t" SMF "v[232:235], v[208:211], %[b2], %[idx]\n\t" \
  "s_waitcnt lgkmcnt(3)\n\t" SMF "v[228:231], v[212:215], %[a3], %[idx]\n\t" SMF "v[232:235], v[212:215], %[b3], %[idx]\n\t" \
    \
  "s_waitcnt lgkmcnt(0)\n\t" \
  "v_readfirstlane_b32 s68, v244\n\t" \
  "v_max3_u32 v244, v216, v217, v218\n\tv_max3_u32 v245, v219, v220, v221\n\tv_max3_u32 v244, v244, v222, v223\n\t" \
  "v_max3_u32 v245, v245, v224, v225\n\tv_max3_u32 v244, v244, v226, v227\n\tv_max_u32 v244, v244, v245\n\t" \
  "v_cmp_eq_u32 s[74:75], -1, v244\n\t" \
  "v_cmp_u_f32 vcc, v228, v229\n\t" \
  "s_or_b64 s[74:75], s[74:75], vcc\n\t" \
  "v_cmp_u_f32 vcc, v234, v235\n\t" \
  "s_or_b64 vcc, s[74:75], vcc\n\t"
#define R3_Y_B \
  SMF "v[228:231], v[216:219], %[a4], %[idx]\n\t" SMF "v[232:235], v[216:219], %[b4], %[idx]\n\t" \
  SMF "v[228:231], v[220:223], %[a5], %[idx]\n\t" SMF "v[228:231], v[224:227], %[a6], %[idx]\n\t" \
  SMF "v[232:235], v[220:223], %[b5], %[idx]\n\t" SMF "v[232:235], v[224:227], %[b6], %[idx]\n\t" \
  "s_nop 1\n\t" \
  "v_permlane32_swap_b32 v228, v229\n\tv_permlane32_swap_b32 v230, v231\n\t" \
  "v_add_f32 v228, v228, v229\n\tv_add_f32 v230, v230, v231\n\t" \
  "v_cvt_pk_bf16_f32 v164, v228, v230\n\t" \
  "s_nop 0\n\t" \
  "v_permlane32_swap_b32 v232, v233\n\tv_permlane32_swap_b32 v234, v235\n\t" \
  "v_add_f32 v232, v232, v233\n\tv_add_f32 v234, v234, v235\n\t" \
  "v_cvt_pk_bf16_f32 v165, v232, v234\n\t" \
  ST(82)
#define R3_POLL \
  WAIT_CYC("s68") \
  "global_load_dwordx4 v[236:239], %[to], s[60:61] sc1\n\t" \
  WAIT_CYC("s69") \
  "global_load_dwordx4 v[240:243], %[tm], s[60:61] sc1\n\t" ST(86)
#define R3_POLL2   \
  "55:\n\ts_memtime vcc\n\ts_waitcnt lgkmcnt(0)\n\ts_sub_i32 s70, s69, vcc_lo\n\ts_cmp_gt_i32 s70, 0\n\ts_cbranch_scc1 55b\n\t" \
  "global_load_dwordx4 v[236:239], %[to], s[60:61] sc1\n\tglobal_load_dwordx4 v[240:243], %[tm], s[60:61] sc1\n\t"
#define R3_S_PHASE(SIG, NS) \
  "s_waitcnt vmcnt(1)\n\t" ST(84) \
  "v_cndmask_b32 v208, 0, v236, s[76:77]\n\tv_cndmask_b32 v209, 0, v237, s[76:77]\n\tv_cndmask_b32 v210, 0, v238, s[76:77]\n\tv_cndmask_b32 v211, 0, v239, s[76:77]\n\t" \
  MF16 "v[220:223], v[160:163], v[236:239], 0\n\t" \
  "v_cndmask_b32 v212, v236, 0, s[76:77]\n\tv_cndmask_b32 v213, v237, 0, s[76:77]\n\tv_cndmask_b32 v214, v238, 0, s[76:77]\n\tv_cndmask_b32 v215, v239, 0, s[76:77]\n\t" \
  MF16 "v[216:219], v[192:195], v[208:211], 0\n\t" \
  "s_nop 0\n\t" \
  MF16 "v[216:219], v[196:199], v[212:215], v[216:219]\n\t" \
  "global_store_dwordx2 %[yo], v[164:165], s[60:61] sc1\n\t"        \
  SIG "s_waitcnt vmcnt(" #NS ")\n\t" ST(88) \
  MF16 "v[232:235], %[selA], v[240:243], 0\n\t" \
  "s_nop 7\n\t" \
  "v_exp_f32 v234, v232\n\tv_exp_f32 v235, v233\n\t" \
  "s_nop 0\n\t" \
  "v_cvt_pk_bf16_f32 v240, v234, v235\n\t" \
  "v_cndmask_b32 v168, 0, v240, s[76:77]\n\tv_cndmask_b32 v169, v240, 0, s[76:77]\n\t" \
  "s_nop 1\n\t" \
  MF16 "v[220:223], v[160:163], v[168:171], v[220:223]\n\t" \
  MF16 "v[244:247], v[164:167], v[168:171], v[216:219]\n\t" \
  "s_nop 6\n\t" \
  "v_rcp_f32 v242, v220\n\t" \
  "v_med3_f32 v241, v220, v172, v173\n\t" \
  "v_cmp_lg_f32 vcc, v241, v220\n\t" \
  "s_cbranch_vccnz 12f\n\t" \
  "11:\n\t" \
  "v_pk_mul_f32 v[244:245], v[244:245], v[242:243] op_sel_hi:[1,0]\n\t" \
  "v_pk_mul_f32 v[246:247], v[246:247], v[242:243] op_sel_hi:[1,0]\n\t" \
  "s_nop 0\n\t" \
  "v_add_f32_dpp v244, v244, v244 row_ror:8 row_mask:0xf bank_mask:0xf\n\tv_add_f32_dpp v245, v245, v245 row_ror:8 row_mask:0xf bank_mask:0xf\n\t" \
  "v_add_f32_dpp v246, v246, v246 row_ror:8 row_mask:0xf bank_mask:0xf\n\tv_add_f32_dpp v247, v247, v247 row_ror:8 row_mask:0xf bank_mask:0xf\n\t" \
  ST(90) "ds_write_b128 %[redw], v[244:247]\n\t" \
  LDSWAIT \
  "s_barrier\n\t" ST(92)
#define R3_S_RARE \
    "12:\n\t" \
  "v_xor_b32 v241, 0x6000, %[to]\n\t" \
  "global_load_dwordx4 v[200:203], v241, s[60:61] sc1\n\t" \
  "s_waitcnt vmcnt(0)\n\t" \
  "v_lshlrev_b32 v208, 16, v200\n\tv_and_b32 v209, 0xffff0000, v200\n\tv_lshlrev_b32 v210, 16, v201\n\tv_and_b32 v211, 0xffff0000, v201\n\t" \
  "v_lshlrev_b32 v212, 16, v202\n\tv_and_b32 v213, 0xffff0000, v202\n\tv_lshlrev_b32 v214, 16, v203\n\tv_and_b32 v215, 0xffff0000, v203\n\t" \
  "v_max3_f32 v240, v208, v209, v210\n\tv_max3_f32 v241, v211, v212, v213\n\tv_max3_f32 v240, v240, v214, v215\n\t" \
  "v_max3_f32 v241, v241, v232, v233\n\tv_max_f32 v240, v240, v241\n\t" \
  XRED("v_max_f32", "v240", "v241") \
  "v_sub_f32 v208, v208, v240\n\tv_sub_f32 v209, v209, v240\n\tv_sub_f32 v210, v210, v240\n\tv_sub_f32 v211, v211, v240\n\t" \
  "v_sub_f32 v212, v212, v240\n\tv_sub_f32 v213, v213, v240\n\tv_sub_f32 v214, v214, v240\n\tv_sub_f32 v215, v215, v240\n\t" \
  "v_sub_f32 v234, v232, v240\n\tv_sub_f32 v235, v233, v240\n\t" \
  "v_exp_f32 v208, v208\n\tv_exp_f32 v209, v209\n\tv_exp_f32 v210, v210\n\tv_exp_f32 v211, v211\n\t" \
  "v_exp_f32 v212, v212\n\tv_exp_f32 v213, v213\n\tv_exp_f32 v214, v214\n\tv_exp_f32 v215, v215\n\t" \
  "v_exp_f32 v234, v234\n\tv_exp_f32 v235, v235\n\ts_nop 1\n\t" \
  "v_cvt_pk_bf16_f32 v200, v208, v209\n\tv_cvt_pk_bf16_f32 v201, v210, v211\n\tv_cvt_pk_bf16_f32 v202, v212, v213\n\tv_cvt_pk_bf16_f32 v203, v214, v215\n\t" \
  "v_cvt_pk_bf16_f32 v240, v234, v235\n\t" \
  "v_cndmask_b32 v208, 0, v200, s[76:77]\n\tv_cndmask_b32 v209, 0, v201, s[76:77]\n\tv_cndmask_b32 v210, 0, v202, s[76:77]\n\tv_cndmask_b32 v211, 0, v203, s[76:77]\n\t" \
  "v_cndmask_b32 v212, v200, 0, s[76:77]\n\tv_cndmask_b32 v213, v201, 0, s[76:77]\n\tv_cndmask_b32 v214, v202, 0, s[76:77]\n\tv_cndmask_b32 v215, v203, 0, s[76:77]\n\t" \
  "v_cndmask_b32 v168, 0, v240, s[76:77]\n\tv_cndmask_b32 v169, v240, 0, s[76:77]\n\ts_nop 1\n\t" \
  MF16 "v[220:223], v[160:163], v[200:203], 0\n\t" \
  MF16 "v[220:223], v[160:163], v[168:171], v[220:223]\n\t" \
  MF16 "v[244:247], v[192:195], v[208:211], 0\n\t" \
  MF16 "v[244:247], v[196:199], v[212:215], v[244:247]\n\t" \
  MF16 "v[244:247], v[164:167], v[168:171], v[244:247]\n\t" \
  "s_nop 7\n\t" \
  "v_rcp_f32 v242, v220\n\ts_nop 0\n\t" \
  "s_branch 11b\n\t"
#define R3_PREP "s_lshl_b32 s44, s65, 2\n\ts_add_i32 s44, s44, s67\n\ts_lshl_b32 s45, s66, 2\n\ts_sub_i32 s45, 108, s45\n\t"
#define R3_FWD "s_add_i32 s66, s66, s47\n\ts_add_i32 s65, s65, s46\n\ts_cmp_ge_i32 s66, 24\n\ts_cselect_b32 s70, 24, 0\n\ts_addc_u32 s65, s65, 0\n\ts_sub_i32 s66, s66, s70\n\t"
#define R3_RT "s_memrealtime vcc\n\ts_waitcnt lgkmcnt(0)\n\ts_sub_i32 s70, s44, vcc_lo\n\ts_cmp_lt_i32 s70, 1\n\t"
#define R3_TW_WAIT \
  R3_RT "s_cbranch_scc1 40f\n\t" \
  "20:\n\t" R3_RT "s_cbranch_scc1 21f\n\t" R3_RT "s_cbranch_scc1 21f\n\t" R3_RT "s_cbranch_scc1 21f\n\t" R3_RT "s_cbranch_scc0 20b\n\t" \
  "21:\n\t" \
  "s_getpc_b64 vcc\n\ts_add_u32 vcc_lo, vcc_lo, s45\n\ts_addc_u32 vcc_hi, vcc_hi, 0\n\ts_setpc_b64 vcc\n\t" \
  ".rept 24\n\ts_nop 0\n\t.endr\n\t" \
  "22:\n\t"
#define R3_TW_LATE \
  "40:\n\t" \
  "s_cmp_eq_u32 s70, 0\n\ts_cbranch_scc1 22b\n\t" \
  "41:\n\ts_add_i32 s65, s65, 26\n\ts_add_i32 s44, s44, 4*26\n\ts_add_i32 s70, s70, 4*26\n\ts_cmp_lt_i32 s70, 0\n\ts_cbranch_scc1 41b\n\t" \
  "s_branch 20b\n\t"
#define R3_STEP(op, uop)   \
  op " %[moff], 0x8000, %[moff]\n\t" op " %[to], 0x8000, %[to]\n\t" op " %[tm], 0x8000, %[tm]\n\t" op " %[sto], 0x8000, %[sto]\n\t" \
  uop " %[uo], 0x4000, %[uo]\n\t" uop " %[yo], 0x1000, %[yo]\n\t"
#define R3_FLIP \
  "s_and_b32 s70, s62, 15\n\ts_cselect_b32 s70, 0, 0x4000\n\ts_add_i32 s71, s62, 1\n\ts_and_b32 s71, s71, 15\n\ts_cselect_b32 s71, 0, 0x4000\n\t" \
  "v_xor_b32 %[moff], s70, %[moff]\n\tv_xor_b32 %[to], s70, %[to]\n\tv_xor_b32 %[tm], s70, %[tm]\n\tv_xor_b32 %[sto], s71, %[sto]\n\t"
#define ULOADS "global_load_dwordx4 v[192:195], %[uo], s[60:61] sc1\n\tglobal_load_dwordx4 v[196:199], %[uo], s[60:61] offset:1024 sc1\n\t"
#define R3_DMA "global_load_lds_dwordx4 %[moff], s[60:61] sc1\n\t"
#define R3_TW_PT "s_lshl_b32 s70, s62, 7\n\ts_add_i32 s70, s70, 0x420080\n\tv_mov_b32 v178, s70\n\tglobal_load_dword v177, v178, s[60:61] sc1\n\t"
#define R3_TW_NEXT "v_readfirstlane_b32 s70, v177\n\ts_cmp_eq_u32 s70, 0\n\ts_cbranch_scc1 45f\n\t" \
  "19:\n\ts_and_b32 s46, s70, 0xff\n\ts_bfe_u32 s47, s70, 0x80008\n\ts_lshr_b32 s48, s70, 16\n\t"
#define R3_TW_OOL        \
  "45:\n\ts_mov_b32 s71, 0x40000\n\t" \
  "46:\n\tglobal_load_dword v177, v178, s[60:61] sc1\n\ts_waitcnt vmcnt(0)\n\tv_readfirstlane_b32 s70, v177\n\t" \
  "s_cmp_eq_u32 s70, 0\n\ts_cbranch_scc0 19b\n\ts_sub_i32 s71, s71, 1\n\ts_cmp_gt_i32 s71, 0\n\ts_cbranch_scc1 46b\n\ts_movk_i32 s70, 40\n\ts_branch 19b\n\t"
#define R3_TW_TIME \
  TBURST \
  "s_sub_i32 s4, s4, s44\n\ts_add_i32 s4, s4, 4\n\ts_mul_i32 s4, s4, 93\n\ts_ashr_i32 s4, s4, 2\n\ts_sub_i32 s58, s58, s4\n\t" \
  "s_lshl_b32 s4, s66, 2\n\ts_add_i32 s58, s58, s4\n\ts_sub_i32 s4, s58, s51\n\ts_mov_b32 s51, s58\n\ts_add_i32 s58, s58, s48\n\t" \
  "v_mov_b32 v200, s58\n\tv_mov_b32 v201, s4\n\tds_write_b64 v188, v[200:201] offset:12296\n\ts_waitcnt lgkmcnt(0)\n\t"
#define R3_WARM(beg, len) \
  "s_getpc_b64 vcc\n\t9:\n\ts_add_u32 vcc_lo, vcc_lo, " beg "-9b\n\ts_addc_u32 vcc_hi, vcc_hi, 0\n\tv_lshlrev_b32 v200, 3, %[rd]\n\tv_min_u32 v200, " len "-64, v200\n\t" \
  "global_load_dword v201, v200, vcc\n\tglobal_load_dword v202, v200, vcc offset:64\n\ts_waitcnt vmcnt(0)\n\t"
#define R3_LOOP(WARM, DMA, TWWAIT, CNTLOAD, CNTPAY, CNTPACK, TWTIME, NW, CNTSIG, NS, TWFWD, FIN, CNTCHK, OUTOFLINE) \
  "s_mov_b64 s[60:61], %[wsp]\n\ts_mov_b32 s62, 0\n\ts_mov_b32 s63, 0\n\ts_mov_b32 s64, 0x10000\n\ts_mov_b32 s78, 0\n\t" \
  "s_mov_b32 s76, 0x00ff00ff\n\ts_mov_b32 s77, s76\n\t" \
  "s_and_b32 s46, %[p0], 0xff\n\ts_bfe_u32 s47, %[p0], 0x80008\n\ts_lshr_b32 s48, %[p0], 16\n\ts_mov_b32 s65, 0\n\ts_mov_b32 s66, 0\n\t" \
  "s_movk_i32 s49, 250\n\ts_lshl_b32 s50, %[p1], 10\n\t" \
  "v_mov_b32 v188, 0\n\tv_mov_b32 v160, 0x3f803f80\n\tv_mov_b32 v161, v160\n\tv_mov_b32 v162, v160\n\tv_mov_b32 v163, v160\n\t" \
  "v_mov_b32 v166, 0\n\tv_mov_b32 v167, 0\n\tv_mov_b32 v170, 0\n\tv_mov_b32 v171, 0\n\tv_mov_b32 v172, 0x1f800000\n\tv_mov_b32 v173, 0x71800000\n\t" \
  R3_PREP \
  WARM \
  "10:\n\t" \
  TWWAIT \
  "s_mov_b32 m0, s50\n\t" \
  "s_waitcnt lgkmcnt(0)\n\ts_barrier\n\t" \
  "s_memtime s[72:73]\n\t" \
  DMA \
  "ds_read_b32 v200, v188 offset:12288\n\t" \
  "s_waitcnt lgkmcnt(0)\n\t" \
  "v_readfirstlane_b32 s74, v200\n\t" \
  "s_cmp_eq_u32 s74, s63\n\ts_cbranch_scc1 50f\n\t" \
  "s_add_i32 s63, s63, 1\n\t" \
  "v_mov_b32 v228, 0\n\tv_mov_b32 v229, 0\n\tv_mov_b32 v230, 0\n\tv_mov_b32 v231, 0\n\t" \
  "v_mov_b32 v232, 0\n\tv_mov_b32 v233, 0\n\tv_mov_b32 v234, 0\n\tv_mov_b32 v235, 0\n\t" \
  ULOADS \
  CNTLOAD TWTIME \
  "s_waitcnt vmcnt(" #NW ")\n\t" \
  "s_barrier\n\t" ST(80) \
  CNTPAY "ds_read_b32 v244, v188 offset:12296\n\t" \
  R3_Y_A \
  "s_cbranch_vccnz 60f\n\t" \
  "14:\n\t" \
  CNTPACK "s_add_i32 s69, s68, s49\n\t" \
  "s_cmp_eq_u32 s62, 63\n\ts_cbranch_scc1 98f\n\t" \
  R3_POLL R3_Y_B \
  TWFWD \
  "15:\n\t" \
  R3_S_PHASE(CNTSIG, NS) FIN CNTCHK \
  "s_add_i32 s62, s62, 1\n\t" \
  R3_STEP("v_add_u32", "v_add_u32") \
  R3_FLIP DIAG_R3 \
  "s_branch 10b\n\t" \
  R3_S_RARE \
    \
  "50:\n\t" \
  "s_sub_i32 s64, s64, 1\n\ts_add_i32 s63, s63, 1\n\ts_bitset1_b32 s78, 28\n\t" \
  R3_FLIP R3_STEP("v_subrev_u32", "v_subrev_u32") \
  "s_sub_i32 s62, s62, 1\n\t" \
  "s_memtime vcc\n\ts_waitcnt lgkmcnt(0)\n\t" \
  "s_sub_i32 s70, s69, s68\n\ts_cmp_eq_u32 s70, s49\n\ts_cselect_b32 s70, 256, 0\n\ts_sub_i32 s69, s69, s70\n\t"       \
  "53:\n\ts_add_i32 s68, s68, 93*26\n\ts_add_i32 s69, s69, 93*26\n\ts_add_i32 s65, s65, 26\n\t" \
  "s_sub_i32 s70, s69, vcc_lo\n\ts_cmp_lt_i32 s70, 0\n\ts_cbranch_scc1 53b\n\t" \
  R3_PREP R3_POLL2 \
  "s_branch 15b\n\t" \
    \
  "60:\n\ts_bitset1_b32 s78, 29\n\ts_sub_i32 s64, s64, 1\n\ts_cmp_gt_i32 s64, 0\n\ts_cbranch_scc0 14b\n\t" \
  "s_add_i32 s65, s65, 26\n\t" R3_PREP "s_branch 10b\n\t" \
  OUTOFLINE \
  "98:\n\t" R3_Y_B \
  "global_store_dwordx2 %[yo], v[164:165], s[60:61] sc1\n\t" \
  "s_waitcnt vmcnt(0)\n\ts_barrier\n\t"
#define R3_FIN \
  "ds_read_b128 v[200:203], %[redr]\n\tds_read_b128 v[204:207], %[redr] offset:512\n\t" \
  "ds_read_b128 v[208:211], %[redr] offset:1024\n\tds_read_b128 v[212:215], %[redr] offset:1536\n\t" \
  "s_waitcnt lgkmcnt(2)\n\t" \
  "v_pk_add_f32 v[200:201], v[200:201], v[204:205]\n\tv_pk_add_f32 v[202:203], v[202:203], v[206:207]\n\t" \
  "s_waitcnt lgkmcnt(1)\n\t" \
  "v_pk_add_f32 v[200:201], v[200:201], v[208:209]\n\tv_pk_add_f32 v[202:203], v[202:203], v[210:211]\n\t" \
  "s_waitcnt lgkmcnt(0)\n\t" \
  "v_pk_add_f32 v[200:201], v[200:201], v[212:213]\n\tv_pk_add_f32 v[202:203], v[202:203], v[214:215]\n\t" \
  "s_nop 1\n\t" \
  "v_permlane32_swap_b32 v200, v202\n\tv_permlane32_swap_b32 v201, v203\n\t" \
  "v_add_f32 v200, v200, v202\n\tv_add_f32 v201, v201, v203\n\t" \
  "v_cvt_pk_bf16_f32 v202, v200, v201\n\t" \
  "v_cmpx_o_f32 vcc, v200, v201\n\t" \
  "global_store_dword %[sto], v202, s[60:61] sc1\n\t" ST(94) \
  "s_memtime s[74:75]\n\t" \
  "s_mov_b64 exec, -1\n\t" \
  "v_mov_b32 v200, -1\n\tv_mov_b32 v201, -1\n\tv_mov_b32 v202, -1\n\tv_mov_b32 v203, -1\n\t" \
  "ds_write_b128 %[redr], v[200:203]\n\tds_write_b128 %[redr], v[200:203] offset:512\n\t" \
  "ds_write_b128 %[redr], v[200:203] offset:1024\n\tds_write_b128 %[redr], v[200:203] offset:1536\n\t" \
  "s_waitcnt lgkmcnt(0)\n\ts_sub_u32 s70, s74, s69\n\tv_mov_b32 v200, s70\n\tds_write_b32 v188, v200 offset:12308\n\t" \
  "s_cmp_eq_u64 vcc, -1\n\ts_cbranch_scc0 80f\n\t" \
  "16:\n\t"
#define R3_FIN_OOL \
  "80:\n\ts_cmp_gt_i32 s64, 0\n\ts_cbranch_scc0 81f\n\t" \
  "v_mov_b32 v200, s63\n\tds_write_b32 v188, v200 offset:12288\n\ts_branch 16b\n\t" \
  "81:\n\tv_mov_b32 v200, 0x7fc07fc0\n\tglobal_store_dword %[sto], v200, s[60:61] sc1\n\ts_branch 16b\n\t"
#define R3_CNT_PAY "ds_read_b32 v174, v188 offset:12300\n\tds_read_b32 v175, v188 offset:12308\n\t"
#define R3_CNT_PACK \
  "v_min_u32 v175, 0xfff, v175\n\tv_min_u32 v174, 0xffff, v174\n\tv_lshl_or_b32 v176, v174, 12, v175\n\tv_or_b32 v176, s78, v176\n\ts_mov_b32 s78, 0\n\t"
#define R3_CNT_SIG "global_store_dword v187, v176, s[60:61] sc1\n\t"
#define R3_CNT_LOAD "v_mov_b32 v187, s43\n\ts_mov_b64 s[38:39], s[40:41]\n\tv_cndmask_b32 v144, %[uo], v186, s[40:41]\n\tglobal_load_dword v145, v144, s[60:61] sc1\n\t"
#define R3_CNT_CHK "v_readfirstlane_b32 s43, %[tm]\n\ts_and_b32 s43, s43, 0xffffe000\n\ts_xor_b32 s43, s43, s42\n\t" \
  "v_cndmask_b32 v189, v189, v145, s[38:39]\n\ts_mov_b64 s[38:39], 0\n\t" \
  "s_add_i32 s70, s62, 3\n\ts_lshr_b32 s70, s70, 1\n\tv_cmp_gt_u32 vcc, s70, v189\n\ts_cbranch_vccnz 85f\n\t17:\n\t" \
  "s_add_i32 s70, s70, 1\n\ts_min_u32 s70, s70, 32\n\tv_cmp_gt_u32 vcc, s70, v189\n\ts_cmp_lg_u64 vcc, 0\n\ts_cselect_b64 s[40:41], -1, 0\n\t"
#define R3_CNT_OOL        \
  "85:\n\ts_mov_b32 s71, 0x40000\n\t" \
  "86:\n\tglobal_load_dword v189, v186, s[60:61] sc1\n\ts_waitcnt vmcnt(0)\n\t" \
  "v_cmp_gt_u32 vcc, s70, v189\n\ts_cbranch_vccz 17b\n\ts_sleep 2\n\ts_sub_i32 s71, s71, 1\n\ts_cmp_gt_i32 s71, 0\n\ts_cbranch_scc1 86b\n\ts_branch 17b\n\t"
#define DIAG_R3
#define DIAG_R3_END
#define DIAG_R3_OUT
#define R3_OUTS [moff] "+v"(moff), [to] "+v"(to), [tm] "+v"(tm), [sto] "+v"(sto), [uo] "+v"(uo), [yo] "+v"(yo) DIAG_R3_OUT
#define R3_INS R1_WOPS, R1_WOPS2, [idx] "v"(idx), [rd] "v"(rd), [redw] "v"(redw), [selA] "v"(selA), [wsp] "s"(ws), [p0] "s"(p0), [p1] "s"(p1)
#define R3_CLOB CLOB_V200_247, "v144","v145","v146","v147","v148","v149","v150","v151", "v160","v161","v162","v163","v164","v165","v166","v167","v168","v169","v170","v171","v172","v173","v174","v175","v176","v177","v178", "v179","v180","v181","v182","v183","s96","s97","v184","v185","v186","v187","v188","v189","v190","v191","v192","v193","v194","v195","v196","v197","v198","v199", \
  "s38","s39","s40","s41","s42","s43","s44","s45","s46","s47","s48","s49","s50","s51","s60","s61","s62","s63","s64","s65","s66","s67","s68","s69","s70","s71", \
  "s72","s73","s74","s75","s76","s77","s78","vcc","scc","memory" ST_CLOB
#define STR2(x) #x
#define STR(x) STR2(x)

template <int KIND>
DEV void r3_body(unsigned char* __restrict__ ws, int v, int wave, int lane, unsigned sel TUNE_ARG) {
  const int g = lane >> 4, hs = (lane >> 3) & 1, i8 = lane & 7;
  const int idx = (lane & 8) ? 0xEEEE : 0x4444;
  const int dw = wave < 7 ? wave : 6;
  const unsigned rd = lane * 16;
  u8v w0[7], w1[7];
  {
    const unsigned char* p = ws + O_WB + (size_t)(2 * wave * 28 + v) * 14336 + lane * 16;
#pragma unroll
    for (int k = 0; k < 7; ++k) {
      w0[k] = cat8(*(const u4v*)(p + k * 2048), *(const u4v*)(p + k * 2048 + 1024));
      w1[k] = cat8(*(const u4v*)(p + 28 * 14336 + k * 2048), *(const u4v*)(p + 28 * 14336 + k * 2048 + 1024));
    }
#pragma unroll
    for (int k = 0; k < 7; ++k) asm volatile("" : "+v"(w0[k]), "+v"(w1[k]));
  }
  RTS(1);
  if (KIND == 3 && lane == 0) lds_w4(L_FLAG, SENT);
  if (KIND == 2) wait_cnt(ws, doneU(ws, 4 * (v >> 1) + (lane & 3)), 1);
  *(LDS u2v*)(size_t)(L_RED + wave * 512 + lane * 8) = (u2v){SENT, SENT};
  RTS(2);
  unsigned moff = hoff(sel, 0, 0) + dw * 1024 + g * 256 + i8 * 32 + hs * 16;
  unsigned to = hoff(sel, 0, 2) + (2 * wave + hs) * 512 + (g >> 1) * 256 + i8 * 32 + (g & 1) * 16;
  unsigned tm = hoff(sel, 64, K_SMK) + (2 * wave + hs) * 512 + g * 128 + i8 * 16;
  u4v selA = {0u, 0u, 0u, 0u};
  if (((lane & 3) >> 1) == 0) {
    const int gq = (lane >> 2) & 3, j = 4 * (gq & 1) + (gq >> 1) + 2 * (lane & 1);
    const unsigned one = (j & 1) ? 0x3f800000u : 0x00003f80u;
    selA = (u4v){(j >> 1) == 0 ? one : 0u, (j >> 1) == 1 ? one : 0u, (j >> 1) == 2 ? one : 0u, (j >> 1) == 3 ? one : 0u};
  }
  unsigned sto = hoff(sel, 1, 0) + v * 256 + i8 * 32 + ((lane >> 3) & 3) * 8 + (lane >> 5) * 4;
  unsigned uo = (unsigned)O_U + v * U_RUN + 2 * wave * 1024 + lane * 16;
  unsigned yo = (unsigned)O_YG + v * Y_RUN + wave * 512 + lane * 8;
  const unsigned redw = L_RED + hs * 4096 + wave * 512 + (g * 8 + i8) * 16;
  const unsigned redr = L_RED + (lane >> 5) * 2048 + (lane & 31) * 16;
  const unsigned p1 = dw;
  {
    const unsigned o = lane < 8 ? yo : lane < 16 ? yo + 63 * 4096 : lane < 24 ? uo : lane < 32 ? uo + 63 * 16384 : lane < 40 ? hoff(sel, 70, 0) : lane < 48 ? hoff(sel, 1, 0) + 7168 : (unsigned)O_FL + 0x3ff00;
    (void)ld_cnt((const unsigned*)(ws + (o & ~3u)));
  }
  int anchor = 0;
  unsigned p0 = 0;
  if (KIND == 3) asm volatile(R3_WARM(".Lr3tw_beg", ".Lr3tw_end-.Lr3tw_beg") :: [rd] "v"(rd) : "v200", "v201", "v202", "vcc", "scc", "memory");
  if (KIND == 3) { wait_cnt(ws, ptp(ws, 0), 1); p0 = __builtin_amdgcn_readfirstlane(ld_cnt(ptp(ws, 0))); }
  bar();
  if (KIND == 3) anchor = get_anchor(ws, sel, 64 + v, lane);
  if (KIND == 0) {
    asm volatile(R3_LOOP(R3_WARM("10f", "98f-10f"), R3_DMA, "", "", "", "", "", 2, "", 1, "", R3_FIN, "", R3_FIN_OOL) DIAG_R3_END : R3_OUTS : R3_INS, [redr] "v"(redr) : R3_CLOB);
  } else if (KIND == 1) {
    asm volatile(R3_LOOP(R3_WARM("10f", "98f-10f"), R3_DMA, "", "", "", "", "", 2, "", 1, "", "", "", "") DIAG_R3_END : R3_OUTS : R3_INS : R3_CLOB);
  } else if (KIND == 2) {
    const unsigned cc = ((unsigned)(3 - K_SMK) << 13) | (unsigned)(v * 128);
    const unsigned uc = (unsigned)O_FL + (C_W + 2 * (56 + 4 * (v >> 1) + (lane & 3))) * 64;
    asm volatile("s_mov_b64 s[40:41], -1\n\ts_mov_b32 s42, %[cc]\n\ts_and_b32 s43, s42, 0x1fff\n\ts_add_i32 s43, s43, 0x43e000\n\tv_mov_b32 v186, %[uc]\n\t"
                 R3_LOOP(R3_WARM("10f", "98f-10f"), R3_DMA, "", R3_CNT_LOAD, R3_CNT_PAY, R3_CNT_PACK, "", 3, R3_CNT_SIG, 2, "", "", R3_CNT_CHK, R3_CNT_OOL) DIAG_R3_END : R3_OUTS : R3_INS, [cc] "s"(cc), [uc] "v"(uc) : R3_CLOB);
  } else {
    asm volatile("s_mov_b32 s67, %[anc]\n\t" R3_LOOP(".Lr3tw_beg:\n\t", "", R3_TW_WAIT, R3_TW_PT, "", "", R3_TW_TIME, 3, "", 1, R3_FWD R3_PREP, "", R3_TW_NEXT, R3_TW_LATE R3_TW_OOL ".Lr3tw_end:\n\t") DIAG_R3_END : R3_OUTS : R3_INS, [anc] "s"(anchor) : R3_CLOB, TBURST_CLOB);
  }
  if (KIND == 2 && lane < 2) st4c_asm(flagY(ws, sel, NSEG - 2 + lane, v), 0u);
  RTS(3);
}
DEV void role_r3(unsigned char* __restrict__ ws, int v, int wave, int lane, unsigned sel TUNE_ARG) {
  if (wave == 0) r3_body<0>(ws, v, wave, lane, sel TUNE_PASS);
  else if (wave == 6) r3_body<2>(ws, v, wave, lane, sel TUNE_PASS);
  else if (wave == 7) r3_body<3>(ws, v, wave, lane, sel TUNE_PASS);
  else r3_body<1>(ws, v, wave, lane, sel TUNE_PASS);
}

#define PF_LAG 4
DEV void role_pf(unsigned char* __restrict__ ws, int k, int wave, int lane, unsigned sel) {
  constexpr int LAG = PF_LAG;
  const rsrc_t ru = mkrsrc(ws + O_U, 30u << 20);
  unsigned have = 0;
  for (int t = 1; t < NSEG - 1; ++t) {
    if (wave == 0) {
      unsigned skip = 0;
      const unsigned need = (t >> 1) + 1;
      if (__any(have < need)) { wait_cnt(ws, doneU(ws, min(lane, 55)), need); have = ld_cnt(doneU(ws, min(lane, 55))); }
      if (t >= LAG) wait_flags(ws, flagY(ws, sel, t - LAG, k));
      else if (t >= LAG - 2) {
        wait_cnt(ws, anchorp(ws, 28 + k), 1);
        const unsigned at = (__builtin_amdgcn_readfirstlane(ld_cnt(anchorp(ws, 28 + k))) & ~1u) + (t - LAG + 2) * 264;
        skip = (int)(((unsigned)rt32() << 1) - at) > 40;
        for (int it = 0; it < 4096 && (int)(at - ((unsigned)rt32() << 1)) > 0; ++it) __builtin_amdgcn_s_sleep(1);
      }
      lds_w4((t & 1) * 4, skip);
    }
    bar();
    if (__builtin_amdgcn_readfirstlane(lds_r4((t & 1) * 4))) continue;
    u4v r[14];
#pragma unroll
    for (int i = 0; i < 14; ++i) { const int n = (k * 8 + wave) * 14 + i; r[i] = ldc16(ru, (n >> 4) * U_RUN + (t * 16 + (n & 15)) * 1024 + lane * 16); }
#pragma unroll
    for (int i = 0; i < 14; ++i) asm volatile("" :: "v"(r[i]));
  }
}

__global__ void __launch_bounds__(512, 2)
main_kernel(const unsigned short* __restrict__ x, const unsigned short* __restrict__ b1, const unsigned short* __restrict__ b2,
            unsigned short* __restrict__ out, unsigned char* __restrict__ ws TUNE_ARG) {
  const int b = blockIdx.x, lane = threadIdx.x & 63;
  const int wave = __builtin_amdgcn_readfirstlane(threadIdx.x >> 6);
  RTS(0);
  const int xc = b & 7, slot = b >> 3;
  if (xc == 0 || xc == 2) {
    const unsigned sel = *(const unsigned*)(ws + O_SEL);
    role_r1(ws, (xc >> 1) * 32 + slot, wave, lane, sel TUNE_PASS);
  } else if (xc == 1) {
    const unsigned sel = *(const unsigned*)(ws + O_SEL);
    if (slot < 28) role_r3(ws, slot, wave, lane, sel TUNE_PASS);
    else role_pf(ws, slot - 28, wave, lane, sel);
  } else {
    const int k = slot * 5 + (xc - 3);
    if (k < 56) role_w1<true>(ws, k, wave, lane);
    else if (k < 112) role_w1<false>(ws, k - 56, wave, lane);
    else {
      if (k < 144) role_ffn(ws, x, b1, b2, k - 112, wave, lane);
      else role_stt(ws, (k - 144) >> 1, k & 1, wave, lane);
      role_w3(ws, out, (159 - k) >> 2, k & 3, wave, lane);
    }
  }
}

extern "C" void kernel_launch(void* const* d_in, const int* in_sizes, int n_in, void* d_out, int out_size, void* d_ws,
                              size_t ws_size, hipStream_t stream) {
  unsigned char* ws = (unsigned char*)(((uintptr_t)d_ws + 2 * MB - 1) & ~(uintptr_t)(2 * MB - 1));
  const unsigned short* x = (const unsigned short*)d_in[0];
  constexpr int LDS_MAIN = 132 * 1024;
  (void)hipFuncSetAttribute((const void*)main_kernel, hipFuncAttributeMaxDynamicSharedMemorySize, LDS_MAIN);
  constexpr int LDS_PREP = 152 * 1024;
  (void)hipFuncSetAttribute((const void*)prep_kernel, hipFuncAttributeMaxDynamicSharedMemorySize, LDS_PREP);
  prep_kernel<<<256, 512, LDS_PREP, stream>>>(x, (const unsigned short*)d_in[1], (const unsigned short*)d_in[2], (const unsigned short*)d_in[3],
                                           (const unsigned short*)d_in[4], (const unsigned short*)d_in[5], (const unsigned short*)d_in[8],
                                           (const unsigned short*)d_in[10], ws);
  main_kernel<<<256, 512, LDS_MAIN, stream>>>(x, (const unsigned short*)d_in[9], (const unsigned short*)d_in[11], (unsigned short*)d_out, ws TUNE_PASS);
}
```

```cpp
#include <hip/hip_runtime.h>
#include <hip/hip_bf16.h>
#include <stdint.h>
#include <stdio.h>

typedef __bf16 bf2 __attribute__((ext_vector_type(2)));
typedef __bf16 bf8 __attribute__((ext_vector_type(8)));
typedef __bf16 bf16x __attribute__((ext_vector_type(16)));
typedef short s4v __attribute__((ext_vector_type(4)));
typedef float f2v __attribute__((ext_vector_type(2)));
typedef float f4v __attribute__((ext_vector_type(4)));
typedef float f16v __attribute__((ext_vector_type(16)));
typedef unsigned u2v __attribute__((ext_vector_type(2)));
typedef unsigned u4v __attribute__((ext_vector_type(4)));
typedef unsigned u8v __attribute__((ext_vector_type(8)));
#define LDS __attribute__((address_space(3)))
#define GLB __attribute__((address_space(1)))
#define DEV __device__ __forceinline__

constexpr int DM = 448, NH = 16, DH = 256, NSEG = 64, FF = 1792;
constexpr unsigned SENT = 0xFFFFFFFFu;

constexpr size_t MB = 1u << 20;
constexpr size_t O_HB = 0;
constexpr size_t O_FL = 4 * MB;
constexpr size_t O_SEL = O_FL + 256 * 1024;
constexpr size_t O_XN = 5 * MB;
constexpr size_t O_WA = 7 * MB;
constexpr size_t O_WB = 14 * MB;
constexpr size_t O_W1 = 21 * MB;
constexpr size_t O_W2 = 23 * MB;
constexpr size_t O_G = 25 * MB;
constexpr size_t O_U = 55 * MB;
constexpr size_t O_ST = 85 * MB;
constexpr size_t O_SM = 87 * MB;
constexpr size_t O_YG = 100 * MB;
constexpr size_t O_F = 111 * MB;
constexpr unsigned G_RUN = 64 * 14336 + 3 * 4096, U_RUN = 64 * 16384 + 5 * 4096, Y_RUN = 64 * 4096 + 4096;
constexpr size_t O_END = 115 * MB;

DEV unsigned pk2(float a, float b) { f2v v = {a, b}; return __builtin_bit_cast(unsigned, __builtin_convertvector(v, bf2)); }
DEV float bflo(unsigned u) { return __builtin_bit_cast(float, u << 16); }
DEV float bfhi(unsigned u) { return __builtin_bit_cast(float, u & 0xffff0000u); }
DEV float rbf(float a) { return bflo(pk2(a, 0.f) & 0xffffu); }
DEV bf8 asbf8(u4v v) { return __builtin_bit_cast(bf8, v); }
DEV u4v lds_r16(unsigned a) { return *(LDS u4v*)(size_t)a; }
DEV void lds_w16(unsigned a, u4v v) { *(LDS u4v*)(size_t)a = v; }
DEV u2v lds_tr(unsigned a) { return __builtin_bit_cast(u2v, __builtin_amdgcn_ds_read_tr16_b64_v4i16((LDS s4v*)(size_t)a)); }
DEV unsigned rdtime() { unsigned long long t; asm volatile("s_memtime %0\n\ts_waitcnt lgkmcnt(0)" : "=s"(t) :: "memory"); return (unsigned)t; }
DEV int swap13(int x) { return (x & 5) | ((x >> 2) & 2) | ((x << 2) & 8); }
DEV void dma16(const void* g, unsigned ldsaddr) {
  unsigned keep;
  asm volatile("s_mov_b32 %0, m0\n\ts_mov_b32 m0, %2\n\ts_nop 0\n\tglobal_load_lds_dwordx4 %1, off\n\ts_mov_b32 m0, %0" : "=&s"(keep) : "v"(g), "s"(ldsaddr) : "memory");
}
DEV void dma16c(const void* g, unsigned ldsaddr) {
  unsigned keep;
  asm volatile("s_mov_b32 %0, m0\n\ts_mov_b32 m0, %2\n\ts_nop 0\n\tglobal_load_lds_dwordx4 %1, off sc1\n\ts_mov_b32 m0, %0" : "=&s"(keep) : "v"(g), "s"(ldsaddr) : "memory");
}
DEV void vm0() { asm volatile("s_waitcnt vmcnt(0)" ::: "memory"); }
DEV void bar() { asm volatile("s_waitcnt lgkmcnt(0)\n\ts_barrier" ::: "memory"); }

DEV void stwt16(void* p, u4v v) { asm volatile("global_store_dwordx4 %0, %1, off sc1\n\ts_nop 1" :: "v"(p), "v"(v) : "memory"); }
DEV float wavesum(float v) {
  v += __builtin_bit_cast(float, __builtin_amdgcn_update_dpp(0, __builtin_bit_cast(int, v), 0xb1, 0xf, 0xf, true));
  v += __builtin_bit_cast(float, __builtin_amdgcn_update_dpp(0, __builtin_bit_cast(int, v), 0x4e, 0xf, 0xf, true));
  v += __builtin_bit_cast(float, __builtin_amdgcn_update_dpp(0, __builtin_bit_cast(int, v), 0x124, 0xf, 0xf, true));
  v += __builtin_bit_cast(float, __builtin_amdgcn_update_dpp(0, __builtin_bit_cast(int, v), 0x128, 0xf, 0xf, true));
  const unsigned u = __builtin_bit_cast(unsigned, v);
  const u2v a = __builtin_amdgcn_permlane16_swap(u, u, false, false);
  const unsigned ax = a.x, ay = a.y;
  const unsigned w = __builtin_bit_cast(unsigned, __builtin_bit_cast(float, ax) + __builtin_bit_cast(float, ay));
  const u2v c = __builtin_amdgcn_permlane32_swap(w, w, false, false);
  const unsigned cx = c.x, cy = c.y;
  return __builtin_bit_cast(float, cx) + __builtin_bit_cast(float, cy);
}
#define K0S(k)
DEV void ln_row(u4v xv, unsigned char* __restrict__ ws, int row, int lane) {
  float e[8];
  if (lane >= 56) xv = (u4v){0u, 0u, 0u, 0u};
#pragma unroll
  for (int j = 0; j < 4; ++j) { e[2 * j] = bflo(xv[j]); e[2 * j + 1] = bfhi(xv[j]); }
  float s = 0.f;
#pragma unroll
  for (int j = 0; j < 8; ++j) s += e[j];
  const float mean = wavesum(s) * (1.f / DM);
  float q = 0.f;
  if (lane < 56) {
#pragma unroll
    for (int j = 0; j < 8; ++j) { const float d = e[j] - mean; q += d * d; }
  }
  const float mb = rbf(mean), vb = rbf(wavesum(q) * (1.f / DM));
  const float den = rbf(__builtin_sqrtf(rbf(vb + 1e-5f)));
  unsigned o4[4];
#pragma unroll
  for (int j = 0; j < 4; ++j) o4[j] = pk2(rbf(e[2 * j] - mb) / den, rbf(e[2 * j + 1] - mb) / den);
  const bool odd = lane & 1;
  const unsigned r0 = __builtin_amdgcn_update_dpp(0u, odd ? o4[0] : o4[2], 0xb1, 0xf, 0xf, true);
  const unsigned r1 = __builtin_amdgcn_update_dpp(0u, odd ? o4[1] : o4[3], 0xb1, 0xf, 0xf, true);
  u4v p;
  if (odd) p = (u4v){r0, o4[2], r1, o4[3]}; else p = (u4v){o4[0], r0, o4[1], r1};
  if (lane < 56) stwt16(ws + O_XN + (size_t)(row >> 5) * 28672 + (lane >> 1) * 1024 + (lane & 1) * 512 + (row & 31) * 16, p);
}
DEV u4v srd(const void* p, unsigned bytes) { const unsigned long long a = (unsigned long long)p; return (u4v){(unsigned)a, (unsigned)(a >> 32), bytes, 0x00020000u}; }
DEV void bdma16(unsigned voff, u4v r, unsigned soff, unsigned ldsaddr) {
  asm volatile("s_mov_b32 m0, %3\n\ts_nop 0\n\tbuffer_load_dwordx4 %0, %1, %2 offen lds" :: "v"(voff), "s"(r), "s"(soff), "s"(ldsaddr) : "memory");
}
constexpr unsigned rowoff(int j) { return (16 * ((j >> 2) & 1) + 8 * (j & 1) + 4 * ((j >> 1) & 1) + 2 * (j >> 3)) * 8192u; }

template <bool ISA>
DEV void prep_gemm(const unsigned short* __restrict__ Rw, const unsigned short* __restrict__ Cw, const unsigned short* __restrict__ x,
                   unsigned char* __restrict__ ws, int b, int h, int cg, int wave, int lane) {
  const int g = lane >> 4, c16 = lane & 15, hl = lane >> 5, sl = lane & 31;
  const int row = b * 8 + wave;
  const unsigned xl = 147456 + wave * 1024;
  dma16(x + (size_t)row * DM + min(lane, 55) * 8, xl);
  if (ISA) {
    const u4v rc = srd(Cw + (size_t)64 * cg * 4096 + h * 256, 64 * 8192);
    const unsigned v0 = hl * 8192 + ((sl ^ hl ^ ((wave & 1) << 3)) << 4), s0 = ((wave >> 1) * 16 + (wave & 1) * 2) * 8192;
#pragma unroll
    for (int j = 0; j < 4; ++j) bdma16(v0 ^ (j << 5), rc, s0 + ((j & 1) * 8 + (j >> 1) * 4) * 8192, (wave * 4 + j) * 1024);
  } else {
    const u4v rc = srd(Cw + (size_t)h * 256 * DM + 64 * cg, 256 * 896);
    const unsigned v0 = (lane >> 3) * 896 + (((lane & 7) ^ (((lane >> 4) & 1) << 1)) << 4);
#pragma unroll
    for (int j = 0; j < 4; ++j) bdma16(v0 ^ ((j & 1) << 6), rc, (wave * 4 + j) * 8 * 896, (wave * 4 + j) * 1024);
  }
  if (wave == 7) {
    __builtin_amdgcn_sched_barrier(0);
    asm volatile("s_waitcnt vmcnt(0)\n\ts_barrier" ::: "memory");
    const u4v ones = {SENT, SENT, SENT, SENT}, zero = {0u, 0u, 0u, 0u};
#pragma unroll
    for (int j = 0; j < 16; ++j) if (b >= 2) stwt16(ws + O_HB + (size_t)b * 16384 + j * 1024 + lane * 16, ones);
    stwt16(ws + O_FL + b * 1024 + lane * 16, zero);
    ln_row(lds_r16(xl + lane * 16), ws, row, lane);
    return;
  }
  const float sc = ISA ? 0.0625f * 1.4426950408889634f : 1.f;
  unsigned char* dst = ws + (ISA ? O_WA : O_WB) + (size_t)(h * 28 + 4 * cg) * 14336 + 2 * wave * 1024 + lane * 16;
  const unsigned reg = 32768 + wave * 16384;
  const int rot = cg * 2;
  const unsigned srot = (16 * ((rot >> 2) & 1) + 8 * (rot & 1) + 4 * ((rot >> 1) & 1) + 2 * (rot >> 3)) * 8192u, mrot = reg | (rot << 10);
  const unsigned vr = hl * 8192 + (((sl ^ hl) << 4) ^ ((rot & 7) << 5));
  const u4v rr = srd(Rw + (size_t)(64 * wave) * 4096 + h * 256, 64 * 8192);
  const __amdgpu_buffer_rsrc_t rq = __builtin_amdgcn_make_buffer_rsrc((void*)(Rw + (size_t)(64 * wave + 32) * 4096 + h * 256), 0, 32 * 8192, 0x00020000);
  u4v nx[16];
#pragma unroll
  for (int j = 0; j < 2; ++j) bdma16(vr ^ ((j & 7) << 5), rr, srot ^ rowoff(j), mrot ^ (j << 10));
#pragma unroll
  for (int j = 0; j < 2; ++j) nx[j] = __builtin_amdgcn_raw_buffer_load_b128(rq, vr ^ ((j & 7) << 5), srot ^ rowoff(j), 0);
  __builtin_amdgcn_sched_barrier(0);
  asm volatile("s_waitcnt vmcnt(8)" ::: "memory");
  ln_row(lds_r16(xl + lane * 16), ws, row, lane);
  __builtin_amdgcn_sched_barrier(0);
  K0S(1);
  asm volatile("s_waitcnt vmcnt(5)\n\ts_barrier" ::: "memory");
  K0S(0);
#pragma unroll
  for (int j = 2; j < 16; ++j) bdma16(vr ^ ((j & 7) << 5), rr, srot ^ rowoff(j), mrot ^ (j << 10));
#pragma unroll
  for (int j = 2; j < 16; ++j) nx[j] = __builtin_amdgcn_raw_buffer_load_b128(rq, vr ^ ((j & 7) << 5), srot ^ rowoff(j), 0);
  __builtin_amdgcn_sched_barrier(0);
  asm volatile("s_waitcnt vmcnt(14)" ::: "memory");
  K0S(2);
  const int q = c16 >> 2, p = c16 & 3;
  const int xr = (q >> 1) | ((g & 1) << 1);
  auto bfrag = [&](int es, int ti) -> u4v {
    if (ISA) return lds_r16((ti * 16 + c16) * 512 + (((es * 4 + g) ^ c16) << 4));
    const unsigned ad = (32 * es + 8 * g + q) * 128 + ((ti ^ xr) << 5) + p * 8;
    const u2v lo = lds_tr(ad), hi = lds_tr(ad + 512);
    return (u4v){lo.x, lo.y, hi.x, hi.y};
  };
  auto afrag = [&](int es, int t) -> u4v { return lds_r16(reg + (t * 16 + c16) * 512 + (((es * 4 + g) ^ c16) << 4)); };
#pragma unroll
  for (int rb = 0; rb < 2; ++rb) {
    u4v bb[2][4], aa[2][2];
#pragma unroll
    for (int ti = 0; ti < 4; ++ti) bb[0][ti] = bfrag(0, ti);
    aa[0][0] = afrag(0, 0); aa[0][1] = afrag(0, 1);
    f4v acc[2][4];
#pragma unroll
    for (int t = 0; t < 2; ++t)
#pragma unroll
      for (int ti = 0; ti < 4; ++ti) acc[t][ti] = (f4v){0.f, 0.f, 0.f, 0.f};
#pragma unroll
    for (int es = 0; es < 8; ++es) {
      if (es < 7) {
        aa[(es + 1) & 1][0] = afrag(es + 1, 0); aa[(es + 1) & 1][1] = afrag(es + 1, 1);
#pragma unroll
        for (int ti = 0; ti < 4; ++ti) bb[(es + 1) & 1][ti] = bfrag(es + 1, ti);
      }
#pragma unroll
      for (int ti = 0; ti < 4; ++ti)
#pragma unroll
        for (int t = 0; t < 2; ++t)
          acc[t][ti] = __builtin_amdgcn_mfma_f32_16x16x32_bf16(asbf8(aa[es & 1][t]), asbf8(bb[es & 1][ti]), acc[t][ti], 0, 0, 0);
      __builtin_amdgcn_sched_barrier(0);
    }
    if (rb == 0) {
#pragma unroll
      for (int j = 0; j < 16; ++j) lds_w16((mrot ^ (j << 10)) + lane * 16, nx[j]);
    }
#pragma unroll
    for (int ti = 0; ti < 4; ++ti) {
      const u4v o = {pk2(acc[0][ti][0] * sc, acc[0][ti][1] * sc), pk2(acc[0][ti][2] * sc, acc[0][ti][3] * sc),
                     pk2(acc[1][ti][0] * sc, acc[1][ti][1] * sc), pk2(acc[1][ti][2] * sc, acc[1][ti][3] * sc)};
      stwt16(dst + (size_t)ti * 14336 + rb * 1024, o);
    }
  }
  K0S(3);
}

__global__ void __launch_bounds__(512, 2)
prep_kernel(const unsigned short* __restrict__ x, const unsigned short* __restrict__ mem0,
            const unsigned short* __restrict__ Wq, const unsigned short* __restrict__ Wk,
            const unsigned short* __restrict__ Wv, const unsigned short* __restrict__ Wo,
            const unsigned short* __restrict__ w1, const unsigned short* __restrict__ w2,
            unsigned char* __restrict__ ws) {
  const int b = blockIdx.x, tid = threadIdx.x, lane = tid & 63;
  const int wave = __builtin_amdgcn_readfirstlane(tid >> 6);
  const int xc = b & 7, slot = b >> 3;
  if (slot < 28) {
    const int m = xc * 4 + slot / 7, cg = slot % 7;
    if (m < 16) prep_gemm<true>(Wk, Wq, x, ws, b, m, cg, wave, lane);
    else prep_gemm<false>(Wv, Wo, x, ws, b, m - 16, cg, wave, lane);
  } else {
    const int row = b * 8 + wave;
    u4v xv = {0u, 0u, 0u, 0u};
    const int misc = (slot - 28) * 8 + xc;
    const int job = (misc - 1) * 8 + wave;
    const unsigned reg = wave * 14336;
    if (misc == 0) {
      if (wave == 7) {
        u4v m0[7];
#pragma unroll
        for (int j = 0; j < 7; ++j) { const int n = j * 64 + lane; m0[j] = *(const u4v*)(mem0 + ((n >> 1) & 7) * DM + (n >> 4) * 16 + (n & 1) * 8); }
        const u4v ones = {SENT, SENT, SENT, SENT};
        stwt16(ws + O_HB + (lane >> 4) * 8192 + (lane & 15) * 64, ones);
        stwt16(ws + O_HB + 2 * MB + (lane >> 4) * 8192 + (lane & 15) * 64, ones);
#pragma unroll
        for (int j = 0; j < 7; ++j) asm volatile("" : "+v"(m0[j]));
        vm0();
        auto timed = [&](const unsigned char* pc) -> unsigned {
          unsigned v;
          const unsigned c0 = rdtime();
          asm volatile("global_load_dword %0, %1, %2 sc1\n\ts_waitcnt vmcnt(0)" : "=&v"(v) : "v"(0u), "s"(pc) : "memory");
          return rdtime() - c0;
        };
        unsigned best[6];
#pragma unroll
        for (int c = 0; c < 6; ++c) best[c] = 0x7fffffff;
#pragma unroll 1
        for (int it = 0; it < 3; ++it) {
#pragma unroll
          for (int c = 0; c < 4; ++c) best[c] = min(best[c], timed(ws + O_HB + c * 8192 + it * 256));
        }
        unsigned sel = 0, bv = best[0];
#pragma unroll
        for (unsigned k = 1; k < 4; ++k) if (best[k] < bv) { bv = best[k]; sel = k; }
#pragma unroll 1
        for (int it = 0; it < 2; ++it) {
#pragma unroll
          for (int c = 0; c < 2; ++c) best[4 + c] = min(best[4 + c], timed(ws + O_HB + 2 * MB + (sel ^ c) * 8192 + it * 256));
        }
        sel |= (sel ^ (best[5] <= best[4] ? 1u : 0u)) << 2;
        if (lane == 0) *(unsigned*)(ws + O_SEL) = sel;
#pragma unroll
        for (int j = 0; j < 7; ++j) stwt16(ws + O_HB + (sel & 3u) * 8192 + j * 1024 + lane * 16, m0[j]);
#pragma unroll
        for (int j = 8; j < 32; ++j) stwt16(ws + O_HB + (((sel & 3u) * 8192) ^ (j * 1024)) + lane * 16, ones);
      }
    } else if (job < 224) {
      const bool first = job < 112;
      const int step = first ? job >> 2 : job - 112;
      const unsigned short* src = first ? w1 + (size_t)step * 16 * FF + (job & 3) * 448 : w2 + (size_t)step * 16 * DM;
      const int ld = first ? FF : DM;
#pragma unroll
      for (int j = 0; j < 14; ++j) { const int n = j * 64 + lane, r = n / 56; dma16(src + (size_t)r * ld + (n - r * 56) * 8, reg + j * 1024); }
    }
    if (lane < 56) xv = *(const u4v*)(x + (size_t)row * DM + lane * 8);
    asm volatile("" : "+v"(xv));
    vm0();
    if (misc > 0 && job < 224) {
      const bool first = job < 112;
      const int hi = lane >> 5, q = (lane & 15) >> 2;
      const int r0 = first ? 4 * hi + 8 * (q >> 1) + (q & 1) : 4 * hi + q, dr = first ? 2 : 8;
      const unsigned ad = reg + r0 * 896 + ((lane >> 4) & 1) * 32 + (lane & 3) * 8;
      unsigned char* dst = ws + (first ? O_W1 + (size_t)((job & 3) * 14 * 28 + (job >> 2)) * 1024 : O_W2 + (size_t)(job - 112) * 1024) + lane * 16;
      const size_t dstep = first ? 28672 : 114688;
#pragma unroll
      for (int t = 0; t < 14; ++t) {
        const u2v lo = lds_tr(ad + t * 64), hv = lds_tr(ad + t * 64 + dr * 896);
        stwt16(dst + t * dstep, (u4v){lo.x, lo.y, hv.x, hv.y});
      }
    }
    const u4v ones = {SENT, SENT, SENT, SENT}, zero = {0u, 0u, 0u, 0u};
    stwt16(ws + O_HB + (size_t)b * 16384 + tid * 16, ones);
    stwt16(ws + O_HB + (size_t)b * 16384 + 8192 + tid * 16, ones);
    if (wave == 0) stwt16(ws + O_FL + b * 1024 + lane * 16, zero);
    ln_row(xv, ws, row, lane);
  }
}
constexpr int C_W = 0  , C_F = 1856, C_ABORT = 1900, C_ANCHOR = 1920;
typedef __amdgpu_buffer_rsrc_t rsrc_t;
DEV rsrc_t mkrsrc(const void* p, unsigned bytes) { return __builtin_amdgcn_make_buffer_rsrc((void*)p, 0, bytes, 0x00020000); }
DEV u4v ldc16(rsrc_t r, unsigned off) { return __builtin_amdgcn_raw_buffer_load_b128(r, off, 0, 16); }
DEV u2v ldc8(rsrc_t r, unsigned off) { return __builtin_amdgcn_raw_buffer_load_b64(r, off, 0, 16); }
DEV void stc16(rsrc_t r, unsigned off, u4v v) { __builtin_amdgcn_raw_buffer_store_b128(v, r, off, 0, 16); }
DEV void stc4(rsrc_t r, unsigned off, unsigned v) { __builtin_amdgcn_raw_buffer_store_b32(v, r, off, 0, 16); }
DEV unsigned* cntp(unsigned char* ws, int idx) { return (unsigned*)(ws + O_FL) + idx * 16; }
DEV unsigned ld_cnt(const unsigned* p) { unsigned v; asm volatile("global_load_dword %0, %1, off sc1\n\ts_waitcnt vmcnt(0)" : "=&v"(v) : "v"(p) : "memory"); return v; }
DEV void add_cnt(unsigned* p, int lane) {
  if (lane == 0) { const unsigned one = 1; asm volatile("global_atomic_add %0, %1, off sc1" :: "v"(p), "v"(one) : "memory"); }
}
DEV void post(unsigned* p, unsigned v, int lane) { if (lane == 0) asm volatile("global_store_dword %0, %1, off sc1" :: "v"(p), "v"(v) : "memory"); }
DEV unsigned* doneG(unsigned char* ws, int j) { return cntp(ws, C_W + 2 * j); }
DEV unsigned* doneU(unsigned char* ws, int j) { return cntp(ws, C_W + 2 * (56 + j)); }
constexpr int SPIN_CAP = 1 << 17;
DEV void wait_cnt(unsigned char* ws, const unsigned* p, unsigned target, int nap = 6) {
  for (int it = 0;; ++it) {
    const unsigned v = ld_cnt(p);
    if (__all(v >= target)) return;
    if ((it & 63) == 63) {
      if (it >= SPIN_CAP) { const unsigned one = 1; asm volatile("global_store_dword %0, %1, off sc1" :: "v"(cntp(ws, C_ABORT)), "v"(one) : "memory"); return; }
      if (__any(ld_cnt(cntp(ws, C_ABORT)) != 0)) return;
    }
    if (nap > 6) __builtin_amdgcn_s_sleep(32); else __builtin_amdgcn_s_sleep(2);
  }
}
DEV unsigned hoff(unsigned sel, int gi, int kind) {
  const unsigned k = ((sel >> (2 * (gi >> 6))) & 3u) ^ ((((unsigned)gi >> 4) & 1u) << 1) ^ (unsigned)kind;
  return (unsigned)O_HB + (unsigned)gi * 32768u + k * 8192u;
}
DEV unsigned char* hblk(unsigned char* ws, unsigned sel, int gi, int kind) { return ws + hoff(sel, gi, kind); }
DEV unsigned char* flagS(unsigned char* ws, unsigned sel, int s, int id) { return hblk(ws, sel, s, 3) + id * 128; }
DEV unsigned char* flagY(unsigned char* ws, unsigned sel, int s, int v) { return hblk(ws, sel, 64 + s, 3) + v * 128; }
DEV unsigned char* flagT(unsigned char* ws, unsigned sel, int s, int h) { return hblk(ws, sel, 64 + s, 1) + h * 128; }
DEV void wait_flags(unsigned char* ws, const unsigned char* p) {
  for (int it = 0;; ++it) {
    if (!__any(ld_cnt((const unsigned*)p) == SENT)) return;
    if ((it & 63) == 63) {
      if (it >= SPIN_CAP) { const unsigned one = 1; asm volatile("global_store_dword %0, %1, off sc1" :: "v"(cntp(ws, C_ABORT)), "v"(one) : "memory"); return; }
      if (__any(ld_cnt(cntp(ws, C_ABORT)) != 0)) return;
    }
    __builtin_amdgcn_s_sleep(6);
  }
}
DEV f16v mfma32(u4v a, u4v b, f16v c) { return __builtin_amdgcn_mfma_f32_32x32x16_bf16(asbf8(a), asbf8(b), c, 0, 0, 0); }
DEV f4v mfma16(u4v a, u4v b, f4v c) { return __builtin_amdgcn_mfma_f32_16x16x32_bf16(asbf8(a), asbf8(b), c, 0, 0, 0); }
DEV void st16c_asm(void* p, u4v v) { asm volatile("global_store_dwordx4 %0, %1, off sc1\n\ts_nop 1" :: "v"(p), "v"(v) : "memory"); }

#define RTS(k)
template <bool ISG>
DEV void role_w1(unsigned char* __restrict__ ws, int j, int wave, int lane) {
  const int ct = wave & 3, rh = wave >> 2, hi = lane >> 5, c32 = lane & 31;
  int h, tile0;
  if (ISG) { const int hp = j / 7, ctile = (j % 7) * 4 + ct; h = 2 * hp + ctile / 14; tile0 = 2 * (ctile % 14); }
  else { const int V = j >> 2; h = 4 * (j & 3) + ct; tile0 = 2 * V; }
  const unsigned char* xn = ws + O_XN + wave * 7168 + lane * 16;
#pragma unroll
  for (int i = 0; i < 7; ++i) dma16(xn + i * 1024, wave * 7168 + i * 1024);
  u4v w[28];
  {
    const unsigned char* p = ws + (ISG ? O_WA : O_WB) + (size_t)(h * 28 + tile0 + (c32 >> 4)) * 14336 + hi * 256 + (c32 & 15) * 16;
#pragma unroll
    for (int s = 0; s < 28; ++s) w[s] = *(const u4v*)(p + (s >> 1) * 1024 + (s & 1) * 512);
  }
  constexpr int SEGB = ISG ? 14336 : 16384;
  unsigned char* out = (ISG ? ws + O_G + (size_t)(h * 2 + (c32 >> 4)) * G_RUN + (tile0 >> 1) * 1024
                            : ws + O_U + (size_t)(tile0 + (c32 >> 4)) * U_RUN + h * 1024) + rh * SEGB + hi * 256 + (c32 & 15) * 16;
  const int tok = ISG ? c32 : ((c32 & 19) | ((c32 >> 1) & 4) | ((c32 << 1) & 8));
  const unsigned rd = rh * 28672 + hi * 512 + tok * 16;
  unsigned* done = ISG ? doneG(ws, j) : doneU(ws, j);
#pragma unroll
  for (int s = 0; s < 28; ++s) asm volatile("" : "+v"(w[s]));
  vm0();
  RTS(1);
  for (int p = 0; p < 32; ++p) {
    asm volatile("s_waitcnt vmcnt(2)\n\ts_barrier" ::: "memory");
    if (wave == 7 && p >= 6) post(done, p - 1, lane);
    const unsigned buf = (p & 1) * 57344;
    const unsigned char* src = xn + (size_t)(p < 31 ? p + 1 : p) * 57344;
    const unsigned dstl = (57344 - buf) + wave * 7168;
    f16v acc;
#pragma unroll
    for (int i = 0; i < 16; ++i) acc[i] = 0.f;
    const LDS u4v* xp = (const LDS u4v*)(size_t)(buf + rd);
    u4v xr[5];
#pragma unroll
    for (int s = 0; s < 4; ++s) xr[s] = xp[s * 64];
#pragma unroll
    for (int s = 0; s < 28; ++s) {
      if (s + 4 < 28) xr[(s + 4) % 5] = xp[(s + 4) * 64];
      acc = ISG ? mfma32(w[s], xr[s % 5], acc) : mfma32(xr[s % 5], w[s], acc);
      __builtin_amdgcn_sched_barrier(0);
      if (s % 3 == 0 && s / 3 < 7) { dma16(src + (s / 3) * 1024, dstl + (s / 3) * 1024); __builtin_amdgcn_sched_barrier(0); }
      if (s == 16) {
        asm volatile("s_waitcnt vmcnt(6)" ::: "memory");
        __builtin_amdgcn_sched_barrier(0);
      }
    }
    const u4v o0 = {pk2(acc[0], acc[1]), pk2(acc[2], acc[3]), pk2(acc[4], acc[5]), pk2(acc[6], acc[7])};
    const u4v o1 = {pk2(acc[8], acc[9]), pk2(acc[10], acc[11]), pk2(acc[12], acc[13]), pk2(acc[14], acc[15])};
    unsigned char* o = out + p * 2 * SEGB;
    st16c_asm(o, o0); st16c_asm(o + 512, o1);
    if (p < 4) {
      RTS(4);
      asm volatile("s_waitcnt vmcnt(0)\n\ts_barrier" ::: "memory");
      if (wave == 7) post(done, p + 1, lane);
      RTS(2);
    }
  }
  asm volatile("s_waitcnt vmcnt(0)\n\ts_barrier" ::: "memory");
  if (wave == 7) post(done, 32, lane);
  RTS(3);
}

template <bool P2>
DEV void ffn_body(unsigned char* __restrict__ ws, const unsigned short* __restrict__ x, const unsigned short* __restrict__ b1,
                  const unsigned short* __restrict__ b2, int job, int wave, int lane) {
  const int hi = lane >> 5, c32 = lane & 31;
  constexpr int NL = P2 ? 60 : 28, RING = P2 ? 12 : 14, DIST = RING - 1;
  const u4v* w1p = (const u4v*)(ws + O_W1 + (size_t)wave * 28672) + lane;
  const u4v* w2p = (const u4v*)(ws + O_W2 + (size_t)(2 * wave) * 114688) + lane;
  auto wload = [&](int c, int i) -> u4v {
    if (i < 28) return w1p[(size_t)c * 14336 + i * 64];
    return w2p[(size_t)c * 1024 + ((i - 28) >> 1) * 64 + ((i - 28) & 1) * 7168];
  };
  u4v wr[RING];
#pragma unroll
  for (int i = 0; i < DIST; ++i) wr[i] = wload(0, i);
  {
    const unsigned char* xn = ws + O_XN + (size_t)job * 57344 + wave * 7168 + lane * 16;
#pragma unroll
    for (int i = 0; i < 7; ++i) dma16(xn + i * 1024, wave * 7168 + i * 1024);
  }
  f16v o[2][2];
#pragma unroll
  for (int a = 0; a < 2; ++a)
#pragma unroll
    for (int bq = 0; bq < 2; ++bq)
#pragma unroll
      for (int i = 0; i < 16; ++i) o[a][bq][i] = 0.f;
  vm0();
  bar();
  const LDS u4v* xp = (const LDS u4v*)(size_t)(lane * 16);
  for (int c = 0; c < 7; ++c) {
    const int cn = c < 6 ? c + 1 : 6;
    const unsigned hbuf = 57344 + (c & 1) * 32768;
    u2v bv[4];
#pragma unroll
    for (int q = 0; q < 4; ++q) bv[q] = *(const u2v*)(b1 + 32 * (8 * c + wave) + 4 * hi + 8 * q);
    f16v a0, a1;
#pragma unroll
    for (int i = 0; i < 16; ++i) { a0[i] = 0.f; a1[i] = 0.f; }
    u4v xa[3], xb[3];
#pragma unroll
    for (int s = 0; s < 2; ++s) { xa[s] = xp[s * 64]; xb[s] = xp[1792 + s * 64]; }
#pragma unroll
    for (int s = 0; s < 28; ++s) {
      wr[(s + DIST) % RING] = (s + DIST < NL) ? wload(c, s + DIST) : wload(cn, s + DIST - NL);
      if (s + 2 < 28) { xa[(s + 2) % 3] = xp[(s + 2) * 64]; xb[(s + 2) % 3] = xp[1792 + (s + 2) * 64]; }
      a0 = mfma32(wr[s % RING], xa[s % 3], a0);
      a1 = mfma32(wr[s % RING], xb[s % 3], a1);
      __builtin_amdgcn_sched_barrier(0);
    }
#pragma unroll
    for (int i = 0; i < 16; ++i) {
      const unsigned bw = bv[i >> 2][(i >> 1) & 1];
      const float bs = (i & 1) ? bfhi(bw) : bflo(bw);
      const float t0 = fmaxf(a0[i] + bs, 0.f), t1 = fmaxf(a1[i] + bs, 0.f);
      a0[i] = t0 * t0; a1[i] = t1 * t1;
    }
#pragma unroll
    for (int bq = 0; bq < 2; ++bq) {
      lds_w16(hbuf + ((wave * 2 + bq) * 2 + 0) * 1024 + lane * 16,
              (u4v){pk2(a0[8 * bq], a0[8 * bq + 1]), pk2(a0[8 * bq + 2], a0[8 * bq + 3]), pk2(a0[8 * bq + 4], a0[8 * bq + 5]), pk2(a0[8 * bq + 6], a0[8 * bq + 7])});
      lds_w16(hbuf + ((wave * 2 + bq) * 2 + 1) * 1024 + lane * 16,
              (u4v){pk2(a1[8 * bq], a1[8 * bq + 1]), pk2(a1[8 * bq + 2], a1[8 * bq + 3]), pk2(a1[8 * bq + 4], a1[8 * bq + 5]), pk2(a1[8 * bq + 6], a1[8 * bq + 7])});
    }
    bar();
    if (P2) {
      const LDS u4v* hp = (const LDS u4v*)(size_t)(hbuf + lane * 16);
      u4v h0[2], h1[2];
      h0[0] = hp[0]; h1[0] = hp[64];
#pragma unroll
      for (int s = 0; s < 16; ++s) {
        const int i = 28 + 2 * s;
        wr[(i + DIST) % RING] = (i + DIST < NL) ? wload(c, i + DIST) : wload(cn, i + DIST - NL);
        if (s + 1 < 16) { h0[(s + 1) & 1] = hp[(s + 1) * 128]; h1[(s + 1) & 1] = hp[(s + 1) * 128 + 64]; }
        o[0][0] = mfma32(wr[i % RING], h0[s & 1], o[0][0]); o[0][1] = mfma32(wr[i % RING], h1[s & 1], o[0][1]);
        __builtin_amdgcn_sched_barrier(0);
        wr[(i + 1 + DIST) % RING] = (i + 1 + DIST < NL) ? wload(c, i + 1 + DIST) : wload(cn, i + 1 + DIST - NL);
        o[1][0] = mfma32(wr[(i + 1) % RING], h0[s & 1], o[1][0]); o[1][1] = mfma32(wr[(i + 1) % RING], h1[s & 1], o[1][1]);
        __builtin_amdgcn_sched_barrier(0);
      }
    }
  }
  if (P2) {
    const rsrc_t rf = mkrsrc(ws + O_F, 4u << 20);
#pragma unroll
    for (int a = 0; a < 2; ++a)
#pragma unroll
      for (int sg = 0; sg < 2; ++sg)
#pragma unroll
        for (int Q = 0; Q < 4; ++Q) {
          const int n = 32 * (2 * wave + a) + 8 * Q + 4 * hi, seg = 2 * job + sg;
          const u2v xv = *(const u2v*)(x + (size_t)(seg * 32 + c32) * DM + n), bv = *(const u2v*)(b2 + n);
          const f4v r = {o[a][sg][4 * Q] + bflo(bv.x) + bflo(xv.x), o[a][sg][4 * Q + 1] + bfhi(bv.x) + bfhi(xv.x),
                         o[a][sg][4 * Q + 2] + bflo(bv.y) + bflo(xv.y), o[a][sg][4 * Q + 3] + bfhi(bv.y) + bfhi(xv.y)};
          const int v = 2 * (2 * wave + a) + (Q >> 1), g = 2 * (Q & 1) + hi;
          stc16(rf, ((seg * 28 + v) * 2 + (c32 >> 4)) * 1024 + (g * 16 + (c32 & 15)) * 16, __builtin_bit_cast(u4v, r));
        }
    vm0();
    add_cnt(cntp(ws, C_F + job), lane);
    RTS(3);
  }
}
DEV void role_ffn(unsigned char* __restrict__ ws, const unsigned short* __restrict__ x, const unsigned short* __restrict__ b1,
                  const unsigned short* __restrict__ b2, int job, int wave, int lane) {
  if (wave < 7) ffn_body<true>(ws, x, b1, b2, job, wave, lane);
  else ffn_body<false>(ws, x, b1, b2, job, wave, lane);
}

DEV float asf(unsigned u) { return __builtin_bit_cast(float, u); }
DEV unsigned asu(float f) { return __builtin_bit_cast(unsigned, f); }
DEV float xmax16_32(float v) {
  const u2v a = __builtin_amdgcn_permlane16_swap(asu(v), asu(v), false, false);
  v = fmaxf(asf(a.x), asf(a.y));
  const u2v c = __builtin_amdgcn_permlane32_swap(asu(v), asu(v), false, false);
  return fmaxf(asf(c.x), asf(c.y));
}
DEV float xsum16_32(float v) {
  const u2v a = __builtin_amdgcn_permlane16_swap(asu(v), asu(v), false, false);
  v = asf(a.x) + asf(a.y);
  const u2v c = __builtin_amdgcn_permlane32_swap(asu(v), asu(v), false, false);
  return asf(c.x) + asf(c.y);
}
DEV float halfsum(float a, float b) {
  const u2v s = __builtin_amdgcn_permlane32_swap(asu(a), asu(b), false, false);
  return asf(s.x) + asf(s.y);
}

DEV void role_stt(unsigned char* __restrict__ ws, int grp, int hg, int wave, int lane) {
  const unsigned sel = *(const unsigned*)(ws + O_SEL);
  const int g = lane >> 4, c16 = lane & 15, h = 8 * hg + wave;
  const rsrc_t rg = mkrsrc(ws + O_G, 30u << 20);
  const LDS u4v* xp = (const LDS u4v*)(size_t)((g >> 1) * 1024 + (g & 1) * 512 + c16 * 16);
  for (int s = grp; s < NSEG; s += 8) {
    if (wave == 7) wait_cnt(ws, doneG(ws, 28 * hg + min(lane, 27)), (s >> 1) + 1, 32);
    bar();
    if (wave < 7) {
#pragma unroll
      for (int i = 0; i < 4; ++i) dma16(ws + O_XN + (size_t)s * 28672 + (wave * 4 + i) * 1024 + lane * 16, (wave * 4 + i) * 1024);
    }
    u4v ga[28];
#pragma unroll
    for (int T = 0; T < 28; ++T) ga[T] = ldc16(rg, (h * 2 + T / 14) * G_RUN + s * 14336 + (T % 14) * 1024 + lane * 16);
    __builtin_amdgcn_sched_barrier(0);
    asm volatile("s_waitcnt vmcnt(28)\n\ts_barrier" ::: "memory");
    f4v acc[2][2];
#pragma unroll
    for (int q = 0; q < 2; ++q) { acc[0][q] = (f4v){0.f, 0.f, 0.f, 0.f}; acc[1][q] = acc[0][q]; }
#pragma unroll
    for (int T = 0; T < 14; ++T) {
      const u4v x0 = xp[T * 128], x1 = xp[T * 128 + 16];
      acc[0][0] = mfma16(ga[T], x0, acc[0][0]); acc[0][1] = mfma16(ga[T], x1, acc[0][1]);
      acc[1][0] = mfma16(ga[14 + T], x0, acc[1][0]); acc[1][1] = mfma16(ga[14 + T], x1, acc[1][1]);
    }
    unsigned char* o = ws + O_ST + (size_t)(s * 16 + h) * 2048 + c16 * 32 + g * 8;
#pragma unroll
    for (int q = 0; q < 2; ++q)
#pragma unroll
      for (int t = 0; t < 2; ++t) {
        const u2v val = {pk2(acc[t][q][0], acc[t][q][1]), pk2(acc[t][q][2], acc[t][q][3])};
        asm volatile("global_store_dwordx2 %0, %1, off sc1" :: "v"(o + q * 1024 + t * 512), "v"(val) : "memory");
      }
    vm0();
    if (lane == 0) { const unsigned zero = 0; asm volatile("global_store_dword %0, %1, off sc1" :: "v"(flagT(ws, sel, s, h)), "v"(zero) : "memory"); }
  }
  RTS(3);
}

constexpr int W3_GROUPS = 12;
DEV void role_w3(unsigned char* __restrict__ ws, unsigned short* __restrict__ out, int grp, int C, int wave, int lane) {
  const unsigned sel = *(const unsigned*)(ws + O_SEL);
  const int g = lane >> 4, c16 = lane & 15, v = 7 * C + (wave < 7 ? wave : 6);
  const rsrc_t ru = mkrsrc(ws + O_U, 30u << 20), rf = mkrsrc(ws + O_F, 4u << 20);
  constexpr unsigned L_P = 0, L_PM = 32768, L_ST = 40960, L_SM = L_ST + 32768, L_Y = L_SM + 32768;
  for (int s = grp; s < NSEG; s += W3_GROUPS) {
    if (wave == 7) {
      wait_cnt(ws, doneU(ws, 4 * ((7 * C) >> 1) + (lane & 15)), (s >> 1) + 1);
      wait_cnt(ws, cntp(ws, C_F + (s >> 1)), 7);
      wait_flags(ws, flagT(ws, sel, s, lane & 15));
    }
    bar();
    u4v uf[16], ff[2];
    if (wave < 7) {
#pragma unroll
      for (int hh = 0; hh < 16; ++hh) uf[hh] = ldc16(ru, v * U_RUN + (s * 16 + hh) * 1024 + lane * 16);
#pragma unroll
      for (int q = 0; q < 2; ++q) ff[q] = ldc16(rf, ((s * 28 + v) * 2 + q) * 1024 + lane * 16);
    } else {
      wait_flags(ws, flagY(ws, sel, s, 7 * C + min(lane, 6)));
      wait_flags(ws, flagS(ws, sel, s, lane));
    }
    __builtin_amdgcn_sched_barrier(0);
    bar();
#pragma unroll
    for (int it = 0; it < 4; ++it) {
      dma16c(ws + O_ST + (size_t)(s * 32 + wave * 4 + it) * 1024 + lane * 16, L_ST + (wave * 4 + it) * 1024);
      dma16c(ws + O_SM + (size_t)(s * 32 + wave * 4 + it) * 1024 + lane * 16, L_SM + (wave * 4 + it) * 1024);
    }
    if (wave < 7) {
#pragma unroll
      for (int H = 0; H < 4; ++H) dma16c(ws + O_YG + (size_t)v * Y_RUN + (s * 4 + H) * 1024 + lane * 16, L_Y + (wave * 4 + H) * 1024);
      asm volatile("s_waitcnt vmcnt(4)" ::: "memory");
    } else vm0();
#pragma unroll
    for (int it = 0; it < 4; ++it) {
      const int q = it & 1, hh = 2 * wave + (it >> 1), tq = 16 * q + c16;
      const u4v st = lds_r16(L_ST + (wave * 4 + it) * 1024 + (g >> 1) * 512 + c16 * 32 + (g & 1) * 16);
      const LDS unsigned* sm = (const LDS unsigned*)(size_t)(L_SM + (wave * 4 + it) * 1024 + lane * 4);
      const unsigned s0 = sm[0], s1 = sm[64], s2 = sm[128], s3 = sm[192];
      float e[8];
      float m0 = (bflo(s0) + bflo(s1)) + (bflo(s2) + bflo(s3));
      float m1 = (bfhi(s0) + bfhi(s1)) + (bfhi(s2) + bfhi(s3));
#pragma unroll
      for (int i = 0; i < 8; ++i) { const unsigned w = st[i >> 1]; e[i] = (8 * g + i <= tq) ? ((i & 1) ? bfhi(w) : bflo(w)) : -__builtin_inff(); }
      float mx = fmaxf(m0, m1);
#pragma unroll
      for (int i = 0; i < 8; ++i) mx = fmaxf(mx, e[i]);
      mx = xmax16_32(mx);
      m0 = __builtin_amdgcn_exp2f(m0 - mx); m1 = __builtin_amdgcn_exp2f(m1 - mx);
      float sum = m0 + m1;
#pragma unroll
      for (int i = 0; i < 8; ++i) { e[i] = __builtin_amdgcn_exp2f(e[i] - mx); sum += e[i]; }
      const float r = __builtin_amdgcn_rcpf(xsum16_32(sum));
      lds_w16(L_P + (q * 16 + hh) * 1024 + lane * 16,
              (u4v){pk2(e[0] * r, e[1] * r), pk2(e[2] * r, e[3] * r), pk2(e[4] * r, e[5] * r), pk2(e[6] * r, e[7] * r)});
      *(LDS unsigned*)(size_t)(L_PM + (q * 4 + (hh >> 2)) * 1024 + lane * 16 + (hh & 3) * 4) = pk2(m0 * r, m1 * r);
    }
    vm0();
    bar();
    if (wave < 7) {
      u4v yf[4];
#pragma unroll
      for (int H = 0; H < 4; ++H) {
        const u2v lo = *(const LDS u2v*)(size_t)(L_Y + (wave * 4 + H) * 1024 + lane * 8), hi = *(const LDS u2v*)(size_t)(L_Y + (wave * 4 + H) * 1024 + 512 + lane * 8);
        yf[H] = (u4v){lo.x, lo.y, hi.x, hi.y};
      }
      f4v a0 = __builtin_bit_cast(f4v, ff[0]), a1 = __builtin_bit_cast(f4v, ff[1]);
      const LDS u4v* pp = (const LDS u4v*)(size_t)(L_P + lane * 16);
      u4v p0[4], p1[4];
#pragma unroll
      for (int k = 0; k < 3; ++k) { p0[k] = pp[k * 64]; p1[k] = pp[(16 + k) * 64]; }
#pragma unroll
      for (int k = 0; k < 20; ++k) {
        const int n = k + 3;
        if (n < 16) { p0[n & 3] = pp[n * 64]; p1[n & 3] = pp[(16 + n) * 64]; }
        else if (n < 20) { p0[n & 3] = pp[(32 + n - 16) * 64]; p1[n & 3] = pp[(36 + n - 16) * 64]; }
        const u4v w = k < 16 ? uf[k] : yf[k - 16];
        a0 = mfma16(w, p0[k & 3], a0); a1 = mfma16(w, p1[k & 3], a1);
        __builtin_amdgcn_sched_barrier(0);
      }
      *(u2v*)(out + (size_t)(s * 32 + c16) * DM + v * 16 + g * 4) = (u2v){pk2(a0[0], a0[1]), pk2(a0[2], a0[3])};
      *(u2v*)(out + (size_t)(s * 32 + 16 + c16) * DM + v * 16 + g * 4) = (u2v){pk2(a1[0], a1[1]), pk2(a1[2], a1[3])};
    }
  }
  RTS(4);
}
#define TUNE_ARG
#define TUNE_PASS
#define K_PERIOD 760
#define K_DS 1560
#define K_MS 43
#define K_MM 50
#define K_LEAD 96
#define K_FIXED 0
constexpr int K_SMK = 2, K_DDE = 250;
constexpr int C_PT = 2048;
DEV unsigned* ptp(unsigned char* ws, int s) { return cntp(ws, C_PT + 2 * s); }
DEV unsigned ptword(int period, int ds) { return (unsigned)(period / 24) | ((unsigned)(period % 24) << 8) | ((unsigned)(ds - K_DDE - 80) << 16); }
DEV unsigned char* flagR(unsigned char* ws, unsigned sel, int id) { return hblk(ws, sel, 64 + (id >> 6), 0) + (id & 63) * 128; }
#define DBG(slot, v)
#define ST(n)
#define ARR(s_)
#define EV(code)
#define EV_OUT(base)
#define ST_CLOB
#define ST_OUT(base, ref)
constexpr int RET_E = 26;
constexpr int TRY_CAP = 1 << 16;

DEV int rt32() { unsigned long long t; asm volatile("s_memrealtime %0\n\ts_waitcnt lgkmcnt(0)" : "=s"(t) :: "memory"); return (int)(unsigned)t; }
struct Lat { int anchor, e, f; };
DEV void lat_fwd(Lat& L, int pe, int pf) { L.f += pf; const int c = L.f >= 24; L.f -= c ? 24 : 0; L.e += pe + c; }
DEV void lat_back(Lat& L, int pe, int pf) { L.f -= pf; const int c = L.f < 0; L.f += c ? 24 : 0; L.e -= pe + c; }
DEV void lat_prep(const Lat& L, int& tgt, int& jmp) { tgt = L.anchor + 4 * L.e; jmp = 108 - 4 * L.f; }
#define TPAIR(r, m) "s_memrealtime s[" #r ":" #r "+1]\n\ts_memtime s[" #m ":" #m "+1]\n\ts_nop 0\n\t"
#define TSEL(r, m) "s_cmp_lg_u32 s" #r ", s4\n\ts_cselect_b32 s58, s" #m ", s58\n\t"
#define TBURST \
  TPAIR(4, 6) TPAIR(8, 10) TPAIR(12, 14) TPAIR(16, 18) TPAIR(20, 22) TPAIR(24, 26) TPAIR(28, 30) TPAIR(52, 54) TPAIR(56, 58) \
  "s_waitcnt lgkmcnt(0)\n\t" TSEL(52, 54) TSEL(28, 30) TSEL(24, 26) TSEL(20, 22) TSEL(16, 18) TSEL(12, 14) TSEL(8, 10)
#define TBURST_CLOB "s4","s5","s6","s7","s8","s9","s10","s11","s12","s13","s14","s15","s16","s17","s18","s19","s20","s21","s22","s23", \
  "s24","s25","s26","s27","s28","s29","s30","s31","s52","s53","s54","s55","s56","s57","s58","s59"
DEV void lat_commit(Lat& L, int tgt, int jmp) {
  if (tgt == (int)0x80000000) return;
  int r = tgt - rt32();
  while (__builtin_expect(r < 0, 0)) { L.e += RET_E; tgt += 4 * RET_E; r += 4 * RET_E; }
  if (r > 0) {
    int d;
#define RT_STEP "s_memrealtime vcc\n\ts_waitcnt lgkmcnt(0)\n\ts_sub_i32 %0, %1, vcc_lo\n\ts_cmp_lt_i32 %0, 1\n\t"
    asm volatile("1:\n\t" RT_STEP "s_cbranch_scc1 2f\n\t" RT_STEP "s_cbranch_scc1 2f\n\t" RT_STEP "s_cbranch_scc1 2f\n\t"
                 RT_STEP "s_cbranch_scc0 1b\n\t"
                 "2:\n\t"
                 "s_getpc_b64 vcc\n\ts_add_u32 vcc_lo, vcc_lo, %2\n\ts_addc_u32 vcc_hi, vcc_hi, 0\n\ts_setpc_b64 vcc\n\t"
                 ".rept 24\n\ts_nop 0\n\t.endr"
                 : "=&s"(d) : "s"(tgt), "s"(jmp) : "scc", "vcc", "memory");
  }
}
DEV unsigned lat_wait(Lat& L) {
  int tgt, jmp;
  unsigned est;
  lat_prep(L, tgt, jmp);
  if (tgt - rt32() < 16) { lat_commit(L, tgt, jmp); return 0; }
  asm volatile("1:\n\ts_memrealtime vcc\n\ts_waitcnt lgkmcnt(0)\n\ts_sub_i32 s4, %1, vcc_lo\n\ts_cmp_gt_i32 s4, 16\n\ts_cbranch_scc1 1b\n\t"
               TBURST
               "s_sub_i32 s4, %1, s4\n\ts_mul_i32 s4, s4, 93\n\ts_lshr_b32 s4, s4, 2\n\t"
               "s_add_i32 s58, s58, s4\n\ts_add_i32 s58, s58, %2\n\ts_or_b32 %0, s58, 1\n\t"
               "s_memtime vcc\n\ts_waitcnt lgkmcnt(0)\n\t"
               "s_sub_i32 s4, s58, vcc_lo\n\ts_max_i32 s4, s4, 0\n\ts_min_i32 s4, s4, 512\n\ts_and_b32 s4, s4, -4\n\ts_sub_i32 s4, 524, s4\n\t"
               "s_getpc_b64 vcc\n\ts_add_u32 vcc_lo, vcc_lo, s4\n\ts_addc_u32 vcc_hi, vcc_hi, 0\n\ts_setpc_b64 vcc\n\t"
               ".rept 128\n\ts_nop 0\n\t.endr"
               : "=&s"(est) : "s"(tgt), "s"(4 * L.f - 93) : TBURST_CLOB, "scc", "vcc", "memory");
  return est;
}
DEV unsigned* anchorp(unsigned char* ws, int id) { return cntp(ws, C_ANCHOR + 2 * (id & 31)); }
DEV int get_anchor(unsigned char* ws, unsigned sel, int id, int lane) {
  post((unsigned*)flagR(ws, sel, id), 0u, lane);
  unsigned v = 0;
  for (int it = 0; it < SPIN_CAP && v == 0; ++it) v = __builtin_amdgcn_readfirstlane(ld_cnt(anchorp(ws, id)));
  const int n = rt32();
  const int d = (int)((v & ~1u) - ((unsigned)n << 1)) >> 1;
  return (d > 4096 || d < -(1 << 22)) ? n : n + d;
}
DEV f4v smf(u4v a, u8v b, f4v c, int idx) {
  return __builtin_amdgcn_smfmac_f32_16x16x64_bf16(asbf8(a), __builtin_bit_cast(bf16x, b), c, idx, 0, 0);
}
DEV unsigned umax4(u4v v) { return max(max(v.x, v.y), max(v.z, v.w)); }
DEV float dpp_ror8(float v) { return __builtin_bit_cast(float, __builtin_amdgcn_update_dpp(0, __builtin_bit_cast(int, v), 0x128, 0xf, 0xf, true)); }
DEV float dpp_xor1(float v) { return __builtin_bit_cast(float, __builtin_amdgcn_update_dpp(0, __builtin_bit_cast(int, v), 0xb1, 0xf, 0xf, true)); }
DEV void st4c_asm(void* p, unsigned v) { asm volatile("global_store_dword %0, %1, off sc1" :: "v"(p), "v"(v) : "memory"); }
DEV void st8c_asm(void* p, u2v v) { asm volatile("global_store_dwordx2 %0, %1, off sc1" :: "v"(p), "v"(v) : "memory"); }
DEV void ld16c_asm(u4v& v, const void* p) { asm volatile("global_load_dwordx4 %0, %1, off sc1" : "=&v"(v) : "v"(p) : "memory"); }
DEV void rawbar() { asm volatile("s_barrier" ::: "memory"); }
DEV unsigned lds_r4(unsigned a) { return *(volatile LDS unsigned*)(size_t)a; }
DEV void lds_w4(unsigned a, unsigned v) { *(volatile LDS unsigned*)(size_t)a = v; }
DEV u8v cat8(u4v a, u4v b) { return (u8v){a.x, a.y, a.z, a.w, b.x, b.y, b.z, b.w}; }
DEV void set_abort(unsigned char* ws) { st4c_asm(cntp(ws, C_ABORT), 1u); }

constexpr unsigned L_MEM = 0, L_KT = 8192, L_FLAG = 12288, L_RED = 16384;
constexpr unsigned L_EST = 12304, L_TS = 12308, L_CG = 12544, L_G = 32768, L_XN = 65536;
#define CLOB_V200_247 "v200","v201","v202","v203","v204","v205","v206","v207","v208","v209","v210","v211","v212","v213","v214","v215", \
  "v216","v217","v218","v219","v220","v221","v222","v223","v224","v225","v226","v227","v228","v229","v230","v231", \
  "v232","v233","v234","v235","v236","v237","v238","v239","v240","v241","v242","v243","v244","v245","v246","v247","v248"
#define SMF "v_smfmac_f32_16x16x64_bf16 "
#define R1_HEAD(o0, o1, o2, o3, o4, o5, o6, FIRST) \
  "v_mov_b32 v228, 0\n\tv_mov_b32 v229, 0\n\tv_mov_b32 v230, 0\n\tv_mov_b32 v231, 0\n\t" \
  "v_mov_b32 v232, 0\n\tv_mov_b32 v233, 0\n\tv_mov_b32 v234, 0\n\tv_mov_b32 v235, 0\n\t" \
  "v_mov_b32 v244, 0\n\tv_mov_b32 v245, 0\n\tv_mov_b32 v246, 0\n\tv_mov_b32 v247, 0\n\t" \
  "s_mov_b32 m0, %[m0v]\n\t" \
  "s_barrier\n\t" ST(80) \
  "global_load_lds_dwordx4 %[moff], %[src] sc1\n\t" \
  "s_waitcnt vmcnt(0)\n\t" \
  "ds_read_b128 v[200:203], %[rd] offset:" #o0 "*1024\n\t" \
  "s_barrier\n\t" ST(82) \
  "ds_read_b128 v[204:207], %[rd] offset:" #o1 "*1024\n\tds_read_b128 v[208:211], %[rd] offset:" #o2 "*1024\n\t" \
  "ds_read_b128 v[212:215], %[rd] offset:" #o3 "*1024\n\tds_read_b128 v[216:219], %[rd] offset:" #o4 "*1024\n\t" \
  "ds_read_b128 v[220:223], %[rd] offset:" #o5 "*1024\n\tds_read_b128 v[224:227], %[rd] offset:" #o6 "*1024\n\t"
#define R1_TWO(n6, n5, n4, n3, n2, n1, n0) \
  "s_waitcnt lgkmcnt(" #n6 ")\n\t" ST(84) SMF "v[228:231], v[200:203], %[a0], %[idx]\n\t" SMF "v[232:235], v[200:203], %[b0], %[idx]\n\t" \
  "s_waitcnt lgkmcnt(" #n5 ")\n\t" SMF "v[228:231], v[204:207], %[a1], %[idx]\n\t" SMF "v[232:235], v[204:207], %[b1], %[idx]\n\t" \
  "s_waitcnt lgkmcnt(" #n4 ")\n\t" SMF "v[228:231], v[208:211], %[a2], %[idx]\n\t" SMF "v[232:235], v[208:211], %[b2], %[idx]\n\t" \
  "s_waitcnt lgkmcnt(" #n3 ")\n\t" SMF "v[228:231], v[212:215], %[a3], %[idx]\n\t" SMF "v[232:235], v[212:215], %[b3], %[idx]\n\t" \
  "s_waitcnt lgkmcnt(" #n2 ")\n\t" SMF "v[228:231], v[216:219], %[a4], %[idx]\n\t" SMF "v[232:235], v[216:219], %[b4], %[idx]\n\t" \
  "s_waitcnt lgkmcnt(" #n1 ")\n\t" SMF "v[228:231], v[220:223], %[a5], %[idx]\n\t" \
  "s_waitcnt lgkmcnt(" #n0 ")\n\t" SMF "v[228:231], v[224:227], %[a6], %[idx]\n\t" \
  SMF "v[232:235], v[220:223], %[b5], %[idx]\n\t" SMF "v[232:235], v[224:227], %[b6], %[idx]\n\t" \
  ST(86) "s_nop 1\n\t" \
  "v_cvt_pk_bf16_f32 v228, v228, v229\n\tv_cvt_pk_bf16_f32 v229, v230, v231\n\t" \
  "ds_write_b64 %[ktw], v[228:229]\n\t" \
  "s_nop 2\n\t" \
  "v_cvt_pk_bf16_f32 v232, v232, v233\n\tv_cvt_pk_bf16_f32 v233, v234, v235\n\t" \
  "ds_write_b64 %[ktw], v[232:233] offset:512\n\t" \
  LDSWAIT ST(88) \
  "s_barrier\n\t" ST(90)
#define LDSWAIT
#define R1_WOPS [a0] "v"(w0[0]), [a1] "v"(w0[1]), [a2] "v"(w0[2]), [a3] "v"(w0[3]), [a4] "v"(w0[4]), [a5] "v"(w0[5]), [a6] "v"(w0[6])
#define R1_WOPS2 [b0] "v"(w1[0]), [b1] "v"(w1[1]), [b2] "v"(w1[2]), [b3] "v"(w1[3]), [b4] "v"(w1[4]), [b5] "v"(w1[5]), [b6] "v"(w1[6])
#define R1_COMMON [idx] "v"(idx), [rd] "v"(rd), [ktw] "v"(ktw), [moff] "v"(moff), [src] "s"(src), [m0v] "s"(m0v)

template <int W>
DEV void r1_mfma(unsigned char* __restrict__ ws, int h, int q, int lane, unsigned sel, int smk) {
  constexpr int wave = W, KIND = W == 0 ? 0 : W == 3 ? 2 : 1;
  const int g = lane >> 4, c16 = lane & 15;
  const int idx = (lane & 8) ? 0xEEEE : 0x4444;
  const unsigned moff = wave * 1024 + g * 256 + (lane & 7) * 32 + ((lane >> 3) & 1) * 16;
  u8v w0[7], w1[7];
  {
    const unsigned char* p = ws + O_WA + (size_t)(h * 28 + 7 * q + 2 * wave) * 14336 + lane * 16;
#pragma unroll
    for (int k = 0; k < 7; ++k) {
      const int o = ((k + W) % 7) * 2048;
      w0[k] = cat8(*(const u4v*)(p + o), *(const u4v*)(p + o + 1024));
      if (KIND != 2) w1[k] = cat8(*(const u4v*)(p + 14336 + o), *(const u4v*)(p + 14336 + o + 1024));
    }
#pragma unroll
    for (int k = 0; k < 7; ++k) { asm volatile("" : "+v"(w0[k])); if (KIND != 2) asm volatile("" : "+v"(w1[k])); }
  }
  RTS(1);
  const unsigned rd = L_MEM + lane * 16, ktw = L_KT + 2 * wave * 512 + c16 * 32 + g * 8, m0v = L_MEM + wave * 1024;
  const unsigned a0a = L_MEM + (7 * q + g) * 256 + c16 * 16, a1a = L_MEM + min(7 * q + 4 + g, 27) * 256 + c16 * 16;
  const unsigned trb = L_KT + (g >> 1) * 512 + (4 * (g & 1) + (c16 >> 2)) * 32 + (c16 & 3) * 8;
  const unsigned sto = h * 512 + q * 128 + (4 * (g & 1) + 2 * (g >> 1) + (c16 >> 3)) * 16 + (c16 & 7) * 2;
  const unsigned long long lo8 = 0x00FF00FF00FF00FFull;
  unsigned ep = 0;
  int budget = TRY_CAP;
  rawbar();
  rawbar();
  for (int s = 0; s < NSEG; ++s) {
    const unsigned char* src = hblk(ws, sel, s, 0);
    unsigned char* dst = hblk(ws, sel, 64 + s, smk);
    for (;;) {
      if (KIND == 0) {
        unsigned long long okm, tst;
        asm volatile(R1_HEAD(0, 1, 2, 3, 4, 5, 6, )
          "ds_read_b128 v[236:239], %[a0a]\n\tds_read_b128 v[240:243], %[a1a]\n\t"
          R1_TWO(8, 7, 6, 5, 4, 3, 2)
          "ds_read_b64_tr_b16 v[200:201], %[trb]\n\tds_read_b64_tr_b16 v[202:203], %[trb] offset:256\n\t"
          "ds_read_b64_tr_b16 v[204:205], %[trb] offset:1024\n\tds_read_b64_tr_b16 v[206:207], %[trb] offset:1280\n\t"
          "ds_read_b64_tr_b16 v[208:209], %[trb] offset:2048\n\tds_read_b64_tr_b16 v[210:211], %[trb] offset:2304\n\t"
          "ds_read_b64_tr_b16 v[212:213], %[trb] offset:3072\n\tds_read_b64_tr_b16 v[214:215], %[trb] offset:3328\n\t"
          "s_waitcnt lgkmcnt(4)\n\t" SMF "v[244:247], v[236:239], v[200:207], %[idx]\n\t"
          "s_waitcnt lgkmcnt(0)\n\t" SMF "v[244:247], v[240:243], v[208:215], %[idx]\n\t"
          "s_nop 7\n\t"
          "v_permlane32_swap_b32 v244, v246\n\tv_permlane32_swap_b32 v245, v247\n\t"
          "v_add_f32 v244, v244, v246\n\tv_add_f32 v245, v245, v247\n\t"
          "v_cndmask_b32 v246, v244, v245, %[lo8]\n\t"
          "v_cndmask_b32 v247, v245, v244, %[lo8]\n\t"
          "s_nop 1\n\t"
          "v_add_f32_dpp v247, v246, v247 row_ror:8 row_mask:0xf bank_mask:0xf\n\t"
          "v_cvt_pk_bf16_f32 v246, v247, v247\n\t"
          "v_cmpx_o_f32 vcc, v247, v247\n\t"
          "global_store_short %[sto], v246, %[dst] sc1\n\t" ST(92)
          "s_memtime %[tst]\n\t"
          "s_mov_b64 exec, -1\n\t"
          "s_mov_b64 %[okm], vcc\n\t"
          "s_waitcnt lgkmcnt(0)\n\t"
          : [okm] "=&s"(okm), [tst] "=&s"(tst)
          : R1_WOPS, R1_WOPS2, R1_COMMON, [a0a] "v"(a0a), [a1a] "v"(a1a), [trb] "v"(trb), [sto] "v"(sto), [dst] "s"(dst), [lo8] "s"(lo8)
          : CLOB_V200_247, "vcc", "memory" ST_CLOB);
        lds_w4(L_TS, (unsigned)tst);
        if (okm != ~0ull) {
          if (budget > 0) { if (lane == 0) lds_w4(L_FLAG, ep); }
          else asm volatile("global_store_short %0, %1, %2 sc1" :: "v"(sto), "v"(0x7FC0u), "s"(dst) : "memory");
        }
      } else if (KIND == 1) {
        if (W == 1) asm volatile(R1_HEAD(1, 2, 3, 4, 5, 6, 0, ) R1_TWO(6, 5, 4, 3, 2, 1, 0) :: R1_WOPS, R1_WOPS2, R1_COMMON : CLOB_V200_247, "memory" ST_CLOB);
        else asm volatile(R1_HEAD(2, 3, 4, 5, 6, 0, 1, ) R1_TWO(6, 5, 4, 3, 2, 1, 0) :: R1_WOPS, R1_WOPS2, R1_COMMON : CLOB_V200_247, "memory" ST_CLOB);
      } else {
        asm volatile(R1_HEAD(3, 4, 5, 6, 0, 1, 2, )
          "s_waitcnt lgkmcnt(6)\n\t" SMF "v[228:231], v[200:203], %[a0], %[idx]\n\t"
          "s_waitcnt lgkmcnt(5)\n\t" SMF "v[228:231], v[204:207], %[a1], %[idx]\n\t"
          "s_waitcnt lgkmcnt(4)\n\t" SMF "v[228:231], v[208:211], %[a2], %[idx]\n\t"
          "s_waitcnt lgkmcnt(3)\n\t" SMF "v[228:231], v[212:215], %[a3], %[idx]\n\t"
          "s_waitcnt lgkmcnt(2)\n\t" SMF "v[228:231], v[216:219], %[a4], %[idx]\n\t"
          "s_waitcnt lgkmcnt(1)\n\t" SMF "v[228:231], v[220:223], %[a5], %[idx]\n\t"
          "s_waitcnt lgkmcnt(0)\n\t" SMF "v[228:231], v[224:227], %[a6], %[idx]\n\t"
          "s_nop 7\n\t"
          "v_cvt_pk_bf16_f32 v228, v228, v229\n\tv_cvt_pk_bf16_f32 v229, v230, v231\n\t"
          "ds_write_b64 %[ktw], v[228:229]\n\t"
          LDSWAIT
          "s_barrier\n\t"
          :: R1_WOPS, R1_COMMON : CLOB_V200_247, "memory" ST_CLOB);
      }
      bar();
      const bool bad = __builtin_amdgcn_readfirstlane(lds_r4(L_FLAG)) == ep;
      ++ep;
      if (!bad) break;
      --budget;
      rawbar();
    }
    ARR(s);
    rawbar();
  }
}

typedef unsigned short h2v __attribute__((ext_vector_type(2)));
DEV unsigned pkmax(unsigned a, unsigned b) { return __builtin_bit_cast(unsigned, __builtin_elementwise_max(__builtin_bit_cast(h2v, a), __builtin_bit_cast(h2v, b))); }
DEV unsigned wavepkmax(unsigned v) {
  v = pkmax(v, __builtin_amdgcn_update_dpp(0u, v, 0xb1, 0xf, 0xf, true));
  v = pkmax(v, __builtin_amdgcn_update_dpp(0u, v, 0x4e, 0xf, 0xf, true));
  v = pkmax(v, __builtin_amdgcn_update_dpp(0u, v, 0x124, 0xf, 0xf, true));
  v = pkmax(v, __builtin_amdgcn_update_dpp(0u, v, 0x128, 0xf, 0xf, true));
  const u2v x = __builtin_amdgcn_permlane16_swap(v, v, false, false);
  v = pkmax(x.x, x.y);
  const u2v y = __builtin_amdgcn_permlane32_swap(v, v, false, false);
  return pkmax(y.x, y.y);
}
DEV void r1_boss(unsigned char* __restrict__ ws, int lane, unsigned sel TUNE_ARG) {
  constexpr unsigned L_FA = 13312, L_FY = 13568;
  int A = 0, V = 0, ms = 4 * K_MS, mm = 4 * K_MM;
  float rho = 0.f;
  bool prevf3 = false;
  int run = 0;
  unsigned ep = 0;
  const unsigned c0 = rdtime();
  const int r0 = rt32();
  rawbar();
  const int P0 = (int)((float)K_PERIOD * (0.3f + 0.7f * fmaxf(23.4f * (float)(rt32() - r0) * __builtin_amdgcn_rcpf((float)(rdtime() - c0)), 1.f)));
  int hist = P0;
  if (lane < 5) st4c_asm(ptp(ws, lane), ptword(P0, K_DS));
  for (int it = 0; it < SPIN_CAP; ++it) {
    unsigned x, y;
    asm volatile("global_load_dword %0, %2, off sc1\n\tglobal_load_dword %1, %3, off sc1\n\ts_waitcnt vmcnt(0)"
                 : "=&v"(x), "=&v"(y) : "v"(flagR(ws, sel, lane)), "v"(flagR(ws, sel, 64 + min(lane, 27))) : "memory");
    if (!__any(max(x, y) == SENT)) break;
  }
  if (lane < 32) st4c_asm(anchorp(ws, lane), ((unsigned)(rt32() + K_LEAD) << 1) | 1u);
  for (int t = 0; t < NSEG; ++t) {
    const int s = max(t - 3, 0);
    const float pa = 4.f * (float)__builtin_amdgcn_readlane(hist, max(s - 1, 0)), py = 4.f * (float)__builtin_amdgcn_readlane(hist, s);
    rawbar(); rawbar();
    if (t >= 3) vm0();
    const unsigned a = lds_r4(L_FA + lane * 4), y = lds_r4(L_FY + lane * 4);
    const float da = (float)((a >> 12) & 0xffffu), dy = (float)((y >> 12) & 0xffffu);
    unsigned pk = min((unsigned)((float)(a & 0xfffu) * pa * __builtin_amdgcn_rcpf(da)), 65535u) |
                  (min((unsigned)((float)(y & 0xfffu) * py * __builtin_amdgcn_rcpf(dy)), 65535u) << 16);
    unsigned fl = (max(a, y) == SENT ? 1u : 0u) | ((a >> 28) != 0 ? 2u : 0u) | ((y >> 26) & 4u) |
                  (fabsf(da - 0.86f * pa) > 0.18f * pa ? 8u : 0u) | (fabsf(dy - 0.86f * py) > 0.18f * py ? 16u : 0u) | ((y >> 25) & 16u);
    float rn = dy * __builtin_amdgcn_rcpf(py);
    asm volatile("s_barrier" : "+v"(pk), "+v"(fl), "+v"(rn) :: "memory");
    pk = wavepkmax(pk);
    asm volatile("s_barrier\n\ts_barrier" : "+v"(pk) :: "memory");
    while (__builtin_amdgcn_readfirstlane(lds_r4(L_FLAG)) == ep++) { rawbar(); rawbar(); rawbar(); rawbar(); rawbar(); }
    if (t >= 3 && t + 2 < NSEG) {
      if (!__any(fl & 1)) {
        const unsigned m = __builtin_amdgcn_readfirstlane(pk);
        const bool f1 = __any(fl & 2), f3 = __any(fl & 4);
        if (!__any(fl & 10)) A = A == 0 ? (int)(m & 0xffffu) : A + (((int)(m & 0xffffu) - A) >> 2);
        if (!__any(fl & 20)) { V = V == 0 ? (int)(m >> 16) : V + (((int)(m >> 16) - V) >> 2); rho = rho == 0.f ? rn : rho + 0.25f * (rn - rho); }
        run = (f1 || f3) ? run + 1 : 0;
        if ((f1 && !prevf3) || run > 3) mm = min(mm + 20, 4 * K_MM + 800);
        if ((f3 && !f1) || run > 3) ms = min(ms + 20, 4 * K_MS + 800);
        prevf3 = f3;
      }
      int P = P0 + ((ms + mm) >> 2) - K_MS - K_MM, DS = K_DS + ms - 4 * K_MS;
      if (A > 0 && V > 0 && !K_FIXED) {
        DS = min(max((int)((float)(A + ms) * rho), 1200), 3000);
        P = min(max((A + ms + V - 84 + mm) >> 2, 600), 1500);
      }
      if (lane == t + 2) hist = P;
      post(ptp(ws, t + 2), ptword(P, DS), lane);
    }
    if (t >= 2) {
      asm volatile("s_mov_b32 m0, %2\n\ts_nop 0\n\tglobal_load_lds_dword %0, off sc1\n\ts_mov_b32 m0, %3\n\ts_nop 0\n\tglobal_load_lds_dword %1, off sc1"
                   :: "v"(flagS(ws, sel, t - 2, lane)), "v"(flagY(ws, sel, t - 2, min(lane, 27))), "s"(L_FA), "s"(L_FY) : "memory");
    }
  }
  vm0();
  rawbar();
}

DEV void role_r1(unsigned char* __restrict__ ws, int id, int wave, int lane, unsigned sel TUNE_ARG) {
  const int h = id >> 2, q = id & 3;
  if (wave == 0) { r1_mfma<0>(ws, h, q, lane, sel, K_SMK); return; }
  if (wave == 1) { r1_mfma<1>(ws, h, q, lane, sel, K_SMK); return; }
  if (wave == 2) { r1_mfma<2>(ws, h, q, lane, sel, K_SMK); return; }
  if (wave == 3) { r1_mfma<3>(ws, h, q, lane, sel, K_SMK); return; }
  if (wave == 7 && id == 3) { r1_boss(ws, lane, sel TUNE_PASS); return; }
  const int g = lane >> 4, c16 = lane & 15;
  const bool act = q < 2;
  const int fr = wave - 1;
  const unsigned moff = fr * 1024 + g * 256 + (lane & 7) * 32 + ((lane >> 3) & 1) * 16, m0v = L_MEM + fr * 1024;
  const unsigned xo = L_XN + (g >> 1) * 1024 + (g & 1) * 512 + c16 * 16;
  unsigned ep = 0;
  if (wave == 7) {
    const int idx = (lane & 8) ? 0xEEEE : 0x4444;
    u8v w0[7];
    const unsigned rd = lane * 16;
    const unsigned sto = h * 512 + (q & 1) * 256 + (4 * (g & 1) + 2 * (g >> 1) + (lane & 1)) * 32 + (c16 >> 1) * 4;
    const unsigned long long odd = 0xAAAAAAAAAAAAAAAAull;
    __builtin_amdgcn_s_setprio(3);
    rawbar();
    for (int s = 0; s < NSEG; ++s) {
      unsigned char* dst = hblk(ws, sel, s, 2);
      unsigned char* raw = hblk(ws, sel, s, 1);
      const bool go = act && s < NSEG - 1;
      if (go) {
#pragma unroll
        for (int k = 0; k < 7; ++k) w0[k] = cat8(lds_r16(L_G + (s & 1) * 14336 + rd + k * 2048), lds_r16(L_G + (s & 1) * 14336 + rd + k * 2048 + 1024));
      }
      for (;;) {
        if (go) {
          asm volatile(
            "v_mov_b32 v228, 0\n\tv_mov_b32 v229, 0\n\tv_mov_b32 v230, 0\n\tv_mov_b32 v231, 0\n\t"
            "s_barrier\n\ts_barrier\n\ts_barrier\n\t" ST(80)
            "ds_read_b128 v[200:203], %[rd]\n\tds_read_b128 v[204:207], %[rd] offset:1024\n\tds_read_b128 v[208:211], %[rd] offset:2048\n\t"
            "ds_read_b128 v[212:215], %[rd] offset:3072\n\tds_read_b128 v[216:219], %[rd] offset:4096\n\tds_read_b128 v[220:223], %[rd] offset:5120\n\t"
            "ds_read_b128 v[224:227], %[rd] offset:6144\n\t"
            "s_waitcnt lgkmcnt(6)\n\t" SMF "v[228:231], v[200:203], %[a0], %[idx]\n\t"
            "s_waitcnt lgkmcnt(5)\n\t" SMF "v[228:231], v[204:207], %[a1], %[idx]\n\t"
            "s_waitcnt lgkmcnt(4)\n\t" SMF "v[228:231], v[208:211], %[a2], %[idx]\n\t"
            "s_waitcnt lgkmcnt(3)\n\t" SMF "v[228:231], v[212:215], %[a3], %[idx]\n\t"
            "s_waitcnt lgkmcnt(2)\n\t" SMF "v[228:231], v[216:219], %[a4], %[idx]\n\t"
            "s_waitcnt lgkmcnt(1)\n\t" SMF "v[228:231], v[220:223], %[a5], %[idx]\n\t"
            "s_waitcnt lgkmcnt(0)\n\t" SMF "v[228:231], v[224:227], %[a6], %[idx]\n\t"
            "s_nop 7\n\t"
            "v_permlane32_swap_b32 v228, v230\n\tv_permlane32_swap_b32 v229, v231\n\t"
            "v_add_f32 v228, v228, v230\n\tv_add_f32 v229, v229, v231\n\t"
            "v_cndmask_b32 v230, v229, v228, %[odd]\n\t"
            "s_nop 1\n\t"
            "v_mov_b32_dpp v230, v230 quad_perm:[1,0,3,2] row_mask:0xf bank_mask:0xf\n\t"
            "v_cndmask_b32 v228, v228, v230, %[odd]\n\tv_cndmask_b32 v229, v230, v229, %[odd]\n\t"
            "v_exp_f32 v230, v228\n\tv_exp_f32 v231, v229\n\t"
            "v_cvt_pk_bf16_f32 v232, v228, v229\n\t"
            "v_cvt_pk_bf16_f32 v233, v230, v231\n\t"
            "v_cmpx_o_f32 vcc, v228, v229\n\t"
            "global_store_dword %[sto], v233, %[dst] sc1\n\t" ST(82)
            "global_store_dword %[sto], v232, %[raw] sc1\n\t"
            "s_mov_b64 exec, -1\n\t"
            "s_barrier\n\t"
            :: R1_WOPS, [idx] "v"(idx), [rd] "v"(rd), [sto] "v"(sto), [dst] "s"(dst), [raw] "s"(raw), [odd] "s"(odd)
            : CLOB_V200_247, "vcc", "memory" ST_CLOB);
          rawbar();
        } else {
          rawbar(); rawbar(); rawbar(); rawbar(); rawbar();
        }
        const bool bad = __builtin_amdgcn_readfirstlane(lds_r4(L_FLAG)) == ep;
        ++ep;
        if (!bad) break;
      }
    }
    rawbar();
  } else if (wave == 4) {
    const unsigned trb = L_KT + (g >> 1) * 512 + (4 * (g & 1) + (c16 >> 2)) * 32 + (c16 & 3) * 8;
    unsigned char* so = ws + O_SM + (size_t)h * 2048 + q * 256 + lane * 4;
    const unsigned x3 = xo, xlast = g < 2 ? 6144 : 5120;
#pragma unroll
    for (int u = 0; u < 8; ++u) *(LDS u2v*)(size_t)(L_KT + u * 512 + lane * 8) = u < 7 ? (u2v){SENT, SENT} : (u2v){0u, 0u};
    if (lane == 0) lds_w4(L_FLAG, SENT);
    rawbar();
    (void)ld_cnt((const unsigned*)(lane < 32 ? so : lane < 48 ? so + 63 * 32768 : hblk(ws, sel, 70, 0)));
    unsigned pay = 0, eprev = 0;
    for (int s = 0; s < NSEG; ++s) {
      bool sig = s > 0;
      const unsigned ep0 = ep;
      for (;;) {
        bar(); rawbar(); rawbar();
        if (sig) { vm0(); if (lane == 0) st4c_asm(flagS(ws, sel, s - 1, id), pay); sig = false; }
        rawbar(); rawbar();
        const bool bad = __builtin_amdgcn_readfirstlane(lds_r4(L_FLAG)) == ep;
        ++ep;
        if (!bad) break;
      }
      {
        const unsigned est = lds_r4(L_EST), tst = lds_r4(L_TS);
        pay = (est ? min(tst - est, 4095u) : 0u) | ((est && eprev) ? min(est - eprev, 65535u) << 12 : 0u);
        pay |= min(ep - ep0 - 1, 7u) << 28;
        eprev = est;
      }
      f4v c0 = {0.f, 0.f, 0.f, 0.f}, c1 = c0;
      u4v ka[4], xa[4], xb[4];
#pragma unroll
      for (int u = 0; u < 4; ++u) {
        const u2v lo = lds_tr(trb + u * 1024), hi = lds_tr(trb + u * 1024 + 256);
        ka[u] = (u4v){lo.x, lo.y, hi.x, hi.y};
        const unsigned ad = x3 + (s & 1) * 8192 + (u < 3 ? u * 2048 : xlast);
        xa[u] = lds_r16(ad); xb[u] = lds_r16(ad + 256);
      }
      __builtin_amdgcn_sched_barrier(0);
#pragma unroll
      for (int u = 0; u < 4; ++u) { c0 = mfma16(ka[u], xa[u], c0); c1 = mfma16(ka[u], xb[u], c1); }
#pragma unroll
      for (int u = 0; u < 7; ++u) *(LDS u2v*)(size_t)(L_KT + u * 512 + lane * 8) = (u2v){SENT, SENT};
      st4c_asm(so + (size_t)s * 32768, pk2(halfsum(c0[0], c0[1]), halfsum(c0[2], c0[3])));
      st4c_asm(so + (size_t)s * 32768 + 1024, pk2(halfsum(c1[0], c1[1]), halfsum(c1[2], c1[3])));
      ARR(s);
    }
    vm0();
    rawbar();
    if (lane == 0) st4c_asm(flagS(ws, sel, NSEG - 1, id), pay);
  } else if (wave == 5) {
    const unsigned char* gp = ws + O_G + (size_t)(h * 2 + (q & 1)) * G_RUN;
    const unsigned v0 = lane * 16, v1 = v0 + 4096, v2 = v0 + 8192, v3 = v0 + 12288;
    auto fetch = [&](int t) {
#define GL4(v) "global_load_lds_dwordx4 " v ", %4 sc1\n\t" "global_load_lds_dwordx4 " v ", %4 offset:1024 sc1\n\t"
#define GL8(v) GL4(v) "global_load_lds_dwordx4 " v ", %4 offset:2048 sc1\n\t" "global_load_lds_dwordx4 " v ", %4 offset:3072 sc1\n\t"
      asm volatile("s_mov_b32 m0, %5\n\ts_nop 0\n\t" GL8("%0") "s_add_u32 m0, m0, 4096\n\ts_nop 0\n\t" GL8("%1")
                   "s_add_u32 m0, m0, 4096\n\ts_nop 0\n\t" GL8("%2") "s_add_u32 m0, m0, 4096\n\ts_nop 0\n\t" GL4("%3")
                   :: "v"(v0), "v"(v1), "v"(v2), "v"(v3), "s"(gp + t * 14336), "s"(L_G + (t & 1) * 14336) : "scc", "memory");
    };
    auto fetchx = [&](int t) {
      asm volatile("s_mov_b32 m0, %3\n\ts_nop 0\n\t"
                   "global_load_lds_dwordx4 %0, %2\n\tglobal_load_lds_dwordx4 %0, %2 offset:1024\n\tglobal_load_lds_dwordx4 %0, %2 offset:2048\n\t"
                   "global_load_lds_dwordx4 %0, %2 offset:3072\n\ts_add_u32 m0, m0, 4096\n\ts_nop 0\n\t"
                   "global_load_lds_dwordx4 %1, %2\n\tglobal_load_lds_dwordx4 %1, %2 offset:1024\n\tglobal_load_lds_dwordx4 %1, %2 offset:2048"
                   :: "v"(v0), "v"(v1), "s"(ws + O_XN + (size_t)t * 28672 + q * 7168), "s"(L_XN + (t & 1) * 8192) : "scc", "memory");
    };
    const unsigned* gdone = doneG(ws, 7 * (h >> 1) + min(lane, 6));
    auto peek = [&](int t) {
      unsigned keep;
      asm volatile("s_mov_b32 %0, m0\n\ts_mov_b32 m0, %2\n\ts_nop 0\n\tglobal_load_lds_dword %1, off sc1\n\ts_mov_b32 m0, %0"
                   : "=&s"(keep) : "v"(lane == 63 ? ptp(ws, t) : gdone), "s"(L_CG) : "memory");
    };
    if (act) {
      wait_cnt(ws, gdone, 1);
      fetch(0); fetch(1);
    }
    fetchx(0);
    vm0();
    RTS(1);
    rawbar();
    for (int s = 0; s < NSEG; ++s) {
      const unsigned char* src = hblk(ws, sel, s, 0) + moff;
      bool todo = act && s + 2 < NSEG - 1, todox = s + 1 < NSEG;
      for (;;) {
        rawbar();
        if (s == 0) peek(0);
        rawbar();
        dma16c(src, m0v);
        vm0();
        rawbar();
        const bool there = __all(lds_r4(L_CG + (lane & 7) * 4) >= (unsigned)(s >> 1) + 2);
        rawbar();
        rawbar();
        if (todo && there) { fetch(s + 2); todo = false; }
        if (todox) { fetchx(s + 1); peek(s + 1); todox = false; }
        const bool bad = __builtin_amdgcn_readfirstlane(lds_r4(L_FLAG)) == ep;
        ++ep;
        if (!bad) break;
      }
      if (todo) {
        wait_cnt(ws, gdone, (s >> 1) + 2);
        fetch(s + 2);
      }
      ARR(s);
    }
    vm0();
    bar();
  } else {
      { Lat W = {rt32(), 6, 0}; (void)lat_wait(W); }
    rawbar();
    Lat L = {get_anchor(ws, sel, id, lane), 0, 0};
    for (int s = 0; s < NSEG; ++s) {
      const unsigned char* src = hblk(ws, sel, s, 0) + moff;
      unsigned pw = 0;
      for (;;) {
        bar();
        const unsigned est = lat_wait(L);
        rawbar();
        asm volatile("s_mov_b32 m0, %1\n\ts_nop 0\n\tglobal_load_lds_dwordx4 %0, off sc1\n\tglobal_load_lds_dwordx4 %0, off offset:1024 sc1\n\ts_waitcnt vmcnt(0)"
                     :: "v"(src), "s"(m0v) : "memory");
        rawbar();
        lds_w4(L_EST, est);
        if (pw == 0) pw = __builtin_amdgcn_readfirstlane(lds_r4(L_CG + 252)) | 0x80000000u;
        bar(); rawbar();
        const bool bad = __builtin_amdgcn_readfirstlane(lds_r4(L_FLAG)) == ep;
        ++ep;
        EV(bad ? 1 : 0);
        if (!bad) break;
        L.e += RET_E;
      }
      if (pw == 0x80000000u && s < NSEG - 1) { wait_cnt(ws, ptp(ws, s), 1); pw = __builtin_amdgcn_readfirstlane(ld_cnt(ptp(ws, s))); }
      lat_fwd(L, pw & 255, (pw >> 8) & 255);
      ARR(s);
    }
    bar();
    EV_OUT(id);
  }
}

#define XRED(op, v, t) \
  "v_mov_b32 " t ", " v "\n\ts_nop 1\n\tv_permlane16_swap_b32 " v ", " t "\n\t" op " " v ", " v ", " t "\n\t" \
  "v_mov_b32 " t ", " v "\n\ts_nop 1\n\tv_permlane32_swap_b32 " v ", " t "\n\t" op " " v ", " v ", " t "\n\t"
#define MF16 "v_mfma_f32_16x16x32_bf16 "
#define WAIT_CYC(T) \
  "s_memtime vcc\n\ts_waitcnt lgkmcnt(0)\n\t" \
  "s_sub_i32 s70, " T ", vcc_lo\n\ts_max_i32 s70, s70, 0\n\ts_mul_i32 s70, s70, 13\n\ts_lshr_b32 s70, s70, 6\n\t" \
  "s_min_i32 s70, s70, 128\n\ts_lshl_b32 s70, s70, 2\n\ts_sub_i32 s70, 524, s70\n\t" \
  "s_getpc_b64 vcc\n\ts_add_u32 vcc_lo, vcc_lo, s70\n\ts_addc_u32 vcc_hi, vcc_hi, 0\n\ts_setpc_b64 vcc\n\t" \
  ".rept 128\n\ts_nop 0\n\t.endr\n\t"
#define R3_Y_A \
  "ds_read_b128 v[200:203], %[rd]\n\tds_read_b128 v[204:207], %[rd] offset:1024\n\tds_read_b128 v[208:211], %[rd] offset:2048\n\t" \
  "ds_read_b128 v[212:215], %[rd] offset:3072\n\tds_read_b128 v[216:219], %[rd] offset:4096\n\tds_read_b128 v[220:223], %[rd] offset:5120\n\t" \
  "ds_read_b128 v[224:227], %[rd] offset:6144\n\t" \
  "s_waitcnt lgkmcnt(6)\n\t" SMF "v[228:231], v[200:203], %[a0], %[idx]\n\t" SMF "v[232:235], v[200:203], %[b0], %[idx]\n\t" \
  "s_waitcnt lgkmcnt(5)\n\t" SMF "v[228:231], v[204:207], %[a1], %[idx]\n\t" SMF "v[232:235], v[204:207], %[b1], %[idx]\n\t" \
  "s_waitcnt lgkmcnt(4)\n\t" SMF "v[228:231], v[208:211], %[a2], %[idx]\n\t" SMF "v[232:235], v[208:211], %[b2], %[idx]\n\t" \
  "s_waitcnt lgkmcnt(3)\n\t" SMF "v[228:231], v[212:215], %[a3], %[idx]\n\t" SMF "v[232:235], v[212:215], %[b3], %[idx]\n\t" \
    \
  "s_waitcnt lgkmcnt(0)\n\t" \
  "v_readfirstlane_b32 s68, v244\n\t" \
  "v_max3_u32 v244, v216, v217, v218\n\tv_max3_u32 v245, v219, v220, v221\n\tv_max3_u32 v244, v244, v222, v223\n\t" \
  "v_max3_u32 v245, v245, v224, v225\n\tv_max3_u32 v244, v244, v226, v227\n\tv_max_u32 v244, v244, v245\n\t" \
  "v_cmp_eq_u32 s[74:75], -1, v244\n\t" \
  "v_cmp_u_f32 vcc, v228, v229\n\t" \
  "s_or_b64 s[74:75], s[74:75], vcc\n\t" \
  "v_cmp_u_f32 vcc, v234, v235\n\t" \
  "s_or_b64 vcc, s[74:75], vcc\n\t"
#define R3_Y_B \
  SMF "v[228:231], v[216:219], %[a4], %[idx]\n\t" SMF "v[232:235], v[216:219], %[b4], %[idx]\n\t" \
  SMF "v[228:231], v[220:223], %[a5], %[idx]\n\t" SMF "v[228:231], v[224:227], %[a6], %[idx]\n\t" \
  SMF "v[232:235], v[220:223], %[b5], %[idx]\n\t" SMF "v[232:235], v[224:227], %[b6], %[idx]\n\t" \
  "s_nop 1\n\t" \
  "v_permlane32_swap_b32 v228, v229\n\tv_permlane32_swap_b32 v230, v231\n\t" \
  "v_add_f32 v228, v228, v229\n\tv_add_f32 v230, v230, v231\n\t" \
  "v_cvt_pk_bf16_f32 v164, v228, v230\n\t" \
  "s_nop 0\n\t" \
  "v_permlane32_swap_b32 v232, v233\n\tv_permlane32_swap_b32 v234, v235\n\t" \
  "v_add_f32 v232, v232, v233\n\tv_add_f32 v234, v234, v235\n\t" \
  "v_cvt_pk_bf16_f32 v165, v232, v234\n\t" \
  ST(82)
#define R3_POLL \
  WAIT_CYC("s68") \
  "global_load_dwordx4 v[236:239], %[to], s[60:61] sc1\n\t" \
  WAIT_CYC("s69") \
  "global_load_dwordx4 v[240:243], %[tm], s[60:61] sc1\n\t" ST(86)
#define R3_POLL2   \
  "55:\n\ts_memtime vcc\n\ts_waitcnt lgkmcnt(0)\n\ts_sub_i32 s70, s69, vcc_lo\n\ts_cmp_gt_i32 s70, 0\n\ts_cbranch_scc1 55b\n\t" \
  "global_load_dwordx4 v[236:239], %[to], s[60:61] sc1\n\tglobal_load_dwordx4 v[240:243], %[tm], s[60:61] sc1\n\t"
#define R3_S_PHASE(SIG, NS) \
  "s_waitcnt vmcnt(1)\n\t" ST(84) \
  "v_cndmask_b32 v208, 0, v236, s[76:77]\n\tv_cndmask_b32 v209, 0, v237, s[76:77]\n\tv_cndmask_b32 v210, 0, v238, s[76:77]\n\tv_cndmask_b32 v211, 0, v239, s[76:77]\n\t" \
  MF16 "v[220:223], v[160:163], v[236:239], 0\n\t" \
  "v_cndmask_b32 v212, v236, 0, s[76:77]\n\tv_cndmask_b32 v213, v237, 0, s[76:77]\n\tv_cndmask_b32 v214, v238, 0, s[76:77]\n\tv_cndmask_b32 v215, v239, 0, s[76:77]\n\t" \
  MF16 "v[216:219], v[192:195], v[208:211], 0\n\t" \
  "s_nop 0\n\t" \
  MF16 "v[216:219], v[196:199], v[212:215], v[216:219]\n\t" \
  "global_store_dwordx2 %[yo], v[164:165], s[60:61] sc1\n\t"        \
  SIG "s_waitcnt vmcnt(" #NS ")\n\t" ST(88) \
  MF16 "v[232:235], %[selA], v[240:243], 0\n\t" \
  "s_nop 7\n\t" \
  "v_exp_f32 v234, v232\n\tv_exp_f32 v235, v233\n\t" \
  "s_nop 0\n\t" \
  "v_cvt_pk_bf16_f32 v240, v234, v235\n\t" \
  "v_cndmask_b32 v168, 0, v240, s[76:77]\n\tv_cndmask_b32 v169, v240, 0, s[76:77]\n\t" \
  "s_nop 1\n\t" \
  MF16 "v[220:223], v[160:163], v[168:171], v[220:223]\n\t" \
  MF16 "v[244:247], v[164:167], v[168:171], v[216:219]\n\t" \
  "s_nop 6\n\t" \
  "v_rcp_f32 v242, v220\n\t" \
  "v_med3_f32 v241, v220, v172, v173\n\t" \
  "v_cmp_lg_f32 vcc, v241, v220\n\t" \
  "s_cbranch_vccnz 12f\n\t" \
  "11:\n\t" \
  "v_pk_mul_f32 v[244:245], v[244:245], v[242:243] op_sel_hi:[1,0]\n\t" \
  "v_pk_mul_f32 v[246:247], v[246:247], v[242:243] op_sel_hi:[1,0]\n\t" \
  "s_nop 0\n\t" \
  "v_add_f32_dpp v244, v244, v244 row_ror:8 row_mask:0xf bank_mask:0xf\n\tv_add_f32_dpp v245, v245, v245 row_ror:8 row_mask:0xf bank_mask:0xf\n\t" \
  "v_add_f32_dpp v246, v246, v246 row_ror:8 row_mask:0xf bank_mask:0xf\n\tv_add_f32_dpp v247, v247, v247 row_ror:8 row_mask:0xf bank_mask:0xf\n\t" \
  ST(90) "ds_write_b128 %[redw], v[244:247]\n\t" \
  LDSWAIT \
  "s_barrier\n\t" ST(92)
#define R3_S_RARE \
    "12:\n\t" \
  "v_xor_b32 v241, 0x6000, %[to]\n\t" \
  "global_load_dwordx4 v[200:203], v241, s[60:61] sc1\n\t" \
  "s_waitcnt vmcnt(0)\n\t" \
  "v_lshlrev_b32 v208, 16, v200\n\tv_and_b32 v209, 0xffff0000, v200\n\tv_lshlrev_b32 v210, 16, v201\n\tv_and_b32 v211, 0xffff0000, v201\n\t" \
  "v_lshlrev_b32 v212, 16, v202\n\tv_and_b32 v213, 0xffff0000, v202\n\tv_lshlrev_b32 v214, 16, v203\n\tv_and_b32 v215, 0xffff0000, v203\n\t" \
  "v_max3_f32 v240, v208, v209, v210\n\tv_max3_f32 v241, v211, v212, v213\n\tv_max3_f32 v240, v240, v214, v215\n\t" \
  "v_max3_f32 v241, v241, v232, v233\n\tv_max_f32 v240, v240, v241\n\t" \
  XRED("v_max_f32", "v240", "v241") \
  "v_sub_f32 v208, v208, v240\n\tv_sub_f32 v209, v209, v240\n\tv_sub_f32 v210, v210, v240\n\tv_sub_f32 v211, v211, v240\n\t" \
  "v_sub_f32 v212, v212, v240\n\tv_sub_f32 v213, v213, v240\n\tv_sub_f32 v214, v214, v240\n\tv_sub_f32 v215, v215, v240\n\t" \
  "v_sub_f32 v234, v232, v240\n\tv_sub_f32 v235, v233, v240\n\t" \
  "v_exp_f32 v208, v208\n\tv_exp_f32 v209, v209\n\tv_exp_f32 v210, v210\n\tv_exp_f32 v211, v211\n\t" \
  "v_exp_f32 v212, v212\n\tv_exp_f32 v213, v213\n\tv_exp_f32 v214, v214\n\tv_exp_f32 v215, v215\n\t" \
  "v_exp_f32 v234, v234\n\tv_exp_f32 v235, v235\n\ts_nop 1\n\t" \
  "v_cvt_pk_bf16_f32 v200, v208, v209\n\tv_cvt_pk_bf16_f32 v201, v210, v211\n\tv_cvt_pk_bf16_f32 v202, v212, v213\n\tv_cvt_pk_bf16_f32 v203, v214, v215\n\t" \
  "v_cvt_pk_bf16_f32 v240, v234, v235\n\t" \
  "v_cndmask_b32 v208, 0, v200, s[76:77]\n\tv_cndmask_b32 v209, 0, v201, s[76:77]\n\tv_cndmask_b32 v210, 0, v202, s[76:77]\n\tv_cndmask_b32 v211, 0, v203, s[76:77]\n\t" \
  "v_cndmask_b32 v212, v200, 0, s[76:77]\n\tv_cndmask_b32 v213, v201, 0, s[76:77]\n\tv_cndmask_b32 v214, v202, 0, s[76:77]\n\tv_cndmask_b32 v215, v203, 0, s[76:77]\n\t" \
  "v_cndmask_b32 v168, 0, v240, s[76:77]\n\tv_cndmask_b32 v169, v240, 0, s[76:77]\n\ts_nop 1\n\t" \
  MF16 "v[220:223], v[160:163], v[200:203], 0\n\t" \
  MF16 "v[220:223], v[160:163], v[168:171], v[220:223]\n\t" \
  MF16 "v[244:247], v[192:195], v[208:211], 0\n\t" \
  MF16 "v[244:247], v[196:199], v[212:215], v[244:247]\n\t" \
  MF16 "v[244:247], v[164:167], v[168:171], v[244:247]\n\t" \
  "s_nop 7\n\t" \
  "v_rcp_f32 v242, v220\n\ts_nop 0\n\t" \
  "s_branch 11b\n\t"
#define R3_PREP "s_lshl_b32 s44, s65, 2\n\ts_add_i32 s44, s44, s67\n\ts_lshl_b32 s45, s66, 2\n\ts_sub_i32 s45, 108, s45\n\t"
#define R3_FWD "s_add_i32 s66, s66, s47\n\ts_add_i32 s65, s65, s46\n\ts_cmp_ge_i32 s66, 24\n\ts_cselect_b32 s70, 24, 0\n\ts_addc_u32 s65, s65, 0\n\ts_sub_i32 s66, s66, s70\n\t"
#define R3_RT "s_memrealtime vcc\n\ts_waitcnt lgkmcnt(0)\n\ts_sub_i32 s70, s44, vcc_lo\n\ts_cmp_lt_i32 s70, 1\n\t"
#define R3_TW_WAIT \
  R3_RT "s_cbranch_scc1 40f\n\t" \
  "20:\n\t" R3_RT "s_cbranch_scc1 21f\n\t" R3_RT "s_cbranch_scc1 21f\n\t" R3_RT "s_cbranch_scc1 21f\n\t" R3_RT "s_cbranch_scc0 20b\n\t" \
  "21:\n\t" \
  "s_getpc_b64 vcc\n\ts_add_u32 vcc_lo, vcc_lo, s45\n\ts_addc_u32 vcc_hi, vcc_hi, 0\n\ts_setpc_b64 vcc\n\t" \
  ".rept 24\n\ts_nop 0\n\t.endr\n\t" \
  "22:\n\t"
#define R3_TW_LATE \
  "40:\n\t" \
  "s_cmp_eq_u32 s70, 0\n\ts_cbranch_scc1 22b\n\t" \
  "41:\n\ts_add_i32 s65, s65, 26\n\ts_add_i32 s44, s44, 4*26\n\ts_add_i32 s70, s70, 4*26\n\ts_cmp_lt_i32 s70, 0\n\ts_cbranch_scc1 41b\n\t" \
  "s_branch 20b\n\t"
#define R3_STEP(op, uop)   \
  op " %[moff], 0x8000, %[moff]\n\t" op " %[to], 0x8000, %[to]\n\t" op " %[tm], 0x8000, %[tm]\n\t" op " %[sto], 0x8000, %[sto]\n\t" \
  uop " %[uo], 0x4000, %[uo]\n\t" uop " %[yo], 0x1000, %[yo]\n\t"
#define R3_FLIP \
  "s_and_b32 s70, s62, 15\n\ts_cselect_b32 s70, 0, 0x4000\n\ts_add_i32 s71, s62, 1\n\ts_and_b32 s71, s71, 15\n\ts_cselect_b32 s71, 0, 0x4000\n\t" \
  "v_xor_b32 %[moff], s70, %[moff]\n\tv_xor_b32 %[to], s70, %[to]\n\tv_xor_b32 %[tm], s70, %[tm]\n\tv_xor_b32 %[sto], s71, %[sto]\n\t"
#define ULOADS "global_load_dwordx4 v[192:195], %[uo], s[60:61] sc1\n\tglobal_load_dwordx4 v[196:199], %[uo], s[60:61] offset:1024 sc1\n\t"
#define R3_DMA "global_load_lds_dwordx4 %[moff], s[60:61] sc1\n\t"
#define R3_TW_PT "s_lshl_b32 s70, s62, 7\n\ts_add_i32 s70, s70, 0x420080\n\tv_mov_b32 v178, s70\n\tglobal_load_dword v177, v178, s[60:61] sc1\n\t"
#define R3_TW_NEXT "v_readfirstlane_b32 s70, v177\n\ts_cmp_eq_u32 s70, 0\n\ts_cbranch_scc1 45f\n\t" \
  "19:\n\ts_and_b32 s46, s70, 0xff\n\ts_bfe_u32 s47, s70, 0x80008\n\ts_lshr_b32 s48, s70, 16\n\t"
#define R3_TW_OOL        \
  "45:\n\ts_mov_b32 s71, 0x40000\n\t" \
  "46:\n\tglobal_load_dword v177, v178, s[60:61] sc1\n\ts_waitcnt vmcnt(0)\n\tv_readfirstlane_b32 s70, v177\n\t" \
  "s_cmp_eq_u32 s70, 0\n\ts_cbranch_scc0 19b\n\ts_sub_i32 s71, s71, 1\n\ts_cmp_gt_i32 s71, 0\n\ts_cbranch_scc1 46b\n\ts_movk_i32 s70, 40\n\ts_branch 19b\n\t"
#define R3_TW_TIME \
  TBURST \
  "s_sub_i32 s4, s4, s44\n\ts_add_i32 s4, s4, 4\n\ts_mul_i32 s4, s4, 93\n\ts_ashr_i32 s4, s4, 2\n\ts_sub_i32 s58, s58, s4\n\t" \
  "s_lshl_b32 s4, s66, 2\n\ts_add_i32 s58, s58, s4\n\ts_sub_i32 s4, s58, s51\n\ts_mov_b32 s51, s58\n\ts_add_i32 s58, s58, s48\n\t" \
  "v_mov_b32 v200, s58\n\tv_mov_b32 v201, s4\n\tds_write_b64 v188, v[200:201] offset:12296\n\ts_waitcnt lgkmcnt(0)\n\t"
#define R3_WARM(beg, len) \
  "s_getpc_b64 vcc\n\t9:\n\ts_add_u32 vcc_lo, vcc_lo, " beg "-9b\n\ts_addc_u32 vcc_hi, vcc_hi, 0\n\tv_lshlrev_b32 v200, 3, %[rd]\n\tv_min_u32 v200, " len "-64, v200\n\t" \
  "global_load_dword v201, v200, vcc\n\tglobal_load_dword v202, v200, vcc offset:64\n\ts_waitcnt vmcnt(0)\n\t"
#define R3_LOOP(WARM, DMA, TWWAIT, CNTLOAD, CNTPAY, CNTPACK, TWTIME, NW, CNTSIG, NS, TWFWD, FIN, CNTCHK, OUTOFLINE) \
  "s_mov_b64 s[60:61], %[wsp]\n\ts_mov_b32 s62, 0\n\ts_mov_b32 s63, 0\n\ts_mov_b32 s64, 0x10000\n\ts_mov_b32 s78, 0\n\t" \
  "s_mov_b32 s76, 0x00ff00ff\n\ts_mov_b32 s77, s76\n\t" \
  "s_and_b32 s46, %[p0], 0xff\n\ts_bfe_u32 s47, %[p0], 0x80008\n\ts_lshr_b32 s48, %[p0], 16\n\ts_mov_b32 s65, 0\n\ts_mov_b32 s66, 0\n\t" \
  "s_movk_i32 s49, 250\n\ts_lshl_b32 s50, %[p1], 10\n\t" \
  "v_mov_b32 v188, 0\n\tv_mov_b32 v160, 0x3f803f80\n\tv_mov_b32 v161, v160\n\tv_mov_b32 v162, v160\n\tv_mov_b32 v163, v160\n\t" \
  "v_mov_b32 v166, 0\n\tv_mov_b32 v167, 0\n\tv_mov_b32 v170, 0\n\tv_mov_b32 v171, 0\n\tv_mov_b32 v172, 0x1f800000\n\tv_mov_b32 v173, 0x71800000\n\t" \
  R3_PREP \
  WARM \
  "10:\n\t" \
  TWWAIT \
  "s_mov_b32 m0, s50\n\t" \
  "s_waitcnt lgkmcnt(0)\n\ts_barrier\n\t" \
  "s_memtime s[72:73]\n\t" \
  DMA \
  "ds_read_b32 v200, v188 offset:12288\n\t" \
  "s_waitcnt lgkmcnt(0)\n\t" \
  "v_readfirstlane_b32 s74, v200\n\t" \
  "s_cmp_eq_u32 s74, s63\n\ts_cbranch_scc1 50f\n\t" \
  "s_add_i32 s63, s63, 1\n\t" \
  "v_mov_b32 v228, 0\n\tv_mov_b32 v229, 0\n\tv_mov_b32 v230, 0\n\tv_mov_b32 v231, 0\n\t" \
  "v_mov_b32 v232, 0\n\tv_mov_b32 v233, 0\n\tv_mov_b32 v234, 0\n\tv_mov_b32 v235, 0\n\t" \
  ULOADS \
  CNTLOAD TWTIME \
  "s_waitcnt vmcnt(" #NW ")\n\t" \
  "s_barrier\n\t" ST(80) \
  CNTPAY "ds_read_b32 v244, v188 offset:12296\n\t" \
  R3_Y_A \
  "s_cbranch_vccnz 60f\n\t" \
  "14:\n\t" \
  CNTPACK "s_add_i32 s69, s68, s49\n\t" \
  "s_cmp_eq_u32 s62, 63\n\ts_cbranch_scc1 98f\n\t" \
  R3_POLL R3_Y_B \
  TWFWD \
  "15:\n\t" \
  R3_S_PHASE(CNTSIG, NS) FIN CNTCHK \
  "s_add_i32 s62, s62, 1\n\t" \
  R3_STEP("v_add_u32", "v_add_u32") \
  R3_FLIP DIAG_R3 \
  "s_branch 10b\n\t" \
  R3_S_RARE \
    \
  "50:\n\t" \
  "s_sub_i32 s64, s64, 1\n\ts_add_i32 s63, s63, 1\n\ts_bitset1_b32 s78, 28\n\t" \
  R3_FLIP R3_STEP("v_subrev_u32", "v_subrev_u32") \
  "s_sub_i32 s62, s62, 1\n\t" \
  "s_memtime vcc\n\ts_waitcnt lgkmcnt(0)\n\t" \
  "s_sub_i32 s70, s69, s68\n\ts_cmp_eq_u32 s70, s49\n\ts_cselect_b32 s70, 256, 0\n\ts_sub_i32 s69, s69, s70\n\t"       \
  "53:\n\ts_add_i32 s68, s68, 93*26\n\ts_add_i32 s69, s69, 93*26\n\ts_add_i32 s65, s65, 26\n\t" \
  "s_sub_i32 s70, s69, vcc_lo\n\ts_cmp_lt_i32 s70, 0\n\ts_cbranch_scc1 53b\n\t" \
  R3_PREP R3_POLL2 \
  "s_branch 15b\n\t" \
    \
  "60:\n\ts_bitset1_b32 s78, 29\n\ts_sub_i32 s64, s64, 1\n\ts_cmp_gt_i32 s64, 0\n\ts_cbranch_scc0 14b\n\t" \
  "s_add_i32 s65, s65, 26\n\t" R3_PREP "s_branch 10b\n\t" \
  OUTOFLINE \
  "98:\n\t" R3_Y_B \
  "global_store_dwordx2 %[yo], v[164:165], s[60:61] sc1\n\t" \
  "s_waitcnt vmcnt(0)\n\ts_barrier\n\t"
#define R3_FIN \
  "ds_read_b128 v[200:203], %[redr]\n\tds_read_b128 v[204:207], %[redr] offset:512\n\t" \
  "ds_read_b128 v[208:211], %[redr] offset:1024\n\tds_read_b128 v[212:215], %[redr] offset:1536\n\t" \
  "s_waitcnt lgkmcnt(2)\n\t" \
  "v_pk_add_f32 v[200:201], v[200:201], v[204:205]\n\tv_pk_add_f32 v[202:203], v[202:203], v[206:207]\n\t" \
  "s_waitcnt lgkmcnt(1)\n\t" \
  "v_pk_add_f32 v[200:201], v[200:201], v[208:209]\n\tv_pk_add_f32 v[202:203], v[202:203], v[210:211]\n\t" \
  "s_waitcnt lgkmcnt(0)\n\t" \
  "v_pk_add_f32 v[200:201], v[200:201], v[212:213]\n\tv_pk_add_f32 v[202:203], v[202:203], v[214:215]\n\t" \
  "s_nop 1\n\t" \
  "v_permlane32_swap_b32 v200, v202\n\tv_permlane32_swap_b32 v201, v203\n\t" \
  "v_add_f32 v200, v200, v202\n\tv_add_f32 v201, v201, v203\n\t" \
  "v_cvt_pk_bf16_f32 v202, v200, v201\n\t" \
  "v_cmpx_o_f32 vcc, v200, v201\n\t" \
  "global_store_dword %[sto], v202, s[60:61] sc1\n\t" ST(94) \
  "s_memtime s[74:75]\n\t" \
  "s_mov_b64 exec, -1\n\t" \
  "v_mov_b32 v200, -1\n\tv_mov_b32 v201, -1\n\tv_mov_b32 v202, -1\n\tv_mov_b32 v203, -1\n\t" \
  "ds_write_b128 %[redr], v[200:203]\n\tds_write_b128 %[redr], v[200:203] offset:512\n\t" \
  "ds_write_b128 %[redr], v[200:203] offset:1024\n\tds_write_b128 %[redr], v[200:203] offset:1536\n\t" \
  "s_waitcnt lgkmcnt(0)\n\ts_sub_u32 s70, s74, s69\n\tv_mov_b32 v200, s70\n\tds_write_b32 v188, v200 offset:12308\n\t" \
  "s_cmp_eq_u64 vcc, -1\n\ts_cbranch_scc0 80f\n\t" \
  "16:\n\t"
#define R3_FIN_OOL \
  "80:\n\ts_cmp_gt_i32 s64, 0\n\ts_cbranch_scc0 81f\n\t" \
  "v_mov_b32 v200, s63\n\tds_write_b32 v188, v200 offset:12288\n\ts_branch 16b\n\t" \
  "81:\n\tv_mov_b32 v200, 0x7fc07fc0\n\tglobal_store_dword %[sto], v200, s[60:61] sc1\n\ts_branch 16b\n\t"
#define R3_CNT_PAY "ds_read_b32 v174, v188 offset:12300\n\tds_read_b32 v175, v188 offset:12308\n\t"
#define R3_CNT_PACK \
  "v_min_u32 v175, 0xfff, v175\n\tv_min_u32 v174, 0xffff, v174\n\tv_lshl_or_b32 v176, v174, 12, v175\n\tv_or_b32 v176, s78, v176\n\ts_mov_b32 s78, 0\n\t"
#define R3_CNT_SIG "global_store_dword v187, v176, s[60:61] sc1\n\t"
#define R3_CNT_LOAD "v_mov_b32 v187, s43\n\ts_mov_b64 s[38:39], s[40:41]\n\tv_cndmask_b32 v144, %[uo], v186, s[40:41]\n\tglobal_load_dword v145, v144, s[60:61] sc1\n\t"
#define R3_CNT_CHK "v_readfirstlane_b32 s43, %[tm]\n\ts_and_b32 s43, s43, 0xffffe000\n\ts_xor_b32 s43, s43, s42\n\t" \
  "v_cndmask_b32 v189, v189, v145, s[38:39]\n\ts_mov_b64 s[38:39], 0\n\t" \
  "s_add_i32 s70, s62, 3\n\ts_lshr_b32 s70, s70, 1\n\tv_cmp_gt_u32 vcc, s70, v189\n\ts_cbranch_vccnz 85f\n\t17:\n\t" \
  "s_add_i32 s70, s70, 1\n\ts_min_u32 s70, s70, 32\n\tv_cmp_gt_u32 vcc, s70, v189\n\ts_cmp_lg_u64 vcc, 0\n\ts_cselect_b64 s[40:41], -1, 0\n\t"
#define R3_CNT_OOL        \
  "85:\n\ts_mov_b32 s71, 0x40000\n\t" \
  "86:\n\tglobal_load_dword v189, v186, s[60:61] sc1\n\ts_waitcnt vmcnt(0)\n\t" \
  "v_cmp_gt_u32 vcc, s70, v189\n\ts_cbranch_vccz 17b\n\ts_sleep 2\n\ts_sub_i32 s71, s71, 1\n\ts_cmp_gt_i32 s71, 0\n\ts_cbranch_scc1 86b\n\ts_branch 17b\n\t"
#define DIAG_R3
#define DIAG_R3_END
#define DIAG_R3_OUT
#define R3_OUTS [moff] "+v"(moff), [to] "+v"(to), [tm] "+v"(tm), [sto] "+v"(sto), [uo] "+v"(uo), [yo] "+v"(yo) DIAG_R3_OUT
#define R3_INS R1_WOPS, R1_WOPS2, [idx] "v"(idx), [rd] "v"(rd), [redw] "v"(redw), [selA] "v"(selA), [wsp] "s"(ws), [p0] "s"(p0), [p1] "s"(p1)
#define R3_CLOB CLOB_V200_247, "v144","v145","v146","v147","v148","v149","v150","v151", "v160","v161","v162","v163","v164","v165","v166","v167","v168","v169","v170","v171","v172","v173","v174","v175","v176","v177","v178", "v179","v180","v181","v182","v183","s96","s97","v184","v185","v186","v187","v188","v189","v190","v191","v192","v193","v194","v195","v196","v197","v198","v199", \
  "s38","s39","s40","s41","s42","s43","s44","s45","s46","s47","s48","s49","s50","s51","s60","s61","s62","s63","s64","s65","s66","s67","s68","s69","s70","s71", \
  "s72","s73","s74","s75","s76","s77","s78","vcc","scc","memory" ST_CLOB
#define STR2(x) #x
#define STR(x) STR2(x)

template <int KIND>
DEV void r3_body(unsigned char* __restrict__ ws, int v, int wave, int lane, unsigned sel TUNE_ARG) {
  const int g = lane >> 4, hs = (lane >> 3) & 1, i8 = lane & 7;
  const int idx = (lane & 8) ? 0xEEEE : 0x4444;
  const int dw = wave < 7 ? wave : 6;
  const unsigned rd = lane * 16;
  u8v w0[7], w1[7];
  {
    const unsigned char* p = ws + O_WB + (size_t)(2 * wave * 28 + v) * 14336 + lane * 16;
#pragma unroll
    for (int k = 0; k < 7; ++k) {
      w0[k] = cat8(*(const u4v*)(p + k * 2048), *(const u4v*)(p + k * 2048 + 1024));
      w1[k] = cat8(*(const u4v*)(p + 28 * 14336 + k * 2048), *(const u4v*)(p + 28 * 14336 + k * 2048 + 1024));
    }
#pragma unroll
    for (int k = 0; k < 7; ++k) asm volatile("" : "+v"(w0[k]), "+v"(w1[k]));
  }
  RTS(1);
  if (KIND == 3 && lane == 0) lds_w4(L_FLAG, SENT);
  if (KIND == 2) wait_cnt(ws, doneU(ws, 4 * (v >> 1) + (lane & 3)), 1);
  *(LDS u2v*)(size_t)(L_RED + wave * 512 + lane * 8) = (u2v){SENT, SENT};
  RTS(2);
  unsigned moff = hoff(sel, 0, 0) + dw * 1024 + g * 256 + i8 * 32 + hs * 16;
  unsigned to = hoff(sel, 0, 2) + (2 * wave + hs) * 512 + (g >> 1) * 256 + i8 * 32 + (g & 1) * 16;
  unsigned tm = hoff(sel, 64, K_SMK) + (2 * wave + hs) * 512 + g * 128 + i8 * 16;
  u4v selA = {0u, 0u, 0u, 0u};
  if (((lane & 3) >> 1) == 0) {
    const int gq = (lane >> 2) & 3, j = 4 * (gq & 1) + (gq >> 1) + 2 * (lane & 1);
    const unsigned one = (j & 1) ? 0x3f800000u : 0x00003f80u;
    selA = (u4v){(j >> 1) == 0 ? one : 0u, (j >> 1) == 1 ? one : 0u, (j >> 1) == 2 ? one : 0u, (j >> 1) == 3 ? one : 0u};
  }
  unsigned sto = hoff(sel, 1, 0) + v * 256 + i8 * 32 + ((lane >> 3) & 3) * 8 + (lane >> 5) * 4;
  unsigned uo = (unsigned)O_U + v * U_RUN + 2 * wave * 1024 + lane * 16;
  unsigned yo = (unsigned)O_YG + v * Y_RUN + wave * 512 + lane * 8;
  const unsigned redw = L_RED + hs * 4096 + wave * 512 + (g * 8 + i8) * 16;
  const unsigned redr = L_RED + (lane >> 5) * 2048 + (lane & 31) * 16;
  const unsigned p1 = dw;
  {
    const unsigned o = lane < 8 ? yo : lane < 16 ? yo + 63 * 4096 : lane < 24 ? uo : lane < 32 ? uo + 63 * 16384 : lane < 40 ? hoff(sel, 70, 0) : lane < 48 ? hoff(sel, 1, 0) + 7168 : (unsigned)O_FL + 0x3ff00;
    (void)ld_cnt((const unsigned*)(ws + (o & ~3u)));
  }
  int anchor = 0;
  unsigned p0 = 0;
  if (KIND == 3) asm volatile(R3_WARM(".Lr3tw_beg", ".Lr3tw_end-.Lr3tw_beg") :: [rd] "v"(rd) : "v200", "v201", "v202", "vcc", "scc", "memory");
  if (KIND == 3) { wait_cnt(ws, ptp(ws, 0), 1); p0 = __builtin_amdgcn_readfirstlane(ld_cnt(ptp(ws, 0))); }
  bar();
  if (KIND == 3) anchor = get_anchor(ws, sel, 64 + v, lane);
  if (KIND == 0) {
    asm volatile(R3_LOOP(R3_WARM("10f", "98f-10f"), R3_DMA, "", "", "", "", "", 2, "", 1, "", R3_FIN, "", R3_FIN_OOL) DIAG_R3_END : R3_OUTS : R3_INS, [redr] "v"(redr) : R3_CLOB);
  } else if (KIND == 1) {
    asm volatile(R3_LOOP(R3_WARM("10f", "98f-10f"), R3_DMA, "", "", "", "", "", 2, "", 1, "", "", "", "") DIAG_R3_END : R3_OUTS : R3_INS : R3_CLOB);
  } else if (KIND == 2) {
    const unsigned cc = ((unsigned)(3 - K_SMK) << 13) | (unsigned)(v * 128);
    const unsigned uc = (unsigned)O_FL + (C_W + 2 * (56 + 4 * (v >> 1) + (lane & 3))) * 64;
    asm volatile("s_mov_b64 s[40:41], -1\n\ts_mov_b32 s42, %[cc]\n\ts_and_b32 s43, s42, 0x1fff\n\ts_add_i32 s43, s43, 0x43e000\n\tv_mov_b32 v186, %[uc]\n\t"
                 R3_LOOP(R3_WARM("10f", "98f-10f"), R3_DMA, "", R3_CNT_LOAD, R3_CNT_PAY, R3_CNT_PACK, "", 3, R3_CNT_SIG, 2, "", "", R3_CNT_CHK, R3_CNT_OOL) DIAG_R3_END : R3_OUTS : R3_INS, [cc] "s"(cc), [uc] "v"(uc) : R3_CLOB);
  } else {
    asm volatile("s_mov_b32 s67, %[anc]\n\t" R3_LOOP(".Lr3tw_beg:\n\t", "", R3_TW_WAIT, R3_TW_PT, "", "", R3_TW_TIME, 3, "", 1, R3_FWD R3_PREP, "", R3_TW_NEXT, R3_TW_LATE R3_TW_OOL ".Lr3tw_end:\n\t") DIAG_R3_END : R3_OUTS : R3_INS, [anc] "s"(anchor) : R3_CLOB, TBURST_CLOB);
  }
  if (KIND == 2 && lane < 2) st4c_asm(flagY(ws, sel, NSEG - 2 + lane, v), 0u);
  RTS(3);
}
DEV void role_r3(unsigned char* __restrict__ ws, int v, int wave, int lane, unsigned sel TUNE_ARG) {
  if (wave == 0) r3_body<0>(ws, v, wave, lane, sel TUNE_PASS);
  else if (wave == 6) r3_body<2>(ws, v, wave, lane, sel TUNE_PASS);
  else if (wave == 7) r3_body<3>(ws, v, wave, lane, sel TUNE_PASS);
  else r3_body<1>(ws, v, wave, lane, sel TUNE_PASS);
}

#define PF_LAG 4
DEV void role_pf(unsigned char* __restrict__ ws, int k, int wave, int lane, unsigned sel) {
  constexpr int LAG = PF_LAG;
  const rsrc_t ru = mkrsrc(ws + O_U, 30u << 20);
  unsigned have = 0;
  for (int t = 1; t < NSEG - 1; ++t) {
    if (wave == 0) {
      unsigned skip = 0;
      const unsigned need = (t >> 1) + 1;
      if (__any(have < need)) { wait_cnt(ws, doneU(ws, min(lane, 55)), need); have = ld_cnt(doneU(ws, min(lane, 55))); }
      if (t >= LAG) wait_flags(ws, flagY(ws, sel, t - LAG, k));
      else if (t >= LAG - 2) {
        wait_cnt(ws, anchorp(ws, 28 + k), 1);
        const unsigned at = (__builtin_amdgcn_readfirstlane(ld_cnt(anchorp(ws, 28 + k))) & ~1u) + (t - LAG + 2) * 264;
        skip = (int)(((unsigned)rt32() << 1) - at) > 40;
        for (int it = 0; it < 4096 && (int)(at - ((unsigned)rt32() << 1)) > 0; ++it) __builtin_amdgcn_s_sleep(1);
      }
      lds_w4((t & 1) * 4, skip);
    }
    bar();
    if (__builtin_amdgcn_readfirstlane(lds_r4((t & 1) * 4))) continue;
    u4v r[14];
#pragma unroll
    for (int i = 0; i < 14; ++i) { const int n = (k * 8 + wave) * 14 + i; r[i] = ldc16(ru, (n >> 4) * U_RUN + (t * 16 + (n & 15)) * 1024 + lane * 16); }
#pragma unroll
    for (int i = 0; i < 14; ++i) asm volatile("" :: "v"(r[i]));
  }
}

__global__ void __launch_bounds__(512, 2)
main_kernel(const unsigned short* __restrict__ x, const unsigned short* __restrict__ b1, const unsigned short* __restrict__ b2,
            unsigned short* __restrict__ out, unsigned char* __restrict__ ws TUNE_ARG) {
  const int b = blockIdx.x, lane = threadIdx.x & 63;
  const int wave = __builtin_amdgcn_readfirstlane(threadIdx.x >> 6);
  RTS(0);
  const int xc = b & 7, slot = b >> 3;
  if (xc == 0 || xc == 2) {
    const unsigned sel = *(const unsigned*)(ws + O_SEL);
    role_r1(ws, (xc >> 1) * 32 + slot, wave, lane, sel TUNE_PASS);
  } else if (xc == 1) {
    const unsigned sel = *(const unsigned*)(ws + O_SEL);
    if (slot < 28) role_r3(ws, slot, wave, lane, sel TUNE_PASS);
    else role_pf(ws, slot - 28, wave, lane, sel);
  } else {
    const int k = slot * 5 + (xc - 3);
    if (k < 56) role_w1<true>(ws, k, wave, lane);
    else if (k < 112) role_w1<false>(ws, k - 56, wave, lane);
    else {
      if (k < 144) role_ffn(ws, x, b1, b2, k - 112, wave, lane);
      else role_stt(ws, (k - 144) >> 1, k & 1, wave, lane);
      role_w3(ws, out, (159 - k) >> 2, k & 3, wave, lane);
    }
  }
}

__global__ void dbgk(unsigned char* ws) {
  if (blockIdx.x != 0) return;
  const int lane = threadIdx.x;
  __shared__ unsigned r[1024];
  for (int k = 0; k < 3; ++k) for (int i = 0; i < 24; ++i) {
    const size_t st = k == 0 ? 4096 : k == 1 ? 65536 : 2097152;
    const unsigned char* p = ws + (130 + 10 * k) * MB + i * st;
    unsigned v; const unsigned c0 = rdtime();
    asm volatile("global_load_dword %0, %1, %2 sc1\n\ts_waitcnt vmcnt(0)" : "=&v"(v) : "v"(0u), "s"(p) : "memory");
    const unsigned c1 = rdtime();
    asm volatile("global_load_dword %0, %1, %2 offset:256 sc1\n\ts_waitcnt vmcnt(0)" : "=&v"(v) : "v"(0u), "s"(p) : "memory");
    const unsigned c2 = rdtime();
    if (lane == 0) { r[k * 48 + 2 * i] = c1 - c0; r[k * 48 + 2 * i + 1] = c2 - c1; }
  }
  for (int g = 0; g < 128; g += 2) for (int c = 0; c < 4; ++c) {
    const unsigned char* p = ws + g * 32768 + c * 8192 + 7680;
    unsigned v = SENT, best = 99999;
    for (int it = 0; it < 2; ++it) {
      asm volatile("global_store_dword %0, %1, %2 sc1\n\ts_waitcnt vmcnt(0)\n\ts_sleep 3" :: "v"(0u), "v"(v), "s"(p) : "memory");
      const unsigned c0 = rdtime();
      asm volatile("global_load_dword %0, %1, %2 sc1\n\ts_waitcnt vmcnt(0)" : "=&v"(v) : "v"(0u), "s"(p) : "memory");
      best = min(best, rdtime() - c0);
    }
    if (lane == 0) r[256 + g * 2 + c] = best;
  }
  if (lane == 0) {
    for (int k = 0; k < 3; ++k) { printf("stride%d:", k); for (int i = 0; i < 24; ++i) printf(" %u/%u", r[k * 48 + 2 * i], r[k * 48 + 2 * i + 1]); printf("\n"); }
    for (int g = 0; g < 128; g += 2) { unsigned b = 0; for (int c = 1; c < 4; ++c) if (r[256 + g * 2 + c] < r[256 + g * 2 + b]) b = c; printf("%u", b); } printf(" ws %p\n", ws);
  }
}
extern "C" void kernel_launch(void* const* d_in, const int* in_sizes, int n_in, void* d_out, int out_size, void* d_ws,
                              size_t ws_size, hipStream_t stream) {
  unsigned char* ws = (unsigned char*)(((uintptr_t)d_ws + 2 * MB - 1) & ~(uintptr_t)(2 * MB - 1));
  const unsigned short* x = (const unsigned short*)d_in[0];
  constexpr int LDS_MAIN = 132 * 1024;
  (void)hipFuncSetAttribute((const void*)main_kernel, hipFuncAttributeMaxDynamicSharedMemorySize, LDS_MAIN);
  dbgk<<<8, 64, 0, stream>>>(ws);
  constexpr int LDS_PREP = 152 * 1024;
  (void)hipFuncSetAttribute((const void*)prep_kernel, hipFuncAttributeMaxDynamicSharedMemorySize, LDS_PREP);
  prep_kernel<<<256, 512, LDS_PREP, stream>>>(x, (const unsigned short*)d_in[1], (const unsigned short*)d_in[2], (const unsigned short*)d_in[3],
                                           (const unsigned short*)d_in[4], (const unsigned short*)d_in[5], (const unsigned short*)d_in[8],
                                           (const unsigned short*)d_in[10], ws);
  main_kernel<<<256, 512, LDS_MAIN, stream>>>(x, (const unsigned short*)d_in[9], (const unsigned short*)d_in[11], (unsigned short*)d_out, ws TUNE_PASS);
}
```

```cpp
#include <hip/hip_runtime.h>
#include <hip/hip_bf16.h>
#include <stdint.h>
#include <stdio.h>

typedef __bf16 bf2 __attribute__((ext_vector_type(2)));
typedef __bf16 bf8 __attribute__((ext_vector_type(8)));
typedef __bf16 bf16x __attribute__((ext_vector_type(16)));
typedef short s4v __attribute__((ext_vector_type(4)));
typedef float f2v __attribute__((ext_vector_type(2)));
typedef float f4v __attribute__((ext_vector_type(4)));
typedef float f16v __attribute__((ext_vector_type(16)));
typedef unsigned u2v __attribute__((ext_vector_type(2)));
typedef unsigned u4v __attribute__((ext_vector_type(4)));
typedef unsigned u8v __attribute__((ext_vector_type(8)));
#define LDS __attribute__((address_space(3)))
#define GLB __attribute__((address_space(1)))
#define DEV __device__ __forceinline__

constexpr int DM = 448, NH = 16, DH = 256, NSEG = 64, FF = 1792;
constexpr unsigned SENT = 0xFFFFFFFFu;

constexpr size_t MB = 1u << 20;
constexpr size_t O_HB = 0;
constexpr size_t O_FL = 4 * MB;
constexpr size_t O_SEL = O_FL + 256 * 1024;
constexpr size_t O_XN = 5 * MB;
constexpr size_t O_WA = 7 * MB;
constexpr size_t O_WB = 14 * MB;
constexpr size_t O_W1 = 21 * MB;
constexpr size_t O_W2 = 23 * MB;
constexpr size_t O_G = 25 * MB;
constexpr size_t O_U = 55 * MB;
constexpr size_t O_ST = 85 * MB;
constexpr size_t O_SM = 87 * MB;
constexpr size_t O_YG = 100 * MB;
constexpr size_t O_F = 111 * MB;
constexpr unsigned G_RUN = 64 * 14336 + 3 * 4096, U_RUN = 64 * 16384 + 5 * 4096, Y_RUN = 64 * 4096 + 4096;
constexpr size_t O_END = 115 * MB;

DEV unsigned pk2(float a, float b) { f2v v = {a, b}; return __builtin_bit_cast(unsigned, __builtin_convertvector(v, bf2)); }
DEV float bflo(unsigned u) { return __builtin_bit_cast(float, u << 16); }
DEV float bfhi(unsigned u) { return __builtin_bit_cast(float, u & 0xffff0000u); }
DEV float rbf(float a) { return bflo(pk2(a, 0.f) & 0xffffu); }
DEV bf8 asbf8(u4v v) { return __builtin_bit_cast(bf8, v); }
DEV u4v lds_r16(unsigned a) { return *(LDS u4v*)(size_t)a; }
DEV void lds_w16(unsigned a, u4v v) { *(LDS u4v*)(size_t)a = v; }
DEV u2v lds_tr(unsigned a) { return __builtin_bit_cast(u2v, __builtin_amdgcn_ds_read_tr16_b64_v4i16((LDS s4v*)(size_t)a)); }
DEV unsigned rdtime() { unsigned long long t; asm volatile("s_memtime %0\n\ts_waitcnt lgkmcnt(0)" : "=s"(t) :: "memory"); return (unsigned)t; }
DEV int swap13(int x) { return (x & 5) | ((x >> 2) & 2) | ((x << 2) & 8); }
DEV void dma16(const void* g, unsigned ldsaddr) {
  unsigned keep;
  asm volatile("s_mov_b32 %0, m0\n\ts_mov_b32 m0, %2\n\ts_nop 0\n\tglobal_load_lds_dwordx4 %1, off\n\ts_mov_b32 m0, %0" : "=&s"(keep) : "v"(g), "s"(ldsaddr) : "memory");
}
DEV void dma16c(const void* g, unsigned ldsaddr) {
  unsigned keep;
  asm volatile("s_mov_b32 %0, m0\n\ts_mov_b32 m0, %2\n\ts_nop 0\n\tglobal_load_lds_dwordx4 %1, off sc1\n\ts_mov_b32 m0, %0" : "=&s"(keep) : "v"(g), "s"(ldsaddr) : "memory");
}
DEV void vm0() { asm volatile("s_waitcnt vmcnt(0)" ::: "memory"); }
DEV void bar() { asm volatile("s_waitcnt lgkmcnt(0)\n\ts_barrier" ::: "memory"); }

DEV void stwt16(void* p, u4v v) { asm volatile("global_store_dwordx4 %0, %1, off sc1\n\ts_nop 1" :: "v"(p), "v"(v) : "memory"); }
DEV float wavesum(float v) {
  v += __builtin_bit_cast(float, __builtin_amdgcn_update_dpp(0, __builtin_bit_cast(int, v), 0xb1, 0xf, 0xf, true));
  v += __builtin_bit_cast(float, __builtin_amdgcn_update_dpp(0, __builtin_bit_cast(int, v), 0x4e, 0xf, 0xf, true));
  v += __builtin_bit_cast(float, __builtin_amdgcn_update_dpp(0, __builtin_bit_cast(int, v), 0x124, 0xf, 0xf, true));
  v += __builtin_bit_cast(float, __builtin_amdgcn_update_dpp(0, __builtin_bit_cast(int, v), 0x128, 0xf, 0xf, true));
  const unsigned u = __builtin_bit_cast(unsigned, v);
  const u2v a = __builtin_amdgcn_permlane16_swap(u, u, false, false);
  const unsigned ax = a.x, ay = a.y;
  const unsigned w = __builtin_bit_cast(unsigned, __builtin_bit_cast(float, ax) + __builtin_bit_cast(float, ay));
  const u2v c = __builtin_amdgcn_permlane32_swap(w, w, false, false);
  const unsigned cx = c.x, cy = c.y;
  return __builtin_bit_cast(float, cx) + __builtin_bit_cast(float, cy);
}
#define K0S(k)
DEV void ln_row(u4v xv, unsigned char* __restrict__ ws, int row, int lane) {
  float e[8];
  if (lane >= 56) xv = (u4v){0u, 0u, 0u, 0u};
#pragma unroll
  for (int j = 0; j < 4; ++j) { e[2 * j] = bflo(xv[j]); e[2 * j + 1] = bfhi(xv[j]); }
  float s = 0.f;
#pragma unroll
  for (int j = 0; j < 8; ++j) s += e[j];
  const float mean = wavesum(s) * (1.f / DM);
  float q = 0.f;
  if (lane < 56) {
#pragma unroll
    for (int j = 0; j < 8; ++j) { const float d = e[j] - mean; q += d * d; }
  }
  const float mb = rbf(mean), vb = rbf(wavesum(q) * (1.f / DM));
  const float den = rbf(__builtin_sqrtf(rbf(vb + 1e-5f)));
  unsigned o4[4];
#pragma unroll
  for (int j = 0; j < 4; ++j) o4[j] = pk2(rbf(e[2 * j] - mb) / den, rbf(e[2 * j + 1] - mb) / den);
  const bool odd = lane & 1;
  const unsigned r0 = __builtin_amdgcn_update_dpp(0u, odd ? o4[0] : o4[2], 0xb1, 0xf, 0xf, true);
  const unsigned r1 = __builtin_amdgcn_update_dpp(0u, odd ? o4[1] : o4[3], 0xb1, 0xf, 0xf, true);
  u4v p;
  if (odd) p = (u4v){r0, o4[2], r1, o4[3]}; else p = (u4v){o4[0], r0, o4[1], r1};
  if (lane < 56) stwt16(ws + O_XN + (size_t)(row >> 5) * 28672 + (lane >> 1) * 1024 + (lane & 1) * 512 + (row & 31) * 16, p);
}
DEV u4v srd(const void* p, unsigned bytes) { const unsigned long long a = (unsigned long long)p; return (u4v){(unsigned)a, (unsigned)(a >> 32), bytes, 0x00020000u}; }
DEV void bdma16(unsigned voff, u4v r, unsigned soff, unsigned ldsaddr) {
  asm volatile("s_mov_b32 m0, %3\n\ts_nop 0\n\tbuffer_load_dwordx4 %0, %1, %2 offen lds" :: "v"(voff), "s"(r), "s"(soff), "s"(ldsaddr) : "memory");
}
constexpr unsigned rowoff(int j) { return (16 * ((j >> 2) & 1) + 8 * (j & 1) + 4 * ((j >> 1) & 1) + 2 * (j >> 3)) * 8192u; }

template <bool ISA>
DEV void prep_gemm(const unsigned short* __restrict__ Rw, const unsigned short* __restrict__ Cw, const unsigned short* __restrict__ x,
                   unsigned char* __restrict__ ws, int b, int h, int cg, int wave, int lane) {
  const int g = lane >> 4, c16 = lane & 15, hl = lane >> 5, sl = lane & 31;
  const int row = b * 8 + wave;
  const unsigned xl = 147456 + wave * 1024;
  dma16(x + (size_t)row * DM + min(lane, 55) * 8, xl);
  if (ISA) {
    const u4v rc = srd(Cw + (size_t)64 * cg * 4096 + h * 256, 64 * 8192);
    const unsigned v0 = hl * 8192 + ((sl ^ hl ^ ((wave & 1) << 3)) << 4), s0 = ((wave >> 1) * 16 + (wave & 1) * 2) * 8192;
#pragma unroll
    for (int j = 0; j < 4; ++j) bdma16(v0 ^ (j << 5), rc, s0 + ((j & 1) * 8 + (j >> 1) * 4) * 8192, (wave * 4 + j) * 1024);
  } else {
    const u4v rc = srd(Cw + (size_t)h * 256 * DM + 64 * cg, 256 * 896);
    const unsigned v0 = (lane >> 3) * 896 + (((lane & 7) ^ (((lane >> 4) & 1) << 1)) << 4);
#pragma unroll
    for (int j = 0; j < 4; ++j) bdma16(v0 ^ ((j & 1) << 6), rc, (wave * 4 + j) * 8 * 896, (wave * 4 + j) * 1024);
  }
  if (wave == 7) {
    __builtin_amdgcn_sched_barrier(0);
    asm volatile("s_waitcnt vmcnt(0)\n\ts_barrier" ::: "memory");
    const u4v ones = {SENT, SENT, SENT, SENT}, zero = {0u, 0u, 0u, 0u};
#pragma unroll
    for (int j = 0; j < 16; ++j) if (b >= 2) stwt16(ws + O_HB + (size_t)b * 16384 + j * 1024 + lane * 16, ones);
    stwt16(ws + O_FL + b * 1024 + lane * 16, zero);
    ln_row(lds_r16(xl + lane * 16), ws, row, lane);
    return;
  }
  const float sc = ISA ? 0.0625f * 1.4426950408889634f : 1.f;
  unsigned char* dst = ws + (ISA ? O_WA : O_WB) + (size_t)(h * 28 + 4 * cg) * 14336 + 2 * wave * 1024 + lane * 16;
  const unsigned reg = 32768 + wave * 16384;
  const int rot = cg * 2;
  const unsigned srot = (16 * ((rot >> 2) & 1) + 8 * (rot & 1) + 4 * ((rot >> 1) & 1) + 2 * (rot >> 3)) * 8192u, mrot = reg | (rot << 10);
  const unsigned vr = hl * 8192 + (((sl ^ hl) << 4) ^ ((rot & 7) << 5));
  const u4v rr = srd(Rw + (size_t)(64 * wave) * 4096 + h * 256, 64 * 8192);
  const __amdgpu_buffer_rsrc_t rq = __builtin_amdgcn_make_buffer_rsrc((void*)(Rw + (size_t)(64 * wave + 32) * 4096 + h * 256), 0, 32 * 8192, 0x00020000);
  u4v nx[16];
#pragma unroll
  for (int j = 0; j < 2; ++j) bdma16(vr ^ ((j & 7) << 5), rr, srot ^ rowoff(j), mrot ^ (j << 10));
#pragma unroll
  for (int j = 0; j < 2; ++j) nx[j] = __builtin_amdgcn_raw_buffer_load_b128(rq, vr ^ ((j & 7) << 5), srot ^ rowoff(j), 0);
  __builtin_amdgcn_sched_barrier(0);
  asm volatile("s_waitcnt vmcnt(8)" ::: "memory");
  ln_row(lds_r16(xl + lane * 16), ws, row, lane);
  __builtin_amdgcn_sched_barrier(0);
  K0S(1);
  asm volatile("s_waitcnt vmcnt(5)\n\ts_barrier" ::: "memory");
  K0S(0);
#pragma unroll
  for (int j = 2; j < 16; ++j) bdma16(vr ^ ((j & 7) << 5), rr, srot ^ rowoff(j), mrot ^ (j << 10));
#pragma unroll
  for (int j = 2; j < 16; ++j) nx[j] = __builtin_amdgcn_raw_buffer_load_b128(rq, vr ^ ((j & 7) << 5), srot ^ rowoff(j), 0);
  __builtin_amdgcn_sched_barrier(0);
  asm volatile("s_waitcnt vmcnt(14)" ::: "memory");
  K0S(2);
  const int q = c16 >> 2, p = c16 & 3;
  const int xr = (q >> 1) | ((g & 1) << 1);
  auto bfrag = [&](int es, int ti) -> u4v {
    if (ISA) return lds_r16((ti * 16 + c16) * 512 + (((es * 4 + g) ^ c16) << 4));
    const unsigned ad = (32 * es + 8 * g + q) * 128 + ((ti ^ xr) << 5) + p * 8;
    const u2v lo = lds_tr(ad), hi = lds_tr(ad + 512);
    return (u4v){lo.x, lo.y, hi.x, hi.y};
  };
  auto afrag = [&](int es, int t) -> u4v { return lds_r16(reg + (t * 16 + c16) * 512 + (((es * 4 + g) ^ c16) << 4)); };
#pragma unroll
  for (int rb = 0; rb < 2; ++rb) {
    u4v bb[2][4], aa[2][2];
#pragma unroll
    for (int ti = 0; ti < 4; ++ti) bb[0][ti] = bfrag(0, ti);
    aa[0][0] = afrag(0, 0); aa[0][1] = afrag(0, 1);
    f4v acc[2][4];
#pragma unroll
    for (int t = 0; t < 2; ++t)
#pragma unroll
      for (int ti = 0; ti < 4; ++ti) acc[t][ti] = (f4v){0.f, 0.f, 0.f, 0.f};
#pragma unroll
    for (int es = 0; es < 8; ++es) {
      if (es < 7) {
        aa[(es + 1) & 1][0] = afrag(es + 1, 0); aa[(es + 1) & 1][1] = afrag(es + 1, 1);
#pragma unroll
        for (int ti = 0; ti < 4; ++ti) bb[(es + 1) & 1][ti] = bfrag(es + 1, ti);
      }
#pragma unroll
      for (int ti = 0; ti < 4; ++ti)
#pragma unroll
        for (int t = 0; t < 2; ++t)
          acc[t][ti] = __builtin_amdgcn_mfma_f32_16x16x32_bf16(asbf8(aa[es & 1][t]), asbf8(bb[es & 1][ti]), acc[t][ti], 0, 0, 0);
      __builtin_amdgcn_sched_barrier(0);
    }
    if (rb == 0) {
#pragma unroll
      for (int j = 0; j < 16; ++j) lds_w16((mrot ^ (j << 10)) + lane * 16, nx[j]);
    }
#pragma unroll
    for (int ti = 0; ti < 4; ++ti) {
      const u4v o = {pk2(acc[0][ti][0] * sc, acc[0][ti][1] * sc), pk2(acc[0][ti][2] * sc, acc[0][ti][3] * sc),
                     pk2(acc[1][ti][0] * sc, acc[1][ti][1] * sc), pk2(acc[1][ti][2] * sc, acc[1][ti][3] * sc)};
      stwt16(dst + (size_t)ti * 14336 + rb * 1024, o);
    }
  }
  K0S(3);
}

__global__ void __launch_bounds__(512, 2)
prep_kernel(const unsigned short* __restrict__ x, const unsigned short* __restrict__ mem0,
            const unsigned short* __restrict__ Wq, const unsigned short* __restrict__ Wk,
            const unsigned short* __restrict__ Wv, const unsigned short* __restrict__ Wo,
            const unsigned short* __restrict__ w1, const unsigned short* __restrict__ w2,
            unsigned char* __restrict__ ws) {
  const int b = blockIdx.x, tid = threadIdx.x, lane = tid & 63;
  const int wave = __builtin_amdgcn_readfirstlane(tid >> 6);
  const int xc = b & 7, slot = b >> 3;
  if (slot < 28) {
    const int m = xc * 4 + slot / 7, cg = slot % 7;
    if (m < 16) prep_gemm<true>(Wk, Wq, x, ws, b, m, cg, wave, lane);
    else prep_gemm<false>(Wv, Wo, x, ws, b, m - 16, cg, wave, lane);
  } else {
    const int row = b * 8 + wave;
    u4v xv = {0u, 0u, 0u, 0u};
    const int misc = (slot - 28) * 8 + xc;
    const int job = (misc - 1) * 8 + wave;
    const unsigned reg = wave * 14336;
    if (misc == 0) {
      if (wave == 7) {
        u4v m0[7];
#pragma unroll
        for (int j = 0; j < 7; ++j) { const int n = j * 64 + lane; m0[j] = *(const u4v*)(mem0 + ((n >> 1) & 7) * DM + (n >> 4) * 16 + (n & 1) * 8); }
        const u4v ones = {SENT, SENT, SENT, SENT};
        stwt16(ws + O_HB + (lane >> 4) * 8192 + (lane & 15) * 64, ones);
        stwt16(ws + O_HB + 2 * MB + (lane >> 4) * 8192 + (lane & 15) * 64, ones);
#pragma unroll
        for (int j = 0; j < 7; ++j) asm volatile("" : "+v"(m0[j]));
        vm0();
        auto timed = [&](const unsigned char* pc) -> unsigned {
          unsigned v;
          const unsigned c0 = rdtime();
          asm volatile("global_load_dword %0, %1, %2 sc1\n\ts_waitcnt vmcnt(0)" : "=&v"(v) : "v"(0u), "s"(pc) : "memory");
          return rdtime() - c0;
        };
        unsigned best[6];
#pragma unroll
        for (int c = 0; c < 6; ++c) best[c] = 0x7fffffff;
#pragma unroll 1
        for (int it = 0; it < 3; ++it) {
#pragma unroll
          for (int c = 0; c < 4; ++c) best[c] = min(best[c], timed(ws + O_HB + c * 8192 + it * 256));
        }
        unsigned sel = 0, bv = best[0];
#pragma unroll
        for (unsigned k = 1; k < 4; ++k) if (best[k] < bv) { bv = best[k]; sel = k; }
#pragma unroll 1
        for (int it = 0; it < 2; ++it) {
#pragma unroll
          for (int c = 0; c < 2; ++c) best[4 + c] = min(best[4 + c], timed(ws + O_HB + 2 * MB + (sel ^ c) * 8192 + it * 256));
        }
        sel |= (sel ^ (best[5] <= best[4] ? 1u : 0u)) << 2;
        if (lane == 0) *(unsigned*)(ws + O_SEL) = sel;
        if (lane == 0) ((unsigned*)(ws + O_SEL))[16] = (unsigned)__builtin_amdgcn_s_getreg(20 | (3 << 11)) & 0xfu;
#pragma unroll
        for (int j = 0; j < 7; ++j) stwt16(ws + O_HB + (sel & 3u) * 8192 + j * 1024 + lane * 16, m0[j]);
#pragma unroll
        for (int j = 8; j < 32; ++j) stwt16(ws + O_HB + (((sel & 3u) * 8192) ^ (j * 1024)) + lane * 16, ones);
      }
    } else if (job < 224) {
      const bool first = job < 112;
      const int step = first ? job >> 2 : job - 112;
      const unsigned short* src = first ? w1 + (size_t)step * 16 * FF + (job & 3) * 448 : w2 + (size_t)step * 16 * DM;
      const int ld = first ? FF : DM;
#pragma unroll
      for (int j = 0; j < 14; ++j) { const int n = j * 64 + lane, r = n / 56; dma16(src + (size_t)r * ld + (n - r * 56) * 8, reg + j * 1024); }
    }
    if (lane < 56) xv = *(const u4v*)(x + (size_t)row * DM + lane * 8);
    asm volatile("" : "+v"(xv));
    vm0();
    if (misc > 0 && job < 224) {
      const bool first = job < 112;
      const int hi = lane >> 5, q = (lane & 15) >> 2;
      const int r0 = first ? 4 * hi + 8 * (q >> 1) + (q & 1) : 4 * hi + q, dr = first ? 2 : 8;
      const unsigned ad = reg + r0 * 896 + ((lane >> 4) & 1) * 32 + (lane & 3) * 8;
      unsigned char* dst = ws + (first ? O_W1 + (size_t)((job & 3) * 14 * 28 + (job >> 2)) * 1024 : O_W2 + (size_t)(job - 112) * 1024) + lane * 16;
      const size_t dstep = first ? 28672 : 114688;
#pragma unroll
      for (int t = 0; t < 14; ++t) {
        const u2v lo = lds_tr(ad + t * 64), hv = lds_tr(ad + t * 64 + dr * 896);
        stwt16(dst + t * dstep, (u4v){lo.x, lo.y, hv.x, hv.y});
      }
    }
    const u4v ones = {SENT, SENT, SENT, SENT}, zero = {0u, 0u, 0u, 0u};
    stwt16(ws + O_HB + (size_t)b * 16384 + tid * 16, ones);
    stwt16(ws + O_HB + (size_t)b * 16384 + 8192 + tid * 16, ones);
    if (wave == 0) stwt16(ws + O_FL + b * 1024 + lane * 16, zero);
    ln_row(xv, ws, row, lane);
  }
}
constexpr int C_W = 0  , C_F = 1856, C_ABORT = 1900, C_ANCHOR = 1920;
typedef __amdgpu_buffer_rsrc_t rsrc_t;
DEV rsrc_t mkrsrc(const void* p, unsigned bytes) { return __builtin_amdgcn_make_buffer_rsrc((void*)p, 0, bytes, 0x00020000); }
DEV u4v ldc16(rsrc_t r, unsigned off) { return __builtin_amdgcn_raw_buffer_load_b128(r, off, 0, 16); }
DEV u2v ldc8(rsrc_t r, unsigned off) { return __builtin_amdgcn_raw_buffer_load_b64(r, off, 0, 16); }
DEV void stc16(rsrc_t r, unsigned off, u4v v) { __builtin_amdgcn_raw_buffer_store_b128(v, r, off, 0, 16); }
DEV void stc4(rsrc_t r, unsigned off, unsigned v) { __builtin_amdgcn_raw_buffer_store_b32(v, r, off, 0, 16); }
DEV unsigned* cntp(unsigned char* ws, int idx) { return (unsigned*)(ws + O_FL) + idx * 16; }
DEV unsigned ld_cnt(const unsigned* p) { unsigned v; asm volatile("global_load_dword %0, %1, off sc1\n\ts_waitcnt vmcnt(0)" : "=&v"(v) : "v"(p) : "memory"); return v; }
DEV void add_cnt(unsigned* p, int lane) {
  if (lane == 0) { const unsigned one = 1; asm volatile("global_atomic_add %0, %1, off sc1" :: "v"(p), "v"(one) : "memory"); }
}
DEV void post(unsigned* p, unsigned v, int lane) { if (lane == 0) asm volatile("global_store_dword %0, %1, off sc1" :: "v"(p), "v"(v) : "memory"); }
DEV unsigned* doneG(unsigned char* ws, int j) { return cntp(ws, C_W + 2 * j); }
DEV unsigned* doneU(unsigned char* ws, int j) { return cntp(ws, C_W + 2 * (56 + j)); }
constexpr int SPIN_CAP = 1 << 17;
DEV void wait_cnt(unsigned char* ws, const unsigned* p, unsigned target, int nap = 6) {
  for (int it = 0;; ++it) {
    const unsigned v = ld_cnt(p);
    if (__all(v >= target)) return;
    if ((it & 63) == 63) {
      if (it >= SPIN_CAP) { const unsigned one = 1; asm volatile("global_store_dword %0, %1, off sc1" :: "v"(cntp(ws, C_ABORT)), "v"(one) : "memory"); return; }
      if (__any(ld_cnt(cntp(ws, C_ABORT)) != 0)) return;
    }
    if (nap > 6) __builtin_amdgcn_s_sleep(32); else __builtin_amdgcn_s_sleep(2);
  }
}
DEV unsigned hoff(unsigned sel, int gi, int kind) {
  const unsigned k = ((sel >> (2 * (gi >> 6))) & 3u) ^ ((((unsigned)gi >> 4) & 1u) << 1) ^ (unsigned)kind;
  return (unsigned)O_HB + (unsigned)gi * 32768u + k * 8192u;
}
DEV unsigned char* hblk(unsigned char* ws, unsigned sel, int gi, int kind) { return ws + hoff(sel, gi, kind); }
DEV unsigned char* flagS(unsigned char* ws, unsigned sel, int s, int id) { return hblk(ws, sel, s, 3) + id * 128; }
DEV unsigned char* flagY(unsigned char* ws, unsigned sel, int s, int v) { return hblk(ws, sel, 64 + s, 3) + v * 128; }
DEV unsigned char* flagT(unsigned char* ws, unsigned sel, int s, int h) { return hblk(ws, sel, 64 + s, 1) + h * 128; }
DEV void wait_flags(unsigned char* ws, const unsigned char* p) {
  for (int it = 0;; ++it) {
    if (!__any(ld_cnt((const unsigned*)p) == SENT)) return;
    if ((it & 63) == 63) {
      if (it >= SPIN_CAP) { const unsigned one = 1; asm volatile("global_store_dword %0, %1, off sc1" :: "v"(cntp(ws, C_ABORT)), "v"(one) : "memory"); return; }
      if (__any(ld_cnt(cntp(ws, C_ABORT)) != 0)) return;
    }
    __builtin_amdgcn_s_sleep(6);
  }
}
DEV f16v mfma32(u4v a, u4v b, f16v c) { return __builtin_amdgcn_mfma_f32_32x32x16_bf16(asbf8(a), asbf8(b), c, 0, 0, 0); }
DEV f4v mfma16(u4v a, u4v b, f4v c) { return __builtin_amdgcn_mfma_f32_16x16x32_bf16(asbf8(a), asbf8(b), c, 0, 0, 0); }
DEV void st16c_asm(void* p, u4v v) { asm volatile("global_store_dwordx4 %0, %1, off sc1\n\ts_nop 1" :: "v"(p), "v"(v) : "memory"); }

#define RTS(k)
template <bool ISG>
DEV void role_w1(unsigned char* __restrict__ ws, int j, int wave, int lane) {
  const int ct = wave & 3, rh = wave >> 2, hi = lane >> 5, c32 = lane & 31;
  int h, tile0;
  if (ISG) { const int hp = j / 7, ctile = (j % 7) * 4 + ct; h = 2 * hp + ctile / 14; tile0 = 2 * (ctile % 14); }
  else { const int V = j >> 2; h = 4 * (j & 3) + ct; tile0 = 2 * V; }
  const unsigned char* xn = ws + O_XN + wave * 7168 + lane * 16;
#pragma unroll
  for (int i = 0; i < 7; ++i) dma16(xn + i * 1024, wave * 7168 + i * 1024);
  u4v w[28];
  {
    const unsigned char* p = ws + (ISG ? O_WA : O_WB) + (size_t)(h * 28 + tile0 + (c32 >> 4)) * 14336 + hi * 256 + (c32 & 15) * 16;
#pragma unroll
    for (int s = 0; s < 28; ++s) w[s] = *(const u4v*)(p + (s >> 1) * 1024 + (s & 1) * 512);
  }
  constexpr int SEGB = ISG ? 14336 : 16384;
  unsigned char* out = (ISG ? ws + O_G + (size_t)(h * 2 + (c32 >> 4)) * G_RUN + (tile0 >> 1) * 1024
                            : ws + O_U + (size_t)(tile0 + (c32 >> 4)) * U_RUN + h * 1024) + rh * SEGB + hi * 256 + (c32 & 15) * 16;
  const int tok = ISG ? c32 : ((c32 & 19) | ((c32 >> 1) & 4) | ((c32 << 1) & 8));
  const unsigned rd = rh * 28672 + hi * 512 + tok * 16;
  unsigned* done = ISG ? doneG(ws, j) : doneU(ws, j);
#pragma unroll
  for (int s = 0; s < 28; ++s) asm volatile("" : "+v"(w[s]));
  vm0();
  RTS(1);
  for (int p = 0; p < 32; ++p) {
    asm volatile("s_waitcnt vmcnt(2)\n\ts_barrier" ::: "memory");
    if (wave == 7 && p >= 6) post(done, p - 1, lane);
    const unsigned buf = (p & 1) * 57344;
    const unsigned char* src = xn + (size_t)(p < 31 ? p + 1 : p) * 57344;
    const unsigned dstl = (57344 - buf) + wave * 7168;
    f16v acc;
#pragma unroll
    for (int i = 0; i < 16; ++i) acc[i] = 0.f;
    const LDS u4v* xp = (const LDS u4v*)(size_t)(buf + rd);
    u4v xr[5];
#pragma unroll
    for (int s = 0; s < 4; ++s) xr[s] = xp[s * 64];
#pragma unroll
    for (int s = 0; s < 28; ++s) {
      if (s + 4 < 28) xr[(s + 4) % 5] = xp[(s + 4) * 64];
      acc = ISG ? mfma32(w[s], xr[s % 5], acc) : mfma32(xr[s % 5], w[s], acc);
      __builtin_amdgcn_sched_barrier(0);
      if (s % 3 == 0 && s / 3 < 7) { dma16(src + (s / 3) * 1024, dstl + (s / 3) * 1024); __builtin_amdgcn_sched_barrier(0); }
      if (s == 16) {
        asm volatile("s_waitcnt vmcnt(6)" ::: "memory");
        __builtin_amdgcn_sched_barrier(0);
      }
    }
    const u4v o0 = {pk2(acc[0], acc[1]), pk2(acc[2], acc[3]), pk2(acc[4], acc[5]), pk2(acc[6], acc[7])};
    const u4v o1 = {pk2(acc[8], acc[9]), pk2(acc[10], acc[11]), pk2(acc[12], acc[13]), pk2(acc[14], acc[15])};
    unsigned char* o = out + p * 2 * SEGB;
    st16c_asm(o, o0); st16c_asm(o + 512, o1);
    if (p < 4) {
      RTS(4);
      asm volatile("s_waitcnt vmcnt(0)\n\ts_barrier" ::: "memory");
      if (wave == 7) post(done, p + 1, lane);
      RTS(2);
    }
  }
  asm volatile("s_waitcnt vmcnt(0)\n\ts_barrier" ::: "memory");
  if (wave == 7) post(done, 32, lane);
  RTS(3);
}

template <bool P2>
DEV void ffn_body(unsigned char* __restrict__ ws, const unsigned short* __restrict__ x, const unsigned short* __restrict__ b1,
                  const unsigned short* __restrict__ b2, int job, int wave, int lane) {
  const int hi = lane >> 5, c32 = lane & 31;
  constexpr int NL = P2 ? 60 : 28, RING = P2 ? 12 : 14, DIST = RING - 1;
  const u4v* w1p = (const u4v*)(ws + O_W1 + (size_t)wave * 28672) + lane;
  const u4v* w2p = (const u4v*)(ws + O_W2 + (size_t)(2 * wave) * 114688) + lane;
  auto wload = [&](int c, int i) -> u4v {
    if (i < 28) return w1p[(size_t)c * 14336 + i * 64];
    return w2p[(size_t)c * 1024 + ((i - 28) >> 1) * 64 + ((i - 28) & 1) * 7168];
  };
  u4v wr[RING];
#pragma unroll
  for (int i = 0; i < DIST; ++i) wr[i] = wload(0, i);
  {
    const unsigned char* xn = ws + O_XN + (size_t)job * 57344 + wave * 7168 + lane * 16;
#pragma unroll
    for (int i = 0; i < 7; ++i) dma16(xn + i * 1024, wave * 7168 + i * 1024);
  }
  f16v o[2][2];
#pragma unroll
  for (int a = 0; a < 2; ++a)
#pragma unroll
    for (int bq = 0; bq < 2; ++bq)
#pragma unroll
      for (int i = 0; i < 16; ++i) o[a][bq][i] = 0.f;
  vm0();
  bar();
  const LDS u4v* xp = (const LDS u4v*)(size_t)(lane * 16);
  for (int c = 0; c < 7; ++c) {
    const int cn = c < 6 ? c + 1 : 6;
    const unsigned hbuf = 57344 + (c & 1) * 32768;
    u2v bv[4];
#pragma unroll
    for (int q = 0; q < 4; ++q) bv[q] = *(const u2v*)(b1 + 32 * (8 * c + wave) + 4 * hi + 8 * q);
    f16v a0, a1;
#pragma unroll
    for (int i = 0; i < 16; ++i) { a0[i] = 0.f; a1[i] = 0.f; }
    u4v xa[3], xb[3];
#pragma unroll
    for (int s = 0; s < 2; ++s) { xa[s] = xp[s * 64]; xb[s] = xp[1792 + s * 64]; }
#pragma unroll
    for (int s = 0; s < 28; ++s) {
      wr[(s + DIST) % RING] = (s + DIST < NL) ? wload(c, s + DIST) : wload(cn, s + DIST - NL);
      if (s + 2 < 28) { xa[(s + 2) % 3] = xp[(s + 2) * 64]; xb[(s + 2) % 3] = xp[1792 + (s + 2) * 64]; }
      a0 = mfma32(wr[s % RING], xa[s % 3], a0);
      a1 = mfma32(wr[s % RING], xb[s % 3], a1);
      __builtin_amdgcn_sched_barrier(0);
    }
#pragma unroll
    for (int i = 0; i < 16; ++i) {
      const unsigned bw = bv[i >> 2][(i >> 1) & 1];
      const float bs = (i & 1) ? bfhi(bw) : bflo(bw);
      const float t0 = fmaxf(a0[i] + bs, 0.f), t1 = fmaxf(a1[i] + bs, 0.f);
      a0[i] = t0 * t0; a1[i] = t1 * t1;
    }
#pragma unroll
    for (int bq = 0; bq < 2; ++bq) {
      lds_w16(hbuf + ((wave * 2 + bq) * 2 + 0) * 1024 + lane * 16,
              (u4v){pk2(a0[8 * bq], a0[8 * bq + 1]), pk2(a0[8 * bq + 2], a0[8 * bq + 3]), pk2(a0[8 * bq + 4], a0[8 * bq + 5]), pk2(a0[8 * bq + 6], a0[8 * bq + 7])});
      lds_w16(hbuf + ((wave * 2 + bq) * 2 + 1) * 1024 + lane * 16,
              (u4v){pk2(a1[8 * bq], a1[8 * bq + 1]), pk2(a1[8 * bq + 2], a1[8 * bq + 3]), pk2(a1[8 * bq + 4], a1[8 * bq + 5]), pk2(a1[8 * bq + 6], a1[8 * bq + 7])});
    }
    bar();
    if (P2) {
      const LDS u4v* hp = (const LDS u4v*)(size_t)(hbuf + lane * 16);
      u4v h0[2], h1[2];
      h0[0] = hp[0]; h1[0] = hp[64];
#pragma unroll
      for (int s = 0; s < 16; ++s) {
        const int i = 28 + 2 * s;
        wr[(i + DIST) % RING] = (i + DIST < NL) ? wload(c, i + DIST) : wload(cn, i + DIST - NL);
        if (s + 1 < 16) { h0[(s + 1) & 1] = hp[(s + 1) * 128]; h1[(s + 1) & 1] = hp[(s + 1) * 128 + 64]; }
        o[0][0] = mfma32(wr[i % RING], h0[s & 1], o[0][0]); o[0][1] = mfma32(wr[i % RING], h1[s & 1], o[0][1]);
        __builtin_amdgcn_sched_barrier(0);
        wr[(i + 1 + DIST) % RING] = (i + 1 + DIST < NL) ? wload(c, i + 1 + DIST) : wload(cn, i + 1 + DIST - NL);
        o[1][0] = mfma32(wr[(i + 1) % RING], h0[s & 1], o[1][0]); o[1][1] = mfma32(wr[(i + 1) % RING], h1[s & 1], o[1][1]);
        __builtin_amdgcn_sched_barrier(0);
      }
    }
  }
  if (P2) {
    const rsrc_t rf = mkrsrc(ws + O_F, 4u << 20);
#pragma unroll
    for (int a = 0; a < 2; ++a)
#pragma unroll
      for (int sg = 0; sg < 2; ++sg)
#pragma unroll
        for (int Q = 0; Q < 4; ++Q) {
          const int n = 32 * (2 * wave + a) + 8 * Q + 4 * hi, seg = 2 * job + sg;
          const u2v xv = *(const u2v*)(x + (size_t)(seg * 32 + c32) * DM + n), bv = *(const u2v*)(b2 + n);
          const f4v r = {o[a][sg][4 * Q] + bflo(bv.x) + bflo(xv.x), o[a][sg][4 * Q + 1] + bfhi(bv.x) + bfhi(xv.x),
                         o[a][sg][4 * Q + 2] + bflo(bv.y) + bflo(xv.y), o[a][sg][4 * Q + 3] + bfhi(bv.y) + bfhi(xv.y)};
          const int v = 2 * (2 * wave + a) + (Q >> 1), g = 2 * (Q & 1) + hi;
          stc16(rf, ((seg * 28 + v) * 2 + (c32 >> 4)) * 1024 + (g * 16 + (c32 & 15)) * 16, __builtin_bit_cast(u4v, r));
        }
    vm0();
    add_cnt(cntp(ws, C_F + job), lane);
    RTS(3);
  }
}
DEV void role_ffn(unsigned char* __restrict__ ws, const unsigned short* __restrict__ x, const unsigned short* __restrict__ b1,
                  const unsigned short* __restrict__ b2, int job, int wave, int lane) {
  if (wave < 7) ffn_body<true>(ws, x, b1, b2, job, wave, lane);
  else ffn_body<false>(ws, x, b1, b2, job, wave, lane);
}

DEV float asf(unsigned u) { return __builtin_bit_cast(float, u); }
DEV unsigned asu(float f) { return __builtin_bit_cast(unsigned, f); }
DEV float xmax16_32(float v) {
  const u2v a = __builtin_amdgcn_permlane16_swap(asu(v), asu(v), false, false);
  v = fmaxf(asf(a.x), asf(a.y));
  const u2v c = __builtin_amdgcn_permlane32_swap(asu(v), asu(v), false, false);
  return fmaxf(asf(c.x), asf(c.y));
}
DEV float xsum16_32(float v) {
  const u2v a = __builtin_amdgcn_permlane16_swap(asu(v), asu(v), false, false);
  v = asf(a.x) + asf(a.y);
  const u2v c = __builtin_amdgcn_permlane32_swap(asu(v), asu(v), false, false);
  return asf(c.x) + asf(c.y);
}
DEV float halfsum(float a, float b) {
  const u2v s = __builtin_amdgcn_permlane32_swap(asu(a), asu(b), false, false);
  return asf(s.x) + asf(s.y);
}

DEV void role_stt(unsigned char* __restrict__ ws, int grp, int hg, int wave, int lane) {
  const unsigned sel = *(const unsigned*)(ws + O_SEL);
  const int g = lane >> 4, c16 = lane & 15, h = 8 * hg + wave;
  const rsrc_t rg = mkrsrc(ws + O_G, 30u << 20);
  const LDS u4v* xp = (const LDS u4v*)(size_t)((g >> 1) * 1024 + (g & 1) * 512 + c16 * 16);
  for (int s = grp; s < NSEG; s += 8) {
    if (wave == 7) wait_cnt(ws, doneG(ws, 28 * hg + min(lane, 27)), (s >> 1) + 1, 32);
    bar();
    if (wave < 7) {
#pragma unroll
      for (int i = 0; i < 4; ++i) dma16(ws + O_XN + (size_t)s * 28672 + (wave * 4 + i) * 1024 + lane * 16, (wave * 4 + i) * 1024);
    }
    u4v ga[28];
#pragma unroll
    for (int T = 0; T < 28; ++T) ga[T] = ldc16(rg, (h * 2 + T / 14) * G_RUN + s * 14336 + (T % 14) * 1024 + lane * 16);
    __builtin_amdgcn_sched_barrier(0);
    asm volatile("s_waitcnt vmcnt(28)\n\ts_barrier" ::: "memory");
    f4v acc[2][2];
#pragma unroll
    for (int q = 0; q < 2; ++q) { acc[0][q] = (f4v){0.f, 0.f, 0.f, 0.f}; acc[1][q] = acc[0][q]; }
#pragma unroll
    for (int T = 0; T < 14; ++T) {
      const u4v x0 = xp[T * 128], x1 = xp[T * 128 + 16];
      acc[0][0] = mfma16(ga[T], x0, acc[0][0]); acc[0][1] = mfma16(ga[T], x1, acc[0][1]);
      acc[1][0] = mfma16(ga[14 + T], x0, acc[1][0]); acc[1][1] = mfma16(ga[14 + T], x1, acc[1][1]);
    }
    unsigned char* o = ws + O_ST + (size_t)(s * 16 + h) * 2048 + c16 * 32 + g * 8;
#pragma unroll
    for (int q = 0; q < 2; ++q)
#pragma unroll
      for (int t = 0; t < 2; ++t) {
        const u2v val = {pk2(acc[t][q][0], acc[t][q][1]), pk2(acc[t][q][2], acc[t][q][3])};
        asm volatile("global_store_dwordx2 %0, %1, off sc1" :: "v"(o + q * 1024 + t * 512), "v"(val) : "memory");
      }
    vm0();
    if (lane == 0) { const unsigned zero = 0; asm volatile("global_store_dword %0, %1, off sc1" :: "v"(flagT(ws, sel, s, h)), "v"(zero) : "memory"); }
  }
  RTS(3);
}

constexpr int W3_GROUPS = 12;
DEV void role_w3(unsigned char* __restrict__ ws, unsigned short* __restrict__ out, int grp, int C, int wave, int lane) {
  const unsigned sel = *(const unsigned*)(ws + O_SEL);
  const int g = lane >> 4, c16 = lane & 15, v = 7 * C + (wave < 7 ? wave : 6);
  const rsrc_t ru = mkrsrc(ws + O_U, 30u << 20), rf = mkrsrc(ws + O_F, 4u << 20);
  constexpr unsigned L_P = 0, L_PM = 32768, L_ST = 40960, L_SM = L_ST + 32768, L_Y = L_SM + 32768;
  for (int s = grp; s < NSEG; s += W3_GROUPS) {
    if (wave == 7) {
      wait_cnt(ws, doneU(ws, 4 * ((7 * C) >> 1) + (lane & 15)), (s >> 1) + 1);
      wait_cnt(ws, cntp(ws, C_F + (s >> 1)), 7);
      wait_flags(ws, flagT(ws, sel, s, lane & 15));
    }
    bar();
    u4v uf[16], ff[2];
    if (wave < 7) {
#pragma unroll
      for (int hh = 0; hh < 16; ++hh) uf[hh] = ldc16(ru, v * U_RUN + (s * 16 + hh) * 1024 + lane * 16);
#pragma unroll
      for (int q = 0; q < 2; ++q) ff[q] = ldc16(rf, ((s * 28 + v) * 2 + q) * 1024 + lane * 16);
    } else {
      wait_flags(ws, flagY(ws, sel, s, 7 * C + min(lane, 6)));
      wait_flags(ws, flagS(ws, sel, s, lane));
    }
    __builtin_amdgcn_sched_barrier(0);
    bar();
#pragma unroll
    for (int it = 0; it < 4; ++it) {
      dma16c(ws + O_ST + (size_t)(s * 32 + wave * 4 + it) * 1024 + lane * 16, L_ST + (wave * 4 + it) * 1024);
      dma16c(ws + O_SM + (size_t)(s * 32 + wave * 4 + it) * 1024 + lane * 16, L_SM + (wave * 4 + it) * 1024);
    }
    if (wave < 7) {
#pragma unroll
      for (int H = 0; H < 4; ++H) dma16c(ws + O_YG + (size_t)v * Y_RUN + (s * 4 + H) * 1024 + lane * 16, L_Y + (wave * 4 + H) * 1024);
      asm volatile("s_waitcnt vmcnt(4)" ::: "memory");
    } else vm0();
#pragma unroll
    for (int it = 0; it < 4; ++it) {
      const int q = it & 1, hh = 2 * wave + (it >> 1), tq = 16 * q + c16;
      const u4v st = lds_r16(L_ST + (wave * 4 + it) * 1024 + (g >> 1) * 512 + c16 * 32 + (g & 1) * 16);
      const LDS unsigned* sm = (const LDS unsigned*)(size_t)(L_SM + (wave * 4 + it) * 1024 + lane * 4);
      const unsigned s0 = sm[0], s1 = sm[64], s2 = sm[128], s3 = sm[192];
      float e[8];
      float m0 = (bflo(s0) + bflo(s1)) + (bflo(s2) + bflo(s3));
      float m1 = (bfhi(s0) + bfhi(s1)) + (bfhi(s2) + bfhi(s3));
#pragma unroll
      for (int i = 0; i < 8; ++i) { const unsigned w = st[i >> 1]; e[i] = (8 * g + i <= tq) ? ((i & 1) ? bfhi(w) : bflo(w)) : -__builtin_inff(); }
      float mx = fmaxf(m0, m1);
#pragma unroll
      for (int i = 0; i < 8; ++i) mx = fmaxf(mx, e[i]);
      mx = xmax16_32(mx);
      m0 = __builtin_amdgcn_exp2f(m0 - mx); m1 = __builtin_amdgcn_exp2f(m1 - mx);
      float sum = m0 + m1;
#pragma unroll
      for (int i = 0; i < 8; ++i) { e[i] = __builtin_amdgcn_exp2f(e[i] - mx); sum += e[i]; }
      const float r = __builtin_amdgcn_rcpf(xsum16_32(sum));
      lds_w16(L_P + (q * 16 + hh) * 1024 + lane * 16,
              (u4v){pk2(e[0] * r, e[1] * r), pk2(e[2] * r, e[3] * r), pk2(e[4] * r, e[5] * r), pk2(e[6] * r, e[7] * r)});
      *(LDS unsigned*)(size_t)(L_PM + (q * 4 + (hh >> 2)) * 1024 + lane * 16 + (hh & 3) * 4) = pk2(m0 * r, m1 * r);
    }
    vm0();
    bar();
    if (wave < 7) {
      u4v yf[4];
#pragma unroll
      for (int H = 0; H < 4; ++H) {
        const u2v lo = *(const LDS u2v*)(size_t)(L_Y + (wave * 4 + H) * 1024 + lane * 8), hi = *(const LDS u2v*)(size_t)(L_Y + (wave * 4 + H) * 1024 + 512 + lane * 8);
        yf[H] = (u4v){lo.x, lo.y, hi.x, hi.y};
      }
      f4v a0 = __builtin_bit_cast(f4v, ff[0]), a1 = __builtin_bit_cast(f4v, ff[1]);
      const LDS u4v* pp = (const LDS u4v*)(size_t)(L_P + lane * 16);
      u4v p0[4], p1[4];
#pragma unroll
      for (int k = 0; k < 3; ++k) { p0[k] = pp[k * 64]; p1[k] = pp[(16 + k) * 64]; }
#pragma unroll
      for (int k = 0; k < 20; ++k) {
        const int n = k + 3;
        if (n < 16) { p0[n & 3] = pp[n * 64]; p1[n & 3] = pp[(16 + n) * 64]; }
        else if (n < 20) { p0[n & 3] = pp[(32 + n - 16) * 64]; p1[n & 3] = pp[(36 + n - 16) * 64]; }
        const u4v w = k < 16 ? uf[k] : yf[k - 16];
        a0 = mfma16(w, p0[k & 3], a0); a1 = mfma16(w, p1[k & 3], a1);
        __builtin_amdgcn_sched_barrier(0);
      }
      *(u2v*)(out + (size_t)(s * 32 + c16) * DM + v * 16 + g * 4) = (u2v){pk2(a0[0], a0[1]), pk2(a0[2], a0[3])};
      *(u2v*)(out + (size_t)(s * 32 + 16 + c16) * DM + v * 16 + g * 4) = (u2v){pk2(a1[0], a1[1]), pk2(a1[2], a1[3])};
    }
  }
  RTS(4);
}
#define TUNE_ARG
#define TUNE_PASS
#define K_PERIOD 760
#define K_DS 1560
#define K_MS 43
#define K_MM 50
#define K_LEAD 96
#define K_FIXED 0
constexpr int K_SMK = 2, K_DDE = 250;
constexpr int C_PT = 2048;
DEV unsigned* ptp(unsigned char* ws, int s) { return cntp(ws, C_PT + 2 * s); }
DEV unsigned ptword(int period, int ds) { return (unsigned)(period / 24) | ((unsigned)(period % 24) << 8) | ((unsigned)(ds - K_DDE - 80) << 16); }
DEV unsigned char* flagR(unsigned char* ws, unsigned sel, int id) { return hblk(ws, sel, 64 + (id >> 6), 0) + (id & 63) * 128; }
#define DBG(slot, v)
#define ST(n)
#define ARR(s_)
#define EV(code)
#define EV_OUT(base)
#define ST_CLOB
#define ST_OUT(base, ref)
constexpr int RET_E = 26;
constexpr int TRY_CAP = 1 << 16;

DEV int rt32() { unsigned long long t; asm volatile("s_memrealtime %0\n\ts_waitcnt lgkmcnt(0)" : "=s"(t) :: "memory"); return (int)(unsigned)t; }
struct Lat { int anchor, e, f; };
DEV void lat_fwd(Lat& L, int pe, int pf) { L.f += pf; const int c = L.f >= 24; L.f -= c ? 24 : 0; L.e += pe + c; }
DEV void lat_back(Lat& L, int pe, int pf) { L.f -= pf; const int c = L.f < 0; L.f += c ? 24 : 0; L.e -= pe + c; }
DEV void lat_prep(const Lat& L, int& tgt, int& jmp) { tgt = L.anchor + 4 * L.e; jmp = 108 - 4 * L.f; }
#define TPAIR(r, m) "s_memrealtime s[" #r ":" #r "+1]\n\ts_memtime s[" #m ":" #m "+1]\n\ts_nop 0\n\t"
#define TSEL(r, m) "s_cmp_lg_u32 s" #r ", s4\n\ts_cselect_b32 s58, s" #m ", s58\n\t"
#define TBURST \
  TPAIR(4, 6) TPAIR(8, 10) TPAIR(12, 14) TPAIR(16, 18) TPAIR(20, 22) TPAIR(24, 26) TPAIR(28, 30) TPAIR(52, 54) TPAIR(56, 58) \
  "s_waitcnt lgkmcnt(0)\n\t" TSEL(52, 54) TSEL(28, 30) TSEL(24, 26) TSEL(20, 22) TSEL(16, 18) TSEL(12, 14) TSEL(8, 10)
#define TBURST_CLOB "s4","s5","s6","s7","s8","s9","s10","s11","s12","s13","s14","s15","s16","s17","s18","s19","s20","s21","s22","s23", \
  "s24","s25","s26","s27","s28","s29","s30","s31","s52","s53","s54","s55","s56","s57","s58","s59"
DEV void lat_commit(Lat& L, int tgt, int jmp) {
  if (tgt == (int)0x80000000) return;
  int r = tgt - rt32();
  while (__builtin_expect(r < 0, 0)) { L.e += RET_E; tgt += 4 * RET_E; r += 4 * RET_E; }
  if (r > 0) {
    int d;
#define RT_STEP "s_memrealtime vcc\n\ts_waitcnt lgkmcnt(0)\n\ts_sub_i32 %0, %1, vcc_lo\n\ts_cmp_lt_i32 %0, 1\n\t"
    asm volatile("1:\n\t" RT_STEP "s_cbranch_scc1 2f\n\t" RT_STEP "s_cbranch_scc1 2f\n\t" RT_STEP "s_cbranch_scc1 2f\n\t"
                 RT_STEP "s_cbranch_scc0 1b\n\t"
                 "2:\n\t"
                 "s_getpc_b64 vcc\n\ts_add_u32 vcc_lo, vcc_lo, %2\n\ts_addc_u32 vcc_hi, vcc_hi, 0\n\ts_setpc_b64 vcc\n\t"
                 ".rept 24\n\ts_nop 0\n\t.endr"
                 : "=&s"(d) : "s"(tgt), "s"(jmp) : "scc", "vcc", "memory");
  }
}
DEV unsigned lat_wait(Lat& L) {
  int tgt, jmp;
  unsigned est;
  lat_prep(L, tgt, jmp);
  if (tgt - rt32() < 16) { lat_commit(L, tgt, jmp); return 0; }
  asm volatile("1:\n\ts_memrealtime vcc\n\ts_waitcnt lgkmcnt(0)\n\ts_sub_i32 s4, %1, vcc_lo\n\ts_cmp_gt_i32 s4, 16\n\ts_cbranch_scc1 1b\n\t"
               TBURST
               "s_sub_i32 s4, %1, s4\n\ts_mul_i32 s4, s4, 93\n\ts_lshr_b32 s4, s4, 2\n\t"
               "s_add_i32 s58, s58, s4\n\ts_add_i32 s58, s58, %2\n\ts_or_b32 %0, s58, 1\n\t"
               "s_memtime vcc\n\ts_waitcnt lgkmcnt(0)\n\t"
               "s_sub_i32 s4, s58, vcc_lo\n\ts_max_i32 s4, s4, 0\n\ts_min_i32 s4, s4, 512\n\ts_and_b32 s4, s4, -4\n\ts_sub_i32 s4, 524, s4\n\t"
               "s_getpc_b64 vcc\n\ts_add_u32 vcc_lo, vcc_lo, s4\n\ts_addc_u32 vcc_hi, vcc_hi, 0\n\ts_setpc_b64 vcc\n\t"
               ".rept 128\n\ts_nop 0\n\t.endr"
               : "=&s"(est) : "s"(tgt), "s"(4 * L.f - 93) : TBURST_CLOB, "scc", "vcc", "memory");
  return est;
}
DEV unsigned* anchorp(unsigned char* ws, int id) { return cntp(ws, C_ANCHOR + 2 * (id & 31)); }
DEV int get_anchor(unsigned char* ws, unsigned sel, int id, int lane) {
  post((unsigned*)flagR(ws, sel, id), 0u, lane);
  unsigned v = 0;
  for (int it = 0; it < SPIN_CAP && v == 0; ++it) v = __builtin_amdgcn_readfirstlane(ld_cnt(anchorp(ws, id)));
  const int n = rt32();
  const int d = (int)((v & ~1u) - ((unsigned)n << 1)) >> 1;
  return (d > 4096 || d < -(1 << 22)) ? n : n + d;
}
DEV f4v smf(u4v a, u8v b, f4v c, int idx) {
  return __builtin_amdgcn_smfmac_f32_16x16x64_bf16(asbf8(a), __builtin_bit_cast(bf16x, b), c, idx, 0, 0);
}
DEV unsigned umax4(u4v v) { return max(max(v.x, v.y), max(v.z, v.w)); }
DEV float dpp_ror8(float v) { return __builtin_bit_cast(float, __builtin_amdgcn_update_dpp(0, __builtin_bit_cast(int, v), 0x128, 0xf, 0xf, true)); }
DEV float dpp_xor1(float v) { return __builtin_bit_cast(float, __builtin_amdgcn_update_dpp(0, __builtin_bit_cast(int, v), 0xb1, 0xf, 0xf, true)); }
DEV void st4c_asm(void* p, unsigned v) { asm volatile("global_store_dword %0, %1, off sc1" :: "v"(p), "v"(v) : "memory"); }
DEV void st8c_asm(void* p, u2v v) { asm volatile("global_store_dwordx2 %0, %1, off sc1" :: "v"(p), "v"(v) : "memory"); }
DEV void ld16c_asm(u4v& v, const void* p) { asm volatile("global_load_dwordx4 %0, %1, off sc1" : "=&v"(v) : "v"(p) : "memory"); }
DEV void rawbar() { asm volatile("s_barrier" ::: "memory"); }
DEV unsigned lds_r4(unsigned a) { return *(volatile LDS unsigned*)(size_t)a; }
DEV void lds_w4(unsigned a, unsigned v) { *(volatile LDS unsigned*)(size_t)a = v; }
DEV u8v cat8(u4v a, u4v b) { return (u8v){a.x, a.y, a.z, a.w, b.x, b.y, b.z, b.w}; }
DEV void set_abort(unsigned char* ws) { st4c_asm(cntp(ws, C_ABORT), 1u); }

constexpr unsigned L_MEM = 0, L_KT = 8192, L_FLAG = 12288, L_RED = 16384;
constexpr unsigned L_EST = 12304, L_TS = 12308, L_CG = 12544, L_G = 32768, L_XN = 65536;
#define CLOB_V200_247 "v200","v201","v202","v203","v204","v205","v206","v207","v208","v209","v210","v211","v212","v213","v214","v215", \
  "v216","v217","v218","v219","v220","v221","v222","v223","v224","v225","v226","v227","v228","v229","v230","v231", \
  "v232","v233","v234","v235","v236","v237","v238","v239","v240","v241","v242","v243","v244","v245","v246","v247","v248"
#define SMF "v_smfmac_f32_16x16x64_bf16 "
#define R1_HEAD(o0, o1, o2, o3, o4, o5, o6, FIRST) \
  "v_mov_b32 v228, 0\n\tv_mov_b32 v229, 0\n\tv_mov_b32 v230, 0\n\tv_mov_b32 v231, 0\n\t" \
  "v_mov_b32 v232, 0\n\tv_mov_b32 v233, 0\n\tv_mov_b32 v234, 0\n\tv_mov_b32 v235, 0\n\t" \
  "v_mov_b32 v244, 0\n\tv_mov_b32 v245, 0\n\tv_mov_b32 v246, 0\n\tv_mov_b32 v247, 0\n\t" \
  "s_mov_b32 m0, %[m0v]\n\t" \
  "s_barrier\n\t" ST(80) \
  "global_load_lds_dwordx4 %[moff], %[src] sc1\n\t" \
  "s_waitcnt vmcnt(0)\n\t" \
  "ds_read_b128 v[200:203], %[rd] offset:" #o0 "*1024\n\t" \
  "s_barrier\n\t" ST(82) \
  "ds_read_b128 v[204:207], %[rd] offset:" #o1 "*1024\n\tds_read_b128 v[208:211], %[rd] offset:" #o2 "*1024\n\t" \
  "ds_read_b128 v[212:215], %[rd] offset:" #o3 "*1024\n\tds_read_b128 v[216:219], %[rd] offset:" #o4 "*1024\n\t" \
  "ds_read_b128 v[220:223], %[rd] offset:" #o5 "*1024\n\tds_read_b128 v[224:227], %[rd] offset:" #o6 "*1024\n\t"
#define R1_TWO(n6, n5, n4, n3, n2, n1, n0) \
  "s_waitcnt lgkmcnt(" #n6 ")\n\t" ST(84) SMF "v[228:231], v[200:203], %[a0], %[idx]\n\t" SMF "v[232:235], v[200:203], %[b0], %[idx]\n\t" \
  "s_waitcnt lgkmcnt(" #n5 ")\n\t" SMF "v[228:231], v[204:207], %[a1], %[idx]\n\t" SMF "v[232:235], v[204:207], %[b1], %[idx]\n\t" \
  "s_waitcnt lgkmcnt(" #n4 ")\n\t" SMF "v[228:231], v[208:211], %[a2], %[idx]\n\t" SMF "v[232:235], v[208:211], %[b2], %[idx]\n\t" \
  "s_waitcnt lgkmcnt(" #n3 ")\n\t" SMF "v[228:231], v[212:215], %[a3], %[idx]\n\t" SMF "v[232:235], v[212:215], %[b3], %[idx]\n\t" \
  "s_waitcnt lgkmcnt(" #n2 ")\n\t" SMF "v[228:231], v[216:219], %[a4], %[idx]\n\t" SMF "v[232:235], v[216:219], %[b4], %[idx]\n\t" \
  "s_waitcnt lgkmcnt(" #n1 ")\n\t" SMF "v[228:231], v[220:223], %[a5], %[idx]\n\t" \
  "s_waitcnt lgkmcnt(" #n0 ")\n\t" SMF "v[228:231], v[224:227], %[a6], %[idx]\n\t" \
  SMF "v[232:235], v[220:223], %[b5], %[idx]\n\t" SMF "v[232:235], v[224:227], %[b6], %[idx]\n\t" \
  ST(86) "s_nop 1\n\t" \
  "v_cvt_pk_bf16_f32 v228, v228, v229\n\tv_cvt_pk_bf16_f32 v229, v230, v231\n\t" \
  "ds_write_b64 %[ktw], v[228:229]\n\t" \
  "s_nop 2\n\t" \
  "v_cvt_pk_bf16_f32 v232, v232, v233\n\tv_cvt_pk_bf16_f32 v233, v234, v235\n\t" \
  "ds_write_b64 %[ktw], v[232:233] offset:512\n\t" \
  LDSWAIT ST(88) \
  "s_barrier\n\t" ST(90)
#define LDSWAIT
#define R1_WOPS [a0] "v"(w0[0]), [a1] "v"(w0[1]), [a2] "v"(w0[2]), [a3] "v"(w0[3]), [a4] "v"(w0[4]), [a5] "v"(w0[5]), [a6] "v"(w0[6])
#define R1_WOPS2 [b0] "v"(w1[0]), [b1] "v"(w1[1]), [b2] "v"(w1[2]), [b3] "v"(w1[3]), [b4] "v"(w1[4]), [b5] "v"(w1[5]), [b6] "v"(w1[6])
#define R1_COMMON [idx] "v"(idx), [rd] "v"(rd), [ktw] "v"(ktw), [moff] "v"(moff), [src] "s"(src), [m0v] "s"(m0v)

template <int W>
DEV void r1_mfma(unsigned char* __restrict__ ws, int h, int q, int lane, unsigned sel, int smk) {
  constexpr int wave = W, KIND = W == 0 ? 0 : W == 3 ? 2 : 1;
  const int g = lane >> 4, c16 = lane & 15;
  const int idx = (lane & 8) ? 0xEEEE : 0x4444;
  const unsigned moff = wave * 1024 + g * 256 + (lane & 7) * 32 + ((lane >> 3) & 1) * 16;
  u8v w0[7], w1[7];
  {
    const unsigned char* p = ws + O_WA + (size_t)(h * 28 + 7 * q + 2 * wave) * 14336 + lane * 16;
#pragma unroll
    for (int k = 0; k < 7; ++k) {
      const int o = ((k + W) % 7) * 2048;
      w0[k] = cat8(*(const u4v*)(p + o), *(const u4v*)(p + o + 1024));
      if (KIND != 2) w1[k] = cat8(*(const u4v*)(p + 14336 + o), *(const u4v*)(p + 14336 + o + 1024));
    }
#pragma unroll
    for (int k = 0; k < 7; ++k) { asm volatile("" : "+v"(w0[k])); if (KIND != 2) asm volatile("" : "+v"(w1[k])); }
  }
  RTS(1);
  const unsigned rd = L_MEM + lane * 16, ktw = L_KT + 2 * wave * 512 + c16 * 32 + g * 8, m0v = L_MEM + wave * 1024;
  const unsigned a0a = L_MEM + (7 * q + g) * 256 + c16 * 16, a1a = L_MEM + min(7 * q + 4 + g, 27) * 256 + c16 * 16;
  const unsigned trb = L_KT + (g >> 1) * 512 + (4 * (g & 1) + (c16 >> 2)) * 32 + (c16 & 3) * 8;
  const unsigned sto = h * 512 + q * 128 + (4 * (g & 1) + 2 * (g >> 1) + (c16 >> 3)) * 16 + (c16 & 7) * 2;
  const unsigned long long lo8 = 0x00FF00FF00FF00FFull;
  unsigned ep = 0;
  int budget = TRY_CAP;
  rawbar();
  rawbar();
  for (int s = 0; s < NSEG; ++s) {
    const unsigned char* src = hblk(ws, sel, s, 0);
    unsigned char* dst = hblk(ws, sel, 64 + s, smk);
    for (;;) {
      if (KIND == 0) {
        unsigned long long okm, tst;
        asm volatile(R1_HEAD(0, 1, 2, 3, 4, 5, 6, )
          "ds_read_b128 v[236:239], %[a0a]\n\tds_read_b128 v[240:243], %[a1a]\n\t"
          R1_TWO(8, 7, 6, 5, 4, 3, 2)
          "ds_read_b64_tr_b16 v[200:201], %[trb]\n\tds_read_b64_tr_b16 v[202:203], %[trb] offset:256\n\t"
          "ds_read_b64_tr_b16 v[204:205], %[trb] offset:1024\n\tds_read_b64_tr_b16 v[206:207], %[trb] offset:1280\n\t"
          "ds_read_b64_tr_b16 v[208:209], %[trb] offset:2048\n\tds_read_b64_tr_b16 v[210:211], %[trb] offset:2304\n\t"
          "ds_read_b64_tr_b16 v[212:213], %[trb] offset:3072\n\tds_read_b64_tr_b16 v[214:215], %[trb] offset:3328\n\t"
          "s_waitcnt lgkmcnt(4)\n\t" SMF "v[244:247], v[236:239], v[200:207], %[idx]\n\t"
          "s_waitcnt lgkmcnt(0)\n\t" SMF "v[244:247], v[240:243], v[208:215], %[idx]\n\t"
          "s_nop 7\n\t"
          "v_permlane32_swap_b32 v244, v246\n\tv_permlane32_swap_b32 v245, v247\n\t"
          "v_add_f32 v244, v244, v246\n\tv_add_f32 v245, v245, v247\n\t"
          "v_cndmask_b32 v246, v244, v245, %[lo8]\n\t"
          "v_cndmask_b32 v247, v245, v244, %[lo8]\n\t"
          "s_nop 1\n\t"
          "v_add_f32_dpp v247, v246, v247 row_ror:8 row_mask:0xf bank_mask:0xf\n\t"
          "v_cvt_pk_bf16_f32 v246, v247, v247\n\t"
          "v_cmpx_o_f32 vcc, v247, v247\n\t"
          "global_store_short %[sto], v246, %[dst] sc1\n\t" ST(92)
          "s_memtime %[tst]\n\t"
          "s_mov_b64 exec, -1\n\t"
          "s_mov_b64 %[okm], vcc\n\t"
          "s_waitcnt lgkmcnt(0)\n\t"
          : [okm] "=&s"(okm), [tst] "=&s"(tst)
          : R1_WOPS, R1_WOPS2, R1_COMMON, [a0a] "v"(a0a), [a1a] "v"(a1a), [trb] "v"(trb), [sto] "v"(sto), [dst] "s"(dst), [lo8] "s"(lo8)
          : CLOB_V200_247, "vcc", "memory" ST_CLOB);
        lds_w4(L_TS, (unsigned)tst);
        if (okm != ~0ull) {
          if (budget > 0) { if (lane == 0) lds_w4(L_FLAG, ep); }
          else asm volatile("global_store_short %0, %1, %2 sc1" :: "v"(sto), "v"(0x7FC0u), "s"(dst) : "memory");
        }
      } else if (KIND == 1) {
        if (W == 1) asm volatile(R1_HEAD(1, 2, 3, 4, 5, 6, 0, ) R1_TWO(6, 5, 4, 3, 2, 1, 0) :: R1_WOPS, R1_WOPS2, R1_COMMON : CLOB_V200_247, "memory" ST_CLOB);
        else asm volatile(R1_HEAD(2, 3, 4, 5, 6, 0, 1, ) R1_TWO(6, 5, 4, 3, 2, 1, 0) :: R1_WOPS, R1_WOPS2, R1_COMMON : CLOB_V200_247, "memory" ST_CLOB);
      } else {
        asm volatile(R1_HEAD(3, 4, 5, 6, 0, 1, 2, )
          "s_waitcnt lgkmcnt(6)\n\t" SMF "v[228:231], v[200:203], %[a0], %[idx]\n\t"
          "s_waitcnt lgkmcnt(5)\n\t" SMF "v[228:231], v[204:207], %[a1], %[idx]\n\t"
          "s_waitcnt lgkmcnt(4)\n\t" SMF "v[228:231], v[208:211], %[a2], %[idx]\n\t"
          "s_waitcnt lgkmcnt(3)\n\t" SMF "v[228:231], v[212:215], %[a3], %[idx]\n\t"
          "s_waitcnt lgkmcnt(2)\n\t" SMF "v[228:231], v[216:219], %[a4], %[idx]\n\t"
          "s_waitcnt lgkmcnt(1)\n\t" SMF "v[228:231], v[220:223], %[a5], %[idx]\n\t"
          "s_waitcnt lgkmcnt(0)\n\t" SMF "v[228:231], v[224:227], %[a6], %[idx]\n\t"
          "s_nop 7\n\t"
          "v_cvt_pk_bf16_f32 v228, v228, v229\n\tv_cvt_pk_bf16_f32 v229, v230, v231\n\t"
          "ds_write_b64 %[ktw], v[228:229]\n\t"
          LDSWAIT
          "s_barrier\n\t"
          :: R1_WOPS, R1_COMMON : CLOB_V200_247, "memory" ST_CLOB);
      }
      bar();
      const bool bad = __builtin_amdgcn_readfirstlane(lds_r4(L_FLAG)) == ep;
      ++ep;
      if (!bad) break;
      --budget;
      rawbar();
    }
    ARR(s);
    rawbar();
  }
}

typedef unsigned short h2v __attribute__((ext_vector_type(2)));
DEV unsigned pkmax(unsigned a, unsigned b) { return __builtin_bit_cast(unsigned, __builtin_elementwise_max(__builtin_bit_cast(h2v, a), __builtin_bit_cast(h2v, b))); }
DEV unsigned wavepkmax(unsigned v) {
  v = pkmax(v, __builtin_amdgcn_update_dpp(0u, v, 0xb1, 0xf, 0xf, true));
  v = pkmax(v, __builtin_amdgcn_update_dpp(0u, v, 0x4e, 0xf, 0xf, true));
  v = pkmax(v, __builtin_amdgcn_update_dpp(0u, v, 0x124, 0xf, 0xf, true));
  v = pkmax(v, __builtin_amdgcn_update_dpp(0u, v, 0x128, 0xf, 0xf, true));
  const u2v x = __builtin_amdgcn_permlane16_swap(v, v, false, false);
  v = pkmax(x.x, x.y);
  const u2v y = __builtin_amdgcn_permlane32_swap(v, v, false, false);
  return pkmax(y.x, y.y);
}
DEV void r1_boss(unsigned char* __restrict__ ws, int lane, unsigned sel TUNE_ARG) {
  constexpr unsigned L_FA = 13312, L_FY = 13568;
  int A = 0, V = 0, ms = 4 * K_MS, mm = 4 * K_MM;
  float rho = 0.f;
  bool prevf3 = false;
  int run = 0;
  unsigned ep = 0;
  const unsigned c0 = rdtime();
  const int r0 = rt32();
  rawbar();
  const int P0 = (int)((float)K_PERIOD * (0.3f + 0.7f * fmaxf(23.4f * (float)(rt32() - r0) * __builtin_amdgcn_rcpf((float)(rdtime() - c0)), 1.f)));
  int hist = P0;
  if (lane < 5) st4c_asm(ptp(ws, lane), ptword(P0, K_DS));
  for (int it = 0; it < SPIN_CAP; ++it) {
    unsigned x, y;
    asm volatile("global_load_dword %0, %2, off sc1\n\tglobal_load_dword %1, %3, off sc1\n\ts_waitcnt vmcnt(0)"
                 : "=&v"(x), "=&v"(y) : "v"(flagR(ws, sel, lane)), "v"(flagR(ws, sel, 64 + min(lane, 27))) : "memory");
    if (!__any(max(x, y) == SENT)) break;
  }
  if (lane < 32) st4c_asm(anchorp(ws, lane), ((unsigned)(rt32() + K_LEAD) << 1) | 1u);
  for (int t = 0; t < NSEG; ++t) {
    const int s = max(t - 3, 0);
    const float pa = 4.f * (float)__builtin_amdgcn_readlane(hist, max(s - 1, 0)), py = 4.f * (float)__builtin_amdgcn_readlane(hist, s);
    rawbar(); rawbar();
    if (t >= 3) vm0();
    const unsigned a = lds_r4(L_FA + lane * 4), y = lds_r4(L_FY + lane * 4);
    const float da = (float)((a >> 12) & 0xffffu), dy = (float)((y >> 12) & 0xffffu);
    unsigned pk = min((unsigned)((float)(a & 0xfffu) * pa * __builtin_amdgcn_rcpf(da)), 65535u) |
                  (min((unsigned)((float)(y & 0xfffu) * py * __builtin_amdgcn_rcpf(dy)), 65535u) << 16);
    unsigned fl = (max(a, y) == SENT ? 1u : 0u) | ((a >> 28) != 0 ? 2u : 0u) | ((y >> 26) & 4u) |
                  (fabsf(da - 0.86f * pa) > 0.18f * pa ? 8u : 0u) | (fabsf(dy - 0.86f * py) > 0.18f * py ? 16u : 0u) | ((y >> 25) & 16u);
    float rn = dy * __builtin_amdgcn_rcpf(py);
    asm volatile("s_barrier" : "+v"(pk), "+v"(fl), "+v"(rn) :: "memory");
    pk = wavepkmax(pk);
    asm volatile("s_barrier\n\ts_barrier" : "+v"(pk) :: "memory");
    while (__builtin_amdgcn_readfirstlane(lds_r4(L_FLAG)) == ep++) { rawbar(); rawbar(); rawbar(); rawbar(); rawbar(); }
    if (t >= 3 && t + 2 < NSEG) {
      if (!__any(fl & 1)) {
        const unsigned m = __builtin_amdgcn_readfirstlane(pk);
        const bool f1 = __any(fl & 2), f3 = __any(fl & 4);
        if (!__any(fl & 10)) A = A == 0 ? (int)(m & 0xffffu) : A + (((int)(m & 0xffffu) - A) >> 2);
        if (!__any(fl & 20)) { V = V == 0 ? (int)(m >> 16) : V + (((int)(m >> 16) - V) >> 2); rho = rho == 0.f ? rn : rho + 0.25f * (rn - rho); }
        run = (f1 || f3) ? run + 1 : 0;
        if ((f1 && !prevf3) || run > 3) mm = min(mm + 20, 4 * K_MM + 800);
        if ((f3 && !f1) || run > 3) ms = min(ms + 20, 4 * K_MS + 800);
        prevf3 = f3;
      }
      int P = P0 + ((ms + mm) >> 2) - K_MS - K_MM, DS = K_DS + ms - 4 * K_MS;
      if (A > 0 && V > 0 && !K_FIXED) {
        DS = min(max((int)((float)(A + ms) * rho), 1200), 3000);
        P = min(max((A + ms + V - 84 + mm) >> 2, 600), 1500);
      }
      if (lane == t + 2) hist = P;
      post(ptp(ws, t + 2), ptword(P, DS), lane);
    }
    if (t >= 2) {
      asm volatile("s_mov_b32 m0, %2\n\ts_nop 0\n\tglobal_load_lds_dword %0, off sc1\n\ts_mov_b32 m0, %3\n\ts_nop 0\n\tglobal_load_lds_dword %1, off sc1"
                   :: "v"(flagS(ws, sel, t - 2, lane)), "v"(flagY(ws, sel, t - 2, min(lane, 27))), "s"(L_FA), "s"(L_FY) : "memory");
    }
  }
  vm0();
  rawbar();
  if (lane == 0) { unsigned* o = (unsigned*)(ws + 126 * MB); o[0] = A; o[1] = V; o[2] = ms; o[3] = mm; o[4] = ep; o[5] = P0; }
}

DEV void role_r1(unsigned char* __restrict__ ws, int id, int wave, int lane, unsigned sel TUNE_ARG) {
  const int h = id >> 2, q = id & 3;
  if (wave == 0) { r1_mfma<0>(ws, h, q, lane, sel, K_SMK); return; }
  if (wave == 1) { r1_mfma<1>(ws, h, q, lane, sel, K_SMK); return; }
  if (wave == 2) { r1_mfma<2>(ws, h, q, lane, sel, K_SMK); return; }
  if (wave == 3) { r1_mfma<3>(ws, h, q, lane, sel, K_SMK); return; }
  if (wave == 7 && id == 3) { r1_boss(ws, lane, sel TUNE_PASS); return; }
  const int g = lane >> 4, c16 = lane & 15;
  const bool act = q < 2;
  const int fr = wave - 1;
  const unsigned moff = fr * 1024 + g * 256 + (lane & 7) * 32 + ((lane >> 3) & 1) * 16, m0v = L_MEM + fr * 1024;
  const unsigned xo = L_XN + (g >> 1) * 1024 + (g & 1) * 512 + c16 * 16;
  unsigned ep = 0;
  if (wave == 7) {
    const int idx = (lane & 8) ? 0xEEEE : 0x4444;
    u8v w0[7];
    const unsigned rd = lane * 16;
    const unsigned sto = h * 512 + (q & 1) * 256 + (4 * (g & 1) + 2 * (g >> 1) + (lane & 1)) * 32 + (c16 >> 1) * 4;
    const unsigned long long odd = 0xAAAAAAAAAAAAAAAAull;
    __builtin_amdgcn_s_setprio(3);
    rawbar();
    for (int s = 0; s < NSEG; ++s) {
      unsigned char* dst = hblk(ws, sel, s, 2);
      unsigned char* raw = hblk(ws, sel, s, 1);
      const bool go = act && s < NSEG - 1;
      if (go) {
#pragma unroll
        for (int k = 0; k < 7; ++k) w0[k] = cat8(lds_r16(L_G + (s & 1) * 14336 + rd + k * 2048), lds_r16(L_G + (s & 1) * 14336 + rd + k * 2048 + 1024));
      }
      for (;;) {
        if (go) {
          asm volatile(
            "v_mov_b32 v228, 0\n\tv_mov_b32 v229, 0\n\tv_mov_b32 v230, 0\n\tv_mov_b32 v231, 0\n\t"
            "s_barrier\n\ts_barrier\n\ts_barrier\n\t" ST(80)
            "ds_read_b128 v[200:203], %[rd]\n\tds_read_b128 v[204:207], %[rd] offset:1024\n\tds_read_b128 v[208:211], %[rd] offset:2048\n\t"
            "ds_read_b128 v[212:215], %[rd] offset:3072\n\tds_read_b128 v[216:219], %[rd] offset:4096\n\tds_read_b128 v[220:223], %[rd] offset:5120\n\t"
            "ds_read_b128 v[224:227], %[rd] offset:6144\n\t"
            "s_waitcnt lgkmcnt(6)\n\t" SMF "v[228:231], v[200:203], %[a0], %[idx]\n\t"
            "s_waitcnt lgkmcnt(5)\n\t" SMF "v[228:231], v[204:207], %[a1], %[idx]\n\t"
            "s_waitcnt lgkmcnt(4)\n\t" SMF "v[228:231], v[208:211], %[a2], %[idx]\n\t"
            "s_waitcnt lgkmcnt(3)\n\t" SMF "v[228:231], v[212:215], %[a3], %[idx]\n\t"
            "s_waitcnt lgkmcnt(2)\n\t" SMF "v[228:231], v[216:219], %[a4], %[idx]\n\t"
            "s_waitcnt lgkmcnt(1)\n\t" SMF "v[228:231], v[220:223], %[a5], %[idx]\n\t"
            "s_waitcnt lgkmcnt(0)\n\t" SMF "v[228:231], v[224:227], %[a6], %[idx]\n\t"
            "s_nop 7\n\t"
            "v_permlane32_swap_b32 v228, v230\n\tv_permlane32_swap_b32 v229, v231\n\t"
            "v_add_f32 v228, v228, v230\n\tv_add_f32 v229, v229, v231\n\t"
            "v_cndmask_b32 v230, v229, v228, %[odd]\n\t"
            "s_nop 1\n\t"
            "v_mov_b32_dpp v230, v230 quad_perm:[1,0,3,2] row_mask:0xf bank_mask:0xf\n\t"
            "v_cndmask_b32 v228, v228, v230, %[odd]\n\tv_cndmask_b32 v229, v230, v229, %[odd]\n\t"
            "v_exp_f32 v230, v228\n\tv_exp_f32 v231, v229\n\t"
            "v_cvt_pk_bf16_f32 v232, v228, v229\n\t"
            "v_cvt_pk_bf16_f32 v233, v230, v231\n\t"
            "v_cmpx_o_f32 vcc, v228, v229\n\t"
            "global_store_dword %[sto], v233, %[dst] sc1\n\t" ST(82)
            "global_store_dword %[sto], v232, %[raw] sc1\n\t"
            "s_mov_b64 exec, -1\n\t"
            "s_barrier\n\t"
            :: R1_WOPS, [idx] "v"(idx), [rd] "v"(rd), [sto] "v"(sto), [dst] "s"(dst), [raw] "s"(raw), [odd] "s"(odd)
            : CLOB_V200_247, "vcc", "memory" ST_CLOB);
          rawbar();
        } else {
          rawbar(); rawbar(); rawbar(); rawbar(); rawbar();
        }
        const bool bad = __builtin_amdgcn_readfirstlane(lds_r4(L_FLAG)) == ep;
        ++ep;
        if (!bad) break;
      }
    }
    rawbar();
  } else if (wave == 4) {
    const unsigned trb = L_KT + (g >> 1) * 512 + (4 * (g & 1) + (c16 >> 2)) * 32 + (c16 & 3) * 8;
    unsigned char* so = ws + O_SM + (size_t)h * 2048 + q * 256 + lane * 4;
    const unsigned x3 = xo, xlast = g < 2 ? 6144 : 5120;
#pragma unroll
    for (int u = 0; u < 8; ++u) *(LDS u2v*)(size_t)(L_KT + u * 512 + lane * 8) = u < 7 ? (u2v){SENT, SENT} : (u2v){0u, 0u};
    if (lane == 0) lds_w4(L_FLAG, SENT);
    rawbar();
    (void)ld_cnt((const unsigned*)(lane < 32 ? so : lane < 48 ? so + 63 * 32768 : hblk(ws, sel, 70, 0)));
    unsigned pay = 0, eprev = 0;
    for (int s = 0; s < NSEG; ++s) {
      bool sig = s > 0;
      const unsigned ep0 = ep;
      for (;;) {
        bar(); rawbar(); rawbar();
        if (sig) { vm0(); if (lane == 0) st4c_asm(flagS(ws, sel, s - 1, id), pay); sig = false; }
        rawbar(); rawbar();
        const bool bad = __builtin_amdgcn_readfirstlane(lds_r4(L_FLAG)) == ep;
        ++ep;
        if (!bad) break;
      }
      {
        const unsigned est = lds_r4(L_EST), tst = lds_r4(L_TS);
        pay = (est ? min(tst - est, 4095u) : 0u) | ((est && eprev) ? min(est - eprev, 65535u) << 12 : 0u);
        pay |= min(ep - ep0 - 1, 7u) << 28;
        eprev = est;
      }
      f4v c0 = {0.f, 0.f, 0.f, 0.f}, c1 = c0;
      u4v ka[4], xa[4], xb[4];
#pragma unroll
      for (int u = 0; u < 4; ++u) {
        const u2v lo = lds_tr(trb + u * 1024), hi = lds_tr(trb + u * 1024 + 256);
        ka[u] = (u4v){lo.x, lo.y, hi.x, hi.y};
        const unsigned ad = x3 + (s & 1) * 8192 + (u < 3 ? u * 2048 : xlast);
        xa[u] = lds_r16(ad); xb[u] = lds_r16(ad + 256);
      }
      __builtin_amdgcn_sched_barrier(0);
#pragma unroll
      for (int u = 0; u < 4; ++u) { c0 = mfma16(ka[u], xa[u], c0); c1 = mfma16(ka[u], xb[u], c1); }
#pragma unroll
      for (int u = 0; u < 7; ++u) *(LDS u2v*)(size_t)(L_KT + u * 512 + lane * 8) = (u2v){SENT, SENT};
      st4c_asm(so + (size_t)s * 32768, pk2(halfsum(c0[0], c0[1]), halfsum(c0[2], c0[3])));
      st4c_asm(so + (size_t)s * 32768 + 1024, pk2(halfsum(c1[0], c1[1]), halfsum(c1[2], c1[3])));
      ARR(s);
    }
    vm0();
    rawbar();
    if (lane == 0) st4c_asm(flagS(ws, sel, NSEG - 1, id), pay);
  } else if (wave == 5) {
    const unsigned char* gp = ws + O_G + (size_t)(h * 2 + (q & 1)) * G_RUN;
    const unsigned v0 = lane * 16, v1 = v0 + 4096, v2 = v0 + 8192, v3 = v0 + 12288;
    auto fetch = [&](int t) {
#define GL4(v) "global_load_lds_dwordx4 " v ", %4 sc1\n\t" "global_load_lds_dwordx4 " v ", %4 offset:1024 sc1\n\t"
#define GL8(v) GL4(v) "global_load_lds_dwordx4 " v ", %4 offset:2048 sc1\n\t" "global_load_lds_dwordx4 " v ", %4 offset:3072 sc1\n\t"
      asm volatile("s_mov_b32 m0, %5\n\ts_nop 0\n\t" GL8("%0") "s_add_u32 m0, m0, 4096\n\ts_nop 0\n\t" GL8("%1")
                   "s_add_u32 m0, m0, 4096\n\ts_nop 0\n\t" GL8("%2") "s_add_u32 m0, m0, 4096\n\ts_nop 0\n\t" GL4("%3")
                   :: "v"(v0), "v"(v1), "v"(v2), "v"(v3), "s"(gp + t * 14336), "s"(L_G + (t & 1) * 14336) : "scc", "memory");
    };
    auto fetchx = [&](int t) {
      asm volatile("s_mov_b32 m0, %3\n\ts_nop 0\n\t"
                   "global_load_lds_dwordx4 %0, %2\n\tglobal_load_lds_dwordx4 %0, %2 offset:1024\n\tglobal_load_lds_dwordx4 %0, %2 offset:2048\n\t"
                   "global_load_lds_dwordx4 %0, %2 offset:3072\n\ts_add_u32 m0, m0, 4096\n\ts_nop 0\n\t"
                   "global_load_lds_dwordx4 %1, %2\n\tglobal_load_lds_dwordx4 %1, %2 offset:1024\n\tglobal_load_lds_dwordx4 %1, %2 offset:2048"
                   :: "v"(v0), "v"(v1), "s"(ws + O_XN + (size_t)t * 28672 + q * 7168), "s"(L_XN + (t & 1) * 8192) : "scc", "memory");
    };
    const unsigned* gdone = doneG(ws, 7 * (h >> 1) + min(lane, 6));
    auto peek = [&](int t) {
      unsigned keep;
      asm volatile("s_mov_b32 %0, m0\n\ts_mov_b32 m0, %2\n\ts_nop 0\n\tglobal_load_lds_dword %1, off sc1\n\ts_mov_b32 m0, %0"
                   : "=&s"(keep) : "v"(lane == 63 ? ptp(ws, t) : gdone), "s"(L_CG) : "memory");
    };
    if (act) {
      wait_cnt(ws, gdone, 1);
      fetch(0); fetch(1);
    }
    fetchx(0);
    vm0();
    RTS(1);
    rawbar();
    for (int s = 0; s < NSEG; ++s) {
      const unsigned char* src = hblk(ws, sel, s, 0) + moff;
      bool todo = act && s + 2 < NSEG - 1, todox = s + 1 < NSEG;
      for (;;) {
        rawbar();
        if (s == 0) peek(0);
        rawbar();
        dma16c(src, m0v);
        vm0();
        rawbar();
        const bool there = __all(lds_r4(L_CG + (lane & 7) * 4) >= (unsigned)(s >> 1) + 2);
        rawbar();
        rawbar();
        if (todo && there) { fetch(s + 2); todo = false; }
        if (todox) { fetchx(s + 1); peek(s + 1); todox = false; }
        const bool bad = __builtin_amdgcn_readfirstlane(lds_r4(L_FLAG)) == ep;
        ++ep;
        if (!bad) break;
      }
      if (todo) {
        wait_cnt(ws, gdone, (s >> 1) + 2);
        fetch(s + 2);
      }
      ARR(s);
    }
    vm0();
    bar();
  } else {
      { Lat W = {rt32(), 6, 0}; (void)lat_wait(W); }
    rawbar();
    Lat L = {get_anchor(ws, sel, id, lane), 0, 0};
    for (int s = 0; s < NSEG; ++s) {
      const unsigned char* src = hblk(ws, sel, s, 0) + moff;
      unsigned pw = 0;
      for (;;) {
        bar();
        const unsigned est = lat_wait(L);
        rawbar();
        asm volatile("s_mov_b32 m0, %1\n\ts_nop 0\n\tglobal_load_lds_dwordx4 %0, off sc1\n\tglobal_load_lds_dwordx4 %0, off offset:1024 sc1\n\ts_waitcnt vmcnt(0)"
                     :: "v"(src), "s"(m0v) : "memory");
        rawbar();
        lds_w4(L_EST, est);
        if (pw == 0) pw = __builtin_amdgcn_readfirstlane(lds_r4(L_CG + 252)) | 0x80000000u;
        bar(); rawbar();
        const bool bad = __builtin_amdgcn_readfirstlane(lds_r4(L_FLAG)) == ep;
        ++ep;
        EV(bad ? 1 : 0);
        if (!bad) break;
        L.e += RET_E;
      }
      if (pw == 0x80000000u && s < NSEG - 1) { wait_cnt(ws, ptp(ws, s), 1); pw = __builtin_amdgcn_readfirstlane(ld_cnt(ptp(ws, s))); }
      lat_fwd(L, pw & 255, (pw >> 8) & 255);
      ARR(s);
    }
    bar();
    EV_OUT(id);
  }
}

#define XRED(op, v, t) \
  "v_mov_b32 " t ", " v "\n\ts_nop 1\n\tv_permlane16_swap_b32 " v ", " t "\n\t" op " " v ", " v ", " t "\n\t" \
  "v_mov_b32 " t ", " v "\n\ts_nop 1\n\tv_permlane32_swap_b32 " v ", " t "\n\t" op " " v ", " v ", " t "\n\t"
#define MF16 "v_mfma_f32_16x16x32_bf16 "
#define WAIT_CYC(T) \
  "s_memtime vcc\n\ts_waitcnt lgkmcnt(0)\n\t" \
  "s_sub_i32 s70, " T ", vcc_lo\n\ts_max_i32 s70, s70, 0\n\ts_mul_i32 s70, s70, 13\n\ts_lshr_b32 s70, s70, 6\n\t" \
  "s_min_i32 s70, s70, 128\n\ts_lshl_b32 s70, s70, 2\n\ts_sub_i32 s70, 524, s70\n\t" \
  "s_getpc_b64 vcc\n\ts_add_u32 vcc_lo, vcc_lo, s70\n\ts_addc_u32 vcc_hi, vcc_hi, 0\n\ts_setpc_b64 vcc\n\t" \
  ".rept 128\n\ts_nop 0\n\t.endr\n\t"
#define R3_Y_A \
  "ds_read_b128 v[200:203], %[rd]\n\tds_read_b128 v[204:207], %[rd] offset:1024\n\tds_read_b128 v[208:211], %[rd] offset:2048\n\t" \
  "ds_read_b128 v[212:215], %[rd] offset:3072\n\tds_read_b128 v[216:219], %[rd] offset:4096\n\tds_read_b128 v[220:223], %[rd] offset:5120\n\t" \
  "ds_read_b128 v[224:227], %[rd] offset:6144\n\t" \
  "s_waitcnt lgkmcnt(6)\n\t" SMF "v[228:231], v[200:203], %[a0], %[idx]\n\t" SMF "v[232:235], v[200:203], %[b0], %[idx]\n\t" \
  "s_waitcnt lgkmcnt(5)\n\t" SMF "v[228:231], v[204:207], %[a1], %[idx]\n\t" SMF "v[232:235], v[204:207], %[b1], %[idx]\n\t" \
  "s_waitcnt lgkmcnt(4)\n\t" SMF "v[228:231], v[208:211], %[a2], %[idx]\n\t" SMF "v[232:235], v[208:211], %[b2], %[idx]\n\t" \
  "s_waitcnt lgkmcnt(3)\n\t" SMF "v[228:231], v[212:215], %[a3], %[idx]\n\t" SMF "v[232:235], v[212:215], %[b3], %[idx]\n\t" \
    \
  "s_waitcnt lgkmcnt(0)\n\t" \
  "v_readfirstlane_b32 s68, v244\n\t" \
  "v_max3_u32 v244, v216, v217, v218\n\tv_max3_u32 v245, v219, v220, v221\n\tv_max3_u32 v244, v244, v222, v223\n\t" \
  "v_max3_u32 v245, v245, v224, v225\n\tv_max3_u32 v244, v244, v226, v227\n\tv_max_u32 v244, v244, v245\n\t" \
  "v_cmp_eq_u32 s[74:75], -1, v244\n\t" \
  "v_cmp_u_f32 vcc, v228, v229\n\t" \
  "s_or_b64 s[74:75], s[74:75], vcc\n\t" \
  "v_cmp_u_f32 vcc, v234, v235\n\t" \
  "s_or_b64 vcc, s[74:75], vcc\n\t"
#define R3_Y_B \
  SMF "v[228:231], v[216:219], %[a4], %[idx]\n\t" SMF "v[232:235], v[216:219], %[b4], %[idx]\n\t" \
  SMF "v[228:231], v[220:223], %[a5], %[idx]\n\t" SMF "v[228:231], v[224:227], %[a6], %[idx]\n\t" \
  SMF "v[232:235], v[220:223], %[b5], %[idx]\n\t" SMF "v[232:235], v[224:227], %[b6], %[idx]\n\t" \
  "s_nop 1\n\t" \
  "v_permlane32_swap_b32 v228, v229\n\tv_permlane32_swap_b32 v230, v231\n\t" \
  "v_add_f32 v228, v228, v229\n\tv_add_f32 v230, v230, v231\n\t" \
  "v_cvt_pk_bf16_f32 v164, v228, v230\n\t" \
  "s_nop 0\n\t" \
  "v_permlane32_swap_b32 v232, v233\n\tv_permlane32_swap_b32 v234, v235\n\t" \
  "v_add_f32 v232, v232, v233\n\tv_add_f32 v234, v234, v235\n\t" \
  "v_cvt_pk_bf16_f32 v165, v232, v234\n\t" \
  ST(82)
#define R3_POLL \
  WAIT_CYC("s68") \
  "global_load_dwordx4 v[236:239], %[to], s[60:61] sc1\n\t" \
  WAIT_CYC("s69") \
  "global_load_dwordx4 v[240:243], %[tm], s[60:61] sc1\n\t" ST(86)
#define R3_POLL2   \
  "55:\n\ts_memtime vcc\n\ts_waitcnt lgkmcnt(0)\n\ts_sub_i32 s70, s69, vcc_lo\n\ts_cmp_gt_i32 s70, 0\n\ts_cbranch_scc1 55b\n\t" \
  "global_load_dwordx4 v[236:239], %[to], s[60:61] sc1\n\tglobal_load_dwordx4 v[240:243], %[tm], s[60:61] sc1\n\t"
#define R3_S_PHASE(SIG, NS) \
  "s_waitcnt vmcnt(1)\n\t" ST(84) \
  "v_cndmask_b32 v208, 0, v236, s[76:77]\n\tv_cndmask_b32 v209, 0, v237, s[76:77]\n\tv_cndmask_b32 v210, 0, v238, s[76:77]\n\tv_cndmask_b32 v211, 0, v239, s[76:77]\n\t" \
  MF16 "v[220:223], v[160:163], v[236:239], 0\n\t" \
  "v_cndmask_b32 v212, v236, 0, s[76:77]\n\tv_cndmask_b32 v213, v237, 0, s[76:77]\n\tv_cndmask_b32 v214, v238, 0, s[76:77]\n\tv_cndmask_b32 v215, v239, 0, s[76:77]\n\t" \
  MF16 "v[216:219], v[192:195], v[208:211], 0\n\t" \
  "s_nop 0\n\t" \
  MF16 "v[216:219], v[196:199], v[212:215], v[216:219]\n\t" \
  "global_store_dwordx2 %[yo], v[164:165], s[60:61] sc1\n\t"        \
  SIG "s_waitcnt vmcnt(" #NS ")\n\t" ST(88) \
  MF16 "v[232:235], %[selA], v[240:243], 0\n\t" \
  "s_nop 7\n\t" \
  "v_exp_f32 v234, v232\n\tv_exp_f32 v235, v233\n\t" \
  "s_nop 0\n\t" \
  "v_cvt_pk_bf16_f32 v240, v234, v235\n\t" \
  "v_cndmask_b32 v168, 0, v240, s[76:77]\n\tv_cndmask_b32 v169, v240, 0, s[76:77]\n\t" \
  "s_nop 1\n\t" \
  MF16 "v[220:223], v[160:163], v[168:171], v[220:223]\n\t" \
  MF16 "v[244:247], v[164:167], v[168:171], v[216:219]\n\t" \
  "s_nop 6\n\t" \
  "v_rcp_f32 v242, v220\n\t" \
  "v_med3_f32 v241, v220, v172, v173\n\t" \
  "v_cmp_lg_f32 vcc, v241, v220\n\t" \
  "s_cbranch_vccnz 12f\n\t" \
  "11:\n\t" \
  "v_pk_mul_f32 v[244:245], v[244:245], v[242:243] op_sel_hi:[1,0]\n\t" \
  "v_pk_mul_f32 v[246:247], v[246:247], v[242:243] op_sel_hi:[1,0]\n\t" \
  "s_nop 0\n\t" \
  "v_add_f32_dpp v244, v244, v244 row_ror:8 row_mask:0xf bank_mask:0xf\n\tv_add_f32_dpp v245, v245, v245 row_ror:8 row_mask:0xf bank_mask:0xf\n\t" \
  "v_add_f32_dpp v246, v246, v246 row_ror:8 row_mask:0xf bank_mask:0xf\n\tv_add_f32_dpp v247, v247, v247 row_ror:8 row_mask:0xf bank_mask:0xf\n\t" \
  ST(90) "ds_write_b128 %[redw], v[244:247]\n\t" \
  LDSWAIT \
  "s_barrier\n\t" ST(92)
#define R3_S_RARE \
    "12:\n\t" \
  "v_xor_b32 v241, 0x6000, %[to]\n\t" \
  "global_load_dwordx4 v[200:203], v241, s[60:61] sc1\n\t" \
  "s_waitcnt vmcnt(0)\n\t" \
  "v_lshlrev_b32 v208, 16, v200\n\tv_and_b32 v209, 0xffff0000, v200\n\tv_lshlrev_b32 v210, 16, v201\n\tv_and_b32 v211, 0xffff0000, v201\n\t" \
  "v_lshlrev_b32 v212, 16, v202\n\tv_and_b32 v213, 0xffff0000, v202\n\tv_lshlrev_b32 v214, 16, v203\n\tv_and_b32 v215, 0xffff0000, v203\n\t" \
  "v_max3_f32 v240, v208, v209, v210\n\tv_max3_f32 v241, v211, v212, v213\n\tv_max3_f32 v240, v240, v214, v215\n\t" \
  "v_max3_f32 v241, v241, v232, v233\n\tv_max_f32 v240, v240, v241\n\t" \
  XRED("v_max_f32", "v240", "v241") \
  "v_sub_f32 v208, v208, v240\n\tv_sub_f32 v209, v209, v240\n\tv_sub_f32 v210, v210, v240\n\tv_sub_f32 v211, v211, v240\n\t" \
  "v_sub_f32 v212, v212, v240\n\tv_sub_f32 v213, v213, v240\n\tv_sub_f32 v214, v214, v240\n\tv_sub_f32 v215, v215, v240\n\t" \
  "v_sub_f32 v234, v232, v240\n\tv_sub_f32 v235, v233, v240\n\t" \
  "v_exp_f32 v208, v208\n\tv_exp_f32 v209, v209\n\tv_exp_f32 v210, v210\n\tv_exp_f32 v211, v211\n\t" \
  "v_exp_f32 v212, v212\n\tv_exp_f32 v213, v213\n\tv_exp_f32 v214, v214\n\tv_exp_f32 v215, v215\n\t" \
  "v_exp_f32 v234, v234\n\tv_exp_f32 v235, v235\n\ts_nop 1\n\t" \
  "v_cvt_pk_bf16_f32 v200, v208, v209\n\tv_cvt_pk_bf16_f32 v201, v210, v211\n\tv_cvt_pk_bf16_f32 v202, v212, v213\n\tv_cvt_pk_bf16_f32 v203, v214, v215\n\t" \
  "v_cvt_pk_bf16_f32 v240, v234, v235\n\t" \
  "v_cndmask_b32 v208, 0, v200, s[76:77]\n\tv_cndmask_b32 v209, 0, v201, s[76:77]\n\tv_cndmask_b32 v210, 0, v202, s[76:77]\n\tv_cndmask_b32 v211, 0, v203, s[76:77]\n\t" \
  "v_cndmask_b32 v212, v200, 0, s[76:77]\n\tv_cndmask_b32 v213, v201, 0, s[76:77]\n\tv_cndmask_b32 v214, v202, 0, s[76:77]\n\tv_cndmask_b32 v215, v203, 0, s[76:77]\n\t" \
  "v_cndmask_b32 v168, 0, v240, s[76:77]\n\tv_cndmask_b32 v169, v240, 0, s[76:77]\n\ts_nop 1\n\t" \
  MF16 "v[220:223], v[160:163], v[200:203], 0\n\t" \
  MF16 "v[220:223], v[160:163], v[168:171], v[220:223]\n\t" \
  MF16 "v[244:247], v[192:195], v[208:211], 0\n\t" \
  MF16 "v[244:247], v[196:199], v[212:215], v[244:247]\n\t" \
  MF16 "v[244:247], v[164:167], v[168:171], v[244:247]\n\t" \
  "s_nop 7\n\t" \
  "v_rcp_f32 v242, v220\n\ts_nop 0\n\t" \
  "s_branch 11b\n\t"
#define R3_PREP "s_lshl_b32 s44, s65, 2\n\ts_add_i32 s44, s44, s67\n\ts_lshl_b32 s45, s66, 2\n\ts_sub_i32 s45, 108, s45\n\t"
#define R3_FWD "s_add_i32 s66, s66, s47\n\ts_add_i32 s65, s65, s46\n\ts_cmp_ge_i32 s66, 24\n\ts_cselect_b32 s70, 24, 0\n\ts_addc_u32 s65, s65, 0\n\ts_sub_i32 s66, s66, s70\n\t"
#define R3_RT "s_memrealtime vcc\n\ts_waitcnt lgkmcnt(0)\n\ts_sub_i32 s70, s44, vcc_lo\n\ts_cmp_lt_i32 s70, 1\n\t"
#define R3_TW_WAIT \
  R3_RT "s_cbranch_scc1 40f\n\t" \
  "20:\n\t" R3_RT "s_cbranch_scc1 21f\n\t" R3_RT "s_cbranch_scc1 21f\n\t" R3_RT "s_cbranch_scc1 21f\n\t" R3_RT "s_cbranch_scc0 20b\n\t" \
  "21:\n\t" \
  "s_getpc_b64 vcc\n\ts_add_u32 vcc_lo, vcc_lo, s45\n\ts_addc_u32 vcc_hi, vcc_hi, 0\n\ts_setpc_b64 vcc\n\t" \
  ".rept 24\n\ts_nop 0\n\t.endr\n\t" \
  "22:\n\t"
#define R3_TW_LATE \
  "40:\n\t" \
  "s_cmp_eq_u32 s70, 0\n\ts_cbranch_scc1 22b\n\t" \
  "41:\n\ts_add_i32 s65, s65, 26\n\ts_add_i32 s44, s44, 4*26\n\ts_add_i32 s70, s70, 4*26\n\ts_cmp_lt_i32 s70, 0\n\ts_cbranch_scc1 41b\n\t" \
  "s_branch 20b\n\t"
#define R3_STEP(op, uop)   \
  op " %[moff], 0x8000, %[moff]\n\t" op " %[to], 0x8000, %[to]\n\t" op " %[tm], 0x8000, %[tm]\n\t" op " %[sto], 0x8000, %[sto]\n\t" \
  uop " %[uo], 0x4000, %[uo]\n\t" uop " %[yo], 0x1000, %[yo]\n\t"
#define R3_FLIP \
  "s_and_b32 s70, s62, 15\n\ts_cselect_b32 s70, 0, 0x4000\n\ts_add_i32 s71, s62, 1\n\ts_and_b32 s71, s71, 15\n\ts_cselect_b32 s71, 0, 0x4000\n\t" \
  "v_xor_b32 %[moff], s70, %[moff]\n\tv_xor_b32 %[to], s70, %[to]\n\tv_xor_b32 %[tm], s70, %[tm]\n\tv_xor_b32 %[sto], s71, %[sto]\n\t"
#define ULOADS "global_load_dwordx4 v[192:195], %[uo], s[60:61] sc1\n\tglobal_load_dwordx4 v[196:199], %[uo], s[60:61] offset:1024 sc1\n\t"
#define R3_DMA "global_load_lds_dwordx4 %[moff], s[60:61] sc1\n\t"
#define R3_TW_PT "s_lshl_b32 s70, s62, 7\n\ts_add_i32 s70, s70, 0x420080\n\tv_mov_b32 v178, s70\n\tglobal_load_dword v177, v178, s[60:61] sc1\n\t"
#define R3_TW_NEXT "v_readfirstlane_b32 s70, v177\n\ts_cmp_eq_u32 s70, 0\n\ts_cbranch_scc1 45f\n\t" \
  "19:\n\ts_and_b32 s46, s70, 0xff\n\ts_bfe_u32 s47, s70, 0x80008\n\ts_lshr_b32 s48, s70, 16\n\t"
#define R3_TW_OOL        \
  "45:\n\ts_mov_b32 s71, 0x40000\n\t" \
  "46:\n\tglobal_load_dword v177, v178, s[60:61] sc1\n\ts_waitcnt vmcnt(0)\n\tv_readfirstlane_b32 s70, v177\n\t" \
  "s_cmp_eq_u32 s70, 0\n\ts_cbranch_scc0 19b\n\ts_sub_i32 s71, s71, 1\n\ts_cmp_gt_i32 s71, 0\n\ts_cbranch_scc1 46b\n\ts_movk_i32 s70, 40\n\ts_branch 19b\n\t"
#define R3_TW_TIME \
  TBURST \
  "s_sub_i32 s4, s4, s44\n\ts_add_i32 s4, s4, 4\n\ts_mul_i32 s4, s4, 93\n\ts_ashr_i32 s4, s4, 2\n\ts_sub_i32 s58, s58, s4\n\t" \
  "s_lshl_b32 s4, s66, 2\n\ts_add_i32 s58, s58, s4\n\ts_sub_i32 s4, s58, s51\n\ts_mov_b32 s51, s58\n\ts_add_i32 s58, s58, s48\n\t" \
  "v_mov_b32 v200, s58\n\tv_mov_b32 v201, s4\n\tds_write_b64 v188, v[200:201] offset:12296\n\ts_waitcnt lgkmcnt(0)\n\t"
#define R3_WARM(beg, len) \
  "s_getpc_b64 vcc\n\t9:\n\ts_add_u32 vcc_lo, vcc_lo, " beg "-9b\n\ts_addc_u32 vcc_hi, vcc_hi, 0\n\tv_lshlrev_b32 v200, 3, %[rd]\n\tv_min_u32 v200, " len "-64, v200\n\t" \
  "global_load_dword v201, v200, vcc\n\tglobal_load_dword v202, v200, vcc offset:64\n\ts_waitcnt vmcnt(0)\n\t"
#define R3_LOOP(WARM, DMA, TWWAIT, CNTLOAD, CNTPAY, CNTPACK, TWTIME, NW, CNTSIG, NS, TWFWD, FIN, CNTCHK, OUTOFLINE) \
  "s_mov_b64 s[60:61], %[wsp]\n\ts_mov_b32 s62, 0\n\ts_mov_b32 s63, 0\n\ts_mov_b32 s64, 0x10000\n\ts_mov_b32 s78, 0\n\t" \
  "s_mov_b32 s76, 0x00ff00ff\n\ts_mov_b32 s77, s76\n\t" \
  "s_and_b32 s46, %[p0], 0xff\n\ts_bfe_u32 s47, %[p0], 0x80008\n\ts_lshr_b32 s48, %[p0], 16\n\ts_mov_b32 s65, 0\n\ts_mov_b32 s66, 0\n\t" \
  "s_movk_i32 s49, 250\n\ts_lshl_b32 s50, %[p1], 10\n\t" \
  "v_mov_b32 v188, 0\n\tv_mov_b32 v160, 0x3f803f80\n\tv_mov_b32 v161, v160\n\tv_mov_b32 v162, v160\n\tv_mov_b32 v163, v160\n\t" \
  "v_mov_b32 v166, 0\n\tv_mov_b32 v167, 0\n\tv_mov_b32 v170, 0\n\tv_mov_b32 v171, 0\n\tv_mov_b32 v172, 0x1f800000\n\tv_mov_b32 v173, 0x71800000\n\t" \
  R3_PREP \
  WARM \
  "10:\n\t" \
  TWWAIT \
  "s_mov_b32 m0, s50\n\t" \
  "s_waitcnt lgkmcnt(0)\n\ts_barrier\n\t" \
  "s_memtime s[72:73]\n\t" \
  DMA \
  "ds_read_b32 v200, v188 offset:12288\n\t" \
  "s_waitcnt lgkmcnt(0)\n\t" \
  "v_readfirstlane_b32 s74, v200\n\t" \
  "s_cmp_eq_u32 s74, s63\n\ts_cbranch_scc1 50f\n\t" \
  "s_add_i32 s63, s63, 1\n\t" \
  "v_mov_b32 v228, 0\n\tv_mov_b32 v229, 0\n\tv_mov_b32 v230, 0\n\tv_mov_b32 v231, 0\n\t" \
  "v_mov_b32 v232, 0\n\tv_mov_b32 v233, 0\n\tv_mov_b32 v234, 0\n\tv_mov_b32 v235, 0\n\t" \
  ULOADS \
  CNTLOAD TWTIME \
  "s_waitcnt vmcnt(" #NW ")\n\t" \
  "s_barrier\n\t" ST(80) \
  CNTPAY "ds_read_b32 v244, v188 offset:12296\n\t" \
  R3_Y_A \
  "s_cbranch_vccnz 60f\n\t" \
  "14:\n\t" \
  CNTPACK "s_add_i32 s69, s68, s49\n\t" \
  "s_cmp_eq_u32 s62, 63\n\ts_cbranch_scc1 98f\n\t" \
  R3_POLL R3_Y_B \
  TWFWD \
  "15:\n\t" \
  R3_S_PHASE(CNTSIG, NS) FIN CNTCHK \
  "s_add_i32 s62, s62, 1\n\t" \
  R3_STEP("v_add_u32", "v_add_u32") \
  R3_FLIP DIAG_R3 \
  "s_branch 10b\n\t" \
  R3_S_RARE \
    \
  "50:\n\t" \
  "s_sub_i32 s64, s64, 1\n\ts_add_i32 s63, s63, 1\n\ts_bitset1_b32 s78, 28\n\t" \
  R3_FLIP R3_STEP("v_subrev_u32", "v_subrev_u32") \
  "s_sub_i32 s62, s62, 1\n\t" \
  "s_memtime vcc\n\ts_waitcnt lgkmcnt(0)\n\t" \
  "s_sub_i32 s70, s69, s68\n\ts_cmp_eq_u32 s70, s49\n\ts_cselect_b32 s70, 256, 0\n\ts_sub_i32 s69, s69, s70\n\t"       \
  "53:\n\ts_add_i32 s68, s68, 93*26\n\ts_add_i32 s69, s69, 93*26\n\ts_add_i32 s65, s65, 26\n\t" \
  "s_sub_i32 s70, s69, vcc_lo\n\ts_cmp_lt_i32 s70, 0\n\ts_cbranch_scc1 53b\n\t" \
  R3_PREP R3_POLL2 \
  "s_branch 15b\n\t" \
    \
  "60:\n\ts_bitset1_b32 s78, 29\n\ts_sub_i32 s64, s64, 1\n\ts_cmp_gt_i32 s64, 0\n\ts_cbranch_scc0 14b\n\t" \
  "s_add_i32 s65, s65, 26\n\t" R3_PREP "s_branch 10b\n\t" \
  OUTOFLINE \
  "98:\n\t" R3_Y_B \
  "global_store_dwordx2 %[yo], v[164:165], s[60:61] sc1\n\t" \
  "s_waitcnt vmcnt(0)\n\ts_barrier\n\t"
#define R3_FIN \
  "ds_read_b128 v[200:203], %[redr]\n\tds_read_b128 v[204:207], %[redr] offset:512\n\t" \
  "ds_read_b128 v[208:211], %[redr] offset:1024\n\tds_read_b128 v[212:215], %[redr] offset:1536\n\t" \
  "s_waitcnt lgkmcnt(2)\n\t" \
  "v_pk_add_f32 v[200:201], v[200:201], v[204:205]\n\tv_pk_add_f32 v[202:203], v[202:203], v[206:207]\n\t" \
  "s_waitcnt lgkmcnt(1)\n\t" \
  "v_pk_add_f32 v[200:201], v[200:201], v[208:209]\n\tv_pk_add_f32 v[202:203], v[202:203], v[210:211]\n\t" \
  "s_waitcnt lgkmcnt(0)\n\t" \
  "v_pk_add_f32 v[200:201], v[200:201], v[212:213]\n\tv_pk_add_f32 v[202:203], v[202:203], v[214:215]\n\t" \
  "s_nop 1\n\t" \
  "v_permlane32_swap_b32 v200, v202\n\tv_permlane32_swap_b32 v201, v203\n\t" \
  "v_add_f32 v200, v200, v202\n\tv_add_f32 v201, v201, v203\n\t" \
  "v_cvt_pk_bf16_f32 v202, v200, v201\n\t" \
  "v_cmpx_o_f32 vcc, v200, v201\n\t" \
  "global_store_dword %[sto], v202, s[60:61] sc1\n\t" ST(94) \
  "s_memtime s[74:75]\n\t" \
  "s_mov_b64 exec, -1\n\t" \
  "v_mov_b32 v200, -1\n\tv_mov_b32 v201, -1\n\tv_mov_b32 v202, -1\n\tv_mov_b32 v203, -1\n\t" \
  "ds_write_b128 %[redr], v[200:203]\n\tds_write_b128 %[redr], v[200:203] offset:512\n\t" \
  "ds_write_b128 %[redr], v[200:203] offset:1024\n\tds_write_b128 %[redr], v[200:203] offset:1536\n\t" \
  "s_waitcnt lgkmcnt(0)\n\ts_sub_u32 s70, s74, s69\n\tv_mov_b32 v200, s70\n\tds_write_b32 v188, v200 offset:12308\n\t" \
  "s_cmp_eq_u64 vcc, -1\n\ts_cbranch_scc0 80f\n\t" \
  "16:\n\t"
#define R3_FIN_OOL \
  "80:\n\ts_cmp_gt_i32 s64, 0\n\ts_cbranch_scc0 81f\n\t" \
  "v_mov_b32 v200, s63\n\tds_write_b32 v188, v200 offset:12288\n\ts_branch 16b\n\t" \
  "81:\n\tv_mov_b32 v200, 0x7fc07fc0\n\tglobal_store_dword %[sto], v200, s[60:61] sc1\n\ts_branch 16b\n\t"
#define R3_CNT_PAY "ds_read_b32 v174, v188 offset:12300\n\tds_read_b32 v175, v188 offset:12308\n\t"
#define R3_CNT_PACK \
  "v_min_u32 v175, 0xfff, v175\n\tv_min_u32 v174, 0xffff, v174\n\tv_lshl_or_b32 v176, v174, 12, v175\n\tv_or_b32 v176, s78, v176\n\ts_mov_b32 s78, 0\n\t"
#define R3_CNT_SIG "global_store_dword v187, v176, s[60:61] sc1\n\t"
#define R3_CNT_LOAD "v_mov_b32 v187, s43\n\ts_mov_b64 s[38:39], s[40:41]\n\tv_cndmask_b32 v144, %[uo], v186, s[40:41]\n\tglobal_load_dword v145, v144, s[60:61] sc1\n\t"
#define R3_CNT_CHK "v_readfirstlane_b32 s43, %[tm]\n\ts_and_b32 s43, s43, 0xffffe000\n\ts_xor_b32 s43, s43, s42\n\t" \
  "v_cndmask_b32 v189, v189, v145, s[38:39]\n\ts_mov_b64 s[38:39], 0\n\t" \
  "s_add_i32 s70, s62, 3\n\ts_lshr_b32 s70, s70, 1\n\tv_cmp_gt_u32 vcc, s70, v189\n\ts_cbranch_vccnz 85f\n\t17:\n\t" \
  "s_add_i32 s70, s70, 1\n\ts_min_u32 s70, s70, 32\n\tv_cmp_gt_u32 vcc, s70, v189\n\ts_cmp_lg_u64 vcc, 0\n\ts_cselect_b64 s[40:41], -1, 0\n\t"
#define R3_CNT_OOL        \
  "85:\n\ts_mov_b32 s71, 0x40000\n\t" \
  "86:\n\tglobal_load_dword v189, v186, s[60:61] sc1\n\ts_waitcnt vmcnt(0)\n\t" \
  "v_cmp_gt_u32 vcc, s70, v189\n\ts_cbranch_vccz 17b\n\ts_sleep 2\n\ts_sub_i32 s71, s71, 1\n\ts_cmp_gt_i32 s71, 0\n\ts_cbranch_scc1 86b\n\ts_branch 17b\n\t"
#define DIAG_R3
#define DIAG_R3_END
#define DIAG_R3_OUT
#define R3_OUTS [moff] "+v"(moff), [to] "+v"(to), [tm] "+v"(tm), [sto] "+v"(sto), [uo] "+v"(uo), [yo] "+v"(yo) DIAG_R3_OUT
#define R3_INS R1_WOPS, R1_WOPS2, [idx] "v"(idx), [rd] "v"(rd), [redw] "v"(redw), [selA] "v"(selA), [wsp] "s"(ws), [p0] "s"(p0), [p1] "s"(p1)
#define R3_CLOB CLOB_V200_247, "v144","v145","v146","v147","v148","v149","v150","v151", "v160","v161","v162","v163","v164","v165","v166","v167","v168","v169","v170","v171","v172","v173","v174","v175","v176","v177","v178", "v179","v180","v181","v182","v183","s96","s97","v184","v185","v186","v187","v188","v189","v190","v191","v192","v193","v194","v195","v196","v197","v198","v199", \
  "s38","s39","s40","s41","s42","s43","s44","s45","s46","s47","s48","s49","s50","s51","s60","s61","s62","s63","s64","s65","s66","s67","s68","s69","s70","s71", \
  "s72","s73","s74","s75","s76","s77","s78","vcc","scc","memory" ST_CLOB
#define STR2(x) #x
#define STR(x) STR2(x)

template <int KIND>
DEV void r3_body(unsigned char* __restrict__ ws, int v, int wave, int lane, unsigned sel TUNE_ARG) {
  const int g = lane >> 4, hs = (lane >> 3) & 1, i8 = lane & 7;
  const int idx = (lane & 8) ? 0xEEEE : 0x4444;
  const int dw = wave < 7 ? wave : 6;
  const unsigned rd = lane * 16;
  u8v w0[7], w1[7];
  {
    const unsigned char* p = ws + O_WB + (size_t)(2 * wave * 28 + v) * 14336 + lane * 16;
#pragma unroll
    for (int k = 0; k < 7; ++k) {
      w0[k] = cat8(*(const u4v*)(p + k * 2048), *(const u4v*)(p + k * 2048 + 1024));
      w1[k] = cat8(*(const u4v*)(p + 28 * 14336 + k * 2048), *(const u4v*)(p + 28 * 14336 + k * 2048 + 1024));
    }
#pragma unroll
    for (int k = 0; k < 7; ++k) asm volatile("" : "+v"(w0[k]), "+v"(w1[k]));
  }
  RTS(1);
  if (KIND == 3 && lane == 0) lds_w4(L_FLAG, SENT);
  if (KIND == 2) wait_cnt(ws, doneU(ws, 4 * (v >> 1) + (lane & 3)), 1);
  *(LDS u2v*)(size_t)(L_RED + wave * 512 + lane * 8) = (u2v){SENT, SENT};
  RTS(2);
  unsigned moff = hoff(sel, 0, 0) + dw * 1024 + g * 256 + i8 * 32 + hs * 16;
  unsigned to = hoff(sel, 0, 2) + (2 * wave + hs) * 512 + (g >> 1) * 256 + i8 * 32 + (g & 1) * 16;
  unsigned tm = hoff(sel, 64, K_SMK) + (2 * wave + hs) * 512 + g * 128 + i8 * 16;
  u4v selA = {0u, 0u, 0u, 0u};
  if (((lane & 3) >> 1) == 0) {
    const int gq = (lane >> 2) & 3, j = 4 * (gq & 1) + (gq >> 1) + 2 * (lane & 1);
    const unsigned one = (j & 1) ? 0x3f800000u : 0x00003f80u;
    selA = (u4v){(j >> 1) == 0 ? one : 0u, (j >> 1) == 1 ? one : 0u, (j >> 1) == 2 ? one : 0u, (j >> 1) == 3 ? one : 0u};
  }
  unsigned sto = hoff(sel, 1, 0) + v * 256 + i8 * 32 + ((lane >> 3) & 3) * 8 + (lane >> 5) * 4;
  unsigned uo = (unsigned)O_U + v * U_RUN + 2 * wave * 1024 + lane * 16;
  unsigned yo = (unsigned)O_YG + v * Y_RUN + wave * 512 + lane * 8;
  const unsigned redw = L_RED + hs * 4096 + wave * 512 + (g * 8 + i8) * 16;
  const unsigned redr = L_RED + (lane >> 5) * 2048 + (lane & 31) * 16;
  const unsigned p1 = dw;
  {
    const unsigned o = lane < 8 ? yo : lane < 16 ? yo + 63 * 4096 : lane < 24 ? uo : lane < 32 ? uo + 63 * 16384 : lane < 40 ? hoff(sel, 70, 0) : lane < 48 ? hoff(sel, 1, 0) + 7168 : (unsigned)O_FL + 0x3ff00;
    (void)ld_cnt((const unsigned*)(ws + (o & ~3u)));
  }
  int anchor = 0;
  unsigned p0 = 0;
  if (KIND == 3) asm volatile(R3_WARM(".Lr3tw_beg", ".Lr3tw_end-.Lr3tw_beg") :: [rd] "v"(rd) : "v200", "v201", "v202", "vcc", "scc", "memory");
  if (KIND == 3) { wait_cnt(ws, ptp(ws, 0), 1); p0 = __builtin_amdgcn_readfirstlane(ld_cnt(ptp(ws, 0))); }
  bar();
  if (KIND == 3) anchor = get_anchor(ws, sel, 64 + v, lane);
  if (KIND == 0) {
    asm volatile(R3_LOOP(R3_WARM("10f", "98f-10f"), R3_DMA, "", "", "", "", "", 2, "", 1, "", R3_FIN, "", R3_FIN_OOL) DIAG_R3_END : R3_OUTS : R3_INS, [redr] "v"(redr) : R3_CLOB);
  } else if (KIND == 1) {
    asm volatile(R3_LOOP(R3_WARM("10f", "98f-10f"), R3_DMA, "", "", "", "", "", 2, "", 1, "", "", "", "") DIAG_R3_END : R3_OUTS : R3_INS : R3_CLOB);
  } else if (KIND == 2) {
    const unsigned cc = ((unsigned)(3 - K_SMK) << 13) | (unsigned)(v * 128);
    const unsigned uc = (unsigned)O_FL + (C_W + 2 * (56 + 4 * (v >> 1) + (lane & 3))) * 64;
    asm volatile("s_mov_b64 s[40:41], -1\n\ts_mov_b32 s42, %[cc]\n\ts_and_b32 s43, s42, 0x1fff\n\ts_add_i32 s43, s43, 0x43e000\n\tv_mov_b32 v186, %[uc]\n\t"
                 R3_LOOP(R3_WARM("10f", "98f-10f"), R3_DMA, "", R3_CNT_LOAD, R3_CNT_PAY, R3_CNT_PACK, "", 3, R3_CNT_SIG, 2, "", "", R3_CNT_CHK, R3_CNT_OOL) DIAG_R3_END : R3_OUTS : R3_INS, [cc] "s"(cc), [uc] "v"(uc) : R3_CLOB);
  } else {
    asm volatile("s_mov_b32 s67, %[anc]\n\t" R3_LOOP(".Lr3tw_beg:\n\t", "", R3_TW_WAIT, R3_TW_PT, "", "", R3_TW_TIME, 3, "", 1, R3_FWD R3_PREP, "", R3_TW_NEXT, R3_TW_LATE R3_TW_OOL ".Lr3tw_end:\n\t") DIAG_R3_END : R3_OUTS : R3_INS, [anc] "s"(anchor) : R3_CLOB, TBURST_CLOB);
  }
  if (KIND == 2 && lane < 2) st4c_asm(flagY(ws, sel, NSEG - 2 + lane, v), 0u);
  RTS(3);
}
DEV void role_r3(unsigned char* __restrict__ ws, int v, int wave, int lane, unsigned sel TUNE_ARG) {
  if (wave == 0) r3_body<0>(ws, v, wave, lane, sel TUNE_PASS);
  else if (wave == 6) r3_body<2>(ws, v, wave, lane, sel TUNE_PASS);
  else if (wave == 7) r3_body<3>(ws, v, wave, lane, sel TUNE_PASS);
  else r3_body<1>(ws, v, wave, lane, sel TUNE_PASS);
}

#define PF_LAG 4
DEV void role_pf(unsigned char* __restrict__ ws, int k, int wave, int lane, unsigned sel) {
  constexpr int LAG = PF_LAG;
  const rsrc_t ru = mkrsrc(ws + O_U, 30u << 20);
  unsigned have = 0;
  for (int t = 1; t < NSEG - 1; ++t) {
    if (wave == 0) {
      unsigned skip = 0;
      const unsigned need = (t >> 1) + 1;
      if (__any(have < need)) { wait_cnt(ws, doneU(ws, min(lane, 55)), need); have = ld_cnt(doneU(ws, min(lane, 55))); }
      if (t >= LAG) wait_flags(ws, flagY(ws, sel, t - LAG, k));
      else if (t >= LAG - 2) {
        wait_cnt(ws, anchorp(ws, 28 + k), 1);
        const unsigned at = (__builtin_amdgcn_readfirstlane(ld_cnt(anchorp(ws, 28 + k))) & ~1u) + (t - LAG + 2) * 264;
        skip = (int)(((unsigned)rt32() << 1) - at) > 40;
        for (int it = 0; it < 4096 && (int)(at - ((unsigned)rt32() << 1)) > 0; ++it) __builtin_amdgcn_s_sleep(1);
      }
      lds_w4((t & 1) * 4, skip);
    }
    bar();
    if (__builtin_amdgcn_readfirstlane(lds_r4((t & 1) * 4))) continue;
    u4v r[14];
#pragma unroll
    for (int i = 0; i < 14; ++i) { const int n = (k * 8 + wave) * 14 + i; r[i] = ldc16(ru, (n >> 4) * U_RUN + (t * 16 + (n & 15)) * 1024 + lane * 16); }
#pragma unroll
    for (int i = 0; i < 14; ++i) asm volatile("" :: "v"(r[i]));
  }
}

__global__ void __launch_bounds__(512, 2)
main_kernel(const unsigned short* __restrict__ x, const unsigned short* __restrict__ b1, const unsigned short* __restrict__ b2,
            unsigned short* __restrict__ out, unsigned char* __restrict__ ws TUNE_ARG) {
  const int b = blockIdx.x, lane = threadIdx.x & 63;
  const int wave = __builtin_amdgcn_readfirstlane(threadIdx.x >> 6);
  RTS(0);
  if (threadIdx.x == 0) ((unsigned*)(ws + 125 * MB))[b] = (unsigned)__builtin_amdgcn_s_getreg(20 | (3 << 11)) & 0xfu;
  const int xc = b & 7, slot = b >> 3;
  if (xc == 0 || xc == 2) {
    const unsigned sel = *(const unsigned*)(ws + O_SEL);
    role_r1(ws, (xc >> 1) * 32 + slot, wave, lane, sel TUNE_PASS);
  } else if (xc == 1) {
    const unsigned sel = *(const unsigned*)(ws + O_SEL);
    if (slot < 28) role_r3(ws, slot, wave, lane, sel TUNE_PASS);
    else role_pf(ws, slot - 28, wave, lane, sel);
  } else {
    const int k = slot * 5 + (xc - 3);
    if (k < 56) role_w1<true>(ws, k, wave, lane);
    else if (k < 112) role_w1<false>(ws, k - 56, wave, lane);
    else {
      if (k < 144) role_ffn(ws, x, b1, b2, k - 112, wave, lane);
      else role_stt(ws, (k - 144) >> 1, k & 1, wave, lane);
      role_w3(ws, out, (159 - k) >> 2, k & 3, wave, lane);
    }
  }
}

__global__ void dbgk(unsigned char* ws) {
  if (threadIdx.x == 0) { unsigned* x = (unsigned*)(ws + 125 * MB); unsigned* o = (unsigned*)(ws + 126 * MB); int bad = 0; for (int b = 0; b < 256; ++b) bad += x[b] != x[b & 7];
    printf("probe xcc %u; main xcc %u %u %u %u %u %u %u %u nonuniform %d; A %u V %u ms %u mm %u tries %u P0 %u\n", ((unsigned*)(ws + O_SEL))[16], x[0], x[1], x[2], x[3], x[4], x[5], x[6], x[7], bad, o[0], o[1], o[2], o[3], o[4], o[5]); }
}
extern "C" void kernel_launch(void* const* d_in, const int* in_sizes, int n_in, void* d_out, int out_size, void* d_ws,
                              size_t ws_size, hipStream_t stream) {
  unsigned char* ws = (unsigned char*)(((uintptr_t)d_ws + 2 * MB - 1) & ~(uintptr_t)(2 * MB - 1));
  const unsigned short* x = (const unsigned short*)d_in[0];
  constexpr int LDS_MAIN = 132 * 1024;
  (void)hipFuncSetAttribute((const void*)main_kernel, hipFuncAttributeMaxDynamicSharedMemorySize, LDS_MAIN);
  constexpr int LDS_PREP = 152 * 1024;
  (void)hipFuncSetAttribute((const void*)prep_kernel, hipFuncAttributeMaxDynamicSharedMemorySize, LDS_PREP);
  prep_kernel<<<256, 512, LDS_PREP, stream>>>(x, (const unsigned short*)d_in[1], (const unsigned short*)d_in[2], (const unsigned short*)d_in[3],
                                           (const unsigned short*)d_in[4], (const unsigned short*)d_in[5], (const unsigned short*)d_in[8],
                                           (const unsigned short*)d_in[10], ws);
  main_kernel<<<256, 512, LDS_MAIN, stream>>>(x, (const unsigned short*)d_in[9], (const unsigned short*)d_in[11], (unsigned short*)d_out, ws TUNE_PASS);
  dbgk<<<1, 64, 0, stream>>>(ws);
}
```

```cpp
#include <hip/hip_runtime.h>
#include <hip/hip_bf16.h>
#include <stdint.h>

typedef __bf16 bf2 __attribute__((ext_vector_type(2)));
typedef __bf16 bf8 __attribute__((ext_vector_type(8)));
typedef __bf16 bf16x __attribute__((ext_vector_type(16)));
typedef short s4v __attribute__((ext_vector_type(4)));
typedef float f2v __attribute__((ext_vector_type(2)));
typedef float f4v __attribute__((ext_vector_type(4)));
typedef float f16v __attribute__((ext_vector_type(16)));
typedef unsigned u2v __attribute__((ext_vector_type(2)));
typedef unsigned u4v __attribute__((ext_vector_type(4)));
typedef unsigned u8v __attribute__((ext_vector_type(8)));
#define LDS __attribute__((address_space(3)))
#define GLB __attribute__((address_space(1)))
#define DEV __device__ __forceinline__

constexpr int DM = 448, NH = 16, DH = 256, NSEG = 64, FF = 1792;
constexpr unsigned SENT = 0xFFFFFFFFu;

constexpr size_t MB = 1u << 20;
constexpr size_t O_HB = 0;
constexpr size_t O_FL = 4 * MB;
constexpr size_t O_SEL = O_FL + 256 * 1024;
constexpr size_t O_XN = 5 * MB;
constexpr size_t O_WA = 7 * MB;
constexpr size_t O_WB = 14 * MB;
constexpr size_t O_W1 = 21 * MB;
constexpr size_t O_W2 = 23 * MB;
constexpr size_t O_G = 25 * MB;
constexpr size_t O_U = 55 * MB;
constexpr size_t O_ST = 85 * MB;
constexpr size_t O_SM = 87 * MB;
constexpr size_t O_YG = 100 * MB;
constexpr size_t O_F = 111 * MB;
constexpr unsigned G_RUN = 64 * 14336 + 3 * 4096, U_RUN = 64 * 16384 + 5 * 4096, Y_RUN = 64 * 4096 + 4096;
constexpr size_t O_END = 115 * MB;

DEV unsigned pk2(float a, float b) { f2v v = {a, b}; return __builtin_bit_cast(unsigned, __builtin_convertvector(v, bf2)); }
DEV float bflo(unsigned u) { return __builtin_bit_cast(float, u << 16); }
DEV float bfhi(unsigned u) { return __builtin_bit_cast(float, u & 0xffff0000u); }
DEV float rbf(float a) { return bflo(pk2(a, 0.f) & 0xffffu); }
DEV bf8 asbf8(u4v v) { return __builtin_bit_cast(bf8, v); }
DEV u4v lds_r16(unsigned a) { return *(LDS u4v*)(size_t)a; }
DEV void lds_w16(unsigned a, u4v v) { *(LDS u4v*)(size_t)a = v; }
DEV u2v lds_tr(unsigned a) { return __builtin_bit_cast(u2v, __builtin_amdgcn_ds_read_tr16_b64_v4i16((LDS s4v*)(size_t)a)); }
DEV unsigned rdtime() { unsigned long long t; asm volatile("s_memtime %0\n\ts_waitcnt lgkmcnt(0)" : "=s"(t) :: "memory"); return (unsigned)t; }
DEV int swap13(int x) { return (x & 5) | ((x >> 2) & 2) | ((x << 2) & 8); }
DEV void dma16(const void* g, unsigned ldsaddr) {
  unsigned keep;
  asm volatile("s_mov_b32 %0, m0\n\ts_mov_b32 m0, %2\n\ts_nop 0\n\tglobal_load_lds_dwordx4 %1, off\n\ts_mov_b32 m0, %0" : "=&s"(keep) : "v"(g), "s"(ldsaddr) : "memory");
}
DEV void dma16c(const void* g, unsigned ldsaddr) {
  unsigned keep;
  asm volatile("s_mov_b32 %0, m0\n\ts_mov_b32 m0, %2\n\ts_nop 0\n\tglobal_load_lds_dwordx4 %1, off sc1\n\ts_mov_b32 m0, %0" : "=&s"(keep) : "v"(g), "s"(ldsaddr) : "memory");
}
DEV void vm0() { asm volatile("s_waitcnt vmcnt(0)" ::: "memory"); }
DEV void bar() { asm volatile("s_waitcnt lgkmcnt(0)\n\ts_barrier" ::: "memory"); }

DEV void stwt16(void* p, u4v v) { asm volatile("global_store_dwordx4 %0, %1, off sc1\n\ts_nop 1" :: "v"(p), "v"(v) : "memory"); }
DEV float wavesum(float v) {
  v += __builtin_bit_cast(float, __builtin_amdgcn_update_dpp(0, __builtin_bit_cast(int, v), 0xb1, 0xf, 0xf, true));
  v += __builtin_bit_cast(float, __builtin_amdgcn_update_dpp(0, __builtin_bit_cast(int, v), 0x4e, 0xf, 0xf, true));
  v += __builtin_bit_cast(float, __builtin_amdgcn_update_dpp(0, __builtin_bit_cast(int, v), 0x124, 0xf, 0xf, true));
  v += __builtin_bit_cast(float, __builtin_amdgcn_update_dpp(0, __builtin_bit_cast(int, v), 0x128, 0xf, 0xf, true));
  const unsigned u = __builtin_bit_cast(unsigned, v);
  const u2v a = __builtin_amdgcn_permlane16_swap(u, u, false, false);
  const unsigned ax = a.x, ay = a.y;
  const unsigned w = __builtin_bit_cast(unsigned, __builtin_bit_cast(float, ax) + __builtin_bit_cast(float, ay));
  const u2v c = __builtin_amdgcn_permlane32_swap(w, w, false, false);
  const unsigned cx = c.x, cy = c.y;
  return __builtin_bit_cast(float, cx) + __builtin_bit_cast(float, cy);
}
#define K0S(k)
DEV void ln_row(u4v xv, unsigned char* __restrict__ ws, int row, int lane) {
  float e[8];
  if (lane >= 56) xv = (u4v){0u, 0u, 0u, 0u};
#pragma unroll
  for (int j = 0; j < 4; ++j) { e[2 * j] = bflo(xv[j]); e[2 * j + 1] = bfhi(xv[j]); }
  float s = 0.f;
#pragma unroll
  for (int j = 0; j < 8; ++j) s += e[j];
  const float mean = wavesum(s) * (1.f / DM);
  float q = 0.f;
  if (lane < 56) {
#pragma unroll
    for (int j = 0; j < 8; ++j) { const float d = e[j] - mean; q += d * d; }
  }
  const float mb = rbf(mean), vb = rbf(wavesum(q) * (1.f / DM));
  const float den = rbf(__builtin_sqrtf(rbf(vb + 1e-5f)));
  unsigned o4[4];
#pragma unroll
  for (int j = 0; j < 4; ++j) o4[j] = pk2(rbf(e[2 * j] - mb) / den, rbf(e[2 * j + 1] - mb) / den);
  const bool odd = lane & 1;
  const unsigned r0 = __builtin_amdgcn_update_dpp(0u, odd ? o4[0] : o4[2], 0xb1, 0xf, 0xf, true);
  const unsigned r1 = __builtin_amdgcn_update_dpp(0u, odd ? o4[1] : o4[3], 0xb1, 0xf, 0xf, true);
  u4v p;
  if (odd) p = (u4v){r0, o4[2], r1, o4[3]}; else p = (u4v){o4[0], r0, o4[1], r1};
  if (lane < 56) stwt16(ws + O_XN + (size_t)(row >> 5) * 28672 + (lane >> 1) * 1024 + (lane & 1) * 512 + (row & 31) * 16, p);
}
DEV u4v srd(const void* p, unsigned bytes) { const unsigned long long a = (unsigned long long)p; return (u4v){(unsigned)a, (unsigned)(a >> 32), bytes, 0x00020000u}; }
DEV void bdma16(unsigned voff, u4v r, unsigned soff, unsigned ldsaddr) {
  asm volatile("s_mov_b32 m0, %3\n\ts_nop 0\n\tbuffer_load_dwordx4 %0, %1, %2 offen lds" :: "v"(voff), "s"(r), "s"(soff), "s"(ldsaddr) : "memory");
}
constexpr unsigned rowoff(int j) { return (16 * ((j >> 2) & 1) + 8 * (j & 1) + 4 * ((j >> 1) & 1) + 2 * (j >> 3)) * 8192u; }

template <bool ISA>
DEV void prep_gemm(const unsigned short* __restrict__ Rw, const unsigned short* __restrict__ Cw, const unsigned short* __restrict__ x,
                   unsigned char* __restrict__ ws, int b, int h, int cg, int wave, int lane) {
  const int g = lane >> 4, c16 = lane & 15, hl = lane >> 5, sl = lane & 31;
  const int row = b * 8 + wave;
  const unsigned xl = 147456 + wave * 1024;
  dma16(x + (size_t)row * DM + min(lane, 55) * 8, xl);
  if (ISA) {
    const u4v rc = srd(Cw + (size_t)64 * cg * 4096 + h * 256, 64 * 8192);
    const unsigned v0 = hl * 8192 + ((sl ^ hl ^ ((wave & 1) << 3)) << 4), s0 = ((wave >> 1) * 16 + (wave & 1) * 2) * 8192;
#pragma unroll
    for (int j = 0; j < 4; ++j) bdma16(v0 ^ (j << 5), rc, s0 + ((j & 1) * 8 + (j >> 1) * 4) * 8192, (wave * 4 + j) * 1024);
  } else {
    const u4v rc = srd(Cw + (size_t)h * 256 * DM + 64 * cg, 256 * 896);
    const unsigned v0 = (lane >> 3) * 896 + (((lane & 7) ^ (((lane >> 4) & 1) << 1)) << 4);
#pragma unroll
    for (int j = 0; j < 4; ++j) bdma16(v0 ^ ((j & 1) << 6), rc, (wave * 4 + j) * 8 * 896, (wave * 4 + j) * 1024);
  }
  if (wave == 7) {
    __builtin_amdgcn_sched_barrier(0);
    asm volatile("s_waitcnt vmcnt(0)\n\ts_barrier" ::: "memory");
    const u4v ones = {SENT, SENT, SENT, SENT}, zero = {0u, 0u, 0u, 0u};
#pragma unroll
    for (int j = 0; j < 16; ++j) stwt16(ws + O_HB + (size_t)b * 16384 + j * 1024 + lane * 16, ones);
    stwt16(ws + O_FL + b * 1024 + lane * 16, zero);
    ln_row(lds_r16(xl + lane * 16), ws, row, lane);
    return;
  }
  const float sc = ISA ? 0.0625f * 1.4426950408889634f : 1.f;
  unsigned char* dst = ws + (ISA ? O_WA : O_WB) + (size_t)(h * 28 + 4 * cg) * 14336 + 2 * wave * 1024 + lane * 16;
  const unsigned reg = 32768 + wave * 16384;
  const int rot = cg * 2;
  const unsigned srot = (16 * ((rot >> 2) & 1) + 8 * (rot & 1) + 4 * ((rot >> 1) & 1) + 2 * (rot >> 3)) * 8192u, mrot = reg | (rot << 10);
  const unsigned vr = hl * 8192 + (((sl ^ hl) << 4) ^ ((rot & 7) << 5));
  const u4v rr = srd(Rw + (size_t)(64 * wave) * 4096 + h * 256, 64 * 8192);
  const __amdgpu_buffer_rsrc_t rq = __builtin_amdgcn_make_buffer_rsrc((void*)(Rw + (size_t)(64 * wave + 32) * 4096 + h * 256), 0, 32 * 8192, 0x00020000);
  u4v nx[16];
#pragma unroll
  for (int j = 0; j < 2; ++j) bdma16(vr ^ ((j & 7) << 5), rr, srot ^ rowoff(j), mrot ^ (j << 10));
#pragma unroll
  for (int j = 0; j < 2; ++j) nx[j] = __builtin_amdgcn_raw_buffer_load_b128(rq, vr ^ ((j & 7) << 5), srot ^ rowoff(j), 0);
  __builtin_amdgcn_sched_barrier(0);
  asm volatile("s_waitcnt vmcnt(8)" ::: "memory");
  ln_row(lds_r16(xl + lane * 16), ws, row, lane);
  __builtin_amdgcn_sched_barrier(0);
  K0S(1);
  asm volatile("s_waitcnt vmcnt(5)\n\ts_barrier" ::: "memory");
  K0S(0);
#pragma unroll
  for (int j = 2; j < 16; ++j) bdma16(vr ^ ((j & 7) << 5), rr, srot ^ rowoff(j), mrot ^ (j << 10));
#pragma unroll
  for (int j = 2; j < 16; ++j) nx[j] = __builtin_amdgcn_raw_buffer_load_b128(rq, vr ^ ((j & 7) << 5), srot ^ rowoff(j), 0);
  __builtin_amdgcn_sched_barrier(0);
  asm volatile("s_waitcnt vmcnt(14)" ::: "memory");
  K0S(2);
  const int q = c16 >> 2, p = c16 & 3;
  const int xr = (q >> 1) | ((g & 1) << 1);
  auto bfrag = [&](int es, int ti) -> u4v {
    if (ISA) return lds_r16((ti * 16 + c16) * 512 + (((es * 4 + g) ^ c16) << 4));
    const unsigned ad = (32 * es + 8 * g + q) * 128 + ((ti ^ xr) << 5) + p * 8;
    const u2v lo = lds_tr(ad), hi = lds_tr(ad + 512);
    return (u4v){lo.x, lo.y, hi.x, hi.y};
  };
  auto afrag = [&](int es, int t) -> u4v { return lds_r16(reg + (t * 16 + c16) * 512 + (((es * 4 + g) ^ c16) << 4)); };
#pragma unroll
  for (int rb = 0; rb < 2; ++rb) {
    u4v bb[2][4], aa[2][2];
#pragma unroll
    for (int ti = 0; ti < 4; ++ti) bb[0][ti] = bfrag(0, ti);
    aa[0][0] = afrag(0, 0); aa[0][1] = afrag(0, 1);
    f4v acc[2][4];
#pragma unroll
    for (int t = 0; t < 2; ++t)
#pragma unroll
      for (int ti = 0; ti < 4; ++ti) acc[t][ti] = (f4v){0.f, 0.f, 0.f, 0.f};
#pragma unroll
    for (int es = 0; es < 8; ++es) {
      if (es < 7) {
        aa[(es + 1) & 1][0] = afrag(es + 1, 0); aa[(es + 1) & 1][1] = afrag(es + 1, 1);
#pragma unroll
        for (int ti = 0; ti < 4; ++ti) bb[(es + 1) & 1][ti] = bfrag(es + 1, ti);
      }
#pragma unroll
      for (int ti = 0; ti < 4; ++ti)
#pragma unroll
        for (int t = 0; t < 2; ++t)
          acc[t][ti] = __builtin_amdgcn_mfma_f32_16x16x32_bf16(asbf8(aa[es & 1][t]), asbf8(bb[es & 1][ti]), acc[t][ti], 0, 0, 0);
      __builtin_amdgcn_sched_barrier(0);
    }
    if (rb == 0) {
#pragma unroll
      for (int j = 0; j < 16; ++j) lds_w16((mrot ^ (j << 10)) + lane * 16, nx[j]);
    }
#pragma unroll
    for (int ti = 0; ti < 4; ++ti) {
      const u4v o = {pk2(acc[0][ti][0] * sc, acc[0][ti][1] * sc), pk2(acc[0][ti][2] * sc, acc[0][ti][3] * sc),
                     pk2(acc[1][ti][0] * sc, acc[1][ti][1] * sc), pk2(acc[1][ti][2] * sc, acc[1][ti][3] * sc)};
      stwt16(dst + (size_t)ti * 14336 + rb * 1024, o);
    }
  }
  K0S(3);
}

__global__ void __launch_bounds__(512, 2)
prep_kernel(const unsigned short* __restrict__ x,
            const unsigned short* __restrict__ Wq, const unsigned short* __restrict__ Wk,
            const unsigned short* __restrict__ Wv, const unsigned short* __restrict__ Wo,
            const unsigned short* __restrict__ w1, const unsigned short* __restrict__ w2,
            unsigned char* __restrict__ ws) {
  const int b = blockIdx.x, tid = threadIdx.x, lane = tid & 63;
  const int wave = __builtin_amdgcn_readfirstlane(tid >> 6);
  const int xc = b & 7, slot = b >> 3;
  if (slot < 28) {
    const int m = xc * 4 + slot / 7, cg = slot % 7;
    if (m < 16) prep_gemm<true>(Wk, Wq, x, ws, b, m, cg, wave, lane);
    else prep_gemm<false>(Wv, Wo, x, ws, b, m - 16, cg, wave, lane);
  } else {
    const int row = b * 8 + wave;
    u4v xv = {0u, 0u, 0u, 0u};
    const bool prober = slot == 28 && wave == 7;
    const int job = slot == 28 ? xc * 7 + wave : 56 + ((slot - 29) * 8 + xc) * 8 + wave;
    const bool work = !prober && job < 224;
    const unsigned reg = wave * 14336;
    if (work) {
      const bool first = job < 112;
      const int step = first ? job >> 2 : job - 112;
      const unsigned short* src = first ? w1 + (size_t)step * 16 * FF + (job & 3) * 448 : w2 + (size_t)step * 16 * DM;
      const int ld = first ? FF : DM;
#pragma unroll
      for (int j = 0; j < 14; ++j) { const int n = j * 64 + lane, r = n / 56; dma16(src + (size_t)r * ld + (n - r * 56) * 8, reg + j * 1024); }
    }
    if (lane < 56) xv = *(const u4v*)(x + (size_t)row * DM + lane * 8);
    if (prober) {
      const u4v ones = {SENT, SENT, SENT, SENT};
      stwt16(ws + O_HB + (lane >> 4) * 8192 + (lane & 15) * 64, ones);
      stwt16(ws + O_HB + 2 * MB + (lane >> 4) * 8192 + (lane & 15) * 64, ones);
    }
    asm volatile("" : "+v"(xv));
    vm0();
    if (slot == 28) bar();
    if (prober) {
      const unsigned xcc = (unsigned)__builtin_amdgcn_s_getreg(20 | (3 << 11)) & 7u;
      unsigned word = xcc << 4;
      if (xcc == 0) {
        auto timed = [&](const unsigned char* pc) -> unsigned {
          unsigned v;
          const unsigned c0 = rdtime();
          asm volatile("global_load_dword %0, %1, %2 sc1\n\ts_waitcnt vmcnt(0)" : "=&v"(v) : "v"(0u), "s"(pc) : "memory");
          return rdtime() - c0;
        };
        unsigned best[6];
#pragma unroll
        for (int c = 0; c < 6; ++c) best[c] = 0x7fffffff;
#pragma unroll 1
        for (int it = 0; it < 3; ++it) {
#pragma unroll
          for (int c = 0; c < 4; ++c) best[c] = min(best[c], timed(ws + O_HB + c * 8192 + it * 256));
        }
        unsigned sel = 0, bv = best[0];
#pragma unroll
        for (unsigned k = 1; k < 4; ++k) if (best[k] < bv) { bv = best[k]; sel = k; }
#pragma unroll 1
        for (int it = 0; it < 2; ++it) {
#pragma unroll
          for (int c = 0; c < 2; ++c) best[4 + c] = min(best[4 + c], timed(ws + O_HB + 2 * MB + (sel ^ c) * 8192 + it * 256));
        }
        word |= 0x80u | sel | ((sel ^ (best[5] <= best[4] ? 1u : 0u)) << 2);
      }
      if (lane == 0) asm volatile("global_store_byte %0, %1, off sc1" :: "v"(ws + O_SEL + xc), "v"(word) : "memory");
    }
    if (work) {
      const bool first = job < 112;
      const int hi = lane >> 5, q = (lane & 15) >> 2;
      const int r0 = first ? 4 * hi + 8 * (q >> 1) + (q & 1) : 4 * hi + q, dr = first ? 2 : 8;
      const unsigned ad = reg + r0 * 896 + ((lane >> 4) & 1) * 32 + (lane & 3) * 8;
      unsigned char* dst = ws + (first ? O_W1 + (size_t)((job & 3) * 14 * 28 + (job >> 2)) * 1024 : O_W2 + (size_t)(job - 112) * 1024) + lane * 16;
      const size_t dstep = first ? 28672 : 114688;
#pragma unroll
      for (int t = 0; t < 14; ++t) {
        const u2v lo = lds_tr(ad + t * 64), hv = lds_tr(ad + t * 64 + dr * 896);
        stwt16(dst + t * dstep, (u4v){lo.x, lo.y, hv.x, hv.y});
      }
    }
    const u4v ones = {SENT, SENT, SENT, SENT}, zero = {0u, 0u, 0u, 0u};
    stwt16(ws + O_HB + (size_t)b * 16384 + tid * 16, ones);
    stwt16(ws + O_HB + (size_t)b * 16384 + 8192 + tid * 16, ones);
    if (wave == 0) stwt16(ws + O_FL + b * 1024 + lane * 16, zero);
    ln_row(xv, ws, row, lane);
  }
}
constexpr int C_W = 0  , C_F = 1856, C_ABORT = 1900, C_ANCHOR = 1920;
typedef __amdgpu_buffer_rsrc_t rsrc_t;
DEV rsrc_t mkrsrc(const void* p, unsigned bytes) { return __builtin_amdgcn_make_buffer_rsrc((void*)p, 0, bytes, 0x00020000); }
DEV u4v ldc16(rsrc_t r, unsigned off) { return __builtin_amdgcn_raw_buffer_load_b128(r, off, 0, 16); }
DEV u2v ldc8(rsrc_t r, unsigned off) { return __builtin_amdgcn_raw_buffer_load_b64(r, off, 0, 16); }
DEV void stc16(rsrc_t r, unsigned off, u4v v) { __builtin_amdgcn_raw_buffer_store_b128(v, r, off, 0, 16); }
DEV void stc4(rsrc_t r, unsigned off, unsigned v) { __builtin_amdgcn_raw_buffer_store_b32(v, r, off, 0, 16); }
DEV unsigned* cntp(unsigned char* ws, int idx) { return (unsigned*)(ws + O_FL) + idx * 16; }
DEV unsigned ld_cnt(const unsigned* p) { unsigned v; asm volatile("global_load_dword %0, %1, off sc1\n\ts_waitcnt vmcnt(0)" : "=&v"(v) : "v"(p) : "memory"); return v; }
DEV void add_cnt(unsigned* p, int lane) {
  if (lane == 0) { const unsigned one = 1; asm volatile("global_atomic_add %0, %1, off sc1" :: "v"(p), "v"(one) : "memory"); }
}
DEV void post(unsigned* p, unsigned v, int lane) { if (lane == 0) asm volatile("global_store_dword %0, %1, off sc1" :: "v"(p), "v"(v) : "memory"); }
DEV unsigned* doneG(unsigned char* ws, int j) { return cntp(ws, C_W + 2 * j); }
DEV unsigned* doneU(unsigned char* ws, int j) { return cntp(ws, C_W + 2 * (56 + j)); }
constexpr int SPIN_CAP = 1 << 17;
DEV void wait_cnt(unsigned char* ws, const unsigned* p, unsigned target, int nap = 6) {
  for (int it = 0;; ++it) {
    const unsigned v = ld_cnt(p);
    if (__all(v >= target)) return;
    if ((it & 63) == 63) {
      if (it >= SPIN_CAP) { const unsigned one = 1; asm volatile("global_store_dword %0, %1, off sc1" :: "v"(cntp(ws, C_ABORT)), "v"(one) : "memory"); return; }
      if (__any(ld_cnt(cntp(ws, C_ABORT)) != 0)) return;
    }
    if (nap > 6) __builtin_amdgcn_s_sleep(32); else __builtin_amdgcn_s_sleep(2);
  }
}
DEV unsigned hoff(unsigned sel, int gi, int kind) {
  const unsigned k = ((sel >> (2 * (gi >> 6))) & 3u) ^ ((((unsigned)gi >> 4) & 1u) << 1) ^ (unsigned)kind;
  return (unsigned)O_HB + (unsigned)gi * 32768u + k * 8192u;
}
DEV unsigned char* hblk(unsigned char* ws, unsigned sel, int gi, int kind) { return ws + hoff(sel, gi, kind); }
DEV unsigned char* flagS(unsigned char* ws, unsigned sel, int s, int id) { return hblk(ws, sel, s, 3) + id * 128; }
DEV unsigned char* flagY(unsigned char* ws, unsigned sel, int s, int v) { return hblk(ws, sel, 64 + s, 3) + v * 128; }
DEV unsigned char* flagT(unsigned char* ws, unsigned sel, int s, int h) { return hblk(ws, sel, 64 + s, 1) + h * 128; }
DEV void wait_flags(unsigned char* ws, const unsigned char* p) {
  for (int it = 0;; ++it) {
    if (!__any(ld_cnt((const unsigned*)p) == SENT)) return;
    if ((it & 63) == 63) {
      if (it >= SPIN_CAP) { const unsigned one = 1; asm volatile("global_store_dword %0, %1, off sc1" :: "v"(cntp(ws, C_ABORT)), "v"(one) : "memory"); return; }
      if (__any(ld_cnt(cntp(ws, C_ABORT)) != 0)) return;
    }
    __builtin_amdgcn_s_sleep(6);
  }
}
DEV f16v mfma32(u4v a, u4v b, f16v c) { return __builtin_amdgcn_mfma_f32_32x32x16_bf16(asbf8(a), asbf8(b), c, 0, 0, 0); }
DEV f4v mfma16(u4v a, u4v b, f4v c) { return __builtin_amdgcn_mfma_f32_16x16x32_bf16(asbf8(a), asbf8(b), c, 0, 0, 0); }
DEV void st16c_asm(void* p, u4v v) { asm volatile("global_store_dwordx4 %0, %1, off sc1\n\ts_nop 1" :: "v"(p), "v"(v) : "memory"); }

#define RTS(k)
template <bool ISG>
DEV void role_w1(unsigned char* __restrict__ ws, int j, int wave, int lane) {
  const int ct = wave & 3, rh = wave >> 2, hi = lane >> 5, c32 = lane & 31;
  int h, tile0;
  if (ISG) { const int hp = j / 7, ctile = (j % 7) * 4 + ct; h = 2 * hp + ctile / 14; tile0 = 2 * (ctile % 14); }
  else { const int V = j >> 2; h = 4 * (j & 3) + ct; tile0 = 2 * V; }
  const unsigned char* xn = ws + O_XN + wave * 7168 + lane * 16;
#pragma unroll
  for (int i = 0; i < 7; ++i) dma16(xn + i * 1024, wave * 7168 + i * 1024);
  u4v w[28];
  {
    const unsigned char* p = ws + (ISG ? O_WA : O_WB) + (size_t)(h * 28 + tile0 + (c32 >> 4)) * 14336 + hi * 256 + (c32 & 15) * 16;
#pragma unroll
    for (int s = 0; s < 28; ++s) w[s] = *(const u4v*)(p + (s >> 1) * 1024 + (s & 1) * 512);
  }
  constexpr int SEGB = ISG ? 14336 : 16384;
  unsigned char* out = (ISG ? ws + O_G + (size_t)(h * 2 + (c32 >> 4)) * G_RUN + (tile0 >> 1) * 1024
                            : ws + O_U + (size_t)(tile0 + (c32 >> 4)) * U_RUN + h * 1024) + rh * SEGB + hi * 256 + (c32 & 15) * 16;
  const int tok = ISG ? c32 : ((c32 & 19) | ((c32 >> 1) & 4) | ((c32 << 1) & 8));
  const unsigned rd = rh * 28672 + hi * 512 + tok * 16;
  unsigned* done = ISG ? doneG(ws, j) : doneU(ws, j);
#pragma unroll
  for (int s = 0; s < 28; ++s) asm volatile("" : "+v"(w[s]));
  vm0();
  RTS(1);
  for (int p = 0; p < 32; ++p) {
    asm volatile("s_waitcnt vmcnt(2)\n\ts_barrier" ::: "memory");
    if (wave == 7 && p >= 6) post(done, p - 1, lane);
    const unsigned buf = (p & 1) * 57344;
    const unsigned char* src = xn + (size_t)(p < 31 ? p + 1 : p) * 57344;
    const unsigned dstl = (57344 - buf) + wave * 7168;
    f16v acc;
#pragma unroll
    for (int i = 0; i < 16; ++i) acc[i] = 0.f;
    const LDS u4v* xp = (const LDS u4v*)(size_t)(buf + rd);
    u4v xr[5];
#pragma unroll
    for (int s = 0; s < 4; ++s) xr[s] = xp[s * 64];
#pragma unroll
    for (int s = 0; s < 28; ++s) {
      if (s + 4 < 28) xr[(s + 4) % 5] = xp[(s + 4) * 64];
      acc = ISG ? mfma32(w[s], xr[s % 5], acc) : mfma32(xr[s % 5], w[s], acc);
      __builtin_amdgcn_sched_barrier(0);
      if (s % 3 == 0 && s / 3 < 7) { dma16(src + (s / 3) * 1024, dstl + (s / 3) * 1024); __builtin_amdgcn_sched_barrier(0); }
      if (s == 16) {
        asm volatile("s_waitcnt vmcnt(6)" ::: "memory");
        __builtin_amdgcn_sched_barrier(0);
      }
    }
    const u4v o0 = {pk2(acc[0], acc[1]), pk2(acc[2], acc[3]), pk2(acc[4], acc[5]), pk2(acc[6], acc[7])};
    const u4v o1 = {pk2(acc[8], acc[9]), pk2(acc[10], acc[11]), pk2(acc[12], acc[13]), pk2(acc[14], acc[15])};
    unsigned char* o = out + p * 2 * SEGB;
    st16c_asm(o, o0); st16c_asm(o + 512, o1);
    if (p < 4) {
      RTS(4);
      asm volatile("s_waitcnt vmcnt(0)\n\ts_barrier" ::: "memory");
      if (wave == 7) post(done, p + 1, lane);
      RTS(2);
    }
  }
  asm volatile("s_waitcnt vmcnt(0)\n\ts_barrier" ::: "memory");
  if (wave == 7) post(done, 32, lane);
  RTS(3);
}

template <bool P2>
DEV void ffn_body(unsigned char* __restrict__ ws, const unsigned short* __restrict__ x, const unsigned short* __restrict__ b1,
                  const unsigned short* __restrict__ b2, int job, int wave, int lane) {
  const int hi = lane >> 5, c32 = lane & 31;
  constexpr int NL = P2 ? 60 : 28, RING = P2 ? 12 : 14, DIST = RING - 1;
  const u4v* w1p = (const u4v*)(ws + O_W1 + (size_t)wave * 28672) + lane;
  const u4v* w2p = (const u4v*)(ws + O_W2 + (size_t)(2 * wave) * 114688) + lane;
  auto wload = [&](int c, int i) -> u4v {
    if (i < 28) return w1p[(size_t)c * 14336 + i * 64];
    return w2p[(size_t)c * 1024 + ((i - 28) >> 1) * 64 + ((i - 28) & 1) * 7168];
  };
  u4v wr[RING];
#pragma unroll
  for (int i = 0; i < DIST; ++i) wr[i] = wload(0, i);
  {
    const unsigned char* xn = ws + O_XN + (size_t)job * 57344 + wave * 7168 + lane * 16;
#pragma unroll
    for (int i = 0; i < 7; ++i) dma16(xn + i * 1024, wave * 7168 + i * 1024);
  }
  f16v o[2][2];
#pragma unroll
  for (int a = 0; a < 2; ++a)
#pragma unroll
    for (int bq = 0; bq < 2; ++bq)
#pragma unroll
      for (int i = 0; i < 16; ++i) o[a][bq][i] = 0.f;
  vm0();
  bar();
  const LDS u4v* xp = (const LDS u4v*)(size_t)(lane * 16);
  for (int c = 0; c < 7; ++c) {
    const int cn = c < 6 ? c + 1 : 6;
    const unsigned hbuf = 57344 + (c & 1) * 32768;
    u2v bv[4];
#pragma unroll
    for (int q = 0; q < 4; ++q) bv[q] = *(const u2v*)(b1 + 32 * (8 * c + wave) + 4 * hi + 8 * q);
    f16v a0, a1;
#pragma unroll
    for (int i = 0; i < 16; ++i) { a0[i] = 0.f; a1[i] = 0.f; }
    u4v xa[3], xb[3];
#pragma unroll
    for (int s = 0; s < 2; ++s) { xa[s] = xp[s * 64]; xb[s] = xp[1792 + s * 64]; }
#pragma unroll
    for (int s = 0; s < 28; ++s) {
      wr[(s + DIST) % RING] = (s + DIST < NL) ? wload(c, s + DIST) : wload(cn, s + DIST - NL);
      if (s + 2 < 28) { xa[(s + 2) % 3] = xp[(s + 2) * 64]; xb[(s + 2) % 3] = xp[1792 + (s + 2) * 64]; }
      a0 = mfma32(wr[s % RING], xa[s % 3], a0);
      a1 = mfma32(wr[s % RING], xb[s % 3], a1);
      __builtin_amdgcn_sched_barrier(0);
    }
#pragma unroll
    for (int i = 0; i < 16; ++i) {
      const unsigned bw = bv[i >> 2][(i >> 1) & 1];
      const float bs = (i & 1) ? bfhi(bw) : bflo(bw);
      const float t0 = fmaxf(a0[i] + bs, 0.f), t1 = fmaxf(a1[i] + bs, 0.f);
      a0[i] = t0 * t0; a1[i] = t1 * t1;
    }
#pragma unroll
    for (int bq = 0; bq < 2; ++bq) {
      lds_w16(hbuf + ((wave * 2 + bq) * 2 + 0) * 1024 + lane * 16,
              (u4v){pk2(a0[8 * bq], a0[8 * bq + 1]), pk2(a0[8 * bq + 2], a0[8 * bq + 3]), pk2(a0[8 * bq + 4], a0[8 * bq + 5]), pk2(a0[8 * bq + 6], a0[8 * bq + 7])});
      lds_w16(hbuf + ((wave * 2 + bq) * 2 + 1) * 1024 + lane * 16,
              (u4v){pk2(a1[8 * bq], a1[8 * bq + 1]), pk2(a1[8 * bq + 2], a1[8 * bq + 3]), pk2(a1[8 * bq + 4], a1[8 * bq + 5]), pk2(a1[8 * bq + 6], a1[8 * bq + 7])});
    }
    bar();
    if (P2) {
      const LDS u4v* hp = (const LDS u4v*)(size_t)(hbuf + lane * 16);
      u4v h0[2], h1[2];
      h0[0] = hp[0]; h1[0] = hp[64];
#pragma unroll
      for (int s = 0; s < 16; ++s) {
        const int i = 28 + 2 * s;
        wr[(i + DIST) % RING] = (i + DIST < NL) ? wload(c, i + DIST) : wload(cn, i + DIST - NL);
        if (s + 1 < 16) { h0[(s + 1) & 1] = hp[(s + 1) * 128]; h1[(s + 1) & 1] = hp[(s + 1) * 128 + 64]; }
        o[0][0] = mfma32(wr[i % RING], h0[s & 1], o[0][0]); o[0][1] = mfma32(wr[i % RING], h1[s & 1], o[0][1]);
        __builtin_amdgcn_sched_barrier(0);
        wr[(i + 1 + DIST) % RING] = (i + 1 + DIST < NL) ? wload(c, i + 1 + DIST) : wload(cn, i + 1 + DIST - NL);
        o[1][0] = mfma32(wr[(i + 1) % RING], h0[s & 1], o[1][0]); o[1][1] = mfma32(wr[(i + 1) % RING], h1[s & 1], o[1][1]);
        __builtin_amdgcn_sched_barrier(0);
      }
    }
  }
  if (P2) {
    const rsrc_t rf = mkrsrc(ws + O_F, 4u << 20);
#pragma unroll
    for (int a = 0; a < 2; ++a)
#pragma unroll
      for (int sg = 0; sg < 2; ++sg)
#pragma unroll
        for (int Q = 0; Q < 4; ++Q) {
          const int n = 32 * (2 * wave + a) + 8 * Q + 4 * hi, seg = 2 * job + sg;
          const u2v xv = *(const u2v*)(x + (size_t)(seg * 32 + c32) * DM + n), bv = *(const u2v*)(b2 + n);
          const f4v r = {o[a][sg][4 * Q] + bflo(bv.x) + bflo(xv.x), o[a][sg][4 * Q + 1] + bfhi(bv.x) + bfhi(xv.x),
                         o[a][sg][4 * Q + 2] + bflo(bv.y) + bflo(xv.y), o[a][sg][4 * Q + 3] + bfhi(bv.y) + bfhi(xv.y)};
          const int v = 2 * (2 * wave + a) + (Q >> 1), g = 2 * (Q & 1) + hi;
          stc16(rf, ((seg * 28 + v) * 2 + (c32 >> 4)) * 1024 + (g * 16 + (c32 & 15)) * 16, __builtin_bit_cast(u4v, r));
        }
    vm0();
    add_cnt(cntp(ws, C_F + job), lane);
    RTS(3);
  }
}
DEV void role_ffn(unsigned char* __restrict__ ws, const unsigned short* __restrict__ x, const unsigned short* __restrict__ b1,
                  const unsigned short* __restrict__ b2, int job, int wave, int lane) {
  if (wave < 7) ffn_body<true>(ws, x, b1, b2, job, wave, lane);
  else ffn_body<false>(ws, x, b1, b2, job, wave, lane);
}

DEV float asf(unsigned u) { return __builtin_bit_cast(float, u); }
DEV unsigned asu(float f) { return __builtin_bit_cast(unsigned, f); }
DEV float xmax16_32(float v) {
  const u2v a = __builtin_amdgcn_permlane16_swap(asu(v), asu(v), false, false);
  v = fmaxf(asf(a.x), asf(a.y));
  const u2v c = __builtin_amdgcn_permlane32_swap(asu(v), asu(v), false, false);
  return fmaxf(asf(c.x), asf(c.y));
}
DEV float xsum16_32(float v) {
  const u2v a = __builtin_amdgcn_permlane16_swap(asu(v), asu(v), false, false);
  v = asf(a.x) + asf(a.y);
  const u2v c = __builtin_amdgcn_permlane32_swap(asu(v), asu(v), false, false);
  return asf(c.x) + asf(c.y);
}
DEV float halfsum(float a, float b) {
  const u2v s = __builtin_amdgcn_permlane32_swap(asu(a), asu(b), false, false);
  return asf(s.x) + asf(s.y);
}

DEV void role_stt(unsigned char* __restrict__ ws, int grp, int hg, int wave, int lane, unsigned sel) {
  const int g = lane >> 4, c16 = lane & 15, h = 8 * hg + wave;
  const rsrc_t rg = mkrsrc(ws + O_G, 30u << 20);
  const LDS u4v* xp = (const LDS u4v*)(size_t)((g >> 1) * 1024 + (g & 1) * 512 + c16 * 16);
  for (int s = grp; s < NSEG; s += 8) {
    if (wave == 7) wait_cnt(ws, doneG(ws, 28 * hg + min(lane, 27)), (s >> 1) + 1, 32);
    bar();
    if (wave < 7) {
#pragma unroll
      for (int i = 0; i < 4; ++i) dma16(ws + O_XN + (size_t)s * 28672 + (wave * 4 + i) * 1024 + lane * 16, (wave * 4 + i) * 1024);
    }
    u4v ga[28];
#pragma unroll
    for (int T = 0; T < 28; ++T) ga[T] = ldc16(rg, (h * 2 + T / 14) * G_RUN + s * 14336 + (T % 14) * 1024 + lane * 16);
    __builtin_amdgcn_sched_barrier(0);
    asm volatile("s_waitcnt vmcnt(28)\n\ts_barrier" ::: "memory");
    f4v acc[2][2];
#pragma unroll
    for (int q = 0; q < 2; ++q) { acc[0][q] = (f4v){0.f, 0.f, 0.f, 0.f}; acc[1][q] = acc[0][q]; }
#pragma unroll
    for (int T = 0; T < 14; ++T) {
      const u4v x0 = xp[T * 128], x1 = xp[T * 128 + 16];
      acc[0][0] = mfma16(ga[T], x0, acc[0][0]); acc[0][1] = mfma16(ga[T], x1, acc[0][1]);
      acc[1][0] = mfma16(ga[14 + T], x0, acc[1][0]); acc[1][1] = mfma16(ga[14 + T], x1, acc[1][1]);
    }
    unsigned char* o = ws + O_ST + (size_t)(s * 16 + h) * 2048 + c16 * 32 + g * 8;
#pragma unroll
    for (int q = 0; q < 2; ++q)
#pragma unroll
      for (int t = 0; t < 2; ++t) {
        const u2v val = {pk2(acc[t][q][0], acc[t][q][1]), pk2(acc[t][q][2], acc[t][q][3])};
        asm volatile("global_store_dwordx2 %0, %1, off sc1" :: "v"(o + q * 1024 + t * 512), "v"(val) : "memory");
      }
    vm0();
    if (lane == 0) { const unsigned zero = 0; asm volatile("global_store_dword %0, %1, off sc1" :: "v"(flagT(ws, sel, s, h)), "v"(zero) : "memory"); }
  }
  RTS(3);
}

constexpr int W3_GROUPS = 12;
DEV void role_w3(unsigned char* __restrict__ ws, unsigned short* __restrict__ out, int grp, int C, int wave, int lane, unsigned sel) {
  const int g = lane >> 4, c16 = lane & 15, v = 7 * C + (wave < 7 ? wave : 6);
  const rsrc_t ru = mkrsrc(ws + O_U, 30u << 20), rf = mkrsrc(ws + O_F, 4u << 20);
  constexpr unsigned L_P = 0, L_PM = 32768, L_ST = 40960, L_SM = L_ST + 32768, L_Y = L_SM + 32768;
  for (int s = grp; s < NSEG; s += W3_GROUPS) {
    if (wave == 7) {
      wait_cnt(ws, doneU(ws, 4 * ((7 * C) >> 1) + (lane & 15)), (s >> 1) + 1);
      wait_cnt(ws, cntp(ws, C_F + (s >> 1)), 7);
      wait_flags(ws, flagT(ws, sel, s, lane & 15));
    }
    bar();
    u4v uf[16], ff[2];
    if (wave < 7) {
#pragma unroll
      for (int hh = 0; hh < 16; ++hh) uf[hh] = ldc16(ru, v * U_RUN + (s * 16 + hh) * 1024 + lane * 16);
#pragma unroll
      for (int q = 0; q < 2; ++q) ff[q] = ldc16(rf, ((s * 28 + v) * 2 + q) * 1024 + lane * 16);
    } else {
      wait_flags(ws, flagY(ws, sel, s, 7 * C + min(lane, 6)));
      wait_flags(ws, flagS(ws, sel, s, lane));
    }
    __builtin_amdgcn_sched_barrier(0);
    bar();
#pragma unroll
    for (int it = 0; it < 4; ++it) {
      dma16c(ws + O_ST + (size_t)(s * 32 + wave * 4 + it) * 1024 + lane * 16, L_ST + (wave * 4 + it) * 1024);
      dma16c(ws + O_SM + (size_t)(s * 32 + wave * 4 + it) * 1024 + lane * 16, L_SM + (wave * 4 + it) * 1024);
    }
    if (wave < 7) {
#pragma unroll
      for (int H = 0; H < 4; ++H) dma16c(ws + O_YG + (size_t)v * Y_RUN + (s * 4 + H) * 1024 + lane * 16, L_Y + (wave * 4 + H) * 1024);
      asm volatile("s_waitcnt vmcnt(4)" ::: "memory");
    } else vm0();
#pragma unroll
    for (int it = 0; it < 4; ++it) {
      const int q = it & 1, hh = 2 * wave + (it >> 1), tq = 16 * q + c16;
      const u4v st = lds_r16(L_ST + (wave * 4 + it) * 1024 + (g >> 1) * 512 + c16 * 32 + (g & 1) * 16);
      const LDS unsigned* sm = (const LDS unsigned*)(size_t)(L_SM + (wave * 4 + it) * 1024 + lane * 4);
      const unsigned s0 = sm[0], s1 = sm[64], s2 = sm[128], s3 = sm[192];
      float e[8];
      float m0 = (bflo(s0) + bflo(s1)) + (bflo(s2) + bflo(s3));
      float m1 = (bfhi(s0) + bfhi(s1)) + (bfhi(s2) + bfhi(s3));
#pragma unroll
      for (int i = 0; i < 8; ++i) { const unsigned w = st[i >> 1]; e[i] = (8 * g + i <= tq) ? ((i & 1) ? bfhi(w) : bflo(w)) : -__builtin_inff(); }
      float mx = fmaxf(m0, m1);
#pragma unroll
      for (int i = 0; i < 8; ++i) mx = fmaxf(mx, e[i]);
      mx = xmax16_32(mx);
      m0 = __builtin_amdgcn_exp2f(m0 - mx); m1 = __builtin_amdgcn_exp2f(m1 - mx);
      float sum = m0 + m1;
#pragma unroll
      for (int i = 0; i < 8; ++i) { e[i] = __builtin_amdgcn_exp2f(e[i] - mx); sum += e[i]; }
      const float r = __builtin_amdgcn_rcpf(xsum16_32(sum));
      lds_w16(L_P + (q * 16 + hh) * 1024 + lane * 16,
              (u4v){pk2(e[0] * r, e[1] * r), pk2(e[2] * r, e[3] * r), pk2(e[4] * r, e[5] * r), pk2(e[6] * r, e[7] * r)});
      *(LDS unsigned*)(size_t)(L_PM + (q * 4 + (hh >> 2)) * 1024 + lane * 16 + (hh & 3) * 4) = pk2(m0 * r, m1 * r);
    }
    vm0();
    bar();
    if (wave < 7) {
      u4v yf[4];
#pragma unroll
      for (int H = 0; H < 4; ++H) {
        const u2v lo = *(const LDS u2v*)(size_t)(L_Y + (wave * 4 + H) * 1024 + lane * 8), hi = *(const LDS u2v*)(size_t)(L_Y + (wave * 4 + H) * 1024 + 512 + lane * 8);
        yf[H] = (u4v){lo.x, lo.y, hi.x, hi.y};
      }
      f4v a0 = __builtin_bit_cast(f4v, ff[0]), a1 = __builtin_bit_cast(f4v, ff[1]);
      const LDS u4v* pp = (const LDS u4v*)(size_t)(L_P + lane * 16);
      u4v p0[4], p1[4];
#pragma unroll
      for (int k = 0; k < 3; ++k) { p0[k] = pp[k * 64]; p1[k] = pp[(16 + k) * 64]; }
#pragma unroll
      for (int k = 0; k < 20; ++k) {
        const int n = k + 3;
        if (n < 16) { p0[n & 3] = pp[n * 64]; p1[n & 3] = pp[(16 + n) * 64]; }
        else if (n < 20) { p0[n & 3] = pp[(32 + n - 16) * 64]; p1[n & 3] = pp[(36 + n - 16) * 64]; }
        const u4v w = k < 16 ? uf[k] : yf[k - 16];
        a0 = mfma16(w, p0[k & 3], a0); a1 = mfma16(w, p1[k & 3], a1);
        __builtin_amdgcn_sched_barrier(0);
      }
      *(u2v*)(out + (size_t)(s * 32 + c16) * DM + v * 16 + g * 4) = (u2v){pk2(a0[0], a0[1]), pk2(a0[2], a0[3])};
      *(u2v*)(out + (size_t)(s * 32 + 16 + c16) * DM + v * 16 + g * 4) = (u2v){pk2(a1[0], a1[1]), pk2(a1[2], a1[3])};
    }
  }
  RTS(4);
}
#define TUNE_ARG
#define TUNE_PASS
#define K_PERIOD 760
#define K_DS 1560
#define K_MS 43
#define K_MM 50
#define K_LEAD 96
#define K_FIXED 0
constexpr int K_SMK = 2, K_DDE = 250;
constexpr int C_PT = 2048;
DEV unsigned* ptp(unsigned char* ws, int s) { return cntp(ws, C_PT + 2 * s); }
DEV unsigned ptword(int period, int ds) { return (unsigned)(period / 24) | ((unsigned)(period % 24) << 8) | ((unsigned)(ds - K_DDE - 80) << 16); }
DEV unsigned char* flagR(unsigned char* ws, unsigned sel, int id) { return hblk(ws, sel, 64 + (id >> 6), 0) + (id & 63) * 128; }
#define DBG(slot, v)
#define ST(n)
#define ARR(s_)
#define EV(code)
#define EV_OUT(base)
#define ST_CLOB
#define ST_OUT(base, ref)
constexpr int RET_E = 26;
constexpr int TRY_CAP = 1 << 16;

DEV int rt32() { unsigned long long t; asm volatile("s_memrealtime %0\n\ts_waitcnt lgkmcnt(0)" : "=s"(t) :: "memory"); return (int)(unsigned)t; }
struct Lat { int anchor, e, f; };
DEV void lat_fwd(Lat& L, int pe, int pf) { L.f += pf; const int c = L.f >= 24; L.f -= c ? 24 : 0; L.e += pe + c; }
DEV void lat_back(Lat& L, int pe, int pf) { L.f -= pf; const int c = L.f < 0; L.f += c ? 24 : 0; L.e -= pe + c; }
DEV void lat_prep(const Lat& L, int& tgt, int& jmp) { tgt = L.anchor + 4 * L.e; jmp = 108 - 4 * L.f; }
#define TPAIR(r, m) "s_memrealtime s[" #r ":" #r "+1]\n\ts_memtime s[" #m ":" #m "+1]\n\ts_nop 0\n\t"
#define TSEL(r, m) "s_cmp_lg_u32 s" #r ", s4\n\ts_cselect_b32 s58, s" #m ", s58\n\t"
#define TBURST \
  TPAIR(4, 6) TPAIR(8, 10) TPAIR(12, 14) TPAIR(16, 18) TPAIR(20, 22) TPAIR(24, 26) TPAIR(28, 30) TPAIR(52, 54) TPAIR(56, 58) \
  "s_waitcnt lgkmcnt(0)\n\t" TSEL(52, 54) TSEL(28, 30) TSEL(24, 26) TSEL(20, 22) TSEL(16, 18) TSEL(12, 14) TSEL(8, 10)
#define TBURST_CLOB "s4","s5","s6","s7","s8","s9","s10","s11","s12","s13","s14","s15","s16","s17","s18","s19","s20","s21","s22","s23", \
  "s24","s25","s26","s27","s28","s29","s30","s31","s52","s53","s54","s55","s56","s57","s58","s59"
DEV void lat_commit(Lat& L, int tgt, int jmp) {
  if (tgt == (int)0x80000000) return;
  int r = tgt - rt32();
  while (__builtin_expect(r < 0, 0)) { L.e += RET_E; tgt += 4 * RET_E; r += 4 * RET_E; }
  if (r > 0) {
    int d;
#define RT_STEP "s_memrealtime vcc\n\ts_waitcnt lgkmcnt(0)\n\ts_sub_i32 %0, %1, vcc_lo\n\ts_cmp_lt_i32 %0, 1\n\t"
    asm volatile("1:\n\t" RT_STEP "s_cbranch_scc1 2f\n\t" RT_STEP "s_cbranch_scc1 2f\n\t" RT_STEP "s_cbranch_scc1 2f\n\t"
                 RT_STEP "s_cbranch_scc0 1b\n\t"
                 "2:\n\t"
                 "s_getpc_b64 vcc\n\ts_add_u32 vcc_lo, vcc_lo, %2\n\ts_addc_u32 vcc_hi, vcc_hi, 0\n\ts_setpc_b64 vcc\n\t"
                 ".rept 24\n\ts_nop 0\n\t.endr"
                 : "=&s"(d) : "s"(tgt), "s"(jmp) : "scc", "vcc", "memory");
  }
}
DEV unsigned lat_wait(Lat& L) {
  int tgt, jmp;
  unsigned est;
  lat_prep(L, tgt, jmp);
  if (tgt - rt32() < 16) { lat_commit(L, tgt, jmp); return 0; }
  asm volatile("1:\n\ts_memrealtime vcc\n\ts_waitcnt lgkmcnt(0)\n\ts_sub_i32 s4, %1, vcc_lo\n\ts_cmp_gt_i32 s4, 16\n\ts_cbranch_scc1 1b\n\t"
               TBURST
               "s_sub_i32 s4, %1, s4\n\ts_mul_i32 s4, s4, 93\n\ts_lshr_b32 s4, s4, 2\n\t"
               "s_add_i32 s58, s58, s4\n\ts_add_i32 s58, s58, %2\n\ts_or_b32 %0, s58, 1\n\t"
               "s_memtime vcc\n\ts_waitcnt lgkmcnt(0)\n\t"
               "s_sub_i32 s4, s58, vcc_lo\n\ts_max_i32 s4, s4, 0\n\ts_min_i32 s4, s4, 512\n\ts_and_b32 s4, s4, -4\n\ts_sub_i32 s4, 524, s4\n\t"
               "s_getpc_b64 vcc\n\ts_add_u32 vcc_lo, vcc_lo, s4\n\ts_addc_u32 vcc_hi, vcc_hi, 0\n\ts_setpc_b64 vcc\n\t"
               ".rept 128\n\ts_nop 0\n\t.endr"
               : "=&s"(est) : "s"(tgt), "s"(4 * L.f - 93) : TBURST_CLOB, "scc", "vcc", "memory");
  return est;
}
DEV unsigned* anchorp(unsigned char* ws, int id) { return cntp(ws, C_ANCHOR + 2 * (id & 31)); }
DEV int get_anchor(unsigned char* ws, unsigned sel, int id, int lane) {
  post((unsigned*)flagR(ws, sel, id), 0u, lane);
  unsigned v = 0;
  for (int it = 0; it < SPIN_CAP && v == 0; ++it) v = __builtin_amdgcn_readfirstlane(ld_cnt(anchorp(ws, id)));
  const int n = rt32();
  const int d = (int)((v & ~1u) - ((unsigned)n << 1)) >> 1;
  return (d > 4096 || d < -(1 << 22)) ? n : n + d;
}
DEV f4v smf(u4v a, u8v b, f4v c, int idx) {
  return __builtin_amdgcn_smfmac_f32_16x16x64_bf16(asbf8(a), __builtin_bit_cast(bf16x, b), c, idx, 0, 0);
}
DEV unsigned umax4(u4v v) { return max(max(v.x, v.y), max(v.z, v.w)); }
DEV float dpp_ror8(float v) { return __builtin_bit_cast(float, __builtin_amdgcn_update_dpp(0, __builtin_bit_cast(int, v), 0x128, 0xf, 0xf, true)); }
DEV float dpp_xor1(float v) { return __builtin_bit_cast(float, __builtin_amdgcn_update_dpp(0, __builtin_bit_cast(int, v), 0xb1, 0xf, 0xf, true)); }
DEV void st4c_asm(void* p, unsigned v) { asm volatile("global_store_dword %0, %1, off sc1" :: "v"(p), "v"(v) : "memory"); }
DEV void st8c_asm(void* p, u2v v) { asm volatile("global_store_dwordx2 %0, %1, off sc1" :: "v"(p), "v"(v) : "memory"); }
DEV void ld16c_asm(u4v& v, const void* p) { asm volatile("global_load_dwordx4 %0, %1, off sc1" : "=&v"(v) : "v"(p) : "memory"); }
DEV void rawbar() { asm volatile("s_barrier" ::: "memory"); }
DEV unsigned lds_r4(unsigned a) { return *(volatile LDS unsigned*)(size_t)a; }
DEV void lds_w4(unsigned a, unsigned v) { *(volatile LDS unsigned*)(size_t)a = v; }
DEV u8v cat8(u4v a, u4v b) { return (u8v){a.x, a.y, a.z, a.w, b.x, b.y, b.z, b.w}; }
DEV void set_abort(unsigned char* ws) { st4c_asm(cntp(ws, C_ABORT), 1u); }

constexpr unsigned L_MEM = 0, L_KT = 8192, L_FLAG = 12288, L_RED = 16384;
constexpr unsigned L_EST = 12304, L_TS = 12308, L_CG = 12544, L_G = 32768, L_XN = 65536;
#define CLOB_V200_247 "v200","v201","v202","v203","v204","v205","v206","v207","v208","v209","v210","v211","v212","v213","v214","v215", \
  "v216","v217","v218","v219","v220","v221","v222","v223","v224","v225","v226","v227","v228","v229","v230","v231", \
  "v232","v233","v234","v235","v236","v237","v238","v239","v240","v241","v242","v243","v244","v245","v246","v247","v248"
#define SMF "v_smfmac_f32_16x16x64_bf16 "
#define R1_HEAD(o0, o1, o2, o3, o4, o5, o6, FIRST) \
  "v_mov_b32 v228, 0\n\tv_mov_b32 v229, 0\n\tv_mov_b32 v230, 0\n\tv_mov_b32 v231, 0\n\t" \
  "v_mov_b32 v232, 0\n\tv_mov_b32 v233, 0\n\tv_mov_b32 v234, 0\n\tv_mov_b32 v235, 0\n\t" \
  "v_mov_b32 v244, 0\n\tv_mov_b32 v245, 0\n\tv_mov_b32 v246, 0\n\tv_mov_b32 v247, 0\n\t" \
  "s_mov_b32 m0, %[m0v]\n\t" \
  "s_barrier\n\t" ST(80) \
  "global_load_lds_dwordx4 %[moff], %[src] sc1\n\t" \
  "s_waitcnt vmcnt(0)\n\t" \
  "ds_read_b128 v[200:203], %[rd] offset:" #o0 "*1024\n\t" \
  "s_barrier\n\t" ST(82) \
  "ds_read_b128 v[204:207], %[rd] offset:" #o1 "*1024\n\tds_read_b128 v[208:211], %[rd] offset:" #o2 "*1024\n\t" \
  "ds_read_b128 v[212:215], %[rd] offset:" #o3 "*1024\n\tds_read_b128 v[216:219], %[rd] offset:" #o4 "*1024\n\t" \
  "ds_read_b128 v[220:223], %[rd] offset:" #o5 "*1024\n\tds_read_b128 v[224:227], %[rd] offset:" #o6 "*1024\n\t"
#define R1_TWO(n6, n5, n4, n3, n2, n1, n0) \
  "s_waitcnt lgkmcnt(" #n6 ")\n\t" ST(84) SMF "v[228:231], v[200:203], %[a0], %[idx]\n\t" SMF "v[232:235], v[200:203], %[b0], %[idx]\n\t" \
  "s_waitcnt lgkmcnt(" #n5 ")\n\t" SMF "v[228:231], v[204:207], %[a1], %[idx]\n\t" SMF "v[232:235], v[204:207], %[b1], %[idx]\n\t" \
  "s_waitcnt lgkmcnt(" #n4 ")\n\t" SMF "v[228:231], v[208:211], %[a2], %[idx]\n\t" SMF "v[232:235], v[208:211], %[b2], %[idx]\n\t" \
  "s_waitcnt lgkmcnt(" #n3 ")\n\t" SMF "v[228:231], v[212:215], %[a3], %[idx]\n\t" SMF "v[232:235], v[212:215], %[b3], %[idx]\n\t" \
  "s_waitcnt lgkmcnt(" #n2 ")\n\t" SMF "v[228:231], v[216:219], %[a4], %[idx]\n\t" SMF "v[232:235], v[216:219], %[b4], %[idx]\n\t" \
  "s_waitcnt lgkmcnt(" #n1 ")\n\t" SMF "v[228:231], v[220:223], %[a5], %[idx]\n\t" \
  "s_waitcnt lgkmcnt(" #n0 ")\n\t" SMF "v[228:231], v[224:227], %[a6], %[idx]\n\t" \
  SMF "v[232:235], v[220:223], %[b5], %[idx]\n\t" SMF "v[232:235], v[224:227], %[b6], %[idx]\n\t" \
  ST(86) "s_nop 1\n\t" \
  "v_cvt_pk_bf16_f32 v228, v228, v229\n\tv_cvt_pk_bf16_f32 v229, v230, v231\n\t" \
  "ds_write_b64 %[ktw], v[228:229]\n\t" \
  "s_nop 2\n\t" \
  "v_cvt_pk_bf16_f32 v232, v232, v233\n\tv_cvt_pk_bf16_f32 v233, v234, v235\n\t" \
  "ds_write_b64 %[ktw], v[232:233] offset:512\n\t" \
  LDSWAIT ST(88) \
  "s_barrier\n\t" ST(90)
#define LDSWAIT
#define R1_WOPS [a0] "v"(w0[0]), [a1] "v"(w0[1]), [a2] "v"(w0[2]), [a3] "v"(w0[3]), [a4] "v"(w0[4]), [a5] "v"(w0[5]), [a6] "v"(w0[6])
#define R1_WOPS2 [b0] "v"(w1[0]), [b1] "v"(w1[1]), [b2] "v"(w1[2]), [b3] "v"(w1[3]), [b4] "v"(w1[4]), [b5] "v"(w1[5]), [b6] "v"(w1[6])
#define R1_COMMON [idx] "v"(idx), [rd] "v"(rd), [ktw] "v"(ktw), [moff] "v"(moff), [src] "s"(src), [m0v] "s"(m0v)

template <int W>
DEV void r1_mfma(unsigned char* __restrict__ ws, int h, int q, int lane, unsigned sel, int smk) {
  constexpr int wave = W, KIND = W == 0 ? 0 : W == 3 ? 2 : 1;
  const int g = lane >> 4, c16 = lane & 15;
  const int idx = (lane & 8) ? 0xEEEE : 0x4444;
  const unsigned moff = wave * 1024 + g * 256 + (lane & 7) * 32 + ((lane >> 3) & 1) * 16;
  u8v w0[7], w1[7];
  {
    const unsigned char* p = ws + O_WA + (size_t)(h * 28 + 7 * q + 2 * wave) * 14336 + lane * 16;
#pragma unroll
    for (int k = 0; k < 7; ++k) {
      const int o = ((k + W) % 7) * 2048;
      w0[k] = cat8(*(const u4v*)(p + o), *(const u4v*)(p + o + 1024));
      if (KIND != 2) w1[k] = cat8(*(const u4v*)(p + 14336 + o), *(const u4v*)(p + 14336 + o + 1024));
    }
#pragma unroll
    for (int k = 0; k < 7; ++k) { asm volatile("" : "+v"(w0[k])); if (KIND != 2) asm volatile("" : "+v"(w1[k])); }
  }
  RTS(1);
  const unsigned rd = L_MEM + lane * 16, ktw = L_KT + 2 * wave * 512 + c16 * 32 + g * 8, m0v = L_MEM + wave * 1024;
  const unsigned a0a = L_MEM + (7 * q + g) * 256 + c16 * 16, a1a = L_MEM + min(7 * q + 4 + g, 27) * 256 + c16 * 16;
  const unsigned trb = L_KT + (g >> 1) * 512 + (4 * (g & 1) + (c16 >> 2)) * 32 + (c16 & 3) * 8;
  const unsigned sto = h * 512 + q * 128 + (4 * (g & 1) + 2 * (g >> 1) + (c16 >> 3)) * 16 + (c16 & 7) * 2;
  const unsigned long long lo8 = 0x00FF00FF00FF00FFull;
  unsigned ep = 0;
  int budget = TRY_CAP;
  rawbar();
  rawbar();
  for (int s = 0; s < NSEG; ++s) {
    const unsigned char* src = hblk(ws, sel, s, 0);
    unsigned char* dst = hblk(ws, sel, 64 + s, smk);
    for (;;) {
      if (KIND == 0) {
        unsigned long long okm, tst;
        asm volatile(R1_HEAD(0, 1, 2, 3, 4, 5, 6, )
          "ds_read_b128 v[236:239], %[a0a]\n\tds_read_b128 v[240:243], %[a1a]\n\t"
          R1_TWO(8, 7, 6, 5, 4, 3, 2)
          "ds_read_b64_tr_b16 v[200:201], %[trb]\n\tds_read_b64_tr_b16 v[202:203], %[trb] offset:256\n\t"
          "ds_read_b64_tr_b16 v[204:205], %[trb] offset:1024\n\tds_read_b64_tr_b16 v[206:207], %[trb] offset:1280\n\t"
          "ds_read_b64_tr_b16 v[208:209], %[trb] offset:2048\n\tds_read_b64_tr_b16 v[210:211], %[trb] offset:2304\n\t"
          "ds_read_b64_tr_b16 v[212:213], %[trb] offset:3072\n\tds_read_b64_tr_b16 v[214:215], %[trb] offset:3328\n\t"
          "s_waitcnt lgkmcnt(4)\n\t" SMF "v[244:247], v[236:239], v[200:207], %[idx]\n\t"
          "s_waitcnt lgkmcnt(0)\n\t" SMF "v[244:247], v[240:243], v[208:215], %[idx]\n\t"
          "s_nop 7\n\t"
          "v_permlane32_swap_b32 v244, v246\n\tv_permlane32_swap_b32 v245, v247\n\t"
          "v_add_f32 v244, v244, v246\n\tv_add_f32 v245, v245, v247\n\t"
          "v_cndmask_b32 v246, v244, v245, %[lo8]\n\t"
          "v_cndmask_b32 v247, v245, v244, %[lo8]\n\t"
          "s_nop 1\n\t"
          "v_add_f32_dpp v247, v246, v247 row_ror:8 row_mask:0xf bank_mask:0xf\n\t"
          "v_cvt_pk_bf16_f32 v246, v247, v247\n\t"
          "v_cmpx_o_f32 vcc, v247, v247\n\t"
          "global_store_short %[sto], v246, %[dst] sc1\n\t" ST(92)
          "s_memtime %[tst]\n\t"
          "s_mov_b64 exec, -1\n\t"
          "s_mov_b64 %[okm], vcc\n\t"
          "s_waitcnt lgkmcnt(0)\n\t"
          : [okm] "=&s"(okm), [tst] "=&s"(tst)
          : R1_WOPS, R1_WOPS2, R1_COMMON, [a0a] "v"(a0a), [a1a] "v"(a1a), [trb] "v"(trb), [sto] "v"(sto), [dst] "s"(dst), [lo8] "s"(lo8)
          : CLOB_V200_247, "vcc", "memory" ST_CLOB);
        lds_w4(L_TS, (unsigned)tst);
        if (okm != ~0ull) {
          if (budget > 0) { if (lane == 0) lds_w4(L_FLAG, ep); }
          else asm volatile("global_store_short %0, %1, %2 sc1" :: "v"(sto), "v"(0x7FC0u), "s"(dst) : "memory");
        }
      } else if (KIND == 1) {
        if (W == 1) asm volatile(R1_HEAD(1, 2, 3, 4, 5, 6, 0, ) R1_TWO(6, 5, 4, 3, 2, 1, 0) :: R1_WOPS, R1_WOPS2, R1_COMMON : CLOB_V200_247, "memory" ST_CLOB);
        else asm volatile(R1_HEAD(2, 3, 4, 5, 6, 0, 1, ) R1_TWO(6, 5, 4, 3, 2, 1, 0) :: R1_WOPS, R1_WOPS2, R1_COMMON : CLOB_V200_247, "memory" ST_CLOB);
      } else {
        asm volatile(R1_HEAD(3, 4, 5, 6, 0, 1, 2, )
          "s_waitcnt lgkmcnt(6)\n\t" SMF "v[228:231], v[200:203], %[a0], %[idx]\n\t"
          "s_waitcnt lgkmcnt(5)\n\t" SMF "v[228:231], v[204:207], %[a1], %[idx]\n\t"
          "s_waitcnt lgkmcnt(4)\n\t" SMF "v[228:231], v[208:211], %[a2], %[idx]\n\t"
          "s_waitcnt lgkmcnt(3)\n\t" SMF "v[228:231], v[212:215], %[a3], %[idx]\n\t"
          "s_waitcnt lgkmcnt(2)\n\t" SMF "v[228:231], v[216:219], %[a4], %[idx]\n\t"
          "s_waitcnt lgkmcnt(1)\n\t" SMF "v[228:231], v[220:223], %[a5], %[idx]\n\t"
          "s_waitcnt lgkmcnt(0)\n\t" SMF "v[228:231], v[224:227], %[a6], %[idx]\n\t"
          "s_nop 7\n\t"
          "v_cvt_pk_bf16_f32 v228, v228, v229\n\tv_cvt_pk_bf16_f32 v229, v230, v231\n\t"
          "ds_write_b64 %[ktw], v[228:229]\n\t"
          LDSWAIT
          "s_barrier\n\t"
          :: R1_WOPS, R1_COMMON : CLOB_V200_247, "memory" ST_CLOB);
      }
      bar();
      const bool bad = __builtin_amdgcn_readfirstlane(lds_r4(L_FLAG)) == ep;
      ++ep;
      if (!bad) break;
      --budget;
      rawbar();
    }
    ARR(s);
    rawbar();
  }
}

typedef unsigned short h2v __attribute__((ext_vector_type(2)));
DEV unsigned pkmax(unsigned a, unsigned b) { return __builtin_bit_cast(unsigned, __builtin_elementwise_max(__builtin_bit_cast(h2v, a), __builtin_bit_cast(h2v, b))); }
DEV unsigned wavepkmax(unsigned v) {
  v = pkmax(v, __builtin_amdgcn_update_dpp(0u, v, 0xb1, 0xf, 0xf, true));
  v = pkmax(v, __builtin_amdgcn_update_dpp(0u, v, 0x4e, 0xf, 0xf, true));
  v = pkmax(v, __builtin_amdgcn_update_dpp(0u, v, 0x124, 0xf, 0xf, true));
  v = pkmax(v, __builtin_amdgcn_update_dpp(0u, v, 0x128, 0xf, 0xf, true));
  const u2v x = __builtin_amdgcn_permlane16_swap(v, v, false, false);
  v = pkmax(x.x, x.y);
  const u2v y = __builtin_amdgcn_permlane32_swap(v, v, false, false);
  return pkmax(y.x, y.y);
}
DEV void r1_boss(unsigned char* __restrict__ ws, const unsigned short* __restrict__ mem0, int lane, unsigned sel TUNE_ARG) {
  constexpr unsigned L_FA = 13312, L_FY = 13568;
  int A = 0, V = 0, ms = 4 * K_MS, mm = 4 * K_MM;
  float rho = 0.f;
  bool prevf3 = false;
  int run = 0;
  unsigned ep = 0;
  const unsigned c0 = rdtime();
  const int r0 = rt32();
#pragma unroll
  for (int j = 0; j < 7; ++j) {
    const int n = j * 64 + lane;
    st16c_asm(hblk(ws, sel, 0, 0) + n * 16, *(const u4v*)(mem0 + ((n >> 1) & 7) * DM + (n >> 4) * 16 + (n & 1) * 8));
  }
  vm0();
  rawbar();
  const int P0 = (int)((float)K_PERIOD * (0.3f + 0.7f * fmaxf(23.4f * (float)(rt32() - r0) * __builtin_amdgcn_rcpf((float)(rdtime() - c0)), 1.f)));
  int hist = P0;
  if (lane < 5) st4c_asm(ptp(ws, lane), ptword(P0, K_DS));
  for (int it = 0; it < SPIN_CAP; ++it) {
    unsigned x, y;
    asm volatile("global_load_dword %0, %2, off sc1\n\tglobal_load_dword %1, %3, off sc1\n\ts_waitcnt vmcnt(0)"
                 : "=&v"(x), "=&v"(y) : "v"(flagR(ws, sel, lane)), "v"(flagR(ws, sel, 64 + min(lane, 27))) : "memory");
    if (!__any(max(x, y) == SENT)) break;
  }
  if (lane < 32) st4c_asm(anchorp(ws, lane), ((unsigned)(rt32() + K_LEAD) << 1) | 1u);
  for (int t = 0; t < NSEG; ++t) {
    const int s = max(t - 3, 0);
    const float pa = 4.f * (float)__builtin_amdgcn_readlane(hist, max(s - 1, 0)), py = 4.f * (float)__builtin_amdgcn_readlane(hist, s);
    rawbar(); rawbar();
    if (t >= 3) vm0();
    const unsigned a = lds_r4(L_FA + lane * 4), y = lds_r4(L_FY + lane * 4);
    const float da = (float)((a >> 12) & 0xffffu), dy = (float)((y >> 12) & 0xffffu);
    unsigned pk = min((unsigned)((float)(a & 0xfffu) * pa * __builtin_amdgcn_rcpf(da)), 65535u) |
                  (min((unsigned)((float)(y & 0xfffu) * py * __builtin_amdgcn_rcpf(dy)), 65535u) << 16);
    unsigned fl = (max(a, y) == SENT ? 1u : 0u) | ((a >> 28) != 0 ? 2u : 0u) | ((y >> 26) & 4u) |
                  (fabsf(da - 0.86f * pa) > 0.18f * pa ? 8u : 0u) | (fabsf(dy - 0.86f * py) > 0.18f * py ? 16u : 0u) | ((y >> 25) & 16u);
    float rn = dy * __builtin_amdgcn_rcpf(py);
    asm volatile("s_barrier" : "+v"(pk), "+v"(fl), "+v"(rn) :: "memory");
    pk = wavepkmax(pk);
    asm volatile("s_barrier\n\ts_barrier" : "+v"(pk) :: "memory");
    while (__builtin_amdgcn_readfirstlane(lds_r4(L_FLAG)) == ep++) { rawbar(); rawbar(); rawbar(); rawbar(); rawbar(); }
    if (t >= 3 && t + 2 < NSEG) {
      if (!__any(fl & 1)) {
        const unsigned m = __builtin_amdgcn_readfirstlane(pk);
        const bool f1 = __any(fl & 2), f3 = __any(fl & 4);
        if (!__any(fl & 10)) A = A == 0 ? (int)(m & 0xffffu) : A + (((int)(m & 0xffffu) - A) >> 2);
        if (!__any(fl & 20)) { V = V == 0 ? (int)(m >> 16) : V + (((int)(m >> 16) - V) >> 2); rho = rho == 0.f ? rn : rho + 0.25f * (rn - rho); }
        run = (f1 || f3) ? run + 1 : 0;
        if ((f1 && !prevf3) || run > 3) mm = min(mm + 20, 4 * K_MM + 800);
        if ((f3 && !f1) || run > 3) ms = min(ms + 20, 4 * K_MS + 800);
        prevf3 = f3;
      }
      int P = P0 + ((ms + mm) >> 2) - K_MS - K_MM, DS = K_DS + ms - 4 * K_MS;
      if (A > 0 && V > 0 && !K_FIXED) {
        DS = min(max((int)((float)(A + ms) * rho), 1200), 3000);
        P = min(max((A + ms + V - 84 + mm) >> 2, 600), 1500);
      }
      if (lane == t + 2) hist = P;
      post(ptp(ws, t + 2), ptword(P, DS), lane);
    }
    if (t >= 2) {
      asm volatile("s_mov_b32 m0, %2\n\ts_nop 0\n\tglobal_load_lds_dword %0, off sc1\n\ts_mov_b32 m0, %3\n\ts_nop 0\n\tglobal_load_lds_dword %1, off sc1"
                   :: "v"(flagS(ws, sel, t - 2, lane)), "v"(flagY(ws, sel, t - 2, min(lane, 27))), "s"(L_FA), "s"(L_FY) : "memory");
    }
  }
  vm0();
  rawbar();
}

DEV void role_r1(unsigned char* __restrict__ ws, const unsigned short* __restrict__ mem0, int id, int wave, int lane, unsigned sel TUNE_ARG) {
  const int h = id >> 2, q = id & 3;
  if (wave == 0) { r1_mfma<0>(ws, h, q, lane, sel, K_SMK); return; }
  if (wave == 1) { r1_mfma<1>(ws, h, q, lane, sel, K_SMK); return; }
  if (wave == 2) { r1_mfma<2>(ws, h, q, lane, sel, K_SMK); return; }
  if (wave == 3) { r1_mfma<3>(ws, h, q, lane, sel, K_SMK); return; }
  if (wave == 7 && id == 3) { r1_boss(ws, mem0, lane, sel TUNE_PASS); return; }
  const int g = lane >> 4, c16 = lane & 15;
  const bool act = q < 2;
  const int fr = wave - 1;
  const unsigned moff = fr * 1024 + g * 256 + (lane & 7) * 32 + ((lane >> 3) & 1) * 16, m0v = L_MEM + fr * 1024;
  const unsigned xo = L_XN + (g >> 1) * 1024 + (g & 1) * 512 + c16 * 16;
  unsigned ep = 0;
  if (wave == 7) {
    const int idx = (lane & 8) ? 0xEEEE : 0x4444;
    u8v w0[7];
    const unsigned rd = lane * 16;
    const unsigned sto = h * 512 + (q & 1) * 256 + (4 * (g & 1) + 2 * (g >> 1) + (lane & 1)) * 32 + (c16 >> 1) * 4;
    const unsigned long long odd = 0xAAAAAAAAAAAAAAAAull;
    __builtin_amdgcn_s_setprio(3);
    rawbar();
    for (int s = 0; s < NSEG; ++s) {
      unsigned char* dst = hblk(ws, sel, s, 2);
      unsigned char* raw = hblk(ws, sel, s, 1);
      const bool go = act && s < NSEG - 1;
      if (go) {
#pragma unroll
        for (int k = 0; k < 7; ++k) w0[k] = cat8(lds_r16(L_G + (s & 1) * 14336 + rd + k * 2048), lds_r16(L_G + (s & 1) * 14336 + rd + k * 2048 + 1024));
      }
      for (;;) {
        if (go) {
          asm volatile(
            "v_mov_b32 v228, 0\n\tv_mov_b32 v229, 0\n\tv_mov_b32 v230, 0\n\tv_mov_b32 v231, 0\n\t"
            "s_barrier\n\ts_barrier\n\ts_barrier\n\t" ST(80)
            "ds_read_b128 v[200:203], %[rd]\n\tds_read_b128 v[204:207], %[rd] offset:1024\n\tds_read_b128 v[208:211], %[rd] offset:2048\n\t"
            "ds_read_b128 v[212:215], %[rd] offset:3072\n\tds_read_b128 v[216:219], %[rd] offset:4096\n\tds_read_b128 v[220:223], %[rd] offset:5120\n\t"
            "ds_read_b128 v[224:227], %[rd] offset:6144\n\t"
            "s_waitcnt lgkmcnt(6)\n\t" SMF "v[228:231], v[200:203], %[a0], %[idx]\n\t"
            "s_waitcnt lgkmcnt(5)\n\t" SMF "v[228:231], v[204:207], %[a1], %[idx]\n\t"
            "s_waitcnt lgkmcnt(4)\n\t" SMF "v[228:231], v[208:211], %[a2], %[idx]\n\t"
            "s_waitcnt lgkmcnt(3)\n\t" SMF "v[228:231], v[212:215], %[a3], %[idx]\n\t"
            "s_waitcnt lgkmcnt(2)\n\t" SMF "v[228:231], v[216:219], %[a4], %[idx]\n\t"
            "s_waitcnt lgkmcnt(1)\n\t" SMF "v[228:231], v[220:223], %[a5], %[idx]\n\t"
            "s_waitcnt lgkmcnt(0)\n\t" SMF "v[228:231], v[224:227], %[a6], %[idx]\n\t"
            "s_nop 7\n\t"
            "v_permlane32_swap_b32 v228, v230\n\tv_permlane32_swap_b32 v229, v231\n\t"
            "v_add_f32 v228, v228, v230\n\tv_add_f32 v229, v229, v231\n\t"
            "v_cndmask_b32 v230, v229, v228, %[odd]\n\t"
            "s_nop 1\n\t"
            "v_mov_b32_dpp v230, v230 quad_perm:[1,0,3,2] row_mask:0xf bank_mask:0xf\n\t"
            "v_cndmask_b32 v228, v228, v230, %[odd]\n\tv_cndmask_b32 v229, v230, v229, %[odd]\n\t"
            "v_exp_f32 v230, v228\n\tv_exp_f32 v231, v229\n\t"
            "v_cvt_pk_bf16_f32 v232, v228, v229\n\t"
            "v_cvt_pk_bf16_f32 v233, v230, v231\n\t"
            "v_cmpx_o_f32 vcc, v228, v229\n\t"
            "global_store_dword %[sto], v233, %[dst] sc1\n\t" ST(82)
            "global_store_dword %[sto], v232, %[raw] sc1\n\t"
            "s_mov_b64 exec, -1\n\t"
            "s_barrier\n\t"
            :: R1_WOPS, [idx] "v"(idx), [rd] "v"(rd), [sto] "v"(sto), [dst] "s"(dst), [raw] "s"(raw), [odd] "s"(odd)
            : CLOB_V200_247, "vcc", "memory" ST_CLOB);
          rawbar();
        } else {
          rawbar(); rawbar(); rawbar(); rawbar(); rawbar();
        }
        const bool bad = __builtin_amdgcn_readfirstlane(lds_r4(L_FLAG)) == ep;
        ++ep;
        if (!bad) break;
      }
    }
    rawbar();
  } else if (wave == 4) {
    const unsigned trb = L_KT + (g >> 1) * 512 + (4 * (g & 1) + (c16 >> 2)) * 32 + (c16 & 3) * 8;
    unsigned char* so = ws + O_SM + (size_t)h * 2048 + q * 256 + lane * 4;
    const unsigned x3 = xo, xlast = g < 2 ? 6144 : 5120;
#pragma unroll
    for (int u = 0; u < 8; ++u) *(LDS u2v*)(size_t)(L_KT + u * 512 + lane * 8) = u < 7 ? (u2v){SENT, SENT} : (u2v){0u, 0u};
    if (lane == 0) lds_w4(L_FLAG, SENT);
    rawbar();
    (void)ld_cnt((const unsigned*)(lane < 32 ? so : lane < 48 ? so + 63 * 32768 : hblk(ws, sel, 70, 0)));
    unsigned pay = 0, eprev = 0;
    for (int s = 0; s < NSEG; ++s) {
      bool sig = s > 0;
      const unsigned ep0 = ep;
      for (;;) {
        bar(); rawbar(); rawbar();
        if (sig) { vm0(); if (lane == 0) st4c_asm(flagS(ws, sel, s - 1, id), pay); sig = false; }
        rawbar(); rawbar();
        const bool bad = __builtin_amdgcn_readfirstlane(lds_r4(L_FLAG)) == ep;
        ++ep;
        if (!bad) break;
      }
      {
        const unsigned est = lds_r4(L_EST), tst = lds_r4(L_TS);
        pay = (est ? min(tst - est, 4095u) : 0u) | ((est && eprev) ? min(est - eprev, 65535u) << 12 : 0u);
        pay |= min(ep - ep0 - 1, 7u) << 28;
        eprev = est;
      }
      f4v c0 = {0.f, 0.f, 0.f, 0.f}, c1 = c0;
      u4v ka[4], xa[4], xb[4];
#pragma unroll
      for (int u = 0; u < 4; ++u) {
        const u2v lo = lds_tr(trb + u * 1024), hi = lds_tr(trb + u * 1024 + 256);
        ka[u] = (u4v){lo.x, lo.y, hi.x, hi.y};
        const unsigned ad = x3 + (s & 1) * 8192 + (u < 3 ? u * 2048 : xlast);
        xa[u] = lds_r16(ad); xb[u] = lds_r16(ad + 256);
      }
      __builtin_amdgcn_sched_barrier(0);
#pragma unroll
      for (int u = 0; u < 4; ++u) { c0 = mfma16(ka[u], xa[u], c0); c1 = mfma16(ka[u], xb[u], c1); }
#pragma unroll
      for (int u = 0; u < 7; ++u) *(LDS u2v*)(size_t)(L_KT + u * 512 + lane * 8) = (u2v){SENT, SENT};
      st4c_asm(so + (size_t)s * 32768, pk2(halfsum(c0[0], c0[1]), halfsum(c0[2], c0[3])));
      st4c_asm(so + (size_t)s * 32768 + 1024, pk2(halfsum(c1[0], c1[1]), halfsum(c1[2], c1[3])));
      ARR(s);
    }
    vm0();
    rawbar();
    if (lane == 0) st4c_asm(flagS(ws, sel, NSEG - 1, id), pay);
  } else if (wave == 5) {
    const unsigned char* gp = ws + O_G + (size_t)(h * 2 + (q & 1)) * G_RUN;
    const unsigned v0 = lane * 16, v1 = v0 + 4096, v2 = v0 + 8192, v3 = v0 + 12288;
    auto fetch = [&](int t) {
#define GL4(v) "global_load_lds_dwordx4 " v ", %4 sc1\n\t" "global_load_lds_dwordx4 " v ", %4 offset:1024 sc1\n\t"
#define GL8(v) GL4(v) "global_load_lds_dwordx4 " v ", %4 offset:2048 sc1\n\t" "global_load_lds_dwordx4 " v ", %4 offset:3072 sc1\n\t"
      asm volatile("s_mov_b32 m0, %5\n\ts_nop 0\n\t" GL8("%0") "s_add_u32 m0, m0, 4096\n\ts_nop 0\n\t" GL8("%1")
                   "s_add_u32 m0, m0, 4096\n\ts_nop 0\n\t" GL8("%2") "s_add_u32 m0, m0, 4096\n\ts_nop 0\n\t" GL4("%3")
                   :: "v"(v0), "v"(v1), "v"(v2), "v"(v3), "s"(gp + t * 14336), "s"(L_G + (t & 1) * 14336) : "scc", "memory");
    };
    auto fetchx = [&](int t) {
      asm volatile("s_mov_b32 m0, %3\n\ts_nop 0\n\t"
                   "global_load_lds_dwordx4 %0, %2\n\tglobal_load_lds_dwordx4 %0, %2 offset:1024\n\tglobal_load_lds_dwordx4 %0, %2 offset:2048\n\t"
                   "global_load_lds_dwordx4 %0, %2 offset:3072\n\ts_add_u32 m0, m0, 4096\n\ts_nop 0\n\t"
                   "global_load_lds_dwordx4 %1, %2\n\tglobal_load_lds_dwordx4 %1, %2 offset:1024\n\tglobal_load_lds_dwordx4 %1, %2 offset:2048"
                   :: "v"(v0), "v"(v1), "s"(ws + O_XN + (size_t)t * 28672 + q * 7168), "s"(L_XN + (t & 1) * 8192) : "scc", "memory");
    };
    const unsigned* gdone = doneG(ws, 7 * (h >> 1) + min(lane, 6));
    auto peek = [&](int t) {
      unsigned keep;
      asm volatile("s_mov_b32 %0, m0\n\ts_mov_b32 m0, %2\n\ts_nop 0\n\tglobal_load_lds_dword %1, off sc1\n\ts_mov_b32 m0, %0"
                   : "=&s"(keep) : "v"(lane == 63 ? ptp(ws, t) : gdone), "s"(L_CG) : "memory");
    };
    if (act) {
      wait_cnt(ws, gdone, 1);
      fetch(0); fetch(1);
    }
    fetchx(0);
    vm0();
    RTS(1);
    rawbar();
    for (int s = 0; s < NSEG; ++s) {
      const unsigned char* src = hblk(ws, sel, s, 0) + moff;
      bool todo = act && s + 2 < NSEG - 1, todox = s + 1 < NSEG;
      for (;;) {
        rawbar();
        if (s == 0) peek(0);
        rawbar();
        dma16c(src, m0v);
        vm0();
        rawbar();
        const bool there = __all(lds_r4(L_CG + (lane & 7) * 4) >= (unsigned)(s >> 1) + 2);
        rawbar();
        rawbar();
        if (todo && there) { fetch(s + 2); todo = false; }
        if (todox) { fetchx(s + 1); peek(s + 1); todox = false; }
        const bool bad = __builtin_amdgcn_readfirstlane(lds_r4(L_FLAG)) == ep;
        ++ep;
        if (!bad) break;
      }
      if (todo) {
        wait_cnt(ws, gdone, (s >> 1) + 2);
        fetch(s + 2);
      }
      ARR(s);
    }
    vm0();
    bar();
  } else {
      { Lat W = {rt32(), 6, 0}; (void)lat_wait(W); }
    rawbar();
    Lat L = {get_anchor(ws, sel, id, lane), 0, 0};
    for (int s = 0; s < NSEG; ++s) {
      const unsigned char* src = hblk(ws, sel, s, 0) + moff;
      unsigned pw = 0;
      for (;;) {
        bar();
        const unsigned est = lat_wait(L);
        rawbar();
        asm volatile("s_mov_b32 m0, %1\n\ts_nop 0\n\tglobal_load_lds_dwordx4 %0, off sc1\n\tglobal_load_lds_dwordx4 %0, off offset:1024 sc1\n\ts_waitcnt vmcnt(0)"
                     :: "v"(src), "s"(m0v) : "memory");
        rawbar();
        lds_w4(L_EST, est);
        if (pw == 0) pw = __builtin_amdgcn_readfirstlane(lds_r4(L_CG + 252)) | 0x80000000u;
        bar(); rawbar();
        const bool bad = __builtin_amdgcn_readfirstlane(lds_r4(L_FLAG)) == ep;
        ++ep;
        EV(bad ? 1 : 0);
        if (!bad) break;
        L.e += RET_E;
      }
      if (pw == 0x80000000u && s < NSEG - 1) { wait_cnt(ws, ptp(ws, s), 1); pw = __builtin_amdgcn_readfirstlane(ld_cnt(ptp(ws, s))); }
      lat_fwd(L, pw & 255, (pw >> 8) & 255);
      ARR(s);
    }
    bar();
    EV_OUT(id);
  }
}

#define XRED(op, v, t) \
  "v_mov_b32 " t ", " v "\n\ts_nop 1\n\tv_permlane16_swap_b32 " v ", " t "\n\t" op " " v ", " v ", " t "\n\t" \
  "v_mov_b32 " t ", " v "\n\ts_nop 1\n\tv_permlane32_swap_b32 " v ", " t "\n\t" op " " v ", " v ", " t "\n\t"
#define MF16 "v_mfma_f32_16x16x32_bf16 "
#define WAIT_CYC(T) \
  "s_memtime vcc\n\ts_waitcnt lgkmcnt(0)\n\t" \
  "s_sub_i32 s70, " T ", vcc_lo\n\ts_max_i32 s70, s70, 0\n\ts_mul_i32 s70, s70, 13\n\ts_lshr_b32 s70, s70, 6\n\t" \
  "s_min_i32 s70, s70, 128\n\ts_lshl_b32 s70, s70, 2\n\ts_sub_i32 s70, 524, s70\n\t" \
  "s_getpc_b64 vcc\n\ts_add_u32 vcc_lo, vcc_lo, s70\n\ts_addc_u32 vcc_hi, vcc_hi, 0\n\ts_setpc_b64 vcc\n\t" \
  ".rept 128\n\ts_nop 0\n\t.endr\n\t"
#define R3_Y_A \
  "ds_read_b128 v[200:203], %[rd]\n\tds_read_b128 v[204:207], %[rd] offset:1024\n\tds_read_b128 v[208:211], %[rd] offset:2048\n\t" \
  "ds_read_b128 v[212:215], %[rd] offset:3072\n\tds_read_b128 v[216:219], %[rd] offset:4096\n\tds_read_b128 v[220:223], %[rd] offset:5120\n\t" \
  "ds_read_b128 v[224:227], %[rd] offset:6144\n\t" \
  "s_waitcnt lgkmcnt(6)\n\t" SMF "v[228:231], v[200:203], %[a0], %[idx]\n\t" SMF "v[232:235], v[200:203], %[b0], %[idx]\n\t" \
  "s_waitcnt lgkmcnt(5)\n\t" SMF "v[228:231], v[204:207], %[a1], %[idx]\n\t" SMF "v[232:235], v[204:207], %[b1], %[idx]\n\t" \
  "s_waitcnt lgkmcnt(4)\n\t" SMF "v[228:231], v[208:211], %[a2], %[idx]\n\t" SMF "v[232:235], v[208:211], %[b2], %[idx]\n\t" \
  "s_waitcnt lgkmcnt(3)\n\t" SMF "v[228:231], v[212:215], %[a3], %[idx]\n\t" SMF "v[232:235], v[212:215], %[b3], %[idx]\n\t" \
    \
  "s_waitcnt lgkmcnt(0)\n\t" \
  "v_readfirstlane_b32 s68, v244\n\t" \
  "v_max3_u32 v244, v216, v217, v218\n\tv_max3_u32 v245, v219, v220, v221\n\tv_max3_u32 v244, v244, v222, v223\n\t" \
  "v_max3_u32 v245, v245, v224, v225\n\tv_max3_u32 v244, v244, v226, v227\n\tv_max_u32 v244, v244, v245\n\t" \
  "v_cmp_eq_u32 s[74:75], -1, v244\n\t" \
  "v_cmp_u_f32 vcc, v228, v229\n\t" \
  "s_or_b64 s[74:75], s[74:75], vcc\n\t" \
  "v_cmp_u_f32 vcc, v234, v235\n\t" \
  "s_or_b64 vcc, s[74:75], vcc\n\t"
#define R3_Y_B \
  SMF "v[228:231], v[216:219], %[a4], %[idx]\n\t" SMF "v[232:235], v[216:219], %[b4], %[idx]\n\t" \
  SMF "v[228:231], v[220:223], %[a5], %[idx]\n\t" SMF "v[228:231], v[224:227], %[a6], %[idx]\n\t" \
  SMF "v[232:235], v[220:223], %[b5], %[idx]\n\t" SMF "v[232:235], v[224:227], %[b6], %[idx]\n\t" \
  "s_nop 1\n\t" \
  "v_permlane32_swap_b32 v228, v229\n\tv_permlane32_swap_b32 v230, v231\n\t" \
  "v_add_f32 v228, v228, v229\n\tv_add_f32 v230, v230, v231\n\t" \
  "v_cvt_pk_bf16_f32 v164, v228, v230\n\t" \
  "s_nop 0\n\t" \
  "v_permlane32_swap_b32 v232, v233\n\tv_permlane32_swap_b32 v234, v235\n\t" \
  "v_add_f32 v232, v232, v233\n\tv_add_f32 v234, v234, v235\n\t" \
  "v_cvt_pk_bf16_f32 v165, v232, v234\n\t" \
  ST(82)
#define R3_POLL \
  WAIT_CYC("s68") \
  "global_load_dwordx4 v[236:239], %[to], s[60:61] sc1\n\t" \
  WAIT_CYC("s69") \
  "global_load_dwordx4 v[240:243], %[tm], s[60:61] sc1\n\t" ST(86)
#define R3_POLL2   \
  "55:\n\ts_memtime vcc\n\ts_waitcnt lgkmcnt(0)\n\ts_sub_i32 s70, s69, vcc_lo\n\ts_cmp_gt_i32 s70, 0\n\ts_cbranch_scc1 55b\n\t" \
  "global_load_dwordx4 v[236:239], %[to], s[60:61] sc1\n\tglobal_load_dwordx4 v[240:243], %[tm], s[60:61] sc1\n\t"
#define R3_S_PHASE(SIG, NS) \
  "s_waitcnt vmcnt(1)\n\t" ST(84) \
  "v_cndmask_b32 v208, 0, v236, s[76:77]\n\tv_cndmask_b32 v209, 0, v237, s[76:77]\n\tv_cndmask_b32 v210, 0, v238, s[76:77]\n\tv_cndmask_b32 v211, 0, v239, s[76:77]\n\t" \
  MF16 "v[220:223], v[160:163], v[236:239], 0\n\t" \
  "v_cndmask_b32 v212, v236, 0, s[76:77]\n\tv_cndmask_b32 v213, v237, 0, s[76:77]\n\tv_cndmask_b32 v214, v238, 0, s[76:77]\n\tv_cndmask_b32 v215, v239, 0, s[76:77]\n\t" \
  MF16 "v[216:219], v[192:195], v[208:211], 0\n\t" \
  "s_nop 0\n\t" \
  MF16 "v[216:219], v[196:199], v[212:215], v[216:219]\n\t" \
  "global_store_dwordx2 %[yo], v[164:165], s[60:61] sc1\n\t"        \
  SIG "s_waitcnt vmcnt(" #NS ")\n\t" ST(88) \
  MF16 "v[232:235], %[selA], v[240:243], 0\n\t" \
  "s_nop 7\n\t" \
  "v_exp_f32 v234, v232\n\tv_exp_f32 v235, v233\n\t" \
  "s_nop 0\n\t" \
  "v_cvt_pk_bf16_f32 v240, v234, v235\n\t" \
  "v_cndmask_b32 v168, 0, v240, s[76:77]\n\tv_cndmask_b32 v169, v240, 0, s[76:77]\n\t" \
  "s_nop 1\n\t" \
  MF16 "v[220:223], v[160:163], v[168:171], v[220:223]\n\t" \
  MF16 "v[244:247], v[164:167], v[168:171], v[216:219]\n\t" \
  "s_nop 6\n\t" \
  "v_rcp_f32 v242, v220\n\t" \
  "v_med3_f32 v241, v220, v172, v173\n\t" \
  "v_cmp_lg_f32 vcc, v241, v220\n\t" \
  "s_cbranch_vccnz 12f\n\t" \
  "11:\n\t" \
  "v_pk_mul_f32 v[244:245], v[244:245], v[242:243] op_sel_hi:[1,0]\n\t" \
  "v_pk_mul_f32 v[246:247], v[246:247], v[242:243] op_sel_hi:[1,0]\n\t" \
  "s_nop 0\n\t" \
  "v_add_f32_dpp v244, v244, v244 row_ror:8 row_mask:0xf bank_mask:0xf\n\tv_add_f32_dpp v245, v245, v245 row_ror:8 row_mask:0xf bank_mask:0xf\n\t" \
  "v_add_f32_dpp v246, v246, v246 row_ror:8 row_mask:0xf bank_mask:0xf\n\tv_add_f32_dpp v247, v247, v247 row_ror:8 row_mask:0xf bank_mask:0xf\n\t" \
  ST(90) "ds_write_b128 %[redw], v[244:247]\n\t" \
  LDSWAIT \
  "s_barrier\n\t" ST(92)
#define R3_S_RARE \
    "12:\n\t" \
  "v_xor_b32 v241, 0x6000, %[to]\n\t" \
  "global_load_dwordx4 v[200:203], v241, s[60:61] sc1\n\t" \
  "s_waitcnt vmcnt(0)\n\t" \
  "v_lshlrev_b32 v208, 16, v200\n\tv_and_b32 v209, 0xffff0000, v200\n\tv_lshlrev_b32 v210, 16, v201\n\tv_and_b32 v211, 0xffff0000, v201\n\t" \
  "v_lshlrev_b32 v212, 16, v202\n\tv_and_b32 v213, 0xffff0000, v202\n\tv_lshlrev_b32 v214, 16, v203\n\tv_and_b32 v215, 0xffff0000, v203\n\t" \
  "v_max3_f32 v240, v208, v209, v210\n\tv_max3_f32 v241, v211, v212, v213\n\tv_max3_f32 v240, v240, v214, v215\n\t" \
  "v_max3_f32 v241, v241, v232, v233\n\tv_max_f32 v240, v240, v241\n\t" \
  XRED("v_max_f32", "v240", "v241") \
  "v_sub_f32 v208, v208, v240\n\tv_sub_f32 v209, v209, v240\n\tv_sub_f32 v210, v210, v240\n\tv_sub_f32 v211, v211, v240\n\t" \
  "v_sub_f32 v212, v212, v240\n\tv_sub_f32 v213, v213, v240\n\tv_sub_f32 v214, v214, v240\n\tv_sub_f32 v215, v215, v240\n\t" \
  "v_sub_f32 v234, v232, v240\n\tv_sub_f32 v235, v233, v240\n\t" \
  "v_exp_f32 v208, v208\n\tv_exp_f32 v209, v209\n\tv_exp_f32 v210, v210\n\tv_exp_f32 v211, v211\n\t" \
  "v_exp_f32 v212, v212\n\tv_exp_f32 v213, v213\n\tv_exp_f32 v214, v214\n\tv_exp_f32 v215, v215\n\t" \
  "v_exp_f32 v234, v234\n\tv_exp_f32 v235, v235\n\ts_nop 1\n\t" \
  "v_cvt_pk_bf16_f32 v200, v208, v209\n\tv_cvt_pk_bf16_f32 v201, v210, v211\n\tv_cvt_pk_bf16_f32 v202, v212, v213\n\tv_cvt_pk_bf16_f32 v203, v214, v215\n\t" \
  "v_cvt_pk_bf16_f32 v240, v234, v235\n\t" \
  "v_cndmask_b32 v208, 0, v200, s[76:77]\n\tv_cndmask_b32 v209, 0, v201, s[76:77]\n\tv_cndmask_b32 v210, 0, v202, s[76:77]\n\tv_cndmask_b32 v211, 0, v203, s[76:77]\n\t" \
  "v_cndmask_b32 v212, v200, 0, s[76:77]\n\tv_cndmask_b32 v213, v201, 0, s[76:77]\n\tv_cndmask_b32 v214, v202, 0, s[76:77]\n\tv_cndmask_b32 v215, v203, 0, s[76:77]\n\t" \
  "v_cndmask_b32 v168, 0, v240, s[76:77]\n\tv_cndmask_b32 v169, v240, 0, s[76:77]\n\ts_nop 1\n\t" \
  MF16 "v[220:223], v[160:163], v[200:203], 0\n\t" \
  MF16 "v[220:223], v[160:163], v[168:171], v[220:223]\n\t" \
  MF16 "v[244:247], v[192:195], v[208:211], 0\n\t" \
  MF16 "v[244:247], v[196:199], v[212:215], v[244:247]\n\t" \
  MF16 "v[244:247], v[164:167], v[168:171], v[244:247]\n\t" \
  "s_nop 7\n\t" \
  "v_rcp_f32 v242, v220\n\ts_nop 0\n\t" \
  "s_branch 11b\n\t"
#define R3_PREP "s_lshl_b32 s44, s65, 2\n\ts_add_i32 s44, s44, s67\n\ts_lshl_b32 s45, s66, 2\n\ts_sub_i32 s45, 108, s45\n\t"
#define R3_FWD "s_add_i32 s66, s66, s47\n\ts_add_i32 s65, s65, s46\n\ts_cmp_ge_i32 s66, 24\n\ts_cselect_b32 s70, 24, 0\n\ts_addc_u32 s65, s65, 0\n\ts_sub_i32 s66, s66, s70\n\t"
#define R3_RT "s_memrealtime vcc\n\ts_waitcnt lgkmcnt(0)\n\ts_sub_i32 s70, s44, vcc_lo\n\ts_cmp_lt_i32 s70, 1\n\t"
#define R3_TW_WAIT \
  R3_RT "s_cbranch_scc1 40f\n\t" \
  "20:\n\t" R3_RT "s_cbranch_scc1 21f\n\t" R3_RT "s_cbranch_scc1 21f\n\t" R3_RT "s_cbranch_scc1 21f\n\t" R3_RT "s_cbranch_scc0 20b\n\t" \
  "21:\n\t" \
  "s_getpc_b64 vcc\n\ts_add_u32 vcc_lo, vcc_lo, s45\n\ts_addc_u32 vcc_hi, vcc_hi, 0\n\ts_setpc_b64 vcc\n\t" \
  ".rept 24\n\ts_nop 0\n\t.endr\n\t" \
  "22:\n\t"
#define R3_TW_LATE \
  "40:\n\t" \
  "s_cmp_eq_u32 s70, 0\n\ts_cbranch_scc1 22b\n\t" \
  "41:\n\ts_add_i32 s65, s65, 26\n\ts_add_i32 s44, s44, 4*26\n\ts_add_i32 s70, s70, 4*26\n\ts_cmp_lt_i32 s70, 0\n\ts_cbranch_scc1 41b\n\t" \
  "s_branch 20b\n\t"
#define R3_STEP(op, uop)   \
  op " %[moff], 0x8000, %[moff]\n\t" op " %[to], 0x8000, %[to]\n\t" op " %[tm], 0x8000, %[tm]\n\t" op " %[sto], 0x8000, %[sto]\n\t" \
  uop " %[uo], 0x4000, %[uo]\n\t" uop " %[yo], 0x1000, %[yo]\n\t"
#define R3_FLIP \
  "s_and_b32 s70, s62, 15\n\ts_cselect_b32 s70, 0, 0x4000\n\ts_add_i32 s71, s62, 1\n\ts_and_b32 s71, s71, 15\n\ts_cselect_b32 s71, 0, 0x4000\n\t" \
  "v_xor_b32 %[moff], s70, %[moff]\n\tv_xor_b32 %[to], s70, %[to]\n\tv_xor_b32 %[tm], s70, %[tm]\n\tv_xor_b32 %[sto], s71, %[sto]\n\t"
#define ULOADS "global_load_dwordx4 v[192:195], %[uo], s[60:61] sc1\n\tglobal_load_dwordx4 v[196:199], %[uo], s[60:61] offset:1024 sc1\n\t"
#define R3_DMA "global_load_lds_dwordx4 %[moff], s[60:61] sc1\n\t"
#define R3_TW_PT "s_lshl_b32 s70, s62, 7\n\ts_add_i32 s70, s70, 0x420080\n\tv_mov_b32 v178, s70\n\tglobal_load_dword v177, v178, s[60:61] sc1\n\t"
#define R3_TW_NEXT "v_readfirstlane_b32 s70, v177\n\ts_cmp_eq_u32 s70, 0\n\ts_cbranch_scc1 45f\n\t" \
  "19:\n\ts_and_b32 s46, s70, 0xff\n\ts_bfe_u32 s47, s70, 0x80008\n\ts_lshr_b32 s48, s70, 16\n\t"
#define R3_TW_OOL        \
  "45:\n\ts_mov_b32 s71, 0x40000\n\t" \
  "46:\n\tglobal_load_dword v177, v178, s[60:61] sc1\n\ts_waitcnt vmcnt(0)\n\tv_readfirstlane_b32 s70, v177\n\t" \
  "s_cmp_eq_u32 s70, 0\n\ts_cbranch_scc0 19b\n\ts_sub_i32 s71, s71, 1\n\ts_cmp_gt_i32 s71, 0\n\ts_cbranch_scc1 46b\n\ts_movk_i32 s70, 40\n\ts_branch 19b\n\t"
#define R3_TW_TIME \
  TBURST \
  "s_sub_i32 s4, s4, s44\n\ts_add_i32 s4, s4, 4\n\ts_mul_i32 s4, s4, 93\n\ts_ashr_i32 s4, s4, 2\n\ts_sub_i32 s58, s58, s4\n\t" \
  "s_lshl_b32 s4, s66, 2\n\ts_add_i32 s58, s58, s4\n\ts_sub_i32 s4, s58, s51\n\ts_mov_b32 s51, s58\n\ts_add_i32 s58, s58, s48\n\t" \
  "v_mov_b32 v200, s58\n\tv_mov_b32 v201, s4\n\tds_write_b64 v188, v[200:201] offset:12296\n\ts_waitcnt lgkmcnt(0)\n\t"
#define R3_WARM(beg, len) \
  "s_getpc_b64 vcc\n\t9:\n\ts_add_u32 vcc_lo, vcc_lo, " beg "-9b\n\ts_addc_u32 vcc_hi, vcc_hi, 0\n\tv_lshlrev_b32 v200, 3, %[rd]\n\tv_min_u32 v200, " len "-64, v200\n\t" \
  "global_load_dword v201, v200, vcc\n\tglobal_load_dword v202, v200, vcc offset:64\n\ts_waitcnt vmcnt(0)\n\t"
#define R3_LOOP(WARM, DMA, TWWAIT, CNTLOAD, CNTPAY, CNTPACK, TWTIME, NW, CNTSIG, NS, TWFWD, FIN, CNTCHK, OUTOFLINE) \
  "s_mov_b64 s[60:61], %[wsp]\n\ts_mov_b32 s62, 0\n\ts_mov_b32 s63, 0\n\ts_mov_b32 s64, 0x10000\n\ts_mov_b32 s78, 0\n\t" \
  "s_mov_b32 s76, 0x00ff00ff\n\ts_mov_b32 s77, s76\n\t" \
  "s_and_b32 s46, %[p0], 0xff\n\ts_bfe_u32 s47, %[p0], 0x80008\n\ts_lshr_b32 s48, %[p0], 16\n\ts_mov_b32 s65, 0\n\ts_mov_b32 s66, 0\n\t" \
  "s_movk_i32 s49, 250\n\ts_lshl_b32 s50, %[p1], 10\n\t" \
  "v_mov_b32 v188, 0\n\tv_mov_b32 v160, 0x3f803f80\n\tv_mov_b32 v161, v160\n\tv_mov_b32 v162, v160\n\tv_mov_b32 v163, v160\n\t" \
  "v_mov_b32 v166, 0\n\tv_mov_b32 v167, 0\n\tv_mov_b32 v170, 0\n\tv_mov_b32 v171, 0\n\tv_mov_b32 v172, 0x1f800000\n\tv_mov_b32 v173, 0x71800000\n\t" \
  R3_PREP \
  WARM \
  "10:\n\t" \
  TWWAIT \
  "s_mov_b32 m0, s50\n\t" \
  "s_waitcnt lgkmcnt(0)\n\ts_barrier\n\t" \
  "s_memtime s[72:73]\n\t" \
  DMA \
  "ds_read_b32 v200, v188 offset:12288\n\t" \
  "s_waitcnt lgkmcnt(0)\n\t" \
  "v_readfirstlane_b32 s74, v200\n\t" \
  "s_cmp_eq_u32 s74, s63\n\ts_cbranch_scc1 50f\n\t" \
  "s_add_i32 s63, s63, 1\n\t" \
  "v_mov_b32 v228, 0\n\tv_mov_b32 v229, 0\n\tv_mov_b32 v230, 0\n\tv_mov_b32 v231, 0\n\t" \
  "v_mov_b32 v232, 0\n\tv_mov_b32 v233, 0\n\tv_mov_b32 v234, 0\n\tv_mov_b32 v235, 0\n\t" \
  ULOADS \
  CNTLOAD TWTIME \
  "s_waitcnt vmcnt(" #NW ")\n\t" \
  "s_barrier\n\t" ST(80) \
  CNTPAY "ds_read_b32 v244, v188 offset:12296\n\t" \
  R3_Y_A \
  "s_cbranch_vccnz 60f\n\t" \
  "14:\n\t" \
  CNTPACK "s_add_i32 s69, s68, s49\n\t" \
  "s_cmp_eq_u32 s62, 63\n\ts_cbranch_scc1 98f\n\t" \
  R3_POLL R3_Y_B \
  TWFWD \
  "15:\n\t" \
  R3_S_PHASE(CNTSIG, NS) FIN CNTCHK \
  "s_add_i32 s62, s62, 1\n\t" \
  R3_STEP("v_add_u32", "v_add_u32") \
  R3_FLIP DIAG_R3 \
  "s_branch 10b\n\t" \
  R3_S_RARE \
    \
  "50:\n\t" \
  "s_sub_i32 s64, s64, 1\n\ts_add_i32 s63, s63, 1\n\ts_bitset1_b32 s78, 28\n\t" \
  R3_FLIP R3_STEP("v_subrev_u32", "v_subrev_u32") \
  "s_sub_i32 s62, s62, 1\n\t" \
  "s_memtime vcc\n\ts_waitcnt lgkmcnt(0)\n\t" \
  "s_sub_i32 s70, s69, s68\n\ts_cmp_eq_u32 s70, s49\n\ts_cselect_b32 s70, 256, 0\n\ts_sub_i32 s69, s69, s70\n\t"       \
  "53:\n\ts_add_i32 s68, s68, 93*26\n\ts_add_i32 s69, s69, 93*26\n\ts_add_i32 s65, s65, 26\n\t" \
  "s_sub_i32 s70, s69, vcc_lo\n\ts_cmp_lt_i32 s70, 0\n\ts_cbranch_scc1 53b\n\t" \
  R3_PREP R3_POLL2 \
  "s_branch 15b\n\t" \
    \
  "60:\n\ts_bitset1_b32 s78, 29\n\ts_sub_i32 s64, s64, 1\n\ts_cmp_gt_i32 s64, 0\n\ts_cbranch_scc0 14b\n\t" \
  "s_add_i32 s65, s65, 26\n\t" R3_PREP "s_branch 10b\n\t" \
  OUTOFLINE \
  "98:\n\t" R3_Y_B \
  "global_store_dwordx2 %[yo], v[164:165], s[60:61] sc1\n\t" \
  "s_waitcnt vmcnt(0)\n\ts_barrier\n\t"
#define R3_FIN \
  "ds_read_b128 v[200:203], %[redr]\n\tds_read_b128 v[204:207], %[redr] offset:512\n\t" \
  "ds_read_b128 v[208:211], %[redr] offset:1024\n\tds_read_b128 v[212:215], %[redr] offset:1536\n\t" \
  "s_waitcnt lgkmcnt(2)\n\t" \
  "v_pk_add_f32 v[200:201], v[200:201], v[204:205]\n\tv_pk_add_f32 v[202:203], v[202:203], v[206:207]\n\t" \
  "s_waitcnt lgkmcnt(1)\n\t" \
  "v_pk_add_f32 v[200:201], v[200:201], v[208:209]\n\tv_pk_add_f32 v[202:203], v[202:203], v[210:211]\n\t" \
  "s_waitcnt lgkmcnt(0)\n\t" \
  "v_pk_add_f32 v[200:201], v[200:201], v[212:213]\n\tv_pk_add_f32 v[202:203], v[202:203], v[214:215]\n\t" \
  "s_nop 1\n\t" \
  "v_permlane32_swap_b32 v200, v202\n\tv_permlane32_swap_b32 v201, v203\n\t" \
  "v_add_f32 v200, v200, v202\n\tv_add_f32 v201, v201, v203\n\t" \
  "v_cvt_pk_bf16_f32 v202, v200, v201\n\t" \
  "v_cmpx_o_f32 vcc, v200, v201\n\t" \
  "global_store_dword %[sto], v202, s[60:61] sc1\n\t" ST(94) \
  "s_memtime s[74:75]\n\t" \
  "s_mov_b64 exec, -1\n\t" \
  "v_mov_b32 v200, -1\n\tv_mov_b32 v201, -1\n\tv_mov_b32 v202, -1\n\tv_mov_b32 v203, -1\n\t" \
  "ds_write_b128 %[redr], v[200:203]\n\tds_write_b128 %[redr], v[200:203] offset:512\n\t" \
  "ds_write_b128 %[redr], v[200:203] offset:1024\n\tds_write_b128 %[redr], v[200:203] offset:1536\n\t" \
  "s_waitcnt lgkmcnt(0)\n\ts_sub_u32 s70, s74, s69\n\tv_mov_b32 v200, s70\n\tds_write_b32 v188, v200 offset:12308\n\t" \
  "s_cmp_eq_u64 vcc, -1\n\ts_cbranch_scc0 80f\n\t" \
  "16:\n\t"
#define R3_FIN_OOL \
  "80:\n\ts_cmp_gt_i32 s64, 0\n\ts_cbranch_scc0 81f\n\t" \
  "v_mov_b32 v200, s63\n\tds_write_b32 v188, v200 offset:12288\n\ts_branch 16b\n\t" \
  "81:\n\tv_mov_b32 v200, 0x7fc07fc0\n\tglobal_store_dword %[sto], v200, s[60:61] sc1\n\ts_branch 16b\n\t"
#define R3_CNT_PAY "ds_read_b32 v174, v188 offset:12300\n\tds_read_b32 v175, v188 offset:12308\n\t"
#define R3_CNT_PACK \
  "v_min_u32 v175, 0xfff, v175\n\tv_min_u32 v174, 0xffff, v174\n\tv_lshl_or_b32 v176, v174, 12, v175\n\tv_or_b32 v176, s78, v176\n\ts_mov_b32 s78, 0\n\t"
#define R3_CNT_SIG "global_store_dword v187, v176, s[60:61] sc1\n\t"
#define R3_CNT_LOAD "v_mov_b32 v187, s43\n\ts_mov_b64 s[38:39], s[40:41]\n\tv_cndmask_b32 v144, %[uo], v186, s[40:41]\n\tglobal_load_dword v145, v144, s[60:61] sc1\n\t"
#define R3_CNT_CHK "v_readfirstlane_b32 s43, %[tm]\n\ts_and_b32 s43, s43, 0xffffe000\n\ts_xor_b32 s43, s43, s42\n\t" \
  "v_cndmask_b32 v189, v189, v145, s[38:39]\n\ts_mov_b64 s[38:39], 0\n\t" \
  "s_add_i32 s70, s62, 3\n\ts_lshr_b32 s70, s70, 1\n\tv_cmp_gt_u32 vcc, s70, v189\n\ts_cbranch_vccnz 85f\n\t17:\n\t" \
  "s_add_i32 s70, s70, 1\n\ts_min_u32 s70, s70, 32\n\tv_cmp_gt_u32 vcc, s70, v189\n\ts_cmp_lg_u64 vcc, 0\n\ts_cselect_b64 s[40:41], -1, 0\n\t"
#define R3_CNT_OOL        \
  "85:\n\ts_mov_b32 s71, 0x40000\n\t" \
  "86:\n\tglobal_load_dword v189, v186, s[60:61] sc1\n\ts_waitcnt vmcnt(0)\n\t" \
  "v_cmp_gt_u32 vcc, s70, v189\n\ts_cbranch_vccz 17b\n\ts_sleep 2\n\ts_sub_i32 s71, s71, 1\n\ts_cmp_gt_i32 s71, 0\n\ts_cbranch_scc1 86b\n\ts_branch 17b\n\t"
#define DIAG_R3
#define DIAG_R3_END
#define DIAG_R3_OUT
#define R3_OUTS [moff] "+v"(moff), [to] "+v"(to), [tm] "+v"(tm), [sto] "+v"(sto), [uo] "+v"(uo), [yo] "+v"(yo) DIAG_R3_OUT
#define R3_INS R1_WOPS, R1_WOPS2, [idx] "v"(idx), [rd] "v"(rd), [redw] "v"(redw), [selA] "v"(selA), [wsp] "s"(ws), [p0] "s"(p0), [p1] "s"(p1)
#define R3_CLOB CLOB_V200_247, "v144","v145","v146","v147","v148","v149","v150","v151", "v160","v161","v162","v163","v164","v165","v166","v167","v168","v169","v170","v171","v172","v173","v174","v175","v176","v177","v178", "v179","v180","v181","v182","v183","s96","s97","v184","v185","v186","v187","v188","v189","v190","v191","v192","v193","v194","v195","v196","v197","v198","v199", \
  "s38","s39","s40","s41","s42","s43","s44","s45","s46","s47","s48","s49","s50","s51","s60","s61","s62","s63","s64","s65","s66","s67","s68","s69","s70","s71", \
  "s72","s73","s74","s75","s76","s77","s78","vcc","scc","memory" ST_CLOB
#define STR2(x) #x
#define STR(x) STR2(x)

template <int KIND>
DEV void r3_body(unsigned char* __restrict__ ws, int v, int wave, int lane, unsigned sel TUNE_ARG) {
  const int g = lane >> 4, hs = (lane >> 3) & 1, i8 = lane & 7;
  const int idx = (lane & 8) ? 0xEEEE : 0x4444;
  const int dw = wave < 7 ? wave : 6;
  const unsigned rd = lane * 16;
  u8v w0[7], w1[7];
  {
    const unsigned char* p = ws + O_WB + (size_t)(2 * wave * 28 + v) * 14336 + lane * 16;
#pragma unroll
    for (int k = 0; k < 7; ++k) {
      w0[k] = cat8(*(const u4v*)(p + k * 2048), *(const u4v*)(p + k * 2048 + 1024));
      w1[k] = cat8(*(const u4v*)(p + 28 * 14336 + k * 2048), *(const u4v*)(p + 28 * 14336 + k * 2048 + 1024));
    }
#pragma unroll
    for (int k = 0; k < 7; ++k) asm volatile("" : "+v"(w0[k]), "+v"(w1[k]));
  }
  RTS(1);
  if (KIND == 3 && lane == 0) lds_w4(L_FLAG, SENT);
  if (KIND == 2) wait_cnt(ws, doneU(ws, 4 * (v >> 1) + (lane & 3)), 1);
  *(LDS u2v*)(size_t)(L_RED + wave * 512 + lane * 8) = (u2v){SENT, SENT};
  RTS(2);
  unsigned moff = hoff(sel, 0, 0) + dw * 1024 + g * 256 + i8 * 32 + hs * 16;
  unsigned to = hoff(sel, 0, 2) + (2 * wave + hs) * 512 + (g >> 1) * 256 + i8 * 32 + (g & 1) * 16;
  unsigned tm = hoff(sel, 64, K_SMK) + (2 * wave + hs) * 512 + g * 128 + i8 * 16;
  u4v selA = {0u, 0u, 0u, 0u};
  if (((lane & 3) >> 1) == 0) {
    const int gq = (lane >> 2) & 3, j = 4 * (gq & 1) + (gq >> 1) + 2 * (lane & 1);
    const unsigned one = (j & 1) ? 0x3f800000u : 0x00003f80u;
    selA = (u4v){(j >> 1) == 0 ? one : 0u, (j >> 1) == 1 ? one : 0u, (j >> 1) == 2 ? one : 0u, (j >> 1) == 3 ? one : 0u};
  }
  unsigned sto = hoff(sel, 1, 0) + v * 256 + i8 * 32 + ((lane >> 3) & 3) * 8 + (lane >> 5) * 4;
  unsigned uo = (unsigned)O_U + v * U_RUN + 2 * wave * 1024 + lane * 16;
  unsigned yo = (unsigned)O_YG + v * Y_RUN + wave * 512 + lane * 8;
  const unsigned redw = L_RED + hs * 4096 + wave * 512 + (g * 8 + i8) * 16;
  const unsigned redr = L_RED + (lane >> 5) * 2048 + (lane & 31) * 16;
  const unsigned p1 = dw;
  {
    const unsigned o = lane < 8 ? yo : lane < 16 ? yo + 63 * 4096 : lane < 24 ? uo : lane < 32 ? uo + 63 * 16384 : lane < 40 ? hoff(sel, 70, 0) : lane < 48 ? hoff(sel, 1, 0) + 7168 : (unsigned)O_FL + 0x3ff00;
    (void)ld_cnt((const unsigned*)(ws + (o & ~3u)));
  }
  int anchor = 0;
  unsigned p0 = 0;
  if (KIND == 3) asm volatile(R3_WARM(".Lr3tw_beg", ".Lr3tw_end-.Lr3tw_beg") :: [rd] "v"(rd) : "v200", "v201", "v202", "vcc", "scc", "memory");
  if (KIND == 3) { wait_cnt(ws, ptp(ws, 0), 1); p0 = __builtin_amdgcn_readfirstlane(ld_cnt(ptp(ws, 0))); }
  bar();
  if (KIND == 3) anchor = get_anchor(ws, sel, 64 + v, lane);
  if (KIND == 0) {
    asm volatile(R3_LOOP(R3_WARM("10f", "98f-10f"), R3_DMA, "", "", "", "", "", 2, "", 1, "", R3_FIN, "", R3_FIN_OOL) DIAG_R3_END : R3_OUTS : R3_INS, [redr] "v"(redr) : R3_CLOB);
  } else if (KIND == 1) {
    asm volatile(R3_LOOP(R3_WARM("10f", "98f-10f"), R3_DMA, "", "", "", "", "", 2, "", 1, "", "", "", "") DIAG_R3_END : R3_OUTS : R3_INS : R3_CLOB);
  } else if (KIND == 2) {
    const unsigned cc = ((unsigned)(3 - K_SMK) << 13) | (unsigned)(v * 128);
    const unsigned uc = (unsigned)O_FL + (C_W + 2 * (56 + 4 * (v >> 1) + (lane & 3))) * 64;
    asm volatile("s_mov_b64 s[40:41], -1\n\ts_mov_b32 s42, %[cc]\n\ts_and_b32 s43, s42, 0x1fff\n\ts_add_i32 s43, s43, 0x43e000\n\tv_mov_b32 v186, %[uc]\n\t"
                 R3_LOOP(R3_WARM("10f", "98f-10f"), R3_DMA, "", R3_CNT_LOAD, R3_CNT_PAY, R3_CNT_PACK, "", 3, R3_CNT_SIG, 2, "", "", R3_CNT_CHK, R3_CNT_OOL) DIAG_R3_END : R3_OUTS : R3_INS, [cc] "s"(cc), [uc] "v"(uc) : R3_CLOB);
  } else {
    asm volatile("s_mov_b32 s67, %[anc]\n\t" R3_LOOP(".Lr3tw_beg:\n\t", "", R3_TW_WAIT, R3_TW_PT, "", "", R3_TW_TIME, 3, "", 1, R3_FWD R3_PREP, "", R3_TW_NEXT, R3_TW_LATE R3_TW_OOL ".Lr3tw_end:\n\t") DIAG_R3_END : R3_OUTS : R3_INS, [anc] "s"(anchor) : R3_CLOB, TBURST_CLOB);
  }
  if (KIND == 2 && lane < 2) st4c_asm(flagY(ws, sel, NSEG - 2 + lane, v), 0u);
  RTS(3);
}
DEV void role_r3(unsigned char* __restrict__ ws, int v, int wave, int lane, unsigned sel TUNE_ARG) {
  if (wave == 0) r3_body<0>(ws, v, wave, lane, sel TUNE_PASS);
  else if (wave == 6) r3_body<2>(ws, v, wave, lane, sel TUNE_PASS);
  else if (wave == 7) r3_body<3>(ws, v, wave, lane, sel TUNE_PASS);
  else r3_body<1>(ws, v, wave, lane, sel TUNE_PASS);
}

#define PF_LAG 4
DEV void role_pf(unsigned char* __restrict__ ws, int k, int wave, int lane, unsigned sel) {
  constexpr int LAG = PF_LAG;
  const rsrc_t ru = mkrsrc(ws + O_U, 30u << 20);
  unsigned have = 0;
  for (int t = 1; t < NSEG - 1; ++t) {
    if (wave == 0) {
      unsigned skip = 0;
      const unsigned need = (t >> 1) + 1;
      if (__any(have < need)) { wait_cnt(ws, doneU(ws, min(lane, 55)), need); have = ld_cnt(doneU(ws, min(lane, 55))); }
      if (t >= LAG) wait_flags(ws, flagY(ws, sel, t - LAG, k));
      else if (t >= LAG - 2) {
        wait_cnt(ws, anchorp(ws, 28 + k), 1);
        const unsigned at = (__builtin_amdgcn_readfirstlane(ld_cnt(anchorp(ws, 28 + k))) & ~1u) + (t - LAG + 2) * 264;
        skip = (int)(((unsigned)rt32() << 1) - at) > 40;
        for (int it = 0; it < 4096 && (int)(at - ((unsigned)rt32() << 1)) > 0; ++it) __builtin_amdgcn_s_sleep(1);
      }
      lds_w4((t & 1) * 4, skip);
    }
    bar();
    if (__builtin_amdgcn_readfirstlane(lds_r4((t & 1) * 4))) continue;
    u4v r[14];
#pragma unroll
    for (int i = 0; i < 14; ++i) { const int n = (k * 8 + wave) * 14 + i; r[i] = ldc16(ru, (n >> 4) * U_RUN + (t * 16 + (n & 15)) * 1024 + lane * 16); }
#pragma unroll
    for (int i = 0; i < 14; ++i) asm volatile("" :: "v"(r[i]));
  }
}

__global__ void __launch_bounds__(512, 2)
main_kernel(const unsigned short* __restrict__ x, const unsigned short* __restrict__ mem0, const unsigned short* __restrict__ b1, const unsigned short* __restrict__ b2,
            unsigned short* __restrict__ out, unsigned char* __restrict__ ws TUNE_ARG) {
  const int b = blockIdx.x, lane = threadIdx.x & 63;
  const int wave = __builtin_amdgcn_readfirstlane(threadIdx.x >> 6);
  RTS(0);
  const unsigned long long tab = *(const unsigned long long*)(ws + O_SEL);
  unsigned sel = 0;
#pragma unroll
  for (int j = 7; j >= 0; --j) if ((tab >> (8 * j + 7)) & 1) sel = (unsigned)(tab >> (8 * j)) & 15u;
  const int xc = (b + (int)(tab >> 4)) & 7, slot = b >> 3;
  if (xc == 0 || xc == 2) {
    role_r1(ws, mem0, (xc >> 1) * 32 + slot, wave, lane, sel TUNE_PASS);
  } else if (xc == 1) {
    if (slot < 28) role_r3(ws, slot, wave, lane, sel TUNE_PASS);
    else role_pf(ws, slot - 28, wave, lane, sel);
  } else {
    const int k = slot * 5 + (xc - 3);
    if (k < 56) role_w1<true>(ws, k, wave, lane);
    else if (k < 112) role_w1<false>(ws, k - 56, wave, lane);
    else {
      if (k < 144) role_ffn(ws, x, b1, b2, k - 112, wave, lane);
      else role_stt(ws, (k - 144) >> 1, k & 1, wave, lane, sel);
      role_w3(ws, out, (159 - k) >> 2, k & 3, wave, lane, sel);
    }
  }
}

extern "C" void kernel_launch(void* const* d_in, const int* in_sizes, int n_in, void* d_out, int out_size, void* d_ws,
                              size_t ws_size, hipStream_t stream) {
  unsigned char* ws = (unsigned char*)(((uintptr_t)d_ws + 2 * MB - 1) & ~(uintptr_t)(2 * MB - 1));
  const unsigned short* x = (const unsigned short*)d_in[0];
  constexpr int LDS_MAIN = 132 * 1024;
  (void)hipFuncSetAttribute((const void*)main_kernel, hipFuncAttributeMaxDynamicSharedMemorySize, LDS_MAIN);
  constexpr int LDS_PREP = 152 * 1024;
  (void)hipFuncSetAttribute((const void*)prep_kernel, hipFuncAttributeMaxDynamicSharedMemorySize, LDS_PREP);
  prep_kernel<<<256, 512, LDS_PREP, stream>>>(x, (const unsigned short*)d_in[2], (const unsigned short*)d_in[3],
                                           (const unsigned short*)d_in[4], (const unsigned short*)d_in[5], (const unsigned short*)d_in[8],
                                           (const unsigned short*)d_in[10], ws);
  main_kernel<<<256, 512, LDS_MAIN, stream>>>(x, (const unsigned short*)d_in[1], (const unsigned short*)d_in[9], (const unsigned short*)d_in[11], (unsigned short*)d_out, ws TUNE_PASS);
}
```

```cpp
#include <hip/hip_runtime.h>
#include <hip/hip_bf16.h>
#include <stdint.h>
#include <type_traits>

typedef __bf16 bf2 __attribute__((ext_vector_type(2)));
typedef __bf16 bf8 __attribute__((ext_vector_type(8)));
typedef __bf16 bf16x __attribute__((ext_vector_type(16)));
typedef short s4v __attribute__((ext_vector_type(4)));
typedef float f2v __attribute__((ext_vector_type(2)));
typedef float f4v __attribute__((ext_vector_type(4)));
typedef float f16v __attribute__((ext_vector_type(16)));
typedef unsigned u2v __attribute__((ext_vector_type(2)));
typedef unsigned u4v __attribute__((ext_vector_type(4)));
typedef unsigned u8v __attribute__((ext_vector_type(8)));
#define LDS __attribute__((address_space(3)))
#define GLB __attribute__((address_space(1)))
#define DEV __device__ __forceinline__

constexpr int DM = 448, NH = 16, DH = 256, NSEG = 64, FF = 1792;
constexpr unsigned SENT = 0xFFFFFFFFu;

constexpr size_t MB = 1u << 20;
constexpr size_t O_HB = 0;
constexpr size_t O_FL = 4 * MB;
constexpr size_t O_SEL = O_FL + 256 * 1024;
constexpr size_t O_XN = 5 * MB;
constexpr size_t O_WA = 7 * MB;
constexpr size_t O_WB = 14 * MB;
constexpr size_t O_W1 = 21 * MB;
constexpr size_t O_W2 = 23 * MB;
constexpr size_t O_G = 25 * MB;
constexpr size_t O_U = 55 * MB;
constexpr size_t O_ST = 85 * MB;
constexpr size_t O_SM = 87 * MB;
constexpr size_t O_YG = 100 * MB;
constexpr size_t O_F = 111 * MB;
constexpr unsigned G_RUN = 64 * 14336 + 3 * 4096, U_RUN = 64 * 16384 + 5 * 4096, Y_RUN = 64 * 4096 + 4096;
constexpr size_t O_END = 115 * MB;

DEV unsigned pk2(float a, float b) { f2v v = {a, b}; return __builtin_bit_cast(unsigned, __builtin_convertvector(v, bf2)); }
DEV float bflo(unsigned u) { return __builtin_bit_cast(float, u << 16); }
DEV float bfhi(unsigned u) { return __builtin_bit_cast(float, u & 0xffff0000u); }
DEV float rbf(float a) { return bflo(pk2(a, 0.f) & 0xffffu); }
DEV bf8 asbf8(u4v v) { return __builtin_bit_cast(bf8, v); }
DEV u4v lds_r16(unsigned a) { return *(LDS u4v*)(size_t)a; }
DEV void lds_w16(unsigned a, u4v v) { *(LDS u4v*)(size_t)a = v; }
DEV u2v lds_tr(unsigned a) { return __builtin_bit_cast(u2v, __builtin_amdgcn_ds_read_tr16_b64_v4i16((LDS s4v*)(size_t)a)); }
DEV unsigned rdtime() { unsigned long long t; asm volatile("s_memtime %0\n\ts_waitcnt lgkmcnt(0)" : "=s"(t) :: "memory"); return (unsigned)t; }
DEV int swap13(int x) { return (x & 5) | ((x >> 2) & 2) | ((x << 2) & 8); }
DEV void dma16(const void* g, unsigned ldsaddr) {
  unsigned keep;
  asm volatile("s_mov_b32 %0, m0\n\ts_mov_b32 m0, %2\n\ts_nop 0\n\tglobal_load_lds_dwordx4 %1, off\n\ts_mov_b32 m0, %0" : "=&s"(keep) : "v"(g), "s"(ldsaddr) : "memory");
}
DEV void dma16c(const void* g, unsigned ldsaddr) {
  unsigned keep;
  asm volatile("s_mov_b32 %0, m0\n\ts_mov_b32 m0, %2\n\ts_nop 0\n\tglobal_load_lds_dwordx4 %1, off sc1\n\ts_mov_b32 m0, %0" : "=&s"(keep) : "v"(g), "s"(ldsaddr) : "memory");
}
DEV void vm0() { asm volatile("s_waitcnt vmcnt(0)" ::: "memory"); }
DEV void bar() { asm volatile("s_waitcnt lgkmcnt(0)\n\ts_barrier" ::: "memory"); }

DEV void stwt16(void* p, u4v v) { asm volatile("global_store_dwordx4 %0, %1, off sc1\n\ts_nop 1" :: "v"(p), "v"(v) : "memory"); }
DEV float wavesum(float v) {
  v += __builtin_bit_cast(float, __builtin_amdgcn_update_dpp(0, __builtin_bit_cast(int, v), 0xb1, 0xf, 0xf, true));
  v += __builtin_bit_cast(float, __builtin_amdgcn_update_dpp(0, __builtin_bit_cast(int, v), 0x4e, 0xf, 0xf, true));
  v += __builtin_bit_cast(float, __builtin_amdgcn_update_dpp(0, __builtin_bit_cast(int, v), 0x124, 0xf, 0xf, true));
  v += __builtin_bit_cast(float, __builtin_amdgcn_update_dpp(0, __builtin_bit_cast(int, v), 0x128, 0xf, 0xf, true));
  const unsigned u = __builtin_bit_cast(unsigned, v);
  const u2v a = __builtin_amdgcn_permlane16_swap(u, u, false, false);
  const unsigned ax = a.x, ay = a.y;
  const unsigned w = __builtin_bit_cast(unsigned, __builtin_bit_cast(float, ax) + __builtin_bit_cast(float, ay));
  const u2v c = __builtin_amdgcn_permlane32_swap(w, w, false, false);
  const unsigned cx = c.x, cy = c.y;
  return __builtin_bit_cast(float, cx) + __builtin_bit_cast(float, cy);
}
#define K0S(k)
DEV void ln_row(u4v xv, unsigned char* __restrict__ ws, int row, int lane) {
  float e[8];
  if (lane >= 56) xv = (u4v){0u, 0u, 0u, 0u};
#pragma unroll
  for (int j = 0; j < 4; ++j) { e[2 * j] = bflo(xv[j]); e[2 * j + 1] = bfhi(xv[j]); }
  float s = 0.f;
#pragma unroll
  for (int j = 0; j < 8; ++j) s += e[j];
  const float mean = wavesum(s) * (1.f / DM);
  float q = 0.f;
  if (lane < 56) {
#pragma unroll
    for (int j = 0; j < 8; ++j) { const float d = e[j] - mean; q += d * d; }
  }
  const float mb = rbf(mean), vb = rbf(wavesum(q) * (1.f / DM));
  const float den = rbf(__builtin_sqrtf(rbf(vb + 1e-5f)));
  unsigned o4[4];
#pragma unroll
  for (int j = 0; j < 4; ++j) o4[j] = pk2(rbf(e[2 * j] - mb) / den, rbf(e[2 * j + 1] - mb) / den);
  const bool odd = lane & 1;
  const unsigned r0 = __builtin_amdgcn_update_dpp(0u, odd ? o4[0] : o4[2], 0xb1, 0xf, 0xf, true);
  const unsigned r1 = __builtin_amdgcn_update_dpp(0u, odd ? o4[1] : o4[3], 0xb1, 0xf, 0xf, true);
  u4v p;
  if (odd) p = (u4v){r0, o4[2], r1, o4[3]}; else p = (u4v){o4[0], r0, o4[1], r1};
  if (lane < 56) stwt16(ws + O_XN + (size_t)(row >> 5) * 28672 + (lane >> 1) * 1024 + (lane & 1) * 512 + (row & 31) * 16, p);
}
DEV u4v srd(const void* p, unsigned bytes) { const unsigned long long a = (unsigned long long)p; return (u4v){(unsigned)a, (unsigned)(a >> 32), bytes, 0x00020000u}; }
DEV void bdma16(unsigned voff, u4v r, unsigned soff, unsigned ldsaddr) {
  asm volatile("s_mov_b32 m0, %3\n\ts_nop 0\n\tbuffer_load_dwordx4 %0, %1, %2 offen lds" :: "v"(voff), "s"(r), "s"(soff), "s"(ldsaddr) : "memory");
}
constexpr unsigned rowoff(int j) { return (16 * ((j >> 2) & 1) + 8 * (j & 1) + 4 * ((j >> 1) & 1) + 2 * (j >> 3)) * 8192u; }

template <bool ISA>
DEV void prep_gemm(const unsigned short* __restrict__ Rw, const unsigned short* __restrict__ Cw, const unsigned short* __restrict__ x,
                   unsigned char* __restrict__ ws, int b, int h, int cg, int wave, int lane) {
  const int g = lane >> 4, c16 = lane & 15, hl = lane >> 5, sl = lane & 31;
  const int row = b * 8 + wave;
  const unsigned xl = 147456 + wave * 1024;
  dma16(x + (size_t)row * DM + min(lane, 55) * 8, xl);
  if (ISA) {
    const u4v rc = srd(Cw + (size_t)64 * cg * 4096 + h * 256, 64 * 8192);
    const unsigned v0 = hl * 8192 + ((sl ^ hl ^ ((wave & 1) << 3)) << 4), s0 = ((wave >> 1) * 16 + (wave & 1) * 2) * 8192;
#pragma unroll
    for (int j = 0; j < 4; ++j) bdma16(v0 ^ (j << 5), rc, s0 + ((j & 1) * 8 + (j >> 1) * 4) * 8192, (wave * 4 + j) * 1024);
  } else {
    const u4v rc = srd(Cw + (size_t)h * 256 * DM + 64 * cg, 256 * 896);
    const unsigned v0 = (lane >> 3) * 896 + (((lane & 7) ^ (((lane >> 4) & 1) << 1)) << 4);
#pragma unroll
    for (int j = 0; j < 4; ++j) bdma16(v0 ^ ((j & 1) << 6), rc, (wave * 4 + j) * 8 * 896, (wave * 4 + j) * 1024);
  }
  if (wave == 7) {
    __builtin_amdgcn_sched_barrier(0);
    asm volatile("s_waitcnt vmcnt(0)\n\ts_barrier" ::: "memory");
    const u4v ones = {SENT, SENT, SENT, SENT}, zero = {0u, 0u, 0u, 0u};
#pragma unroll
    for (int j = 0; j < 16; ++j) stwt16(ws + O_HB + (size_t)b * 16384 + j * 1024 + lane * 16, ones);
    stwt16(ws + O_FL + b * 1024 + lane * 16, zero);
    ln_row(lds_r16(xl + lane * 16), ws, row, lane);
    return;
  }
  const float sc = ISA ? 0.0625f * 1.4426950408889634f : 1.f;
  unsigned char* dst = ws + (ISA ? O_WA : O_WB) + (size_t)(h * 28 + 4 * cg) * 14336 + 2 * wave * 1024 + lane * 16;
  const unsigned reg = 32768 + wave * 16384;
  const int rot = cg * 2;
  const unsigned srot = (16 * ((rot >> 2) & 1) + 8 * (rot & 1) + 4 * ((rot >> 1) & 1) + 2 * (rot >> 3)) * 8192u, mrot = reg | (rot << 10);
  const unsigned vr = hl * 8192 + (((sl ^ hl) << 4) ^ ((rot & 7) << 5));
  const u4v rr = srd(Rw + (size_t)(64 * wave) * 4096 + h * 256, 64 * 8192);
  const __amdgpu_buffer_rsrc_t rq = __builtin_amdgcn_make_buffer_rsrc((void*)(Rw + (size_t)(64 * wave + 32) * 4096 + h * 256), 0, 32 * 8192, 0x00020000);
  u4v nx[16];
#pragma unroll
  for (int j = 0; j < 2; ++j) bdma16(vr ^ ((j & 7) << 5), rr, srot ^ rowoff(j), mrot ^ (j << 10));
#pragma unroll
  for (int j = 0; j < 2; ++j) nx[j] = __builtin_amdgcn_raw_buffer_load_b128(rq, vr ^ ((j & 7) << 5), srot ^ rowoff(j), 0);
  __builtin_amdgcn_sched_barrier(0);
  asm volatile("s_waitcnt vmcnt(8)" ::: "memory");
  ln_row(lds_r16(xl + lane * 16), ws, row, lane);
  __builtin_amdgcn_sched_barrier(0);
  K0S(1);
  asm volatile("s_waitcnt vmcnt(5)\n\ts_barrier" ::: "memory");
  K0S(0);
#pragma unroll
  for (int j = 2; j < 16; ++j) bdma16(vr ^ ((j & 7) << 5), rr, srot ^ rowoff(j), mrot ^ (j << 10));
#pragma unroll
  for (int j = 2; j < 16; ++j) nx[j] = __builtin_amdgcn_raw_buffer_load_b128(rq, vr ^ ((j & 7) << 5), srot ^ rowoff(j), 0);
  __builtin_amdgcn_sched_barrier(0);
  asm volatile("s_waitcnt vmcnt(14)" ::: "memory");
  K0S(2);
  const int q = c16 >> 2, p = c16 & 3;
  const int xr = (q >> 1) | ((g & 1) << 1);
  auto bfrag = [&](int es, int ti) -> u4v {
    if (ISA) return lds_r16((ti * 16 + c16) * 512 + (((es * 4 + g) ^ c16) << 4));
    const unsigned ad = (32 * es + 8 * g + q) * 128 + ((ti ^ xr) << 5) + p * 8;
    const u2v lo = lds_tr(ad), hi = lds_tr(ad + 512);
    return (u4v){lo.x, lo.y, hi.x, hi.y};
  };
  auto afrag = [&](int es, int t) -> u4v { return lds_r16(reg + (t * 16 + c16) * 512 + (((es * 4 + g) ^ c16) << 4)); };
#pragma unroll
  for (int rb = 0; rb < 2; ++rb) {
    u4v bb[2][4], aa[2][2];
#pragma unroll
    for (int ti = 0; ti < 4; ++ti) bb[0][ti] = bfrag(0, ti);
    aa[0][0] = afrag(0, 0); aa[0][1] = afrag(0, 1);
    f4v acc[2][4];
#pragma unroll
    for (int t = 0; t < 2; ++t)
#pragma unroll
      for (int ti = 0; ti < 4; ++ti) acc[t][ti] = (f4v){0.f, 0.f, 0.f, 0.f};
#pragma unroll
    for (int es = 0; es < 8; ++es) {
      if (es < 7) {
        aa[(es + 1) & 1][0] = afrag(es + 1, 0); aa[(es + 1) & 1][1] = afrag(es + 1, 1);
#pragma unroll
        for (int ti = 0; ti < 4; ++ti) bb[(es + 1) & 1][ti] = bfrag(es + 1, ti);
      }
#pragma unroll
      for (int ti = 0; ti < 4; ++ti)
#pragma unroll
        for (int t = 0; t < 2; ++t)
          acc[t][ti] = __builtin_amdgcn_mfma_f32_16x16x32_bf16(asbf8(aa[es & 1][t]), asbf8(bb[es & 1][ti]), acc[t][ti], 0, 0, 0);
      __builtin_amdgcn_sched_barrier(0);
    }
    if (rb == 0) {
#pragma unroll
      for (int j = 0; j < 16; ++j) lds_w16((mrot ^ (j << 10)) + lane * 16, nx[j]);
    }
#pragma unroll
    for (int ti = 0; ti < 4; ++ti) {
      const u4v o = {pk2(acc[0][ti][0] * sc, acc[0][ti][1] * sc), pk2(acc[0][ti][2] * sc, acc[0][ti][3] * sc),
                     pk2(acc[1][ti][0] * sc, acc[1][ti][1] * sc), pk2(acc[1][ti][2] * sc, acc[1][ti][3] * sc)};
      stwt16(dst + (size_t)ti * 14336 + rb * 1024, o);
    }
  }
  K0S(3);
}

__global__ void __launch_bounds__(512, 2)
prep_kernel(const unsigned short* __restrict__ x,
            const unsigned short* __restrict__ Wq, const unsigned short* __restrict__ Wk,
            const unsigned short* __restrict__ Wv, const unsigned short* __restrict__ Wo,
            const unsigned short* __restrict__ w1, const unsigned short* __restrict__ w2,
            unsigned char* __restrict__ ws) {
  const int b = blockIdx.x, tid = threadIdx.x, lane = tid & 63;
  const int wave = __builtin_amdgcn_readfirstlane(tid >> 6);
  const int xc = b & 7, slot = b >> 3;
  if (slot < 28) {
    const int m = xc * 4 + slot / 7, cg = slot % 7;
    if (m < 16) prep_gemm<true>(Wk, Wq, x, ws, b, m, cg, wave, lane);
    else prep_gemm<false>(Wv, Wo, x, ws, b, m - 16, cg, wave, lane);
  } else {
    const int row = b * 8 + wave;
    u4v xv = {0u, 0u, 0u, 0u};
    const bool prober = slot == 28 && wave == 7;
    const int job = slot == 28 ? xc * 7 + wave : 56 + ((slot - 29) * 8 + xc) * 8 + wave;
    const bool work = !prober && job < 224;
    const unsigned reg = wave * 14336;
    if (work) {
      const bool first = job < 112;
      const int step = first ? job >> 2 : job - 112;
      const unsigned short* src = first ? w1 + (size_t)step * 16 * FF + (job & 3) * 448 : w2 + (size_t)step * 16 * DM;
      const int ld = first ? FF : DM;
#pragma unroll
      for (int j = 0; j < 14; ++j) { const int n = j * 64 + lane, r = n / 56; dma16(src + (size_t)r * ld + (n - r * 56) * 8, reg + j * 1024); }
    }
    if (lane < 56) xv = *(const u4v*)(x + (size_t)row * DM + lane * 8);
    if (prober) {
      const u4v ones = {SENT, SENT, SENT, SENT};
      stwt16(ws + O_HB + (lane >> 4) * 8192 + (lane & 15) * 64, ones);
      stwt16(ws + O_HB + 2 * MB + (lane >> 4) * 8192 + (lane & 15) * 64, ones);
    }
    asm volatile("" : "+v"(xv));
    vm0();
    if (slot == 28) bar();
    if (prober) {
      const unsigned xcc = (unsigned)__builtin_amdgcn_s_getreg(20 | (3 << 11)) & 7u;
      unsigned word = xcc << 4;
      if (xcc == 0) {
        auto timed = [&](const unsigned char* pc) -> unsigned {
          unsigned v;
          const unsigned c0 = rdtime();
          asm volatile("global_load_dword %0, %1, %2 sc1\n\ts_waitcnt vmcnt(0)" : "=&v"(v) : "v"(0u), "s"(pc) : "memory");
          return rdtime() - c0;
        };
        unsigned best[6];
#pragma unroll
        for (int c = 0; c < 6; ++c) best[c] = 0x7fffffff;
#pragma unroll 1
        for (int it = 0; it < 3; ++it) {
#pragma unroll
          for (int c = 0; c < 4; ++c) best[c] = min(best[c], timed(ws + O_HB + c * 8192 + it * 256));
        }
        unsigned sel = 0, bv = best[0];
#pragma unroll
        for (unsigned k = 1; k < 4; ++k) if (best[k] < bv) { bv = best[k]; sel = k; }
#pragma unroll 1
        for (int it = 0; it < 2; ++it) {
#pragma unroll
          for (int c = 0; c < 2; ++c) best[4 + c] = min(best[4 + c], timed(ws + O_HB + 2 * MB + (sel ^ c) * 8192 + it * 256));
        }
        word |= 0x80u | sel | ((sel ^ (best[5] <= best[4] ? 1u : 0u)) << 2);
      }
      if (lane == 0) asm volatile("global_store_byte %0, %1, off sc1" :: "v"(ws + O_SEL + xc), "v"(word) : "memory");
    }
    if (work) {
      const bool first = job < 112;
      const int hi = lane >> 5, q = (lane & 15) >> 2;
      const int r0 = first ? 4 * hi + 8 * (q >> 1) + (q & 1) : 4 * hi + q, dr = first ? 2 : 8;
      const unsigned ad = reg + r0 * 896 + ((lane >> 4) & 1) * 32 + (lane & 3) * 8;
      unsigned char* dst = ws + (first ? O_W1 + (size_t)((job & 3) * 14 * 28 + (job >> 2)) * 1024 : O_W2 + (size_t)(job - 112) * 1024) + lane * 16;
      const size_t dstep = first ? 28672 : 114688;
#pragma unroll
      for (int t = 0; t < 14; ++t) {
        const u2v lo = lds_tr(ad + t * 64), hv = lds_tr(ad + t * 64 + dr * 896);
        stwt16(dst + t * dstep, (u4v){lo.x, lo.y, hv.x, hv.y});
      }
    }
    const u4v ones = {SENT, SENT, SENT, SENT}, zero = {0u, 0u, 0u, 0u};
    stwt16(ws + O_HB + (size_t)b * 16384 + tid * 16, ones);
    stwt16(ws + O_HB + (size_t)b * 16384 + 8192 + tid * 16, ones);
    if (wave == 0) stwt16(ws + O_FL + b * 1024 + lane * 16, zero);
    ln_row(xv, ws, row, lane);
  }
}
constexpr int C_W = 0  , C_F = 1856, C_ABORT = 1900, C_ANCHOR = 1920;
typedef __amdgpu_buffer_rsrc_t rsrc_t;
DEV rsrc_t mkrsrc(const void* p, unsigned bytes) { return __builtin_amdgcn_make_buffer_rsrc((void*)p, 0, bytes, 0x00020000); }
DEV u4v ldc16(rsrc_t r, unsigned off) { return __builtin_amdgcn_raw_buffer_load_b128(r, off, 0, 16); }
DEV u2v ldc8(rsrc_t r, unsigned off) { return __builtin_amdgcn_raw_buffer_load_b64(r, off, 0, 16); }
DEV void stc16(rsrc_t r, unsigned off, u4v v) { __builtin_amdgcn_raw_buffer_store_b128(v, r, off, 0, 16); }
DEV void stc4(rsrc_t r, unsigned off, unsigned v) { __builtin_amdgcn_raw_buffer_store_b32(v, r, off, 0, 16); }
DEV unsigned* cntp(unsigned char* ws, int idx) { return (unsigned*)(ws + O_FL) + idx * 16; }
DEV unsigned ld_cnt(const unsigned* p) { unsigned v; asm volatile("global_load_dword %0, %1, off sc1\n\ts_waitcnt vmcnt(0)" : "=&v"(v) : "v"(p) : "memory"); return v; }
DEV void add_cnt(unsigned* p, int lane) {
  if (lane == 0) { const unsigned one = 1; asm volatile("global_atomic_add %0, %1, off sc1" :: "v"(p), "v"(one) : "memory"); }
}
DEV void post(unsigned* p, unsigned v, int lane) { if (lane == 0) asm volatile("global_store_dword %0, %1, off sc1" :: "v"(p), "v"(v) : "memory"); }
DEV unsigned* doneG(unsigned char* ws, int j) { return cntp(ws, C_W + 2 * j); }
DEV unsigned* doneU(unsigned char* ws, int j) { return cntp(ws, C_W + 2 * (56 + j)); }
constexpr int SPIN_CAP = 1 << 17;
DEV void wait_cnt(unsigned char* ws, const unsigned* p, unsigned target, int nap = 6) {
  for (int it = 0;; ++it) {
    const unsigned v = ld_cnt(p);
    if (__all(v >= target)) return;
    if ((it & 63) == 63) {
      if (it >= SPIN_CAP) { const unsigned one = 1; asm volatile("global_store_dword %0, %1, off sc1" :: "v"(cntp(ws, C_ABORT)), "v"(one) : "memory"); return; }
      if (__any(ld_cnt(cntp(ws, C_ABORT)) != 0)) return;
    }
    if (nap > 6) __builtin_amdgcn_s_sleep(32); else __builtin_amdgcn_s_sleep(2);
  }
}
DEV unsigned hoff(unsigned sel, int gi, int kind) {
  const unsigned k = ((sel >> (2 * (gi >> 6))) & 3u) ^ ((((unsigned)gi >> 4) & 1u) << 1) ^ (unsigned)kind;
  return (unsigned)O_HB + (unsigned)gi * 32768u + k * 8192u;
}
DEV unsigned char* hblk(unsigned char* ws, unsigned sel, int gi, int kind) { return ws + hoff(sel, gi, kind); }
DEV unsigned char* flagS(unsigned char* ws, unsigned sel, int s, int id) { return hblk(ws, sel, s, 3) + id * 128; }
DEV unsigned char* flagY(unsigned char* ws, unsigned sel, int s, int v) { return hblk(ws, sel, 64 + s, 3) + v * 128; }
DEV unsigned char* flagT(unsigned char* ws, unsigned sel, int s, int h) { return hblk(ws, sel, 64 + s, 1) + h * 128; }
DEV void wait_flags(unsigned char* ws, const unsigned char* p) {
  for (int it = 0;; ++it) {
    if (!__any(ld_cnt((const unsigned*)p) == SENT)) return;
    if ((it & 63) == 63) {
      if (it >= SPIN_CAP) { const unsigned one = 1; asm volatile("global_store_dword %0, %1, off sc1" :: "v"(cntp(ws, C_ABORT)), "v"(one) : "memory"); return; }
      if (__any(ld_cnt(cntp(ws, C_ABORT)) != 0)) return;
    }
    __builtin_amdgcn_s_sleep(6);
  }
}
DEV void wait_flags2(unsigned char* ws, const unsigned char* p, const unsigned char* q) {
  for (int it = 0; it < SPIN_CAP; ++it) {
    unsigned x, y;
    asm volatile("global_load_dword %0, %2, off sc1\n\tglobal_load_dword %1, %3, off sc1\n\ts_waitcnt vmcnt(0)" : "=&v"(x), "=&v"(y) : "v"(p), "v"(q) : "memory");
    if (!__any(max(x, y) == SENT)) return;
    if ((it & 63) == 63 && __any(ld_cnt(cntp(ws, C_ABORT)) != 0)) return;
    __builtin_amdgcn_s_sleep(2);
  }
}
DEV f16v mfma32(u4v a, u4v b, f16v c) { return __builtin_amdgcn_mfma_f32_32x32x16_bf16(asbf8(a), asbf8(b), c, 0, 0, 0); }
DEV f4v mfma16(u4v a, u4v b, f4v c) { return __builtin_amdgcn_mfma_f32_16x16x32_bf16(asbf8(a), asbf8(b), c, 0, 0, 0); }
DEV void st16c_asm(void* p, u4v v) { asm volatile("global_store_dwordx4 %0, %1, off sc1\n\ts_nop 1" :: "v"(p), "v"(v) : "memory"); }

#define RTS(k)
#define W1_PRE 4
template <bool ISG>
DEV void role_w1(unsigned char* __restrict__ ws, int j, int wave, int lane) {
  const int ct = wave & 3, rh = wave >> 2, hi = lane >> 5, c32 = lane & 31;
  int h, tile0;
  if (ISG) { const int hp = j / 7, ctile = (j % 7) * 4 + ct; h = 2 * hp + ctile / 14; tile0 = 2 * (ctile % 14); }
  else { const int V = j >> 2; h = 4 * (j & 3) + ct; tile0 = 2 * V; }
  const unsigned char* xn = ws + O_XN + wave * 7168 + lane * 16;
#pragma unroll
  for (int i = 0; i < 7; ++i) dma16(xn + i * 1024, wave * 7168 + i * 1024);
  u4v w[28];
  {
    const unsigned char* p = ws + (ISG ? O_WA : O_WB) + (size_t)(h * 28 + tile0 + (c32 >> 4)) * 14336 + hi * 256 + (c32 & 15) * 16;
#pragma unroll
    for (int s = 0; s < 28; ++s) {
      w[s] = *(const u4v*)(p + (s >> 1) * 1024 + (s & 1) * 512);
      if (s == W1_PRE - 1) {
        __builtin_amdgcn_sched_barrier(0);
        asm volatile("s_waitcnt vmcnt(%0)\n\ts_barrier" :: "i"(W1_PRE) : "memory");
        __builtin_amdgcn_sched_barrier(0);
      }
    }
  }
  constexpr int SEGB = ISG ? 14336 : 16384;
  unsigned char* out = (ISG ? ws + O_G + (size_t)(h * 2 + (c32 >> 4)) * G_RUN + (tile0 >> 1) * 1024
                            : ws + O_U + (size_t)(tile0 + (c32 >> 4)) * U_RUN + h * 1024) + rh * SEGB + hi * 256 + (c32 & 15) * 16;
  const int tok = ISG ? c32 : ((c32 & 19) | ((c32 >> 1) & 4) | ((c32 << 1) & 8));
  const unsigned rd = rh * 28672 + hi * 512 + tok * 16;
  unsigned* done = ISG ? doneG(ws, j) : doneU(ws, j);
  auto pass = [&](int p, auto firstc) {
    constexpr bool FIRST = decltype(firstc)::value;
    const unsigned buf = (p & 1) * 57344;
    const unsigned char* src = xn + (size_t)(p < 31 ? p + 1 : p) * 57344;
    const unsigned dstl = (57344 - buf) + wave * 7168;
    f16v acc;
#pragma unroll
    for (int i = 0; i < 16; ++i) acc[i] = 0.f;
    const LDS u4v* xp = (const LDS u4v*)(size_t)(buf + rd);
    u4v xr[5];
#pragma unroll
    for (int s = 0; s < 4; ++s) xr[s] = xp[s * 64];
#pragma unroll
    for (int s = 0; s < 28; ++s) {
      if (s + 4 < 28) xr[(s + 4) % 5] = xp[(s + 4) * 64];
      acc = ISG ? mfma32(w[s], xr[s % 5], acc) : mfma32(xr[s % 5], w[s], acc);
      __builtin_amdgcn_sched_barrier(0);
      if (!FIRST && s % 3 == 0 && s / 3 < 7) { dma16(src + (s / 3) * 1024, dstl + (s / 3) * 1024); __builtin_amdgcn_sched_barrier(0); }
      if (!FIRST && s == 16) {
        asm volatile("s_waitcnt vmcnt(6)" ::: "memory");
        __builtin_amdgcn_sched_barrier(0);
      }
    }
    const u4v o0 = {pk2(acc[0], acc[1]), pk2(acc[2], acc[3]), pk2(acc[4], acc[5]), pk2(acc[6], acc[7])};
    const u4v o1 = {pk2(acc[8], acc[9]), pk2(acc[10], acc[11]), pk2(acc[12], acc[13]), pk2(acc[14], acc[15])};
    unsigned char* o = out + p * 2 * SEGB;
    st16c_asm(o, o0); st16c_asm(o + 512, o1);
    if (FIRST) {
#pragma unroll
      for (int i = 0; i < 7; ++i) dma16(src + i * 1024, dstl + i * 1024);
    }
    if (p < 4) {
      RTS(4);
      if (FIRST) asm volatile("s_waitcnt vmcnt(7)\n\ts_barrier" ::: "memory");
      else asm volatile("s_waitcnt vmcnt(0)\n\ts_barrier" ::: "memory");
      if (wave == 7) post(done, p + 1, lane);
      if (FIRST) { RTS(2); vm0(); }
    }
  };
  __builtin_amdgcn_sched_barrier(0);
  RTS(1);
  pass(0, std::true_type{});
  for (int p = 1; p < 32; ++p) {
    asm volatile("s_waitcnt vmcnt(2)\n\ts_barrier" ::: "memory");
    if (wave == 7 && p >= 6) post(done, p - 1, lane);
    pass(p, std::false_type{});
  }
  asm volatile("s_waitcnt vmcnt(0)\n\ts_barrier" ::: "memory");
  if (wave == 7) post(done, 32, lane);
  RTS(3);
}

template <bool P2>
DEV void ffn_body(unsigned char* __restrict__ ws, const unsigned short* __restrict__ x, const unsigned short* __restrict__ b1,
                  const unsigned short* __restrict__ b2, int job, int wave, int lane) {
  const int hi = lane >> 5, c32 = lane & 31;
  constexpr int NL = P2 ? 60 : 28, RING = P2 ? 12 : 14, DIST = RING - 1;
  const u4v* w1p = (const u4v*)(ws + O_W1 + (size_t)wave * 28672) + lane;
  const u4v* w2p = (const u4v*)(ws + O_W2 + (size_t)(2 * wave) * 114688) + lane;
  auto wload = [&](int c, int i) -> u4v {
    if (i < 28) return w1p[(size_t)c * 14336 + i * 64];
    return w2p[(size_t)c * 1024 + ((i - 28) >> 1) * 64 + ((i - 28) & 1) * 7168];
  };
  u4v wr[RING];
#pragma unroll
  for (int i = 0; i < DIST; ++i) wr[i] = wload(0, i);
  {
    const unsigned char* xn = ws + O_XN + (size_t)job * 57344 + wave * 7168 + lane * 16;
#pragma unroll
    for (int i = 0; i < 7; ++i) dma16(xn + i * 1024, wave * 7168 + i * 1024);
  }
  f16v o[2][2];
#pragma unroll
  for (int a = 0; a < 2; ++a)
#pragma unroll
    for (int bq = 0; bq < 2; ++bq)
#pragma unroll
      for (int i = 0; i < 16; ++i) o[a][bq][i] = 0.f;
  vm0();
  bar();
  const LDS u4v* xp = (const LDS u4v*)(size_t)(lane * 16);
  for (int c = 0; c < 7; ++c) {
    const int cn = c < 6 ? c + 1 : 6;
    const unsigned hbuf = 57344 + (c & 1) * 32768;
    u2v bv[4];
#pragma unroll
    for (int q = 0; q < 4; ++q) bv[q] = *(const u2v*)(b1 + 32 * (8 * c + wave) + 4 * hi + 8 * q);
    f16v a0, a1;
#pragma unroll
    for (int i = 0; i < 16; ++i) { a0[i] = 0.f; a1[i] = 0.f; }
    u4v xa[3], xb[3];
#pragma unroll
    for (int s = 0; s < 2; ++s) { xa[s] = xp[s * 64]; xb[s] = xp[1792 + s * 64]; }
#pragma unroll
    for (int s = 0; s < 28; ++s) {
      wr[(s + DIST) % RING] = (s + DIST < NL) ? wload(c, s + DIST) : wload(cn, s + DIST - NL);
      if (s + 2 < 28) { xa[(s + 2) % 3] = xp[(s + 2) * 64]; xb[(s + 2) % 3] = xp[1792 + (s + 2) * 64]; }
      a0 = mfma32(wr[s % RING], xa[s % 3], a0);
      a1 = mfma32(wr[s % RING], xb[s % 3], a1);
      __builtin_amdgcn_sched_barrier(0);
    }
#pragma unroll
    for (int i = 0; i < 16; ++i) {
      const unsigned bw = bv[i >> 2][(i >> 1) & 1];
      const float bs = (i & 1) ? bfhi(bw) : bflo(bw);
      const float t0 = fmaxf(a0[i] + bs, 0.f), t1 = fmaxf(a1[i] + bs, 0.f);
      a0[i] = t0 * t0; a1[i] = t1 * t1;
    }
#pragma unroll
    for (int bq = 0; bq < 2; ++bq) {
      lds_w16(hbuf + ((wave * 2 + bq) * 2 + 0) * 1024 + lane * 16,
              (u4v){pk2(a0[8 * bq], a0[8 * bq + 1]), pk2(a0[8 * bq + 2], a0[8 * bq + 3]), pk2(a0[8 * bq + 4], a0[8 * bq + 5]), pk2(a0[8 * bq + 6], a0[8 * bq + 7])});
      lds_w16(hbuf + ((wave * 2 + bq) * 2 + 1) * 1024 + lane * 16,
              (u4v){pk2(a1[8 * bq], a1[8 * bq + 1]), pk2(a1[8 * bq + 2], a1[8 * bq + 3]), pk2(a1[8 * bq + 4], a1[8 * bq + 5]), pk2(a1[8 * bq + 6], a1[8 * bq + 7])});
    }
    bar();
    if (P2) {
      const LDS u4v* hp = (const LDS u4v*)(size_t)(hbuf + lane * 16);
      u4v h0[2], h1[2];
      h0[0] = hp[0]; h1[0] = hp[64];
#pragma unroll
      for (int s = 0; s < 16; ++s) {
        const int i = 28 + 2 * s;
        wr[(i + DIST) % RING] = (i + DIST < NL) ? wload(c, i + DIST) : wload(cn, i + DIST - NL);
        if (s + 1 < 16) { h0[(s + 1) & 1] = hp[(s + 1) * 128]; h1[(s + 1) & 1] = hp[(s + 1) * 128 + 64]; }
        o[0][0] = mfma32(wr[i % RING], h0[s & 1], o[0][0]); o[0][1] = mfma32(wr[i % RING], h1[s & 1], o[0][1]);
        __builtin_amdgcn_sched_barrier(0);
        wr[(i + 1 + DIST) % RING] = (i + 1 + DIST < NL) ? wload(c, i + 1 + DIST) : wload(cn, i + 1 + DIST - NL);
        o[1][0] = mfma32(wr[(i + 1) % RING], h0[s & 1], o[1][0]); o[1][1] = mfma32(wr[(i + 1) % RING], h1[s & 1], o[1][1]);
        __builtin_amdgcn_sched_barrier(0);
      }
    }
  }
  if (P2) {
    const rsrc_t rf = mkrsrc(ws + O_F, 4u << 20);
#pragma unroll
    for (int a = 0; a < 2; ++a)
#pragma unroll
      for (int sg = 0; sg < 2; ++sg)
#pragma unroll
        for (int Q = 0; Q < 4; ++Q) {
          const int n = 32 * (2 * wave + a) + 8 * Q + 4 * hi, seg = 2 * job + sg;
          const u2v xv = *(const u2v*)(x + (size_t)(seg * 32 + c32) * DM + n), bv = *(const u2v*)(b2 + n);
          const f4v r = {o[a][sg][4 * Q] + bflo(bv.x) + bflo(xv.x), o[a][sg][4 * Q + 1] + bfhi(bv.x) + bfhi(xv.x),
                         o[a][sg][4 * Q + 2] + bflo(bv.y) + bflo(xv.y), o[a][sg][4 * Q + 3] + bfhi(bv.y) + bfhi(xv.y)};
          const int v = 2 * (2 * wave + a) + (Q >> 1), g = 2 * (Q & 1) + hi;
          stc16(rf, ((seg * 28 + v) * 2 + (c32 >> 4)) * 1024 + (g * 16 + (c32 & 15)) * 16, __builtin_bit_cast(u4v, r));
        }
    vm0();
    add_cnt(cntp(ws, C_F + job), lane);
    RTS(3);
  }
}
DEV void role_ffn(unsigned char* __restrict__ ws, const unsigned short* __restrict__ x, const unsigned short* __restrict__ b1,
                  const unsigned short* __restrict__ b2, int job, int wave, int lane) {
  if (wave < 7) ffn_body<true>(ws, x, b1, b2, job, wave, lane);
  else ffn_body<false>(ws, x, b1, b2, job, wave, lane);
}

DEV float asf(unsigned u) { return __builtin_bit_cast(float, u); }
DEV unsigned asu(float f) { return __builtin_bit_cast(unsigned, f); }
DEV float xmax16_32(float v) {
  const u2v a = __builtin_amdgcn_permlane16_swap(asu(v), asu(v), false, false);
  v = fmaxf(asf(a.x), asf(a.y));
  const u2v c = __builtin_amdgcn_permlane32_swap(asu(v), asu(v), false, false);
  return fmaxf(asf(c.x), asf(c.y));
}
DEV float xsum16_32(float v) {
  const u2v a = __builtin_amdgcn_permlane16_swap(asu(v), asu(v), false, false);
  v = asf(a.x) + asf(a.y);
  const u2v c = __builtin_amdgcn_permlane32_swap(asu(v), asu(v), false, false);
  return asf(c.x) + asf(c.y);
}
DEV float halfsum(float a, float b) {
  const u2v s = __builtin_amdgcn_permlane32_swap(asu(a), asu(b), false, false);
  return asf(s.x) + asf(s.y);
}

DEV void role_stt(unsigned char* __restrict__ ws, int grp, int hg, int wave, int lane, unsigned sel) {
  const int g = lane >> 4, c16 = lane & 15, h = 8 * hg + wave;
  const rsrc_t rg = mkrsrc(ws + O_G, 30u << 20);
  const LDS u4v* xp = (const LDS u4v*)(size_t)((g >> 1) * 1024 + (g & 1) * 512 + c16 * 16);
  for (int s = grp; s < NSEG; s += 8) {
    if (wave == 7) wait_cnt(ws, doneG(ws, 28 * hg + min(lane, 27)), (s >> 1) + 1, 32);
    bar();
    if (wave < 7) {
#pragma unroll
      for (int i = 0; i < 4; ++i) dma16(ws + O_XN + (size_t)s * 28672 + (wave * 4 + i) * 1024 + lane * 16, (wave * 4 + i) * 1024);
    }
    u4v ga[28];
#pragma unroll
    for (int T = 0; T < 28; ++T) ga[T] = ldc16(rg, (h * 2 + T / 14) * G_RUN + s * 14336 + (T % 14) * 1024 + lane * 16);
    __builtin_amdgcn_sched_barrier(0);
    asm volatile("s_waitcnt vmcnt(28)\n\ts_barrier" ::: "memory");
    f4v acc[2][2];
#pragma unroll
    for (int q = 0; q < 2; ++q) { acc[0][q] = (f4v){0.f, 0.f, 0.f, 0.f}; acc[1][q] = acc[0][q]; }
#pragma unroll
    for (int T = 0; T < 14; ++T) {
      const u4v x0 = xp[T * 128], x1 = xp[T * 128 + 16];
      acc[0][0] = mfma16(ga[T], x0, acc[0][0]); acc[0][1] = mfma16(ga[T], x1, acc[0][1]);
      acc[1][0] = mfma16(ga[14 + T], x0, acc[1][0]); acc[1][1] = mfma16(ga[14 + T], x1, acc[1][1]);
    }
    unsigned char* o = ws + O_ST + (size_t)(s * 16 + h) * 2048 + c16 * 32 + g * 8;
#pragma unroll
    for (int q = 0; q < 2; ++q)
#pragma unroll
      for (int t = 0; t < 2; ++t) {
        const u2v val = {pk2(acc[t][q][0], acc[t][q][1]), pk2(acc[t][q][2], acc[t][q][3])};
        asm volatile("global_store_dwordx2 %0, %1, off sc1" :: "v"(o + q * 1024 + t * 512), "v"(val) : "memory");
      }
    vm0();
    if (lane == 0) { const unsigned zero = 0; asm volatile("global_store_dword %0, %1, off sc1" :: "v"(flagT(ws, sel, s, h)), "v"(zero) : "memory"); }
  }
  RTS(3);
}

constexpr int W3_GROUPS = 12;
DEV void role_w3(unsigned char* __restrict__ ws, unsigned short* __restrict__ out, int grp, int C, int wave, int lane, unsigned sel) {
  const int g = lane >> 4, c16 = lane & 15, v = 7 * C + (wave < 7 ? wave : 6);
  const rsrc_t ru = mkrsrc(ws + O_U, 30u << 20), rf = mkrsrc(ws + O_F, 4u << 20);
  constexpr unsigned L_P = 0, L_PM = 32768, L_ST = 40960, L_SM = L_ST + 32768, L_Y = L_SM + 32768;
  for (int s = grp; s < NSEG; s += W3_GROUPS) {
    if (wave == 7) {
      wait_cnt(ws, doneU(ws, 4 * ((7 * C) >> 1) + (lane & 15)), (s >> 1) + 1);
      wait_cnt(ws, cntp(ws, C_F + (s >> 1)), 7);
      wait_flags(ws, flagT(ws, sel, s, lane & 15));
    }
    bar();
    u4v uf[16], ff[2];
    if (wave < 7) {
#pragma unroll
      for (int hh = 0; hh < 16; ++hh) uf[hh] = ldc16(ru, v * U_RUN + (s * 16 + hh) * 1024 + lane * 16);
#pragma unroll
      for (int q = 0; q < 2; ++q) ff[q] = ldc16(rf, ((s * 28 + v) * 2 + q) * 1024 + lane * 16);
    }
#pragma unroll
    for (int it = 0; it < 4; ++it) dma16c(ws + O_ST + (size_t)(s * 32 + wave * 4 + it) * 1024 + lane * 16, L_ST + (wave * 4 + it) * 1024);
    if (wave == 7) wait_flags2(ws, flagY(ws, sel, s, 7 * C + min(lane, 6)), flagS(ws, sel, s, lane));
    __builtin_amdgcn_sched_barrier(0);
    bar();
#pragma unroll
    for (int it = 0; it < 4; ++it) dma16c(ws + O_SM + (size_t)(s * 32 + wave * 4 + it) * 1024 + lane * 16, L_SM + (wave * 4 + it) * 1024);
    if (wave < 7) {
#pragma unroll
      for (int H = 0; H < 4; ++H) dma16c(ws + O_YG + (size_t)v * Y_RUN + (s * 4 + H) * 1024 + lane * 16, L_Y + (wave * 4 + H) * 1024);
      asm volatile("s_waitcnt vmcnt(4)" ::: "memory");
    } else vm0();
#pragma unroll
    for (int it = 0; it < 4; ++it) {
      const int q = it & 1, hh = 2 * wave + (it >> 1), tq = 16 * q + c16;
      const u4v st = lds_r16(L_ST + (wave * 4 + it) * 1024 + (g >> 1) * 512 + c16 * 32 + (g & 1) * 16);
      const LDS unsigned* sm = (const LDS unsigned*)(size_t)(L_SM + (wave * 4 + it) * 1024 + lane * 4);
      const unsigned s0 = sm[0], s1 = sm[64], s2 = sm[128], s3 = sm[192];
      float e[8];
      float m0 = (bflo(s0) + bflo(s1)) + (bflo(s2) + bflo(s3));
      float m1 = (bfhi(s0) + bfhi(s1)) + (bfhi(s2) + bfhi(s3));
#pragma unroll
      for (int i = 0; i < 8; ++i) { const unsigned w = st[i >> 1]; e[i] = (8 * g + i <= tq) ? ((i & 1) ? bfhi(w) : bflo(w)) : -__builtin_inff(); }
      float mx = fmaxf(m0, m1);
#pragma unroll
      for (int i = 0; i < 8; ++i) mx = fmaxf(mx, e[i]);
      mx = xmax16_32(mx);
      m0 = __builtin_amdgcn_exp2f(m0 - mx); m1 = __builtin_amdgcn_exp2f(m1 - mx);
      float sum = m0 + m1;
#pragma unroll
      for (int i = 0; i < 8; ++i) { e[i] = __builtin_amdgcn_exp2f(e[i] - mx); sum += e[i]; }
      const float r = __builtin_amdgcn_rcpf(xsum16_32(sum));
      lds_w16(L_P + (q * 16 + hh) * 1024 + lane * 16,
              (u4v){pk2(e[0] * r, e[1] * r), pk2(e[2] * r, e[3] * r), pk2(e[4] * r, e[5] * r), pk2(e[6] * r, e[7] * r)});
      *(LDS unsigned*)(size_t)(L_PM + (q * 4 + (hh >> 2)) * 1024 + lane * 16 + (hh & 3) * 4) = pk2(m0 * r, m1 * r);
    }
    vm0();
    bar();
    if (wave < 7) {
      u4v yf[4];
#pragma unroll
      for (int H = 0; H < 4; ++H) {
        const u2v lo = *(const LDS u2v*)(size_t)(L_Y + (wave * 4 + H) * 1024 + lane * 8), hi = *(const LDS u2v*)(size_t)(L_Y + (wave * 4 + H) * 1024 + 512 + lane * 8);
        yf[H] = (u4v){lo.x, lo.y, hi.x, hi.y};
      }
      f4v a0 = __builtin_bit_cast(f4v, ff[0]), a1 = __builtin_bit_cast(f4v, ff[1]);
      const LDS u4v* pp = (const LDS u4v*)(size_t)(L_P + lane * 16);
      u4v p0[4], p1[4];
#pragma unroll
      for (int k = 0; k < 3; ++k) { p0[k] = pp[k * 64]; p1[k] = pp[(16 + k) * 64]; }
#pragma unroll
      for (int k = 0; k < 20; ++k) {
        const int n = k + 3;
        if (n < 16) { p0[n & 3] = pp[n * 64]; p1[n & 3] = pp[(16 + n) * 64]; }
        else if (n < 20) { p0[n & 3] = pp[(32 + n - 16) * 64]; p1[n & 3] = pp[(36 + n - 16) * 64]; }
        const u4v w = k < 16 ? uf[k] : yf[k - 16];
        a0 = mfma16(w, p0[k & 3], a0); a1 = mfma16(w, p1[k & 3], a1);
        __builtin_amdgcn_sched_barrier(0);
      }
      *(u2v*)(out + (size_t)(s * 32 + c16) * DM + v * 16 + g * 4) = (u2v){pk2(a0[0], a0[1]), pk2(a0[2], a0[3])};
      *(u2v*)(out + (size_t)(s * 32 + 16 + c16) * DM + v * 16 + g * 4) = (u2v){pk2(a1[0], a1[1]), pk2(a1[2], a1[3])};
    }
  }
  RTS(4);
}
#define TUNE_ARG
#define TUNE_PASS
#define K_PERIOD 760
#define K_DS 1560
#define K_MS 43
#define K_MM 50
#define K_LEAD 96
#define K_FIXED 0
constexpr int K_SMK = 2, K_DDE = 250;
constexpr int C_PT = 2048;
DEV unsigned* ptp(unsigned char* ws, int s) { return cntp(ws, C_PT + 2 * s); }
DEV unsigned ptword(int period, int ds) { return (unsigned)(period / 24) | ((unsigned)(period % 24) << 8) | ((unsigned)(ds - K_DDE - 80) << 16); }
DEV unsigned char* flagR(unsigned char* ws, unsigned sel, int id) { return hblk(ws, sel, 64 + (id >> 6), 0) + (id & 63) * 128; }
#define DBG(slot, v)
#define ST(n)
#define ARR(s_)
#define EV(code)
#define EV_OUT(base)
#define ST_CLOB
#define ST_OUT(base, ref)
constexpr int RET_E = 26;
constexpr int TRY_CAP = 1 << 16;

DEV int rt32() { unsigned long long t; asm volatile("s_memrealtime %0\n\ts_waitcnt lgkmcnt(0)" : "=s"(t) :: "memory"); return (int)(unsigned)t; }
struct Lat { int anchor, e, f; };
DEV void lat_fwd(Lat& L, int pe, int pf) { L.f += pf; const int c = L.f >= 24; L.f -= c ? 24 : 0; L.e += pe + c; }
DEV void lat_back(Lat& L, int pe, int pf) { L.f -= pf; const int c = L.f < 0; L.f += c ? 24 : 0; L.e -= pe + c; }
DEV void lat_prep(const Lat& L, int& tgt, int& jmp) { tgt = L.anchor + 4 * L.e; jmp = 108 - 4 * L.f; }
#define TPAIR(r, m) "s_memrealtime s[" #r ":" #r "+1]\n\ts_memtime s[" #m ":" #m "+1]\n\ts_nop 0\n\t"
#define TSEL(r, m) "s_cmp_lg_u32 s" #r ", s4\n\ts_cselect_b32 s58, s" #m ", s58\n\t"
#define TBURST \
  TPAIR(4, 6) TPAIR(8, 10) TPAIR(12, 14) TPAIR(16, 18) TPAIR(20, 22) TPAIR(24, 26) TPAIR(28, 30) TPAIR(52, 54) TPAIR(56, 58) \
  "s_waitcnt lgkmcnt(0)\n\t" TSEL(52, 54) TSEL(28, 30) TSEL(24, 26) TSEL(20, 22) TSEL(16, 18) TSEL(12, 14) TSEL(8, 10)
#define TBURST_CLOB "s4","s5","s6","s7","s8","s9","s10","s11","s12","s13","s14","s15","s16","s17","s18","s19","s20","s21","s22","s23", \
  "s24","s25","s26","s27","s28","s29","s30","s31","s52","s53","s54","s55","s56","s57","s58","s59"
DEV void lat_commit(Lat& L, int tgt, int jmp) {
  if (tgt == (int)0x80000000) return;
  int r = tgt - rt32();
  while (__builtin_expect(r < 0, 0)) { L.e += RET_E; tgt += 4 * RET_E; r += 4 * RET_E; }
  if (r > 0) {
    int d;
#define RT_STEP "s_memrealtime vcc\n\ts_waitcnt lgkmcnt(0)\n\ts_sub_i32 %0, %1, vcc_lo\n\ts_cmp_lt_i32 %0, 1\n\t"
    asm volatile("1:\n\t" RT_STEP "s_cbranch_scc1 2f\n\t" RT_STEP "s_cbranch_scc1 2f\n\t" RT_STEP "s_cbranch_scc1 2f\n\t"
                 RT_STEP "s_cbranch_scc0 1b\n\t"
                 "2:\n\t"
                 "s_getpc_b64 vcc\n\ts_add_u32 vcc_lo, vcc_lo, %2\n\ts_addc_u32 vcc_hi, vcc_hi, 0\n\ts_setpc_b64 vcc\n\t"
                 ".rept 24\n\ts_nop 0\n\t.endr"
                 : "=&s"(d) : "s"(tgt), "s"(jmp) : "scc", "vcc", "memory");
  }
}
DEV unsigned lat_wait(Lat& L) {
  int tgt, jmp;
  unsigned est;
  lat_prep(L, tgt, jmp);
  if (tgt - rt32() < 16) { lat_commit(L, tgt, jmp); return 0; }
  asm volatile("1:\n\ts_memrealtime vcc\n\ts_waitcnt lgkmcnt(0)\n\ts_sub_i32 s4, %1, vcc_lo\n\ts_cmp_gt_i32 s4, 16\n\ts_cbranch_scc1 1b\n\t"
               TBURST
               "s_sub_i32 s4, %1, s4\n\ts_mul_i32 s4, s4, 93\n\ts_lshr_b32 s4, s4, 2\n\t"
               "s_add_i32 s58, s58, s4\n\ts_add_i32 s58, s58, %2\n\ts_or_b32 %0, s58, 1\n\t"
               "s_memtime vcc\n\ts_waitcnt lgkmcnt(0)\n\t"
               "s_sub_i32 s4, s58, vcc_lo\n\ts_max_i32 s4, s4, 0\n\ts_min_i32 s4, s4, 512\n\ts_and_b32 s4, s4, -4\n\ts_sub_i32 s4, 524, s4\n\t"
               "s_getpc_b64 vcc\n\ts_add_u32 vcc_lo, vcc_lo, s4\n\ts_addc_u32 vcc_hi, vcc_hi, 0\n\ts_setpc_b64 vcc\n\t"
               ".rept 128\n\ts_nop 0\n\t.endr"
               : "=&s"(est) : "s"(tgt), "s"(4 * L.f - 93) : TBURST_CLOB, "scc", "vcc", "memory");
  return est;
}
DEV unsigned* anchorp(unsigned char* ws, int id) { return cntp(ws, C_ANCHOR + 2 * (id & 31)); }
DEV int get_anchor(unsigned char* ws, unsigned sel, int id, int lane) {
  post((unsigned*)flagR(ws, sel, id), 0u, lane);
  unsigned v = 0;
  for (int it = 0; it < SPIN_CAP && v == 0; ++it) v = __builtin_amdgcn_readfirstlane(ld_cnt(anchorp(ws, id)));
  const int n = rt32();
  const int d = (int)((v & ~1u) - ((unsigned)n << 1)) >> 1;
  return (d > 4096 || d < -(1 << 22)) ? n : n + d;
}
DEV f4v smf(u4v a, u8v b, f4v c, int idx) {
  return __builtin_amdgcn_smfmac_f32_16x16x64_bf16(asbf8(a), __builtin_bit_cast(bf16x, b), c, idx, 0, 0);
}
DEV unsigned umax4(u4v v) { return max(max(v.x, v.y), max(v.z, v.w)); }
DEV float dpp_ror8(float v) { return __builtin_bit_cast(float, __builtin_amdgcn_update_dpp(0, __builtin_bit_cast(int, v), 0x128, 0xf, 0xf, true)); }
DEV float dpp_xor1(float v) { return __builtin_bit_cast(float, __builtin_amdgcn_update_dpp(0, __builtin_bit_cast(int, v), 0xb1, 0xf, 0xf, true)); }
DEV void st4c_asm(void* p, unsigned v) { asm volatile("global_store_dword %0, %1, off sc1" :: "v"(p), "v"(v) : "memory"); }
DEV void st8c_asm(void* p, u2v v) { asm volatile("global_store_dwordx2 %0, %1, off sc1" :: "v"(p), "v"(v) : "memory"); }
DEV void ld16c_asm(u4v& v, const void* p) { asm volatile("global_load_dwordx4 %0, %1, off sc1" : "=&v"(v) : "v"(p) : "memory"); }
DEV void rawbar() { asm volatile("s_barrier" ::: "memory"); }
DEV unsigned lds_r4(unsigned a) { return *(volatile LDS unsigned*)(size_t)a; }
DEV void lds_w4(unsigned a, unsigned v) { *(volatile LDS unsigned*)(size_t)a = v; }
DEV u8v cat8(u4v a, u4v b) { return (u8v){a.x, a.y, a.z, a.w, b.x, b.y, b.z, b.w}; }
DEV void set_abort(unsigned char* ws) { st4c_asm(cntp(ws, C_ABORT), 1u); }

constexpr unsigned L_MEM = 0, L_KT = 8192, L_FLAG = 12288, L_RED = 16384;
constexpr unsigned L_EST = 12304, L_TS = 12308, L_CG = 12544, L_G = 32768, L_XN = 65536;
#define CLOB_V200_247 "v200","v201","v202","v203","v204","v205","v206","v207","v208","v209","v210","v211","v212","v213","v214","v215", \
  "v216","v217","v218","v219","v220","v221","v222","v223","v224","v225","v226","v227","v228","v229","v230","v231", \
  "v232","v233","v234","v235","v236","v237","v238","v239","v240","v241","v242","v243","v244","v245","v246","v247","v248"
#define SMF "v_smfmac_f32_16x16x64_bf16 "
#define R1_HEAD(o0, o1, o2, o3, o4, o5, o6, FIRST) \
  "v_mov_b32 v228, 0\n\tv_mov_b32 v229, 0\n\tv_mov_b32 v230, 0\n\tv_mov_b32 v231, 0\n\t" \
  "v_mov_b32 v232, 0\n\tv_mov_b32 v233, 0\n\tv_mov_b32 v234, 0\n\tv_mov_b32 v235, 0\n\t" \
  "v_mov_b32 v244, 0\n\tv_mov_b32 v245, 0\n\tv_mov_b32 v246, 0\n\tv_mov_b32 v247, 0\n\t" \
  "s_mov_b32 m0, %[m0v]\n\t" \
  "s_barrier\n\t" ST(80) \
  "global_load_lds_dwordx4 %[moff], %[src] sc1\n\t" \
  "s_waitcnt vmcnt(0)\n\t" \
  "ds_read_b128 v[200:203], %[rd] offset:" #o0 "*1024\n\t" \
  "s_barrier\n\t" ST(82) \
  "ds_read_b128 v[204:207], %[rd] offset:" #o1 "*1024\n\tds_read_b128 v[208:211], %[rd] offset:" #o2 "*1024\n\t" \
  "ds_read_b128 v[212:215], %[rd] offset:" #o3 "*1024\n\tds_read_b128 v[216:219], %[rd] offset:" #o4 "*1024\n\t" \
  "ds_read_b128 v[220:223], %[rd] offset:" #o5 "*1024\n\tds_read_b128 v[224:227], %[rd] offset:" #o6 "*1024\n\t"
#define R1_TWO(n6, n5, n4, n3, n2, n1, n0) \
  "s_waitcnt lgkmcnt(" #n6 ")\n\t" ST(84) SMF "v[228:231], v[200:203], %[a0], %[idx]\n\t" SMF "v[232:235], v[200:203], %[b0], %[idx]\n\t" \
  "s_waitcnt lgkmcnt(" #n5 ")\n\t" SMF "v[228:231], v[204:207], %[a1], %[idx]\n\t" SMF "v[232:235], v[204:207], %[b1], %[idx]\n\t" \
  "s_waitcnt lgkmcnt(" #n4 ")\n\t" SMF "v[228:231], v[208:211], %[a2], %[idx]\n\t" SMF "v[232:235], v[208:211], %[b2], %[idx]\n\t" \
  "s_waitcnt lgkmcnt(" #n3 ")\n\t" SMF "v[228:231], v[212:215], %[a3], %[idx]\n\t" SMF "v[232:235], v[212:215], %[b3], %[idx]\n\t" \
  "s_waitcnt lgkmcnt(" #n2 ")\n\t" SMF "v[228:231], v[216:219], %[a4], %[idx]\n\t" SMF "v[232:235], v[216:219], %[b4], %[idx]\n\t" \
  "s_waitcnt lgkmcnt(" #n1 ")\n\t" SMF "v[228:231], v[220:223], %[a5], %[idx]\n\t" \
  "s_waitcnt lgkmcnt(" #n0 ")\n\t" SMF "v[228:231], v[224:227], %[a6], %[idx]\n\t" \
  SMF "v[232:235], v[220:223], %[b5], %[idx]\n\t" SMF "v[232:235], v[224:227], %[b6], %[idx]\n\t" \
  ST(86) "s_nop 1\n\t" \
  "v_cvt_pk_bf16_f32 v228, v228, v229\n\tv_cvt_pk_bf16_f32 v229, v230, v231\n\t" \
  "ds_write_b64 %[ktw], v[228:229]\n\t" \
  "s_nop 2\n\t" \
  "v_cvt_pk_bf16_f32 v232, v232, v233\n\tv_cvt_pk_bf16_f32 v233, v234, v235\n\t" \
  "ds_write_b64 %[ktw], v[232:233] offset:512\n\t" \
  LDSWAIT ST(88) \
  "s_barrier\n\t" ST(90)
#define LDSWAIT
#define R1_WOPS [a0] "v"(w0[0]), [a1] "v"(w0[1]), [a2] "v"(w0[2]), [a3] "v"(w0[3]), [a4] "v"(w0[4]), [a5] "v"(w0[5]), [a6] "v"(w0[6])
#define R1_WOPS2 [b0] "v"(w1[0]), [b1] "v"(w1[1]), [b2] "v"(w1[2]), [b3] "v"(w1[3]), [b4] "v"(w1[4]), [b5] "v"(w1[5]), [b6] "v"(w1[6])
#define R1_COMMON [idx] "v"(idx), [rd] "v"(rd), [ktw] "v"(ktw), [moff] "v"(moff), [src] "s"(src), [m0v] "s"(m0v)

template <int W>
DEV void r1_mfma(unsigned char* __restrict__ ws, int h, int q, int lane, unsigned sel, int smk) {
  constexpr int wave = W, KIND = W == 0 ? 0 : W == 3 ? 2 : 1;
  const int g = lane >> 4, c16 = lane & 15;
  const int idx = (lane & 8) ? 0xEEEE : 0x4444;
  const unsigned moff = wave * 1024 + g * 256 + (lane & 7) * 32 + ((lane >> 3) & 1) * 16;
  u8v w0[7], w1[7];
  {
    const unsigned char* p = ws + O_WA + (size_t)(h * 28 + 7 * q + 2 * wave) * 14336 + lane * 16;
#pragma unroll
    for (int k = 0; k < 7; ++k) {
      const int o = ((k + W) % 7) * 2048;
      w0[k] = cat8(*(const u4v*)(p + o), *(const u4v*)(p + o + 1024));
      if (KIND != 2) w1[k] = cat8(*(const u4v*)(p + 14336 + o), *(const u4v*)(p + 14336 + o + 1024));
    }
#pragma unroll
    for (int k = 0; k < 7; ++k) { asm volatile("" : "+v"(w0[k])); if (KIND != 2) asm volatile("" : "+v"(w1[k])); }
  }
  RTS(1);
  const unsigned rd = L_MEM + lane * 16, ktw = L_KT + 2 * wave * 512 + c16 * 32 + g * 8, m0v = L_MEM + wave * 1024;
  const unsigned a0a = L_MEM + (7 * q + g) * 256 + c16 * 16, a1a = L_MEM + min(7 * q + 4 + g, 27) * 256 + c16 * 16;
  const unsigned trb = L_KT + (g >> 1) * 512 + (4 * (g & 1) + (c16 >> 2)) * 32 + (c16 & 3) * 8;
  const unsigned sto = h * 512 + q * 128 + (4 * (g & 1) + 2 * (g >> 1) + (c16 >> 3)) * 16 + (c16 & 7) * 2;
  const unsigned long long lo8 = 0x00FF00FF00FF00FFull;
  unsigned ep = 0;
  int budget = TRY_CAP;
  rawbar();
  rawbar();
  rawbar();
  for (int s = 0; s < NSEG; ++s) {
    const unsigned char* src = hblk(ws, sel, s, 0);
    unsigned char* dst = hblk(ws, sel, 64 + s, smk);
    for (;;) {
      if (KIND == 0) {
        unsigned long long okm, tst;
        asm volatile(R1_HEAD(0, 1, 2, 3, 4, 5, 6, )
          "ds_read_b128 v[236:239], %[a0a]\n\tds_read_b128 v[240:243], %[a1a]\n\t"
          R1_TWO(8, 7, 6, 5, 4, 3, 2)
          "ds_read_b64_tr_b16 v[200:201], %[trb]\n\tds_read_b64_tr_b16 v[202:203], %[trb] offset:256\n\t"
          "ds_read_b64_tr_b16 v[204:205], %[trb] offset:1024\n\tds_read_b64_tr_b16 v[206:207], %[trb] offset:1280\n\t"
          "ds_read_b64_tr_b16 v[208:209], %[trb] offset:2048\n\tds_read_b64_tr_b16 v[210:211], %[trb] offset:2304\n\t"
          "ds_read_b64_tr_b16 v[212:213], %[trb] offset:3072\n\tds_read_b64_tr_b16 v[214:215], %[trb] offset:3328\n\t"
          "s_waitcnt lgkmcnt(4)\n\t" SMF "v[244:247], v[236:239], v[200:207], %[idx]\n\t"
          "s_waitcnt lgkmcnt(0)\n\t" SMF "v[244:247], v[240:243], v[208:215], %[idx]\n\t"
          "s_nop 7\n\t"
          "v_permlane32_swap_b32 v244, v246\n\tv_permlane32_swap_b32 v245, v247\n\t"
          "v_add_f32 v244, v244, v246\n\tv_add_f32 v245, v245, v247\n\t"
          "v_cndmask_b32 v246, v244, v245, %[lo8]\n\t"
          "v_cndmask_b32 v247, v245, v244, %[lo8]\n\t"
          "s_nop 1\n\t"
          "v_add_f32_dpp v247, v246, v247 row_ror:8 row_mask:0xf bank_mask:0xf\n\t"
          "v_cvt_pk_bf16_f32 v246, v247, v247\n\t"
          "v_cmpx_o_f32 vcc, v247, v247\n\t"
          "global_store_short %[sto], v246, %[dst] sc1\n\t" ST(92)
          "s_memtime %[tst]\n\t"
          "s_mov_b64 exec, -1\n\t"
          "s_mov_b64 %[okm], vcc\n\t"
          "s_waitcnt lgkmcnt(0)\n\t"
          : [okm] "=&s"(okm), [tst] "=&s"(tst)
          : R1_WOPS, R1_WOPS2, R1_COMMON, [a0a] "v"(a0a), [a1a] "v"(a1a), [trb] "v"(trb), [sto] "v"(sto), [dst] "s"(dst), [lo8] "s"(lo8)
          : CLOB_V200_247, "vcc", "memory" ST_CLOB);
        lds_w4(L_TS, (unsigned)tst);
        if (okm != ~0ull) {
          if (budget > 0) { if (lane == 0) lds_w4(L_FLAG, ep); }
          else asm volatile("global_store_short %0, %1, %2 sc1" :: "v"(sto), "v"(0x7FC0u), "s"(dst) : "memory");
        }
      } else if (KIND == 1) {
        if (W == 1) asm volatile(R1_HEAD(1, 2, 3, 4, 5, 6, 0, ) R1_TWO(6, 5, 4, 3, 2, 1, 0) :: R1_WOPS, R1_WOPS2, R1_COMMON : CLOB_V200_247, "memory" ST_CLOB);
        else asm volatile(R1_HEAD(2, 3, 4, 5, 6, 0, 1, ) R1_TWO(6, 5, 4, 3, 2, 1, 0) :: R1_WOPS, R1_WOPS2, R1_COMMON : CLOB_V200_247, "memory" ST_CLOB);
      } else {
        asm volatile(R1_HEAD(3, 4, 5, 6, 0, 1, 2, )
          "s_waitcnt lgkmcnt(6)\n\t" SMF "v[228:231], v[200:203], %[a0], %[idx]\n\t"
          "s_waitcnt lgkmcnt(5)\n\t" SMF "v[228:231], v[204:207], %[a1], %[idx]\n\t"
          "s_waitcnt lgkmcnt(4)\n\t" SMF "v[228:231], v[208:211], %[a2], %[idx]\n\t"
          "s_waitcnt lgkmcnt(3)\n\t" SMF "v[228:231], v[212:215], %[a3], %[idx]\n\t"
          "s_waitcnt lgkmcnt(2)\n\t" SMF "v[228:231], v[216:219], %[a4], %[idx]\n\t"
          "s_waitcnt lgkmcnt(1)\n\t" SMF "v[228:231], v[220:223], %[a5], %[idx]\n\t"
          "s_waitcnt lgkmcnt(0)\n\t" SMF "v[228:231], v[224:227], %[a6], %[idx]\n\t"
          "s_nop 7\n\t"
          "v_cvt_pk_bf16_f32 v228, v228, v229\n\tv_cvt_pk_bf16_f32 v229, v230, v231\n\t"
          "ds_write_b64 %[ktw], v[228:229]\n\t"
          LDSWAIT
          "s_barrier\n\t"
          :: R1_WOPS, R1_COMMON : CLOB_V200_247, "memory" ST_CLOB);
      }
      bar();
      const bool bad = __builtin_amdgcn_readfirstlane(lds_r4(L_FLAG)) == ep;
      ++ep;
      if (!bad) break;
      --budget;
      rawbar();
    }
    ARR(s);
    rawbar();
  }
}

typedef unsigned short h2v __attribute__((ext_vector_type(2)));
DEV unsigned pkmax(unsigned a, unsigned b) { return __builtin_bit_cast(unsigned, __builtin_elementwise_max(__builtin_bit_cast(h2v, a), __builtin_bit_cast(h2v, b))); }
DEV unsigned wavepkmax(unsigned v) {
  v = pkmax(v, __builtin_amdgcn_update_dpp(0u, v, 0xb1, 0xf, 0xf, true));
  v = pkmax(v, __builtin_amdgcn_update_dpp(0u, v, 0x4e, 0xf, 0xf, true));
  v = pkmax(v, __builtin_amdgcn_update_dpp(0u, v, 0x124, 0xf, 0xf, true));
  v = pkmax(v, __builtin_amdgcn_update_dpp(0u, v, 0x128, 0xf, 0xf, true));
  const u2v x = __builtin_amdgcn_permlane16_swap(v, v, false, false);
  v = pkmax(x.x, x.y);
  const u2v y = __builtin_amdgcn_permlane32_swap(v, v, false, false);
  return pkmax(y.x, y.y);
}
DEV void r1_boss(unsigned char* __restrict__ ws, const unsigned short* __restrict__ mem0, int lane, unsigned sel TUNE_ARG) {
  constexpr unsigned L_FA = 13312, L_FY = 13568;
  int A = 0, V = 0, ms = 4 * K_MS, mm = 4 * K_MM;
  float rho = 0.f;
  bool prevf3 = false;
  int run = 0;
  unsigned ep = 0;
  const unsigned c0 = rdtime();
  const int r0 = rt32();
#pragma unroll
  for (int j = 0; j < 7; ++j) {
    const int n = j * 64 + lane;
    st16c_asm(hblk(ws, sel, 0, 0) + n * 16, *(const u4v*)(mem0 + ((n >> 1) & 7) * DM + (n >> 4) * 16 + (n & 1) * 8));
  }
  vm0();
  rawbar();
  const int P0 = (int)((float)K_PERIOD * (0.3f + 0.7f * fmaxf(23.4f * (float)(rt32() - r0) * __builtin_amdgcn_rcpf((float)(rdtime() - c0)), 1.f)));
  int hist = P0;
  if (lane < 5) st4c_asm(ptp(ws, lane), ptword(P0, K_DS));
  for (int it = 0; it < SPIN_CAP; ++it) {
    unsigned x, y;
    asm volatile("global_load_dword %0, %2, off sc1\n\tglobal_load_dword %1, %3, off sc1\n\ts_waitcnt vmcnt(0)"
                 : "=&v"(x), "=&v"(y) : "v"(flagR(ws, sel, lane)), "v"(flagR(ws, sel, 64 + min(lane, 27))) : "memory");
    if (!__any(max(x, y) == SENT)) break;
  }
  for (int it = 0; it < SPIN_CAP; ++it) {
    unsigned x, y;
    asm volatile("global_load_dword %0, %2, off sc1\n\tglobal_load_dword %1, %3, off sc1\n\ts_waitcnt vmcnt(0)"
                 : "=&v"(x), "=&v"(y) : "v"(doneG(ws, min(lane, 55))), "v"(doneU(ws, min(lane, 55))) : "memory");
    if (!__any(min(x, y) == 0)) break;
  }
  if (lane < 32) st4c_asm(anchorp(ws, lane), ((unsigned)(rt32() + K_LEAD) << 1) | 1u);
  rawbar();
  for (int t = 0; t < NSEG; ++t) {
    const int s = max(t - 3, 0);
    const float pa = 4.f * (float)__builtin_amdgcn_readlane(hist, max(s - 1, 0)), py = 4.f * (float)__builtin_amdgcn_readlane(hist, s);
    rawbar(); rawbar();
    if (t >= 3) vm0();
    const unsigned a = lds_r4(L_FA + lane * 4), y = lds_r4(L_FY + lane * 4);
    const float da = (float)((a >> 12) & 0xffffu), dy = (float)((y >> 12) & 0xffffu);
    unsigned pk = min((unsigned)((float)(a & 0xfffu) * pa * __builtin_amdgcn_rcpf(da)), 65535u) |
                  (min((unsigned)((float)(y & 0xfffu) * py * __builtin_amdgcn_rcpf(dy)), 65535u) << 16);
    unsigned fl = (max(a, y) == SENT ? 1u : 0u) | ((a >> 28) != 0 ? 2u : 0u) | ((y >> 26) & 4u) |
                  (fabsf(da - 0.86f * pa) > 0.18f * pa ? 8u : 0u) | (fabsf(dy - 0.86f * py) > 0.18f * py ? 16u : 0u) | ((y >> 25) & 16u);
    float rn = dy * __builtin_amdgcn_rcpf(py);
    asm volatile("s_barrier" : "+v"(pk), "+v"(fl), "+v"(rn) :: "memory");
    pk = wavepkmax(pk);
    asm volatile("s_barrier\n\ts_barrier" : "+v"(pk) :: "memory");
    while (__builtin_amdgcn_readfirstlane(lds_r4(L_FLAG)) == ep++) { rawbar(); rawbar(); rawbar(); rawbar(); rawbar(); }
    if (t >= 3 && t + 2 < NSEG) {
      if (!__any(fl & 1)) {
        const unsigned m = __builtin_amdgcn_readfirstlane(pk);
        const bool f1 = __any(fl & 2), f3 = __any(fl & 4);
        if (!__any(fl & 10)) A = A == 0 ? (int)(m & 0xffffu) : A + (((int)(m & 0xffffu) - A) >> 2);
        if (!__any(fl & 20)) { V = V == 0 ? (int)(m >> 16) : V + (((int)(m >> 16) - V) >> 2); rho = rho == 0.f ? rn : rho + 0.25f * (rn - rho); }
        run = (f1 || f3) ? run + 1 : 0;
        if ((f1 && !prevf3) || run > 3) mm = min(mm + 20, 4 * K_MM + 800);
        if ((f3 && !f1) || run > 3) ms = min(ms + 20, 4 * K_MS + 800);
        prevf3 = f3;
      }
      int P = P0 + ((ms + mm) >> 2) - K_MS - K_MM, DS = K_DS + ms - 4 * K_MS;
      if (A > 0 && V > 0 && !K_FIXED) {
        DS = min(max((int)((float)(A + ms) * rho), 1200), 3000);
        P = min(max((A + ms + V - 84 + mm) >> 2, 600), 1500);
      }
      if (lane == t + 2) hist = P;
      post(ptp(ws, t + 2), ptword(P, DS), lane);
    }
    if (t >= 2) {
      asm volatile("s_mov_b32 m0, %2\n\ts_nop 0\n\tglobal_load_lds_dword %0, off sc1\n\ts_mov_b32 m0, %3\n\ts_nop 0\n\tglobal_load_lds_dword %1, off sc1"
                   :: "v"(flagS(ws, sel, t - 2, lane)), "v"(flagY(ws, sel, t - 2, min(lane, 27))), "s"(L_FA), "s"(L_FY) : "memory");
    }
  }
  vm0();
  rawbar();
}

DEV void role_r1(unsigned char* __restrict__ ws, const unsigned short* __restrict__ mem0, int id, int wave, int lane, unsigned sel TUNE_ARG) {
  const int h = id >> 2, q = id & 3;
  if (wave == 0) { r1_mfma<0>(ws, h, q, lane, sel, K_SMK); return; }
  if (wave == 1) { r1_mfma<1>(ws, h, q, lane, sel, K_SMK); return; }
  if (wave == 2) { r1_mfma<2>(ws, h, q, lane, sel, K_SMK); return; }
  if (wave == 3) { r1_mfma<3>(ws, h, q, lane, sel, K_SMK); return; }
  if (wave == 7 && id == 3) { r1_boss(ws, mem0, lane, sel TUNE_PASS); return; }
  const int g = lane >> 4, c16 = lane & 15;
  const bool act = q < 2;
  const int fr = wave - 1;
  const unsigned moff = fr * 1024 + g * 256 + (lane & 7) * 32 + ((lane >> 3) & 1) * 16, m0v = L_MEM + fr * 1024;
  const unsigned xo = L_XN + (g >> 1) * 1024 + (g & 1) * 512 + c16 * 16;
  unsigned ep = 0;
  if (wave == 7) {
    const int idx = (lane & 8) ? 0xEEEE : 0x4444;
    u8v w0[7];
    const unsigned rd = lane * 16;
    const unsigned sto = h * 512 + (q & 1) * 256 + (4 * (g & 1) + 2 * (g >> 1) + (lane & 1)) * 32 + (c16 >> 1) * 4;
    const unsigned long long odd = 0xAAAAAAAAAAAAAAAAull;
    __builtin_amdgcn_s_setprio(3);
    rawbar(); rawbar();
    for (int s = 0; s < NSEG; ++s) {
      unsigned char* dst = hblk(ws, sel, s, 2);
      unsigned char* raw = hblk(ws, sel, s, 1);
      const bool go = act && s < NSEG - 1;
      if (go) {
#pragma unroll
        for (int k = 0; k < 7; ++k) w0[k] = cat8(lds_r16(L_G + (s & 1) * 14336 + rd + k * 2048), lds_r16(L_G + (s & 1) * 14336 + rd + k * 2048 + 1024));
      }
      for (;;) {
        if (go) {
          asm volatile(
            "v_mov_b32 v228, 0\n\tv_mov_b32 v229, 0\n\tv_mov_b32 v230, 0\n\tv_mov_b32 v231, 0\n\t"
            "s_barrier\n\ts_barrier\n\ts_barrier\n\t" ST(80)
            "ds_read_b128 v[200:203], %[rd]\n\tds_read_b128 v[204:207], %[rd] offset:1024\n\tds_read_b128 v[208:211], %[rd] offset:2048\n\t"
            "ds_read_b128 v[212:215], %[rd] offset:3072\n\tds_read_b128 v[216:219], %[rd] offset:4096\n\tds_read_b128 v[220:223], %[rd] offset:5120\n\t"
            "ds_read_b128 v[224:227], %[rd] offset:6144\n\t"
            "s_waitcnt lgkmcnt(6)\n\t" SMF "v[228:231], v[200:203], %[a0], %[idx]\n\t"
            "s_waitcnt lgkmcnt(5)\n\t" SMF "v[228:231], v[204:207], %[a1], %[idx]\n\t"
            "s_waitcnt lgkmcnt(4)\n\t" SMF "v[228:231], v[208:211], %[a2], %[idx]\n\t"
            "s_waitcnt lgkmcnt(3)\n\t" SMF "v[228:231], v[212:215], %[a3], %[idx]\n\t"
            "s_waitcnt lgkmcnt(2)\n\t" SMF "v[228:231], v[216:219], %[a4], %[idx]\n\t"
            "s_waitcnt lgkmcnt(1)\n\t" SMF "v[228:231], v[220:223], %[a5], %[idx]\n\t"
            "s_waitcnt lgkmcnt(0)\n\t" SMF "v[228:231], v[224:227], %[a6], %[idx]\n\t"
            "s_nop 7\n\t"
            "v_permlane32_swap_b32 v228, v230\n\tv_permlane32_swap_b32 v229, v231\n\t"
            "v_add_f32 v228, v228, v230\n\tv_add_f32 v229, v229, v231\n\t"
            "v_cndmask_b32 v230, v229, v228, %[odd]\n\t"
            "s_nop 1\n\t"
            "v_mov_b32_dpp v230, v230 quad_perm:[1,0,3,2] row_mask:0xf bank_mask:0xf\n\t"
            "v_cndmask_b32 v228, v228, v230, %[odd]\n\tv_cndmask_b32 v229, v230, v229, %[odd]\n\t"
            "v_exp_f32 v230, v228\n\tv_exp_f32 v231, v229\n\t"
            "v_cvt_pk_bf16_f32 v232, v228, v229\n\t"
            "v_cvt_pk_bf16_f32 v233, v230, v231\n\t"
            "v_cmpx_o_f32 vcc, v228, v229\n\t"
            "global_store_dword %[sto], v233, %[dst] sc1\n\t" ST(82)
            "global_store_dword %[sto], v232, %[raw] sc1\n\t"
            "s_mov_b64 exec, -1\n\t"
            "s_barrier\n\t"
            :: R1_WOPS, [idx] "v"(idx), [rd] "v"(rd), [sto] "v"(sto), [dst] "s"(dst), [raw] "s"(raw), [odd] "s"(odd)
            : CLOB_V200_247, "vcc", "memory" ST_CLOB);
          rawbar();
        } else {
          rawbar(); rawbar(); rawbar(); rawbar(); rawbar();
        }
        const bool bad = __builtin_amdgcn_readfirstlane(lds_r4(L_FLAG)) == ep;
        ++ep;
        if (!bad) break;
      }
    }
    rawbar();
  } else if (wave == 4) {
    const unsigned trb = L_KT + (g >> 1) * 512 + (4 * (g & 1) + (c16 >> 2)) * 32 + (c16 & 3) * 8;
    unsigned char* so = ws + O_SM + (size_t)h * 2048 + q * 256 + lane * 4;
    const unsigned x3 = xo, xlast = g < 2 ? 6144 : 5120;
#pragma unroll
    for (int u = 0; u < 8; ++u) *(LDS u2v*)(size_t)(L_KT + u * 512 + lane * 8) = u < 7 ? (u2v){SENT, SENT} : (u2v){0u, 0u};
    if (lane == 0) lds_w4(L_FLAG, SENT);
    rawbar(); rawbar();
    (void)ld_cnt((const unsigned*)(lane < 32 ? so : lane < 48 ? so + 63 * 32768 : hblk(ws, sel, 70, 0)));
    unsigned pay = 0, eprev = 0;
    for (int s = 0; s < NSEG; ++s) {
      bool sig = s > 0;
      const unsigned ep0 = ep;
      for (;;) {
        bar(); rawbar(); rawbar();
        if (sig) { vm0(); if (lane == 0) st4c_asm(flagS(ws, sel, s - 1, id), pay); sig = false; }
        rawbar(); rawbar();
        const bool bad = __builtin_amdgcn_readfirstlane(lds_r4(L_FLAG)) == ep;
        ++ep;
        if (!bad) break;
      }
      {
        const unsigned est = lds_r4(L_EST), tst = lds_r4(L_TS);
        pay = (est ? min(tst - est, 4095u) : 0u) | ((est && eprev) ? min(est - eprev, 65535u) << 12 : 0u);
        pay |= min(ep - ep0 - 1, 7u) << 28;
        eprev = est;
      }
      f4v c0 = {0.f, 0.f, 0.f, 0.f}, c1 = c0;
      u4v ka[4], xa[4], xb[4];
#pragma unroll
      for (int u = 0; u < 4; ++u) {
        const u2v lo = lds_tr(trb + u * 1024), hi = lds_tr(trb + u * 1024 + 256);
        ka[u] = (u4v){lo.x, lo.y, hi.x, hi.y};
        const unsigned ad = x3 + (s & 1) * 8192 + (u < 3 ? u * 2048 : xlast);
        xa[u] = lds_r16(ad); xb[u] = lds_r16(ad + 256);
      }
      __builtin_amdgcn_sched_barrier(0);
#pragma unroll
      for (int u = 0; u < 4; ++u) { c0 = mfma16(ka[u], xa[u], c0); c1 = mfma16(ka[u], xb[u], c1); }
#pragma unroll
      for (int u = 0; u < 7; ++u) *(LDS u2v*)(size_t)(L_KT + u * 512 + lane * 8) = (u2v){SENT, SENT};
      st4c_asm(so + (size_t)s * 32768, pk2(halfsum(c0[0], c0[1]), halfsum(c0[2], c0[3])));
      st4c_asm(so + (size_t)s * 32768 + 1024, pk2(halfsum(c1[0], c1[1]), halfsum(c1[2], c1[3])));
      ARR(s);
    }
    vm0();
    rawbar();
    if (lane == 0) st4c_asm(flagS(ws, sel, NSEG - 1, id), pay);
  } else if (wave == 5) {
    const unsigned char* gp = ws + O_G + (size_t)(h * 2 + (q & 1)) * G_RUN;
    const unsigned v0 = lane * 16, v1 = v0 + 4096, v2 = v0 + 8192, v3 = v0 + 12288;
    auto fetch = [&](int t) {
#define GL4(v) "global_load_lds_dwordx4 " v ", %4 sc1\n\t" "global_load_lds_dwordx4 " v ", %4 offset:1024 sc1\n\t"
#define GL8(v) GL4(v) "global_load_lds_dwordx4 " v ", %4 offset:2048 sc1\n\t" "global_load_lds_dwordx4 " v ", %4 offset:3072 sc1\n\t"
      asm volatile("s_mov_b32 m0, %5\n\ts_nop 0\n\t" GL8("%0") "s_add_u32 m0, m0, 4096\n\ts_nop 0\n\t" GL8("%1")
                   "s_add_u32 m0, m0, 4096\n\ts_nop 0\n\t" GL8("%2") "s_add_u32 m0, m0, 4096\n\ts_nop 0\n\t" GL4("%3")
                   :: "v"(v0), "v"(v1), "v"(v2), "v"(v3), "s"(gp + t * 14336), "s"(L_G + (t & 1) * 14336) : "scc", "memory");
    };
    auto fetchx = [&](int t) {
      asm volatile("s_mov_b32 m0, %3\n\ts_nop 0\n\t"
                   "global_load_lds_dwordx4 %0, %2\n\tglobal_load_lds_dwordx4 %0, %2 offset:1024\n\tglobal_load_lds_dwordx4 %0, %2 offset:2048\n\t"
                   "global_load_lds_dwordx4 %0, %2 offset:3072\n\ts_add_u32 m0, m0, 4096\n\ts_nop 0\n\t"
                   "global_load_lds_dwordx4 %1, %2\n\tglobal_load_lds_dwordx4 %1, %2 offset:1024\n\tglobal_load_lds_dwordx4 %1, %2 offset:2048"
                   :: "v"(v0), "v"(v1), "s"(ws + O_XN + (size_t)t * 28672 + q * 7168), "s"(L_XN + (t & 1) * 8192) : "scc", "memory");
    };
    const unsigned* gdone = doneG(ws, 7 * (h >> 1) + min(lane, 6));
    auto peek = [&](int t) {
      unsigned keep;
      asm volatile("s_mov_b32 %0, m0\n\ts_mov_b32 m0, %2\n\ts_nop 0\n\tglobal_load_lds_dword %1, off sc1\n\ts_mov_b32 m0, %0"
                   : "=&s"(keep) : "v"(lane == 63 ? ptp(ws, t) : gdone), "s"(L_CG) : "memory");
    };
    fetchx(0);
    rawbar();
    if (act) {
      wait_cnt(ws, gdone, 1);
      fetch(0); fetch(1);
      asm volatile("s_waitcnt vmcnt(14)" ::: "memory");
    } else vm0();
    RTS(1);
    rawbar();
    for (int s = 0; s < NSEG; ++s) {
      const unsigned char* src = hblk(ws, sel, s, 0) + moff;
      bool todo = act && s + 2 < NSEG - 1, todox = s + 1 < NSEG;
      for (;;) {
        rawbar();
        if (s == 0) peek(0);
        rawbar();
        dma16c(src, m0v);
        vm0();
        rawbar();
        const bool there = __all(lds_r4(L_CG + (lane & 7) * 4) >= (unsigned)(s >> 1) + 2);
        rawbar();
        rawbar();
        if (todo && there) { fetch(s + 2); todo = false; }
        if (todox) { fetchx(s + 1); peek(s + 1); todox = false; }
        const bool bad = __builtin_amdgcn_readfirstlane(lds_r4(L_FLAG)) == ep;
        ++ep;
        if (!bad) break;
      }
      if (todo) {
        wait_cnt(ws, gdone, (s >> 1) + 2);
        fetch(s + 2);
      }
      ARR(s);
    }
    vm0();
    bar();
  } else {
      { Lat W = {rt32(), 6, 0}; (void)lat_wait(W); }
    rawbar();
    Lat L = {get_anchor(ws, sel, id, lane), 0, 0};
    rawbar();
    for (int s = 0; s < NSEG; ++s) {
      const unsigned char* src = hblk(ws, sel, s, 0) + moff;
      unsigned pw = 0;
      for (;;) {
        bar();
        const unsigned est = lat_wait(L);
        rawbar();
        asm volatile("s_mov_b32 m0, %1\n\ts_nop 0\n\tglobal_load_lds_dwordx4 %0, off sc1\n\tglobal_load_lds_dwordx4 %0, off offset:1024 sc1\n\ts_waitcnt vmcnt(0)"
                     :: "v"(src), "s"(m0v) : "memory");
        rawbar();
        lds_w4(L_EST, est);
        if (pw == 0) pw = __builtin_amdgcn_readfirstlane(lds_r4(L_CG + 252)) | 0x80000000u;
        bar(); rawbar();
        const bool bad = __builtin_amdgcn_readfirstlane(lds_r4(L_FLAG)) == ep;
        ++ep;
        EV(bad ? 1 : 0);
        if (!bad) break;
        L.e += RET_E;
      }
      if (pw == 0x80000000u && s < NSEG - 1) { wait_cnt(ws, ptp(ws, s), 1); pw = __builtin_amdgcn_readfirstlane(ld_cnt(ptp(ws, s))); }
      lat_fwd(L, pw & 255, (pw >> 8) & 255);
      ARR(s);
    }
    bar();
    EV_OUT(id);
  }
}

#define XRED(op, v, t) \
  "v_mov_b32 " t ", " v "\n\ts_nop 1\n\tv_permlane16_swap_b32 " v ", " t "\n\t" op " " v ", " v ", " t "\n\t" \
  "v_mov_b32 " t ", " v "\n\ts_nop 1\n\tv_permlane32_swap_b32 " v ", " t "\n\t" op " " v ", " v ", " t "\n\t"
#define MF16 "v_mfma_f32_16x16x32_bf16 "
#define WAIT_CYC(T) \
  "s_memtime vcc\n\ts_waitcnt lgkmcnt(0)\n\t" \
  "s_sub_i32 s70, " T ", vcc_lo\n\ts_max_i32 s70, s70, 0\n\ts_mul_i32 s70, s70, 13\n\ts_lshr_b32 s70, s70, 6\n\t" \
  "s_min_i32 s70, s70, 128\n\ts_lshl_b32 s70, s70, 2\n\ts_sub_i32 s70, 524, s70\n\t" \
  "s_getpc_b64 vcc\n\ts_add_u32 vcc_lo, vcc_lo, s70\n\ts_addc_u32 vcc_hi, vcc_hi, 0\n\ts_setpc_b64 vcc\n\t" \
  ".rept 128\n\ts_nop 0\n\t.endr\n\t"
#define R3_Y_A \
  "ds_read_b128 v[200:203], %[rd]\n\tds_read_b128 v[204:207], %[rd] offset:1024\n\tds_read_b128 v[208:211], %[rd] offset:2048\n\t" \
  "ds_read_b128 v[212:215], %[rd] offset:3072\n\tds_read_b128 v[216:219], %[rd] offset:4096\n\tds_read_b128 v[220:223], %[rd] offset:5120\n\t" \
  "ds_read_b128 v[224:227], %[rd] offset:6144\n\t" \
  "s_waitcnt lgkmcnt(6)\n\t" SMF "v[228:231], v[200:203], %[a0], %[idx]\n\t" SMF "v[232:235], v[200:203], %[b0], %[idx]\n\t" \
  "s_waitcnt lgkmcnt(5)\n\t" SMF "v[228:231], v[204:207], %[a1], %[idx]\n\t" SMF "v[232:235], v[204:207], %[b1], %[idx]\n\t" \
  "s_waitcnt lgkmcnt(4)\n\t" SMF "v[228:231], v[208:211], %[a2], %[idx]\n\t" SMF "v[232:235], v[208:211], %[b2], %[idx]\n\t" \
  "s_waitcnt lgkmcnt(3)\n\t" SMF "v[228:231], v[212:215], %[a3], %[idx]\n\t" SMF "v[232:235], v[212:215], %[b3], %[idx]\n\t" \
    \
  "s_waitcnt lgkmcnt(0)\n\t" \
  "v_readfirstlane_b32 s68, v244\n\t" \
  "v_max3_u32 v244, v216, v217, v218\n\tv_max3_u32 v245, v219, v220, v221\n\tv_max3_u32 v244, v244, v222, v223\n\t" \
  "v_max3_u32 v245, v245, v224, v225\n\tv_max3_u32 v244, v244, v226, v227\n\tv_max_u32 v244, v244, v245\n\t" \
  "v_cmp_eq_u32 s[74:75], -1, v244\n\t" \
  "v_cmp_u_f32 vcc, v228, v229\n\t" \
  "s_or_b64 s[74:75], s[74:75], vcc\n\t" \
  "v_cmp_u_f32 vcc, v234, v235\n\t" \
  "s_or_b64 vcc, s[74:75], vcc\n\t"
#define R3_Y_B \
  SMF "v[228:231], v[216:219], %[a4], %[idx]\n\t" SMF "v[232:235], v[216:219], %[b4], %[idx]\n\t" \
  SMF "v[228:231], v[220:223], %[a5], %[idx]\n\t" SMF "v[228:231], v[224:227], %[a6], %[idx]\n\t" \
  SMF "v[232:235], v[220:223], %[b5], %[idx]\n\t" SMF "v[232:235], v[224:227], %[b6], %[idx]\n\t" \
  "s_nop 1\n\t" \
  "v_permlane32_swap_b32 v228, v229\n\tv_permlane32_swap_b32 v230, v231\n\t" \
  "v_add_f32 v228, v228, v229\n\tv_add_f32 v230, v230, v231\n\t" \
  "v_cvt_pk_bf16_f32 v164, v228, v230\n\t" \
  "s_nop 0\n\t" \
  "v_permlane32_swap_b32 v232, v233\n\tv_permlane32_swap_b32 v234, v235\n\t" \
  "v_add_f32 v232, v232, v233\n\tv_add_f32 v234, v234, v235\n\t" \
  "v_cvt_pk_bf16_f32 v165, v232, v234\n\t" \
  ST(82)
#define R3_POLL \
  WAIT_CYC("s68") \
  "global_load_dwordx4 v[236:239], %[to], s[60:61] sc1\n\t" \
  WAIT_CYC("s69") \
  "global_load_dwordx4 v[240:243], %[tm], s[60:61] sc1\n\t" ST(86)
#define R3_POLL2   \
  "55:\n\ts_memtime vcc\n\ts_waitcnt lgkmcnt(0)\n\ts_sub_i32 s70, s69, vcc_lo\n\ts_cmp_gt_i32 s70, 0\n\ts_cbranch_scc1 55b\n\t" \
  "global_load_dwordx4 v[236:239], %[to], s[60:61] sc1\n\tglobal_load_dwordx4 v[240:243], %[tm], s[60:61] sc1\n\t"
#define R3_S_PHASE(SIG, NS) \
  "s_waitcnt vmcnt(1)\n\t" ST(84) \
  "v_cndmask_b32 v208, 0, v236, s[76:77]\n\tv_cndmask_b32 v209, 0, v237, s[76:77]\n\tv_cndmask_b32 v210, 0, v238, s[76:77]\n\tv_cndmask_b32 v211, 0, v239, s[76:77]\n\t" \
  MF16 "v[220:223], v[160:163], v[236:239], 0\n\t" \
  "v_cndmask_b32 v212, v236, 0, s[76:77]\n\tv_cndmask_b32 v213, v237, 0, s[76:77]\n\tv_cndmask_b32 v214, v238, 0, s[76:77]\n\tv_cndmask_b32 v215, v239, 0, s[76:77]\n\t" \
  MF16 "v[216:219], v[192:195], v[208:211], 0\n\t" \
  "s_nop 0\n\t" \
  MF16 "v[216:219], v[196:199], v[212:215], v[216:219]\n\t" \
  "global_store_dwordx2 %[yo], v[164:165], s[60:61] sc1\n\t"        \
  SIG "s_waitcnt vmcnt(" #NS ")\n\t" ST(88) \
  MF16 "v[232:235], %[selA], v[240:243], 0\n\t" \
  "s_nop 7\n\t" \
  "v_exp_f32 v234, v232\n\tv_exp_f32 v235, v233\n\t" \
  "s_nop 0\n\t" \
  "v_cvt_pk_bf16_f32 v240, v234, v235\n\t" \
  "v_cndmask_b32 v168, 0, v240, s[76:77]\n\tv_cndmask_b32 v169, v240, 0, s[76:77]\n\t" \
  "s_nop 1\n\t" \
  MF16 "v[220:223], v[160:163], v[168:171], v[220:223]\n\t" \
  MF16 "v[244:247], v[164:167], v[168:171], v[216:219]\n\t" \
  "s_nop 6\n\t" \
  "v_rcp_f32 v242, v220\n\t" \
  "v_med3_f32 v241, v220, v172, v173\n\t" \
  "v_cmp_lg_f32 vcc, v241, v220\n\t" \
  "s_cbranch_vccnz 12f\n\t" \
  "11:\n\t" \
  "v_pk_mul_f32 v[244:245], v[244:245], v[242:243] op_sel_hi:[1,0]\n\t" \
  "v_pk_mul_f32 v[246:247], v[246:247], v[242:243] op_sel_hi:[1,0]\n\t" \
  "s_nop 0\n\t" \
  "v_add_f32_dpp v244, v244, v244 row_ror:8 row_mask:0xf bank_mask:0xf\n\tv_add_f32_dpp v245, v245, v245 row_ror:8 row_mask:0xf bank_mask:0xf\n\t" \
  "v_add_f32_dpp v246, v246, v246 row_ror:8 row_mask:0xf bank_mask:0xf\n\tv_add_f32_dpp v247, v247, v247 row_ror:8 row_mask:0xf bank_mask:0xf\n\t" \
  ST(90) "ds_write_b128 %[redw], v[244:247]\n\t" \
  LDSWAIT \
  "s_barrier\n\t" ST(92)
#define R3_S_RARE \
    "12:\n\t" \
  "v_xor_b32 v241, 0x6000, %[to]\n\t" \
  "global_load_dwordx4 v[200:203], v241, s[60:61] sc1\n\t" \
  "s_waitcnt vmcnt(0)\n\t" \
  "v_lshlrev_b32 v208, 16, v200\n\tv_and_b32 v209, 0xffff0000, v200\n\tv_lshlrev_b32 v210, 16, v201\n\tv_and_b32 v211, 0xffff0000, v201\n\t" \
  "v_lshlrev_b32 v212, 16, v202\n\tv_and_b32 v213, 0xffff0000, v202\n\tv_lshlrev_b32 v214, 16, v203\n\tv_and_b32 v215, 0xffff0000, v203\n\t" \
  "v_max3_f32 v240, v208, v209, v210\n\tv_max3_f32 v241, v211, v212, v213\n\tv_max3_f32 v240, v240, v214, v215\n\t" \
  "v_max3_f32 v241, v241, v232, v233\n\tv_max_f32 v240, v240, v241\n\t" \
  XRED("v_max_f32", "v240", "v241") \
  "v_sub_f32 v208, v208, v240\n\tv_sub_f32 v209, v209, v240\n\tv_sub_f32 v210, v210, v240\n\tv_sub_f32 v211, v211, v240\n\t" \
  "v_sub_f32 v212, v212, v240\n\tv_sub_f32 v213, v213, v240\n\tv_sub_f32 v214, v214, v240\n\tv_sub_f32 v215, v215, v240\n\t" \
  "v_sub_f32 v234, v232, v240\n\tv_sub_f32 v235, v233, v240\n\t" \
  "v_exp_f32 v208, v208\n\tv_exp_f32 v209, v209\n\tv_exp_f32 v210, v210\n\tv_exp_f32 v211, v211\n\t" \
  "v_exp_f32 v212, v212\n\tv_exp_f32 v213, v213\n\tv_exp_f32 v214, v214\n\tv_exp_f32 v215, v215\n\t" \
  "v_exp_f32 v234, v234\n\tv_exp_f32 v235, v235\n\ts_nop 1\n\t" \
  "v_cvt_pk_bf16_f32 v200, v208, v209\n\tv_cvt_pk_bf16_f32 v201, v210, v211\n\tv_cvt_pk_bf16_f32 v202, v212, v213\n\tv_cvt_pk_bf16_f32 v203, v214, v215\n\t" \
  "v_cvt_pk_bf16_f32 v240, v234, v235\n\t" \
  "v_cndmask_b32 v208, 0, v200, s[76:77]\n\tv_cndmask_b32 v209, 0, v201, s[76:77]\n\tv_cndmask_b32 v210, 0, v202, s[76:77]\n\tv_cndmask_b32 v211, 0, v203, s[76:77]\n\t" \
  "v_cndmask_b32 v212, v200, 0, s[76:77]\n\tv_cndmask_b32 v213, v201, 0, s[76:77]\n\tv_cndmask_b32 v214, v202, 0, s[76:77]\n\tv_cndmask_b32 v215, v203, 0, s[76:77]\n\t" \
  "v_cndmask_b32 v168, 0, v240, s[76:77]\n\tv_cndmask_b32 v169, v240, 0, s[76:77]\n\ts_nop 1\n\t" \
  MF16 "v[220:223], v[160:163], v[200:203], 0\n\t" \
  MF16 "v[220:223], v[160:163], v[168:171], v[220:223]\n\t" \
  MF16 "v[244:247], v[192:195], v[208:211], 0\n\t" \
  MF16 "v[244:247], v[196:199], v[212:215], v[244:247]\n\t" \
  MF16 "v[244:247], v[164:167], v[168:171], v[244:247]\n\t" \
  "s_nop 7\n\t" \
  "v_rcp_f32 v242, v220\n\ts_nop 0\n\t" \
  "s_branch 11b\n\t"
#define R3_PREP "s_lshl_b32 s44, s65, 2\n\ts_add_i32 s44, s44, s67\n\ts_lshl_b32 s45, s66, 2\n\ts_sub_i32 s45, 108, s45\n\t"
#define R3_FWD "s_add_i32 s66, s66, s47\n\ts_add_i32 s65, s65, s46\n\ts_cmp_ge_i32 s66, 24\n\ts_cselect_b32 s70, 24, 0\n\ts_addc_u32 s65, s65, 0\n\ts_sub_i32 s66, s66, s70\n\t"
#define R3_RT "s_memrealtime vcc\n\ts_waitcnt lgkmcnt(0)\n\ts_sub_i32 s70, s44, vcc_lo\n\ts_cmp_lt_i32 s70, 1\n\t"
#define R3_TW_WAIT \
  R3_RT "s_cbranch_scc1 40f\n\t" \
  "20:\n\t" R3_RT "s_cbranch_scc1 21f\n\t" R3_RT "s_cbranch_scc1 21f\n\t" R3_RT "s_cbranch_scc1 21f\n\t" R3_RT "s_cbranch_scc0 20b\n\t" \
  "21:\n\t" \
  "s_getpc_b64 vcc\n\ts_add_u32 vcc_lo, vcc_lo, s45\n\ts_addc_u32 vcc_hi, vcc_hi, 0\n\ts_setpc_b64 vcc\n\t" \
  ".rept 24\n\ts_nop 0\n\t.endr\n\t" \
  "22:\n\t"
#define R3_TW_LATE \
  "40:\n\t" \
  "s_cmp_eq_u32 s70, 0\n\ts_cbranch_scc1 22b\n\t" \
  "41:\n\ts_add_i32 s65, s65, 26\n\ts_add_i32 s44, s44, 4*26\n\ts_add_i32 s70, s70, 4*26\n\ts_cmp_lt_i32 s70, 0\n\ts_cbranch_scc1 41b\n\t" \
  "s_branch 20b\n\t"
#define R3_STEP(op, uop)   \
  op " %[moff], 0x8000, %[moff]\n\t" op " %[to], 0x8000, %[to]\n\t" op " %[tm], 0x8000, %[tm]\n\t" op " %[sto], 0x8000, %[sto]\n\t" \
  uop " %[uo], 0x4000, %[uo]\n\t" uop " %[yo], 0x1000, %[yo]\n\t"
#define R3_FLIP \
  "s_and_b32 s70, s62, 15\n\ts_cselect_b32 s70, 0, 0x4000\n\ts_add_i32 s71, s62, 1\n\ts_and_b32 s71, s71, 15\n\ts_cselect_b32 s71, 0, 0x4000\n\t" \
  "v_xor_b32 %[moff], s70, %[moff]\n\tv_xor_b32 %[to], s70, %[to]\n\tv_xor_b32 %[tm], s70, %[tm]\n\tv_xor_b32 %[sto], s71, %[sto]\n\t"
#define ULOADS "global_load_dwordx4 v[192:195], %[uo], s[60:61] sc1\n\tglobal_load_dwordx4 v[196:199], %[uo], s[60:61] offset:1024 sc1\n\t"
#define R3_DMA "global_load_lds_dwordx4 %[moff], s[60:61] sc1\n\t"
#define R3_TW_PT "s_lshl_b32 s70, s62, 7\n\ts_add_i32 s70, s70, 0x420080\n\tv_mov_b32 v178, s70\n\tglobal_load_dword v177, v178, s[60:61] sc1\n\t"
#define R3_TW_NEXT "v_readfirstlane_b32 s70, v177\n\ts_cmp_eq_u32 s70, 0\n\ts_cbranch_scc1 45f\n\t" \
  "19:\n\ts_and_b32 s46, s70, 0xff\n\ts_bfe_u32 s47, s70, 0x80008\n\ts_lshr_b32 s48, s70, 16\n\t"
#define R3_TW_OOL        \
  "45:\n\ts_mov_b32 s71, 0x40000\n\t" \
  "46:\n\tglobal_load_dword v177, v178, s[60:61] sc1\n\ts_waitcnt vmcnt(0)\n\tv_readfirstlane_b32 s70, v177\n\t" \
  "s_cmp_eq_u32 s70, 0\n\ts_cbranch_scc0 19b\n\ts_sub_i32 s71, s71, 1\n\ts_cmp_gt_i32 s71, 0\n\ts_cbranch_scc1 46b\n\ts_movk_i32 s70, 40\n\ts_branch 19b\n\t"
#define R3_TW_TIME \
  TBURST \
  "s_sub_i32 s4, s4, s44\n\ts_add_i32 s4, s4, 4\n\ts_mul_i32 s4, s4, 93\n\ts_ashr_i32 s4, s4, 2\n\ts_sub_i32 s58, s58, s4\n\t" \
  "s_lshl_b32 s4, s66, 2\n\ts_add_i32 s58, s58, s4\n\ts_sub_i32 s4, s58, s51\n\ts_mov_b32 s51, s58\n\ts_add_i32 s58, s58, s48\n\t" \
  "v_mov_b32 v200, s58\n\tv_mov_b32 v201, s4\n\tds_write_b64 v188, v[200:201] offset:12296\n\ts_waitcnt lgkmcnt(0)\n\t"
#define R3_WARM(beg, len) \
  "s_getpc_b64 vcc\n\t9:\n\ts_add_u32 vcc_lo, vcc_lo, " beg "-9b\n\ts_addc_u32 vcc_hi, vcc_hi, 0\n\tv_lshlrev_b32 v200, 3, %[rd]\n\tv_min_u32 v200, " len "-64, v200\n\t" \
  "global_load_dword v201, v200, vcc\n\tglobal_load_dword v202, v200, vcc offset:64\n\ts_waitcnt vmcnt(0)\n\t"
#define R3_LOOP(WARM, DMA, TWWAIT, CNTLOAD, CNTPAY, CNTPACK, TWTIME, NW, CNTSIG, NS, TWFWD, FIN, CNTCHK, OUTOFLINE) \
  "s_mov_b64 s[60:61], %[wsp]\n\ts_mov_b32 s62, 0\n\ts_mov_b32 s63, 0\n\ts_mov_b32 s64, 0x10000\n\ts_mov_b32 s78, 0\n\t" \
  "s_mov_b32 s76, 0x00ff00ff\n\ts_mov_b32 s77, s76\n\t" \
  "s_and_b32 s46, %[p0], 0xff\n\ts_bfe_u32 s47, %[p0], 0x80008\n\ts_lshr_b32 s48, %[p0], 16\n\ts_mov_b32 s65, 0\n\ts_mov_b32 s66, 0\n\t" \
  "s_movk_i32 s49, 250\n\ts_lshl_b32 s50, %[p1], 10\n\t" \
  "v_mov_b32 v188, 0\n\tv_mov_b32 v160, 0x3f803f80\n\tv_mov_b32 v161, v160\n\tv_mov_b32 v162, v160\n\tv_mov_b32 v163, v160\n\t" \
  "v_mov_b32 v166, 0\n\tv_mov_b32 v167, 0\n\tv_mov_b32 v170, 0\n\tv_mov_b32 v171, 0\n\tv_mov_b32 v172, 0x1f800000\n\tv_mov_b32 v173, 0x71800000\n\t" \
  R3_PREP \
  WARM \
  "10:\n\t" \
  TWWAIT \
  "s_mov_b32 m0, s50\n\t" \
  "s_waitcnt lgkmcnt(0)\n\ts_barrier\n\t" \
  "s_memtime s[72:73]\n\t" \
  DMA \
  "ds_read_b32 v200, v188 offset:12288\n\t" \
  "s_waitcnt lgkmcnt(0)\n\t" \
  "v_readfirstlane_b32 s74, v200\n\t" \
  "s_cmp_eq_u32 s74, s63\n\ts_cbranch_scc1 50f\n\t" \
  "s_add_i32 s63, s63, 1\n\t" \
  "v_mov_b32 v228, 0\n\tv_mov_b32 v229, 0\n\tv_mov_b32 v230, 0\n\tv_mov_b32 v231, 0\n\t" \
  "v_mov_b32 v232, 0\n\tv_mov_b32 v233, 0\n\tv_mov_b32 v234, 0\n\tv_mov_b32 v235, 0\n\t" \
  ULOADS \
  CNTLOAD TWTIME \
  "s_waitcnt vmcnt(" #NW ")\n\t" \
  "s_barrier\n\t" ST(80) \
  CNTPAY "ds_read_b32 v244, v188 offset:12296\n\t" \
  R3_Y_A \
  "s_cbranch_vccnz 60f\n\t" \
  "14:\n\t" \
  CNTPACK "s_add_i32 s69, s68, s49\n\t" \
  "s_cmp_eq_u32 s62, 63\n\ts_cbranch_scc1 98f\n\t" \
  R3_POLL R3_Y_B \
  TWFWD \
  "15:\n\t" \
  R3_S_PHASE(CNTSIG, NS) FIN CNTCHK \
  "s_add_i32 s62, s62, 1\n\t" \
  R3_STEP("v_add_u32", "v_add_u32") \
  R3_FLIP DIAG_R3 \
  "s_branch 10b\n\t" \
  R3_S_RARE \
    \
  "50:\n\t" \
  "s_sub_i32 s64, s64, 1\n\ts_add_i32 s63, s63, 1\n\ts_bitset1_b32 s78, 28\n\t" \
  R3_FLIP R3_STEP("v_subrev_u32", "v_subrev_u32") \
  "s_sub_i32 s62, s62, 1\n\t" \
  "s_memtime vcc\n\ts_waitcnt lgkmcnt(0)\n\t" \
  "s_sub_i32 s70, s69, s68\n\ts_cmp_eq_u32 s70, s49\n\ts_cselect_b32 s70, 256, 0\n\ts_sub_i32 s69, s69, s70\n\t"       \
  "53:\n\ts_add_i32 s68, s68, 93*26\n\ts_add_i32 s69, s69, 93*26\n\ts_add_i32 s65, s65, 26\n\t" \
  "s_sub_i32 s70, s69, vcc_lo\n\ts_cmp_lt_i32 s70, 0\n\ts_cbranch_scc1 53b\n\t" \
  R3_PREP R3_POLL2 \
  "s_branch 15b\n\t" \
    \
  "60:\n\ts_bitset1_b32 s78, 29\n\ts_sub_i32 s64, s64, 1\n\ts_cmp_gt_i32 s64, 0\n\ts_cbranch_scc0 14b\n\t" \
  "s_add_i32 s65, s65, 26\n\t" R3_PREP "s_branch 10b\n\t" \
  OUTOFLINE \
  "98:\n\t" R3_Y_B \
  "global_store_dwordx2 %[yo], v[164:165], s[60:61] sc1\n\t" \
  "s_waitcnt vmcnt(0)\n\ts_barrier\n\t"
#define R3_FIN \
  "ds_read_b128 v[200:203], %[redr]\n\tds_read_b128 v[204:207], %[redr] offset:512\n\t" \
  "ds_read_b128 v[208:211], %[redr] offset:1024\n\tds_read_b128 v[212:215], %[redr] offset:1536\n\t" \
  "s_waitcnt lgkmcnt(2)\n\t" \
  "v_pk_add_f32 v[200:201], v[200:201], v[204:205]\n\tv_pk_add_f32 v[202:203], v[202:203], v[206:207]\n\t" \
  "s_waitcnt lgkmcnt(1)\n\t" \
  "v_pk_add_f32 v[200:201], v[200:201], v[208:209]\n\tv_pk_add_f32 v[202:203], v[202:203], v[210:211]\n\t" \
  "s_waitcnt lgkmcnt(0)\n\t" \
  "v_pk_add_f32 v[200:201], v[200:201], v[212:213]\n\tv_pk_add_f32 v[202:203], v[202:203], v[214:215]\n\t" \
  "s_nop 1\n\t" \
  "v_permlane32_swap_b32 v200, v202\n\tv_permlane32_swap_b32 v201, v203\n\t" \
  "v_add_f32 v200, v200, v202\n\tv_add_f32 v201, v201, v203\n\t" \
  "v_cvt_pk_bf16_f32 v202, v200, v201\n\t" \
  "v_cmpx_o_f32 vcc, v200, v201\n\t" \
  "global_store_dword %[sto], v202, s[60:61] sc1\n\t" ST(94) \
  "s_memtime s[74:75]\n\t" \
  "s_mov_b64 exec, -1\n\t" \
  "v_mov_b32 v200, -1\n\tv_mov_b32 v201, -1\n\tv_mov_b32 v202, -1\n\tv_mov_b32 v203, -1\n\t" \
  "ds_write_b128 %[redr], v[200:203]\n\tds_write_b128 %[redr], v[200:203] offset:512\n\t" \
  "ds_write_b128 %[redr], v[200:203] offset:1024\n\tds_write_b128 %[redr], v[200:203] offset:1536\n\t" \
  "s_waitcnt lgkmcnt(0)\n\ts_sub_u32 s70, s74, s69\n\tv_mov_b32 v200, s70\n\tds_write_b32 v188, v200 offset:12308\n\t" \
  "s_cmp_eq_u64 vcc, -1\n\ts_cbranch_scc0 80f\n\t" \
  "16:\n\t"
#define R3_FIN_OOL \
  "80:\n\ts_cmp_gt_i32 s64, 0\n\ts_cbranch_scc0 81f\n\t" \
  "v_mov_b32 v200, s63\n\tds_write_b32 v188, v200 offset:12288\n\ts_branch 16b\n\t" \
  "81:\n\tv_mov_b32 v200, 0x7fc07fc0\n\tglobal_store_dword %[sto], v200, s[60:61] sc1\n\ts_branch 16b\n\t"
#define R3_CNT_PAY "ds_read_b32 v174, v188 offset:12300\n\tds_read_b32 v175, v188 offset:12308\n\t"
#define R3_CNT_PACK \
  "v_min_u32 v175, 0xfff, v175\n\tv_min_u32 v174, 0xffff, v174\n\tv_lshl_or_b32 v176, v174, 12, v175\n\tv_or_b32 v176, s78, v176\n\ts_mov_b32 s78, 0\n\t"
#define R3_CNT_SIG "global_store_dword v187, v176, s[60:61] sc1\n\t"
#define R3_CNT_LOAD "v_mov_b32 v187, s43\n\ts_mov_b64 s[38:39], s[40:41]\n\tv_cndmask_b32 v144, %[uo], v186, s[40:41]\n\tglobal_load_dword v145, v144, s[60:61] sc1\n\t"
#define R3_CNT_CHK "v_readfirstlane_b32 s43, %[tm]\n\ts_and_b32 s43, s43, 0xffffe000\n\ts_xor_b32 s43, s43, s42\n\t" \
  "v_cndmask_b32 v189, v189, v145, s[38:39]\n\ts_mov_b64 s[38:39], 0\n\t" \
  "s_add_i32 s70, s62, 3\n\ts_lshr_b32 s70, s70, 1\n\tv_cmp_gt_u32 vcc, s70, v189\n\ts_cbranch_vccnz 85f\n\t17:\n\t" \
  "s_add_i32 s70, s70, 1\n\ts_min_u32 s70, s70, 32\n\tv_cmp_gt_u32 vcc, s70, v189\n\ts_cmp_lg_u64 vcc, 0\n\ts_cselect_b64 s[40:41], -1, 0\n\t"
#define R3_CNT_OOL        \
  "85:\n\ts_mov_b32 s71, 0x40000\n\t" \
  "86:\n\tglobal_load_dword v189, v186, s[60:61] sc1\n\ts_waitcnt vmcnt(0)\n\t" \
  "v_cmp_gt_u32 vcc, s70, v189\n\ts_cbranch_vccz 17b\n\ts_sleep 2\n\ts_sub_i32 s71, s71, 1\n\ts_cmp_gt_i32 s71, 0\n\ts_cbranch_scc1 86b\n\ts_branch 17b\n\t"
#define DIAG_R3
#define DIAG_R3_END
#define DIAG_R3_OUT
#define R3_OUTS [moff] "+v"(moff), [to] "+v"(to), [tm] "+v"(tm), [sto] "+v"(sto), [uo] "+v"(uo), [yo] "+v"(yo) DIAG_R3_OUT
#define R3_INS R1_WOPS, R1_WOPS2, [idx] "v"(idx), [rd] "v"(rd), [redw] "v"(redw), [selA] "v"(selA), [wsp] "s"(ws), [p0] "s"(p0), [p1] "s"(p1)
#define R3_CLOB CLOB_V200_247, "v144","v145","v146","v147","v148","v149","v150","v151", "v160","v161","v162","v163","v164","v165","v166","v167","v168","v169","v170","v171","v172","v173","v174","v175","v176","v177","v178", "v179","v180","v181","v182","v183","s96","s97","v184","v185","v186","v187","v188","v189","v190","v191","v192","v193","v194","v195","v196","v197","v198","v199", \
  "s38","s39","s40","s41","s42","s43","s44","s45","s46","s47","s48","s49","s50","s51","s60","s61","s62","s63","s64","s65","s66","s67","s68","s69","s70","s71", \
  "s72","s73","s74","s75","s76","s77","s78","vcc","scc","memory" ST_CLOB
#define STR2(x) #x
#define STR(x) STR2(x)

template <int KIND>
DEV void r3_body(unsigned char* __restrict__ ws, int v, int wave, int lane, unsigned sel TUNE_ARG) {
  const int g = lane >> 4, hs = (lane >> 3) & 1, i8 = lane & 7;
  const int idx = (lane & 8) ? 0xEEEE : 0x4444;
  const int dw = wave < 7 ? wave : 6;
  const unsigned rd = lane * 16;
  u8v w0[7], w1[7];
  {
    const unsigned char* p = ws + O_WB + (size_t)(2 * wave * 28 + v) * 14336 + lane * 16;
#pragma unroll
    for (int k = 0; k < 7; ++k) {
      w0[k] = cat8(*(const u4v*)(p + k * 2048), *(const u4v*)(p + k * 2048 + 1024));
      w1[k] = cat8(*(const u4v*)(p + 28 * 14336 + k * 2048), *(const u4v*)(p + 28 * 14336 + k * 2048 + 1024));
    }
#pragma unroll
    for (int k = 0; k < 7; ++k) asm volatile("" : "+v"(w0[k]), "+v"(w1[k]));
  }
  RTS(1);
  if (KIND == 3 && lane == 0) lds_w4(L_FLAG, SENT);
  *(LDS u2v*)(size_t)(L_RED + wave * 512 + lane * 8) = (u2v){SENT, SENT};
  RTS(2);
  unsigned moff = hoff(sel, 0, 0) + dw * 1024 + g * 256 + i8 * 32 + hs * 16;
  unsigned to = hoff(sel, 0, 2) + (2 * wave + hs) * 512 + (g >> 1) * 256 + i8 * 32 + (g & 1) * 16;
  unsigned tm = hoff(sel, 64, K_SMK) + (2 * wave + hs) * 512 + g * 128 + i8 * 16;
  u4v selA = {0u, 0u, 0u, 0u};
  if (((lane & 3) >> 1) == 0) {
    const int gq = (lane >> 2) & 3, j = 4 * (gq & 1) + (gq >> 1) + 2 * (lane & 1);
    const unsigned one = (j & 1) ? 0x3f800000u : 0x00003f80u;
    selA = (u4v){(j >> 1) == 0 ? one : 0u, (j >> 1) == 1 ? one : 0u, (j >> 1) == 2 ? one : 0u, (j >> 1) == 3 ? one : 0u};
  }
  unsigned sto = hoff(sel, 1, 0) + v * 256 + i8 * 32 + ((lane >> 3) & 3) * 8 + (lane >> 5) * 4;
  unsigned uo = (unsigned)O_U + v * U_RUN + 2 * wave * 1024 + lane * 16;
  unsigned yo = (unsigned)O_YG + v * Y_RUN + wave * 512 + lane * 8;
  const unsigned redw = L_RED + hs * 4096 + wave * 512 + (g * 8 + i8) * 16;
  const unsigned redr = L_RED + (lane >> 5) * 2048 + (lane & 31) * 16;
  const unsigned p1 = dw;
  {
    const unsigned o = lane < 8 ? yo : lane < 16 ? yo + 63 * 4096 : lane < 24 ? uo : lane < 32 ? uo + 63 * 16384 : lane < 40 ? hoff(sel, 70, 0) : lane < 48 ? hoff(sel, 1, 0) + 7168 : (unsigned)O_FL + 0x3ff00;
    (void)ld_cnt((const unsigned*)(ws + (o & ~3u)));
  }
  int anchor = 0;
  unsigned p0 = 0;
  if (KIND == 3) asm volatile(R3_WARM(".Lr3tw_beg", ".Lr3tw_end-.Lr3tw_beg") :: [rd] "v"(rd) : "v200", "v201", "v202", "vcc", "scc", "memory");
  if (KIND == 3) { wait_cnt(ws, ptp(ws, 0), 1); p0 = __builtin_amdgcn_readfirstlane(ld_cnt(ptp(ws, 0))); }
  bar();
  if (KIND == 3) anchor = get_anchor(ws, sel, 64 + v, lane);
  if (KIND == 2) wait_cnt(ws, doneU(ws, 4 * (v >> 1) + (lane & 3)), 1);
  if (KIND == 0) {
    asm volatile(R3_LOOP(R3_WARM("10f", "98f-10f"), R3_DMA, "", "", "", "", "", 2, "", 1, "", R3_FIN, "", R3_FIN_OOL) DIAG_R3_END : R3_OUTS : R3_INS, [redr] "v"(redr) : R3_CLOB);
  } else if (KIND == 1) {
    asm volatile(R3_LOOP(R3_WARM("10f", "98f-10f"), R3_DMA, "", "", "", "", "", 2, "", 1, "", "", "", "") DIAG_R3_END : R3_OUTS : R3_INS : R3_CLOB);
  } else if (KIND == 2) {
    const unsigned cc = ((unsigned)(3 - K_SMK) << 13) | (unsigned)(v * 128);
    const unsigned uc = (unsigned)O_FL + (C_W + 2 * (56 + 4 * (v >> 1) + (lane & 3))) * 64;
    asm volatile("s_mov_b64 s[40:41], -1\n\ts_mov_b32 s42, %[cc]\n\ts_and_b32 s43, s42, 0x1fff\n\ts_add_i32 s43, s43, 0x43e000\n\tv_mov_b32 v186, %[uc]\n\t"
                 R3_LOOP(R3_WARM("10f", "98f-10f"), R3_DMA, "", R3_CNT_LOAD, R3_CNT_PAY, R3_CNT_PACK, "", 3, R3_CNT_SIG, 2, "", "", R3_CNT_CHK, R3_CNT_OOL) DIAG_R3_END : R3_OUTS : R3_INS, [cc] "s"(cc), [uc] "v"(uc) : R3_CLOB);
  } else {
    asm volatile("s_mov_b32 s67, %[anc]\n\t" R3_LOOP(".Lr3tw_beg:\n\t", "", R3_TW_WAIT, R3_TW_PT, "", "", R3_TW_TIME, 3, "", 1, R3_FWD R3_PREP, "", R3_TW_NEXT, R3_TW_LATE R3_TW_OOL ".Lr3tw_end:\n\t") DIAG_R3_END : R3_OUTS : R3_INS, [anc] "s"(anchor) : R3_CLOB, TBURST_CLOB);
  }
  if (KIND == 2 && lane < 2) st4c_asm(flagY(ws, sel, NSEG - 2 + lane, v), 0u);
  RTS(3);
}
DEV void role_r3(unsigned char* __restrict__ ws, int v, int wave, int lane, unsigned sel TUNE_ARG) {
  if (wave == 0) r3_body<0>(ws, v, wave, lane, sel TUNE_PASS);
  else if (wave == 6) r3_body<2>(ws, v, wave, lane, sel TUNE_PASS);
  else if (wave == 7) r3_body<3>(ws, v, wave, lane, sel TUNE_PASS);
  else r3_body<1>(ws, v, wave, lane, sel TUNE_PASS);
}

#define PF_LAG 4
DEV void role_pf(unsigned char* __restrict__ ws, int k, int wave, int lane, unsigned sel) {
  constexpr int LAG = PF_LAG;
  const rsrc_t ru = mkrsrc(ws + O_U, 30u << 20);
  unsigned have = 0;
  for (int t = 1; t < NSEG - 1; ++t) {
    if (wave == 0) {
      unsigned skip = 0;
      const unsigned need = (t >> 1) + 1;
      if (__any(have < need)) { wait_cnt(ws, doneU(ws, min(lane, 55)), need); have = ld_cnt(doneU(ws, min(lane, 55))); }
      if (t >= LAG) wait_flags(ws, flagY(ws, sel, t - LAG, k));
      else if (t >= LAG - 2) {
        wait_cnt(ws, anchorp(ws, 28 + k), 1);
        const unsigned at = (__builtin_amdgcn_readfirstlane(ld_cnt(anchorp(ws, 28 + k))) & ~1u) + (t - LAG + 2) * 264;
        skip = (int)(((unsigned)rt32() << 1) - at) > 40;
        for (int it = 0; it < 4096 && (int)(at - ((unsigned)rt32() << 1)) > 0; ++it) __builtin_amdgcn_s_sleep(1);
      }
      lds_w4((t & 1) * 4, skip);
    }
    bar();
    if (__builtin_amdgcn_readfirstlane(lds_r4((t & 1) * 4))) continue;
    u4v r[14];
#pragma unroll
    for (int i = 0; i < 14; ++i) { const int n = (k * 8 + wave) * 14 + i; r[i] = ldc16(ru, (n >> 4) * U_RUN + (t * 16 + (n & 15)) * 1024 + lane * 16); }
#pragma unroll
    for (int i = 0; i < 14; ++i) asm volatile("" :: "v"(r[i]));
  }
}

__global__ void __launch_bounds__(512, 2)
main_kernel(const unsigned short* __restrict__ x, const unsigned short* __restrict__ mem0, const unsigned short* __restrict__ b1, const unsigned short* __restrict__ b2,
            unsigned short* __restrict__ out, unsigned char* __restrict__ ws TUNE_ARG) {
  const int b = blockIdx.x, lane = threadIdx.x & 63;
  const int wave = __builtin_amdgcn_readfirstlane(threadIdx.x >> 6);
  RTS(0);
  const unsigned long long tab = *(const unsigned long long*)(ws + O_SEL);
  unsigned sel = 0;
#pragma unroll
  for (int j = 7; j >= 0; --j) if ((tab >> (8 * j + 7)) & 1) sel = (unsigned)(tab >> (8 * j)) & 15u;
  const int xc = (b + (int)(tab >> 4)) & 7, slot = b >> 3;
  if (xc == 0 || xc == 2) {
    role_r1(ws, mem0, (xc >> 1) * 32 + slot, wave, lane, sel TUNE_PASS);
  } else if (xc == 1) {
    if (slot < 28) role_r3(ws, slot, wave, lane, sel TUNE_PASS);
    else role_pf(ws, slot - 28, wave, lane, sel);
  } else {
    const int k = slot * 5 + (xc - 3);
    if (k < 56) role_w1<true>(ws, k, wave, lane);
    else if (k < 112) role_w1<false>(ws, k - 56, wave, lane);
    else {
      if (k < 144) role_ffn(ws, x, b1, b2, k - 112, wave, lane);
      else role_stt(ws, (k - 144) >> 1, k & 1, wave, lane, sel);
      role_w3(ws, out, (159 - k) >> 2, k & 3, wave, lane, sel);
    }
  }
}

extern "C" void kernel_launch(void* const* d_in, const int* in_sizes, int n_in, void* d_out, int out_size, void* d_ws,
                              size_t ws_size, hipStream_t stream) {
  unsigned char* ws = (unsigned char*)(((uintptr_t)d_ws + 2 * MB - 1) & ~(uintptr_t)(2 * MB - 1));
  const unsigned short* x = (const unsigned short*)d_in[0];
  constexpr int LDS_MAIN = 132 * 1024;
  (void)hipFuncSetAttribute((const void*)main_kernel, hipFuncAttributeMaxDynamicSharedMemorySize, LDS_MAIN);
  constexpr int LDS_PREP = 152 * 1024;
  (void)hipFuncSetAttribute((const void*)prep_kernel, hipFuncAttributeMaxDynamicSharedMemorySize, LDS_PREP);
  prep_kernel<<<256, 512, LDS_PREP, stream>>>(x, (const unsigned short*)d_in[2], (const unsigned short*)d_in[3],
                                           (const unsigned short*)d_in[4], (const unsigned short*)d_in[5], (const unsigned short*)d_in[8],
                                           (const unsigned short*)d_in[10], ws);
  main_kernel<<<256, 512, LDS_MAIN, stream>>>(x, (const unsigned short*)d_in[1], (const unsigned short*)d_in[9], (const unsigned short*)d_in[11], (unsigned short*)d_out, ws TUNE_PASS);
}
```
